```python
import torch

def _softmax(x, axis=-1):
    return torch.softmax(x, dim=axis)
def _gelu(x):
    return torch.nn.functional.gelu(x, approximate="tanh")
def _silu(x):
    return torch.nn.functional.silu(x)
def _sigmoid(x):
    return torch.sigmoid(x)
def _layernorm(x, axis=-1, eps=1e-5):
    m = x.mean(dim=axis, keepdim=True)
    v = x.var(dim=axis, keepdim=True, unbiased=False)
    return (x - m) / torch.sqrt(v + eps)
def _scatter_add(tgt, idx, src, axis=0):
    return tgt.clone().index_add_(axis, idx, src)
def _segment_sum(src, seg, n):
    out = torch.zeros((n,) + src.shape[1:], dtype=src.dtype)
    return out.index_add_(0, seg, src)
def _pad(x, pw):
    flat = tuple(v for pair in reversed(pw) for v in pair)
    return torch.nn.functional.pad(x, flat)

kernel_name = 'sequential_preLN__sliding_block__mixer_s_mlp__prompt'

def setup_inputs():

    _w = (lambda a: (lambda t: t.to(torch.bfloat16) if t.is_floating_point() else t)(torch.as_tensor(a)))
    def _A_setup():
        B, S, D = 1, 2048, 448
        return {
            "x": _w((lambda *s: torch.randn(*s, dtype=torch.bfloat16) * 0.05)(B, S, D)),
            "wqkv": _w((lambda *s: torch.randn(*s, dtype=torch.bfloat16) * 0.05)(D, 3 * D)),
            "wo": _w((lambda *s: torch.randn(*s, dtype=torch.bfloat16) * 0.05)(D, D)),
        }
    _A_in = _A_setup()

    def _B_setup():
        B, S, D, F = 1, 2048, 448, 1792
        return {
            "x":  _w((lambda *s: torch.randn(*s, dtype=torch.bfloat16) * 0.05)(B, S, D)),
            "w1": _w((lambda *s: torch.randn(*s, dtype=torch.bfloat16) * 3.2)(D, D) * (D ** -0.5)),
            "w2": _w((lambda *s: torch.randn(*s, dtype=torch.bfloat16) * 3.2)(D, D) * (D ** -0.5)),
            "w3": _w((lambda *s: torch.randn(*s, dtype=torch.bfloat16) * 3.2)(D, F) * (D ** -0.5)),
            "w4": _w((lambda *s: torch.randn(*s, dtype=torch.bfloat16) * 3.2)(F, D) * (F ** -0.5)),
        }
    _B_in = _B_setup()

    return {
        "A_x": _A_in["x"],
        "A_wqkv": _A_in["wqkv"],
        "A_wo": _A_in["wo"],
        "B_x": _B_in["x"],
        "B_w1": _B_in["w1"],
        "B_w2": _B_in["w2"],
        "B_w3": _B_in["w3"],
        "B_w4": _B_in["w4"],
    }

def reference(A_x, A_wqkv, A_wo, B_x, B_w1, B_w2, B_w3, B_w4):
    def _A_ref(x, wqkv, wo):
        B, S, D = x.shape
        H = 16; Dh = D // H; W = 256
        qkv = (lambda x, *d: x.permute(*d))((x @ wqkv).reshape(B, S, 3, H, Dh), 2, 0, 3, 1, 4)
        q, k, v = qkv[0], qkv[1], qkv[2]
        scores = torch.einsum("bhqd,bhkd->bhqk", q, k) * (Dh ** -0.5)
        idx = torch.arange(S)
        mask = (idx[:, None] - idx[None, :] >= 0) & (idx[:, None] - idx[None, :] < W)
        scores = torch.where(mask[None, None], scores, -1e9)
        p = _softmax(scores, axis=-1)
        o = torch.einsum("bhqk,bhkd->bhqd", p, v)
        return (lambda x, *d: x.permute(*d))(o, 0, 2, 1, 3).reshape(B, S, D) @ wo

    def _B_ref(x, w1, w2, w3, w4):
        B, S, D = x.shape
        h = _gelu(_layernorm(x, axis=-1) @ w1)
        c = D // 4
        g0 = _pad(h[:, :, 0*c:1*c], ((0, 0), (1, 0), (0, 0)))[:, :S, :]
        g1 = _pad(h[:, :, 1*c:2*c], ((0, 0), (0, 1), (0, 0)))[:, 1:, :]
        g2 = _pad(h[:, :, 2*c:3*c], ((0, 0), (2, 0), (0, 0)))[:, :S, :]
        g3 = _pad(h[:, :, 3*c:   ], ((0, 0), (0, 2), (0, 0)))[:, 2:, :]
        hs = torch.concatenate([g0, g1, g2, g3], axis=-1) @ w2
        x1 = x + hs
        h2 = _gelu(_layernorm(x1, axis=-1) @ w3) @ w4
        return x1 + h2

    x = A_x
    h = x + _A_ref(_layernorm(x, axis=-1), A_wqkv, A_wo)
    h2 = h + _B_ref(_layernorm(h, axis=-1), B_w1, B_w2, B_w3, B_w4)
    return h2
```

```cpp
#include <hip/hip_runtime.h>
#include <hip/hip_bf16.h>
#include <stdint.h>

typedef __attribute__((ext_vector_type(8))) short bf16x8;
typedef __attribute__((ext_vector_type(4))) float f32x4;
typedef unsigned short u16;

constexpr int S = 2048, D = 448, H = 16, DH = 28, WIN = 256, F = 1792;

__device__ __forceinline__ float bf2f(u16 b) { return __uint_as_float(((unsigned)b) << 16); }
__device__ __forceinline__ u16 f2bf(float f) {
    __hip_bfloat16 h = __float2bfloat16(f);
    return *reinterpret_cast<u16*>(&h);
}
__device__ __forceinline__ float gelu_t(float x) {
    const float k0 = 0.7978845608028654f, k1 = 0.044715f;
    float u = k0 * (x + k1 * x * x * x);
    float e = __expf(2.f * u);
    float t = 1.f - 2.f / (e + 1.f);
    return 0.5f * x * (1.f + t);
}

__global__ void k_ln(const u16* __restrict__ in, u16* __restrict__ out1, u16* __restrict__ out2) {
    int row = blockIdx.x * 4 + (threadIdx.x >> 6), lane = threadIdx.x & 63;
    float v[7];
    float s = 0.f;
    for (int i = 0; i < 7; i++) { v[i] = bf2f(in[row * D + lane + 64 * i]); s += v[i]; }
    for (int o = 32; o; o >>= 1) s += __shfl_xor(s, o);
    float m = s / D, q = 0.f;
    for (int i = 0; i < 7; i++) { v[i] -= m; q += v[i] * v[i]; }
    for (int o = 32; o; o >>= 1) q += __shfl_xor(q, o);
    float r = 1.f / sqrtf(q / D + 1e-5f);
    for (int i = 0; i < 7; i++) { u16 b = f2bf(v[i] * r); out1[row * D + lane + 64 * i] = b; v[i] = bf2f(b); }
    if (out2) {
        s = 0.f;
        for (int i = 0; i < 7; i++) s += v[i];
        for (int o = 32; o; o >>= 1) s += __shfl_xor(s, o);
        m = s / D; q = 0.f;
        for (int i = 0; i < 7; i++) { v[i] -= m; q += v[i] * v[i]; }
        for (int o = 32; o; o >>= 1) q += __shfl_xor(q, o);
        r = 1.f / sqrtf(q / D + 1e-5f);
        for (int i = 0; i < 7; i++) out2[row * D + lane + 64 * i] = f2bf(v[i] * r);
    }
}

template <int EPI, bool SHIFT>
__global__ __launch_bounds__(256) void k_gemm(const u16* __restrict__ A, const u16* __restrict__ W, u16* __restrict__ C,
                                              const u16* __restrict__ R1, const u16* __restrict__ R2, int N, int K) {
    __shared__ u16 sA[64][40];
    __shared__ u16 sB[64][40];
    int bm = blockIdx.y * 64, bn = blockIdx.x * 64;
    int tid = threadIdx.x, lane = tid & 63, w = tid >> 6;
    int wm = (w >> 1) * 32, wn = (w & 1) * 32;
    f32x4 acc[2][2] = {};
    for (int k0 = 0; k0 < K; k0 += 32) {
        {
            int r = tid >> 2, c = (tid & 3) * 8;
            int row = bm + r;
            bool ok = true;
            if (SHIFT) {
                int g = (k0 + c) / 112;
                int sh = (g == 0) ? -1 : (g == 1) ? 1 : (g == 2) ? -2 : 2;
                row += sh;
                ok = (row >= 0 && row < S);
            }
            uint4 v = make_uint4(0, 0, 0, 0);
            if (ok) v = *reinterpret_cast<const uint4*>(A + (size_t)row * K + k0 + c);
            *reinterpret_cast<uint4*>(&sA[r][c]) = v;
        }
        {
            int kk = tid >> 3, c = (tid & 7) * 8;
            uint4 v = *reinterpret_cast<const uint4*>(W + (size_t)(k0 + kk) * N + bn + c);
            const u16* p = reinterpret_cast<const u16*>(&v);
            for (int j = 0; j < 8; j++) sB[c + j][kk] = p[j];
        }
        __syncthreads();
        bf16x8 a[2], b[2];
        for (int i = 0; i < 2; i++) {
            a[i] = *reinterpret_cast<const bf16x8*>(&sA[wm + i * 16 + (lane & 15)][(lane >> 4) * 8]);
            b[i] = *reinterpret_cast<const bf16x8*>(&sB[wn + i * 16 + (lane & 15)][(lane >> 4) * 8]);
        }
        for (int i = 0; i < 2; i++)
            for (int j = 0; j < 2; j++) acc[i][j] = __builtin_amdgcn_mfma_f32_16x16x32_bf16(a[i], b[j], acc[i][j], 0, 0, 0);
        __syncthreads();
    }
    for (int i = 0; i < 2; i++)
        for (int j = 0; j < 2; j++)
            for (int r = 0; r < 4; r++) {
                int row = bm + wm + i * 16 + (lane >> 4) * 4 + r, col = bn + wn + j * 16 + (lane & 15);
                float v = acc[i][j][r];
                size_t idx = (size_t)row * N + col;
                if (EPI == 1) v = gelu_t(bf2f(f2bf(v)));
                if (EPI == 2) v = bf2f(f2bf(v)) + bf2f(R1[idx]);
                if (EPI == 3) v = bf2f(f2bf(bf2f(f2bf(v)) + bf2f(R1[idx]))) + bf2f(R2[idx]);
                C[idx] = f2bf(v);
            }
}

__global__ __launch_bounds__(256) void k_attn(const u16* __restrict__ qkv, u16* __restrict__ o) {
    int q = blockIdx.x * 4 + (threadIdx.x >> 6), h = blockIdx.y, lane = threadIdx.x & 63;
    float qv[DH];
    for (int d = 0; d < DH; d++) qv[d] = bf2f(qkv[(size_t)q * 3 * D + h * DH + d]);
    float sc[4];
    float mx = -1e30f;
    for (int i = 0; i < 4; i++) {
        int k = q - (lane + 64 * i);
        float s = -1e30f;
        if (k >= 0) {
            s = 0.f;
            for (int d = 0; d < DH; d++) s += qv[d] * bf2f(qkv[(size_t)k * 3 * D + D + h * DH + d]);
            s *= 0.18898223650461362f;
        }
        sc[i] = s;
        mx = fmaxf(mx, s);
    }
    for (int of = 32; of; of >>= 1) mx = fmaxf(mx, __shfl_xor(mx, of));
    float sum = 0.f;
    for (int i = 0; i < 4; i++) { int k = q - (lane + 64 * i); sc[i] = (k >= 0) ? __expf(sc[i] - mx) : 0.f; sum += sc[i]; }
    for (int of = 32; of; of >>= 1) sum += __shfl_xor(sum, of);
    float inv = 1.f / sum;
    for (int d = 0; d < DH; d++) {
        float acc = 0.f;
        for (int i = 0; i < 4; i++) { int k = q - (lane + 64 * i); if (k >= 0) acc += sc[i] * bf2f(qkv[(size_t)k * 3 * D + 2 * D + h * DH + d]); }
        for (int of = 32; of; of >>= 1) acc += __shfl_xor(acc, of);
        if (lane == 0) o[(size_t)q * D + h * DH + d] = f2bf(acc * inv);
    }
}

extern "C" void kernel_launch(void* const* d_in, const int* in_sizes, int n_in, void* d_out, int out_size, void* d_ws,
                              size_t ws_size, hipStream_t stream) {
    const u16* x = (const u16*)d_in[0];
    const u16* wqkv = (const u16*)d_in[1];
    const u16* wo = (const u16*)d_in[2];
    const u16* w1 = (const u16*)d_in[4];
    const u16* w2 = (const u16*)d_in[5];
    const u16* w3 = (const u16*)d_in[6];
    const u16* w4 = (const u16*)d_in[7];
    u16* out = (u16*)d_out;
    u16* ws = (u16*)d_ws;
    u16* ln1 = ws;
    u16* qkv = ln1 + S * D;
    u16* o = qkv + S * 3 * D;
    u16* h = o + S * D;
    u16* ln2 = h + S * D;
    u16* ln2b = ln2 + S * D;
    u16* hh = ln2b + S * D;
    u16* x1 = hh + S * D;
    u16* ln3 = x1 + S * D;
    u16* u = ln3 + S * D;
    k_ln<<<S / 4, 256, 0, stream>>>(x, ln1, nullptr);
    k_gemm<0, false><<<dim3(3 * D / 64, S / 64), 256, 0, stream>>>(ln1, wqkv, qkv, nullptr, nullptr, 3 * D, D);
    k_attn<<<dim3(S / 4, H), 256, 0, stream>>>(qkv, o);
    k_gemm<2, false><<<dim3(D / 64, S / 64), 256, 0, stream>>>(o, wo, h, x, nullptr, D, D);
    k_ln<<<S / 4, 256, 0, stream>>>(h, ln2, ln2b);
    k_gemm<1, false><<<dim3(D / 64, S / 64), 256, 0, stream>>>(ln2b, w1, hh, nullptr, nullptr, D, D);
    k_gemm<2, true><<<dim3(D / 64, S / 64), 256, 0, stream>>>(hh, w2, x1, ln2, nullptr, D, D);
    k_ln<<<S / 4, 256, 0, stream>>>(x1, ln3, nullptr);
    k_gemm<1, false><<<dim3(F / 64, S / 64), 256, 0, stream>>>(ln3, w3, u, nullptr, nullptr, F, D);
    k_gemm<3, false><<<dim3(D / 64, S / 64), 256, 0, stream>>>(u, w4, out, x1, h, D, F);
}
```

```cpp
#include <hip/hip_runtime.h>
#include <hip/hip_bf16.h>
#include <stdint.h>

typedef unsigned short u16;
typedef __attribute__((ext_vector_type(8))) short bf16x8;
typedef __attribute__((ext_vector_type(4))) short s16x4;
typedef __attribute__((ext_vector_type(16))) float f32x16;
typedef __attribute__((ext_vector_type(4))) float f32x4;
typedef __attribute__((ext_vector_type(2))) float f32x2;
typedef __attribute__((ext_vector_type(4))) unsigned u32x4;
typedef __attribute__((ext_vector_type(2))) unsigned u32x2;
typedef __attribute__((ext_vector_type(2))) __bf16 bf2;
#define LAS __attribute__((address_space(3)))
#define GAS __attribute__((address_space(1)))
#define DEVI __device__ __forceinline__

#define MK_STAMP 0

namespace mk {
constexpr int S = 2048, D = 448;
constexpr unsigned WS_EP = 0;
constexpr unsigned WS_FLAG = 4096;
constexpr unsigned WS_KV = 262144;
constexpr unsigned WS_O = WS_KV + 4194304;
constexpr unsigned WS_H = WS_O + 2097152;
constexpr unsigned WS_HH = WS_H + 1835008;
constexpr unsigned WS_X1 = WS_HH + 1835008;
constexpr unsigned WS_U = WS_X1 + 1835008;
constexpr unsigned WS_STH = WS_U + 7340032;
constexpr unsigned WS_STX = WS_STH + 262144;
constexpr unsigned WS_STAMP = WS_STX + 262144;
constexpr unsigned WS_END = WS_STAMP + 256 * 32 * 8;
constexpr unsigned L_SLOT = 28672;
constexpr unsigned L_OWNKV = 143360;
constexpr unsigned L_MISC = 159744;
constexpr unsigned L_KV = 110592;
constexpr unsigned L_WST = 25088;
constexpr unsigned L_RED = 100352;
constexpr unsigned L_M2 = 149504;
constexpr float EPS = 1e-5f;
constexpr unsigned SPIN_CAP = 1u << 18;

struct Params {
    const u16* x; const u16* wqkv; const u16* wo; const u16* w1; const u16* w2; const u16* w3; const u16* w4;
    u16* out; char* ws;
};

DEVI unsigned pk(float lo, float hi) { bf2 v = {(__bf16)lo, (__bf16)hi}; return __builtin_bit_cast(unsigned, v); }
DEVI float blo(unsigned u) { return __uint_as_float(u << 16); }
DEVI float bhi(unsigned u) { return __uint_as_float(u & 0xffff0000u); }
DEVI float dot2(unsigned a, unsigned b, float c) { return __builtin_amdgcn_fdot2_f32_bf16(__builtin_bit_cast(bf2, a), __builtin_bit_cast(bf2, b), c, false); }
constexpr unsigned ONES = 0x3f803f80u;
DEVI float hsum(float v) { auto r = __builtin_amdgcn_permlane32_swap(__float_as_uint(v), __float_as_uint(v), false, false); return __uint_as_float(r[0]) + __uint_as_float(r[1]); }
DEVI float hmax(float v) { auto r = __builtin_amdgcn_permlane32_swap(__float_as_uint(v), __float_as_uint(v), false, false); return fmaxf(__uint_as_float(r[0]), __uint_as_float(r[1])); }
DEVI f32x16 mfma(u32x4 a, u32x4 b, f32x16 c) { return __builtin_amdgcn_mfma_f32_32x32x16_bf16(__builtin_bit_cast(bf16x8, a), __builtin_bit_cast(bf16x8, b), c, 0, 0, 0); }
DEVI u32x4 lds128(unsigned a) { return *(const LAS u32x4*)(uintptr_t)a; }
DEVI f32x4 ldsf4(unsigned a) { return *(const LAS f32x4*)(uintptr_t)a; }
DEVI void sts128(unsigned a, u32x4 v) { *(LAS u32x4*)(uintptr_t)a = v; }
DEVI void stsf4(unsigned a, f32x4 v) { *(LAS f32x4*)(uintptr_t)a = v; }
DEVI u32x2 ldstr(unsigned a) { return __builtin_bit_cast(u32x2, __builtin_amdgcn_ds_read_tr16_b64_v4i16((LAS s16x4*)(uintptr_t)a)); }
DEVI u32x4 wfrag(unsigned a0, unsigned a1) { u32x2 p = ldstr(a0), q = ldstr(a1); return (u32x4){p.x, p.y, q.x, q.y}; }
DEVI u32x4 pack8(const f32x16& a, int s) { return (u32x4){pk(a[8 * s], a[8 * s + 1]), pk(a[8 * s + 2], a[8 * s + 3]), pk(a[8 * s + 4], a[8 * s + 5]), pk(a[8 * s + 6], a[8 * s + 7])}; }
constexpr int SC1 = 16;
DEVI u32x4 gld(__amdgpu_buffer_rsrc_t rs, unsigned voff, unsigned soff) { return __builtin_amdgcn_raw_buffer_load_b128(rs, voff, soff, SC1); }
DEVI u32x2 gld8(__amdgpu_buffer_rsrc_t rs, unsigned voff, unsigned soff) { return __builtin_amdgcn_raw_buffer_load_b64(rs, voff, soff, SC1); }
DEVI void gst(__amdgpu_buffer_rsrc_t rs, u32x4 v, unsigned voff, unsigned soff) { __builtin_amdgcn_raw_buffer_store_b128(v, rs, voff, soff, SC1); }
DEVI void gst8(__amdgpu_buffer_rsrc_t rs, u32x2 v, unsigned voff, unsigned soff) { __builtin_amdgcn_raw_buffer_store_b64(v, rs, voff, soff, SC1); }
#define WAIT_VM(N) asm volatile("s_waitcnt vmcnt(" #N ")" ::: "memory")
#define WAIT_BAR(N) asm volatile("s_waitcnt vmcnt(" #N ") lgkmcnt(0)\n\ts_barrier" ::: "memory")
#define LBAR() asm volatile("s_waitcnt lgkmcnt(0)\n\ts_barrier" ::: "memory")

DEVI void dma_run(const char* sbase, unsigned sstep, unsigned voff, unsigned lds, unsigned lstep, unsigned n) {
    unsigned keep;
    asm volatile(
        "s_nop 4\n\t"
        "s_mov_b32 %[keep], m0\n"
        "1:\n\t"
        "s_mov_b32 m0, %[lds]\n\t"
        "s_sub_u32 %[n], %[n], 1\n\t"
        "global_load_lds_dwordx4 %[voff], %[sb]\n\t"
        "v_add_u32 %[voff], %[sstep], %[voff]\n\t"
        "s_add_u32 %[lds], %[lds], %[lstep]\n\t"
        "s_cmp_lg_u32 %[n], 0\n\t"
        "s_cbranch_scc1 1b\n\t"
        "s_mov_b32 m0, %[keep]"
        : [keep] "=&s"(keep), [lds] "+s"(lds), [n] "+s"(n), [voff] "+v"(voff)
        : [sb] "s"(sbase), [sstep] "s"(sstep), [lstep] "s"(lstep)
        : "memory", "scc");
}

DEVI void wait_flags(const char* ws, unsigned foff, int n, unsigned e, int lane) {
    const GAS unsigned* p = (const GAS unsigned*)(ws + foff) + (lane < n ? lane : 0) * 32;
    for (unsigned sp = 0; sp < SPIN_CAP; ++sp) {
        unsigned v = __hip_atomic_load(p, __ATOMIC_RELAXED, __HIP_MEMORY_SCOPE_AGENT);
        if (__all(v == e)) break;
    }
    __builtin_amdgcn_fence(__ATOMIC_ACQUIRE, "wavefront");
}
DEVI void set_flag(char* ws, unsigned foff, unsigned e) { __hip_atomic_store((GAS unsigned*)(ws + foff), e, __ATOMIC_RELAXED, __HIP_MEMORY_SCOPE_AGENT); }
DEVI unsigned flag_off(int ph, int idx) { return WS_FLAG + (ph * 256 + idx) * 128; }

DEVI float gelu_t(float x) {
    const float c0 = -2.f * 0.7978845608028654f * 1.4426950408889634f, c1 = c0 * 0.044715f;
    float z = x * __builtin_fmaf(x * x, c1, c0);
    return x * __builtin_amdgcn_rcpf(1.f + __builtin_amdgcn_exp2f(z));
}

#define STAMP(k) do {} while (0)

__global__ __launch_bounds__(256, 1) void fused_fwd(Params P) {
    __shared__ __attribute__((aligned(16))) char smem[163840];
    const unsigned L0 = (unsigned)(uintptr_t)(LAS char*)smem;
    const int tid = threadIdx.x, lane = tid & 63, w = __builtin_amdgcn_readfirstlane(tid >> 6);
    const int r32 = lane & 31, hh = lane >> 5;
    const int b = blockIdx.x, xcd = b & 7, slot = b >> 3;
    const unsigned e = *(const unsigned*)(P.ws + WS_EP) + 1u;
    const __amdgpu_buffer_rsrc_t rs = __builtin_amdgcn_make_buffer_rsrc((void*)P.ws, 0, WS_END, 0x00020000);
    const int trq = (lane & 15) >> 2, trQ = ((lane >> 4) & 1) * 4 + (lane & 3);
    const int gq = (trQ & 4) | ((trQ & 1) << 1) | ((trQ >> 1) & 1);
    STAMP(0);

    const int m = 2 * xcd + (slot >> 4), h = slot & 15;
    u32x4 qf0, qf1;
    {
        const unsigned xoffE = (lane >> 3) * 896 + (((lane & 7) ^ (lane >> 4)) << 4);
        const unsigned xoffO = (lane >> 3) * 896 + (((lane & 7) ^ (4 + (lane >> 4))) << 4);
        const unsigned colb = (56 * h) & ~15u, shift = (56 * h) & 15u;
        unsigned woff[3];
#pragma unroll
        for (int j = 0; j < 3; ++j) {
            const int bl = 4 * j + (lane >> 4), kql = bl / 3, tile = bl % 3;
            woff[j] = (4 * kql + ((lane >> 2) & 3)) * 2688 + tile * 896 + colb + (lane & 3) * 16;
        }
        const char* xb = (const char*)P.x + (size_t)(128 * m + 32 * w) * 896;
        const char* wb = (const char*)P.wqkv + (size_t)(16 * w) * 2688;
        auto issue = [&](int kc, unsigned sl) {
            const unsigned dx = L0 + sl * L_SLOT + 4 * w * 1024, dw = L0 + sl * L_SLOT + 16384 + 3 * w * 1024;
            dma_run(xb + kc * 128, 16 * 896, xoffE, dx, 2048, 2);
            dma_run(xb + kc * 128 + 8 * 896, 16 * 896, xoffO, dx + 1024, 2048, 2);
            dma_run(wb + (size_t)kc * 64 * 2688, 0, woff[0], dw, 0, 1);
            dma_run(wb + (size_t)kc * 64 * 2688, 0, woff[1], dw + 1024, 0, 1);
            dma_run(wb + (size_t)kc * 64 * 2688, 0, woff[2], dw + 2048, 0, 1);
        };
#pragma unroll 1
        for (int c = 0; c < 5; ++c) issue(c, c);
        f32x16 aq = {}, ak = {}, av = {};
        float s1 = 0.f, s2 = 0.f, csq = 0.f, csk = 0.f, csv = 0.f;
        const unsigned xrd = (32 * w + r32) * 128 + ((hh ^ ((r32 >> 1) & 7)) << 4);
        const unsigned wrd = 16384 + hh * 6 * 256 + trq * 64 + ((shift + gq * 8) & 63);
#pragma unroll 1
        for (int c = 0; c < 7; ++c) {
            if (c == 0) WAIT_BAR(28); else if (c <= 3) WAIT_BAR(21); else if (c == 4) WAIT_BAR(14); else if (c == 5) WAIT_BAR(7); else WAIT_BAR(0);
            if (c == 1 || c == 2) issue(c + 4, c - 1);
            const unsigned sl = L0 + (c >= 5 ? c - 5 : c) * L_SLOT;
            const unsigned xa = sl + xrd, wa = sl + wrd;
#pragma unroll
            for (int ks = 0; ks < 4; ++ks) {
                const u32x4 xf = lds128(xa ^ (ks << 5));
                const u32x4 wq = wfrag(wa + (4 * ks) * 768, wa + (4 * ks + 1) * 768);
                const u32x4 wk = wfrag(wa + (4 * ks) * 768 + 256, wa + (4 * ks + 1) * 768 + 256);
                const u32x4 wv = wfrag(wa + (4 * ks) * 768 + 512, wa + (4 * ks + 1) * 768 + 512);
                aq = mfma(wq, xf, aq); ak = mfma(wk, xf, ak); av = mfma(xf, wv, av);
#pragma unroll
                for (int i = 0; i < 4; ++i) { s1 = dot2(xf[i], ONES, s1); s2 = dot2(xf[i], xf[i], s2); }
                if (ks == w) {
#pragma unroll
                    for (int i = 0; i < 4; ++i) { csq = dot2(wq[i], ONES, csq); csk = dot2(wk[i], ONES, csk); csv = dot2(wv[i], ONES, csv); }
                }
            }
        }
        STAMP(1);
        s1 = hsum(s1); s2 = hsum(s2); csq = hsum(csq); csk = hsum(csk); csv = hsum(csv);
        const float mean = s1 * (1.f / D), var = fmaxf(s2 * (1.f / D) - mean * mean, 0.f), rstd = __builtin_amdgcn_rsqf(var + EPS);
        LAS float* csp = (LAS float*)(uintptr_t)(L0 + L_MISC);
        LAS float* csw = (LAS float*)(uintptr_t)(L0 + L_MISC + 1536 + w * 384);
        LAS float* stw = (LAS float*)(uintptr_t)(L0 + L_MISC + 3072 + w * 256);
        if (hh == 0) { csp[w * 96 + r32] = csq; csp[w * 96 + 32 + r32] = csk; csp[w * 96 + 64 + r32] = csv; stw[r32] = mean * rstd; stw[32 + r32] = rstd; }
        LBAR();
        float cq = 0.f, ck = 0.f, cv = 0.f;
#pragma unroll
        for (int ww = 0; ww < 4; ++ww) { cq += csp[ww * 96 + r32]; ck += csp[ww * 96 + 32 + r32]; cv += csp[ww * 96 + 64 + r32]; }
        if (hh == 0) { csw[r32] = cq; csw[32 + r32] = ck; }
        asm volatile("s_waitcnt lgkmcnt(0)" ::: "memory");
        const float qs = rstd * (0.18898223650461362f * 1.4426950408889634f);
#pragma unroll
        for (int g = 0; g < 4; ++g) {
            const f32x4 c1 = *(const LAS f32x4*)(csw + 8 * g + 4 * hh), c2 = *(const LAS f32x4*)(csw + 32 + 8 * g + 4 * hh);
            const f32x4 mr = *(const LAS f32x4*)(stw + 8 * g + 4 * hh), rr = *(const LAS f32x4*)(stw + 32 + 8 * g + 4 * hh);
#pragma unroll
            for (int i = 0; i < 4; ++i) {
                aq[4 * g + i] = (aq[4 * g + i] - mean * c1[i]) * qs;
                ak[4 * g + i] = (ak[4 * g + i] - mean * c2[i]) * rstd;
                av[4 * g + i] = av[4 * g + i] * rr[i] - mr[i] * cv;
            }
        }
        if (hh == 1) {
#pragma unroll
            for (int i = 12; i < 16; ++i) { aq[i] = 0.f; ak[i] = 0.f; }
        }
        if (r32 >= 28) {
            const float pv = r32 == 28 ? 1.f : 0.f;
#pragma unroll
            for (int i = 0; i < 16; ++i) av[i] = pv;
        }
        qf0 = pack8(aq, 0); qf1 = pack8(aq, 1);
        const u32x4 k0 = pack8(ak, 0), k1 = pack8(ak, 1), v0 = pack8(av, 0), v1 = pack8(av, 1);
        const unsigned lo = L0 + L_OWNKV + w * 4096 + lane * 16;
        sts128(lo, k0); sts128(lo + 1024, k1); sts128(lo + 2048, v0); sts128(lo + 3072, v1);
        const unsigned go = WS_KV + (h * 64 + 4 * m + w) * 4096;
        gst(rs, k0, lane * 16, go); gst(rs, k1, lane * 16, go + 1024); gst(rs, v0, lane * 16, go + 2048); gst(rs, v1, lane * 16, go + 3072);
        WAIT_BAR(0);
        if (tid == 0) set_flag(P.ws, flag_off(0, m * 16 + h), e);
        STAMP(2);
    }
    {
        const int mt = m - 2 + (w >> 1);
        u32x4 kv[8];
        if (mt >= 0) {
            wait_flags(P.ws, flag_off(0, mt * 16 + h), 1, e, lane);
            const unsigned go = WS_KV + (h * 64 + 4 * m - 8 + 2 * w) * 4096;
#pragma unroll
            for (int i = 0; i < 8; ++i) kv[i] = gld(rs, lane * 16, go + i * 1024);
        }
        const int g3 = slot & 3;
        const unsigned wvoff = lane < 48 ? ((lane >> 2) & 3) * 896 + (lane >> 4) * 64 + (lane & 3) * 16 : ((lane - 48) >> 1) * 896 + 192 + (lane & 1) * 16;
        if (lane < 56) dma_run((const char*)P.wo + (size_t)(112 * w) * 896 + g3 * 224, 4 * 896, wvoff, L0 + w * L_WST, 896, 28);
        if (mt >= 0) {
#pragma unroll
            for (int i = 0; i < 8; ++i) sts128(L0 + L_KV + 2 * w * 4096 + i * 1024 + lane * 16, kv[i]);
        }
        LBAR();
        STAMP(3);
        const unsigned kvb = L0 + L_KV + lane * 16;
        const int ulo = (8 - 4 * m) > w + 1 ? (8 - 4 * m) : w + 1;
        const bool has_old = (4 * m - 8 + w) >= 0;
        f32x16 cdiag, cold;
#pragma unroll
        for (int i = 0; i < 16; ++i) { const int kr = (i & 3) + 8 * (i >> 2) + 4 * hh; cdiag[i] = kr <= r32 ? 0.f : -1e30f; cold[i] = kr > r32 ? 0.f : -1e30f; }
        auto smax = [&](const f32x16& s) { float a = fmaxf(fmaxf(s[0], s[1]), s[2]);
#pragma unroll
            for (int i = 3; i < 15; i += 2) a = fmaxf(fmaxf(a, s[i]), s[i + 1]);
            return fmaxf(a, s[15]); };
        float mx;
        { const unsigned a = kvb + (w + 8) * 4096; f32x16 s = mfma(lds128(a), qf0, cdiag); s = mfma(lds128(a + 1024), qf1, s); mx = smax(s); }
        if (has_old) { const unsigned a = kvb + w * 4096; f32x16 s = mfma(lds128(a), qf0, cold); s = mfma(lds128(a + 1024), qf1, s); mx = fmaxf(mx, smax(s)); }
        const f32x16 zero16 = {};
#pragma unroll 1
        for (int u = ulo; u < w + 8; ++u) { const unsigned a = kvb + u * 4096; f32x16 s = mfma(lds128(a), qf0, zero16); s = mfma(lds128(a + 1024), qf1, s); mx = fmaxf(mx, smax(s)); }
        mx = hmax(mx);
        f32x16 o = {}, cm;
#pragma unroll
        for (int i = 0; i < 16; ++i) { cm[i] = -mx; cdiag[i] -= mx; cold[i] -= mx; }
        auto unit = [&](unsigned a, const f32x16& c) {
            f32x16 s = mfma(lds128(a), qf0, c); s = mfma(lds128(a + 1024), qf1, s);
#pragma unroll
            for (int i = 0; i < 16; ++i) s[i] = __builtin_amdgcn_exp2f(s[i]);
            o = mfma(lds128(a + 2048), pack8(s, 0), o); o = mfma(lds128(a + 3072), pack8(s, 1), o);
        };
        unit(kvb + (w + 8) * 4096, cdiag);
        if (has_old) unit(kvb + w * 4096, cold);
#pragma unroll 1
        for (int u = ulo; u < w + 8; ++u) unit(kvb + u * 4096, cm);
        auto lr = __builtin_amdgcn_permlane32_swap(__float_as_uint(o[12]), __float_as_uint(o[12]), false, false);
        const float inv = __builtin_amdgcn_rcpf(__uint_as_float(lr[1]));
#pragma unroll
        for (int i = 0; i < 16; ++i) o[i] *= inv;
        if (hh == 1) { o[12] = 0.f; o[13] = 0.f; o[14] = 0.f; o[15] = 0.f; }
        const unsigned vo = (hh * 2048 + r32) * 16, so = WS_O + ((4 * h) * 2048 + 128 * m + 32 * w) * 16;
        gst(rs, pack8(o, 0), vo, so); gst(rs, pack8(o, 1), vo, so + 2 * 2048 * 16);
        WAIT_BAR(0);
        if (tid == 0) set_flag(P.ws, flag_off(1, m * 16 + h), e);
        STAMP(4);
    }
    const int ti = 8 * xcd + (slot >> 2), g = slot & 3;
    const unsigned wvoff = lane < 48 ? ((lane >> 2) & 3) * 896 + (lane >> 4) * 64 + (lane & 3) * 16 : ((lane - 48) >> 1) * 896 + 192 + (lane & 1) * 16;
    const unsigned trA = L0 + w * L_WST + trq * 64 + gq * 8;
    const unsigned trB = L0 + w * L_WST + 768 + trq * 32 + (gq & 3) * 8;
    auto reduce = [&](f32x16 (&acc)[4]) -> f32x16 {
#pragma unroll
        for (int t = 0; t < 4; ++t) {
            if (t != w) {
                const unsigned a = L0 + L_RED + t * 12288 + (w < t ? w : w - 1) * (t < 3 ? 4096 : 2048) + lane * 16;
#pragma unroll
                for (int q = 0; q < (t < 3 ? 4 : 2); ++q) stsf4(a + q * 1024, (f32x4){acc[t][4 * q], acc[t][4 * q + 1], acc[t][4 * q + 2], acc[t][4 * q + 3]});
            }
        }
        LBAR();
        f32x16 r = w == 0 ? acc[0] : w == 1 ? acc[1] : w == 2 ? acc[2] : acc[3];
        const unsigned st = w < 3 ? 4096 : 2048, a = L0 + L_RED + w * 12288 + lane * 16;
#pragma unroll
        for (int j = 0; j < 3; ++j)
#pragma unroll
            for (int q = 0; q < 4; ++q) {
                if (q < 2 || w < 3) { const f32x4 v = ldsf4(a + j * st + q * 1024); r[4 * q] += v[0]; r[4 * q + 1] += v[1]; r[4 * q + 2] += v[2]; r[4 * q + 3] += v[3]; }
            }
        return r;
    };
    const unsigned vx = (hh * 2048 + r32) * 16;
    const int ns = w < 3 ? 2 : 1;
    u32x4 hp[2];
    float meanH, rstdH;
    {
        u32x4 wr[8][4];
#pragma unroll
        for (int hl = 0; hl < 4; ++hl)
#pragma unroll
            for (int s = 0; s < 2; ++s) {
                const int kq0 = 7 * hl + 4 * s;
                const unsigned o0 = (kq0 + 2 * hh) * 896;
                const unsigned o1 = (hl == 3 && s == 1) ? (hh ? 27 * 896 : o0 + 896) : o0 + 896;
#pragma unroll
                for (int t = 0; t < 3; ++t) wr[2 * hl + s][t] = wfrag(trA + o0 + t * 256, trA + o1 + t * 256);
                wr[2 * hl + s][3] = wfrag(trB + o0, trB + o1);
            }
        asm volatile("s_waitcnt lgkmcnt(0)" ::: "memory");
        u32x4 xr[2];
        const char* xp = (const char*)P.x + (size_t)(32 * ti + r32) * 896 + 224 * g + 64 * w + 16 * hh;
        xr[0] = *(const GAS u32x4*)xp; xr[1] = w < 3 ? *(const GAS u32x4*)(xp + 32) : xr[0];
        wait_flags(P.ws, flag_off(1, (ti >> 2) * 16 + 4 * w), 4, e, lane);
        u32x4 xf[8];
#pragma unroll
        for (int k = 0; k < 8; ++k) xf[k] = gld(rs, vx, WS_O + ((16 * w + 2 * k) * 2048 + 32 * ti) * 16);
        if (lane < 56) dma_run((const char*)P.w1 + (size_t)(112 * w) * 896 + g * 224, 4 * 896, wvoff, L0 + w * L_WST, 896, 28);
        STAMP(5);
        f32x16 acc[4] = {};
#pragma unroll
        for (int k = 0; k < 8; ++k)
#pragma unroll
            for (int t = 0; t < 4; ++t) acc[t] = mfma(wr[k][t], xf[k], acc[t]);
        f32x16 r = reduce(acc);
        float s1 = 0.f, s2 = 0.f;
#pragma unroll
        for (int s = 0; s < 2; ++s) {
#pragma unroll
            for (int i = 0; i < 4; ++i) {
                hp[s][i] = pk(r[8 * s + 2 * i] + blo(xr[s][i]), r[8 * s + 2 * i + 1] + bhi(xr[s][i]));
                if (s < ns) { s1 = dot2(hp[s][i], ONES, s1); s2 = dot2(hp[s][i], hp[s][i], s2); }
            }
            if (s < ns) gst(rs, hp[s], vx, WS_H + ((14 * g + 4 * w + 2 * s) * 2048 + 32 * ti) * 16);
        }
        s1 = hsum(s1); s2 = hsum(s2);
        if (hh == 0) gst8(rs, (u32x2){__float_as_uint(s1), __float_as_uint(s2)}, r32 * 8, WS_STH + ((g * 4 + w) * 2048 + 32 * ti) * 8);
        WAIT_BAR(0);
        if (tid == 0) set_flag(P.ws, flag_off(2, ti * 4 + g), e);
        STAMP(6);
    }
    auto load_wr = [&](u32x4 (&wr)[7][4]) {
#pragma unroll
        for (int k = 0; k < 7; ++k) {
            const unsigned o0 = (4 * k + 2 * hh) * 896;
#pragma unroll
            for (int t = 0; t < 3; ++t) wr[k][t] = wfrag(trA + o0 + t * 256, trA + o0 + 896 + t * 256);
            wr[k][3] = wfrag(trB + o0, trB + o0 + 896);
        }
    };
    LAS float* m2 = (LAS float*)(uintptr_t)(L0 + L_M2);
    {
        u32x4 wr[7][4];
        load_wr(wr);
        float cs[4] = {0.f, 0.f, 0.f, 0.f};
#pragma unroll
        for (int k = 0; k < 7; ++k)
#pragma unroll
            for (int t = 0; t < 4; ++t)
#pragma unroll
                for (int i = 0; i < 4; ++i) cs[t] = dot2(wr[k][t][i], ONES, cs[t]);
#pragma unroll
        for (int t = 0; t < 4; ++t) { cs[t] = hsum(cs[t]); if (hh == 0) m2[w * 128 + t * 32 + r32] = cs[t]; }
        asm volatile("s_waitcnt lgkmcnt(0)" ::: "memory");
        wait_flags(P.ws, flag_off(2, ti * 4 + w), 1, e, lane);
        u32x4 xf[7];
#pragma unroll
        for (int k = 0; k < 7; ++k) xf[k] = gld(rs, vx, WS_H + ((14 * w + 2 * k) * 2048 + 32 * ti) * 16);
        const unsigned sv = (2 * hh * 2048 + r32) * 8, so = WS_STH + (w * 4 * 2048 + 32 * ti) * 8;
        const u32x2 sa = gld8(rs, sv, so), sb = gld8(rs, sv, so + 2048 * 8);
        if (lane < 56) dma_run((const char*)P.w2 + (size_t)(112 * w) * 896 + g * 224, 4 * 896, wvoff, L0 + w * L_WST, 896, 28);
        STAMP(7);
        f32x16 acc[4] = {};
#pragma unroll
        for (int k = 0; k < 7; ++k)
#pragma unroll
            for (int t = 0; t < 4; ++t) acc[t] = mfma(wr[k][t], xf[k], acc[t]);
        { const float a = hsum(__uint_as_float(sa.x) + __uint_as_float(sb.x)), c = hsum(__uint_as_float(sa.y) + __uint_as_float(sb.y));
          if (hh == 0) { m2[512 + w * 64 + 2 * r32] = a; m2[512 + w * 64 + 2 * r32 + 1] = c; } }
        f32x16 r = reduce(acc);
        float t1 = 0.f, t2 = 0.f;
#pragma unroll
        for (int ww = 0; ww < 4; ++ww) { t1 += m2[512 + ww * 64 + 2 * r32]; t2 += m2[512 + ww * 64 + 2 * r32 + 1]; }
        meanH = t1 * (1.f / D);
        const float var = fmaxf(t2 * (1.f / D) - meanH * meanH, 0.f);
        rstdH = __builtin_amdgcn_rsqf(var + EPS);
        const float sc = rstdH * __builtin_amdgcn_rsqf(var * rstdH * rstdH + EPS);
#pragma unroll
        for (int q = 0; q < 4; ++q) {
            f32x4 c = {0.f, 0.f, 0.f, 0.f};
#pragma unroll
            for (int ww = 0; ww < 4; ++ww) c += *(const LAS f32x4*)(m2 + ww * 128 + w * 32 + 8 * q + 4 * hh);
#pragma unroll
            for (int i = 0; i < 4; ++i) r[4 * q + i] = gelu_t((r[4 * q + i] - meanH * c[i]) * sc);
        }
#pragma unroll
        for (int s = 0; s < 2; ++s) if (s < ns) gst(rs, pack8(r, s), vx, WS_HH + ((14 * g + 4 * w + 2 * s) * 2048 + 32 * ti) * 16);
        WAIT_BAR(0);
        if (tid == 0) set_flag(P.ws, flag_off(3, ti * 4 + g), e);
        STAMP(8);
    }
    const int s16 = slot & 15;
    const unsigned wvoff3 = lane < 48 ? ((lane >> 2) & 3) * 3584 + (lane >> 4) * 64 + (lane & 3) * 16 : ((lane - 48) >> 1) * 3584 + 192 + (lane & 1) * 16;
    {
        u32x4 wr[7][4];
        load_wr(wr);
        LBAR();
        const int sh = w == 0 ? -1 : w == 1 ? 1 : w == 2 ? -2 : 2;
        const int tn = ti + (sh < 0 ? -1 : 1);
        {
            const bool nb = tn >= 0 && tn < 64;
            const GAS unsigned* p = (const GAS unsigned*)(P.ws + flag_off(3, ((lane == 1 && nb) ? tn : ti) * 4 + w));
            for (unsigned sp = 0; sp < SPIN_CAP; ++sp) { unsigned v = __hip_atomic_load(p, __ATOMIC_RELAXED, __HIP_MEMORY_SCOPE_AGENT); if (__all(v == e)) break; }
            __builtin_amdgcn_fence(__ATOMIC_ACQUIRE, "wavefront");
        }
        const int tok = 32 * ti + r32 + sh;
        const bool ok = tok >= 0 && tok < S;
        const unsigned vxs = (hh * 2048 + (ok ? tok : 0)) * 16;
        u32x4 xf[7];
#pragma unroll
        for (int k = 0; k < 7; ++k) { xf[k] = gld(rs, vxs, WS_HH + (14 * w + 2 * k) * 2048 * 16); }
        if (lane < 56) dma_run((const char*)P.w3 + (size_t)(112 * w) * 3584 + s16 * 224, 4 * 3584, wvoff3, L0 + w * L_WST, 896, 28);
        STAMP(9);
        f32x16 acc[4] = {};
#pragma unroll
        for (int k = 0; k < 7; ++k) {
            if (!ok) xf[k] = (u32x4){0u, 0u, 0u, 0u};
#pragma unroll
            for (int t = 0; t < 4; ++t) acc[t] = mfma(wr[k][t], xf[k], acc[t]);
        }
        f32x16 r = reduce(acc);
        float s1 = 0.f, s2 = 0.f;
#pragma unroll
        for (int s = 0; s < 2; ++s) {
            u32x4 xp;
#pragma unroll
            for (int i = 0; i < 4; ++i) {
                xp[i] = pk(r[8 * s + 2 * i] + (blo(hp[s][i]) - meanH) * rstdH, r[8 * s + 2 * i + 1] + (bhi(hp[s][i]) - meanH) * rstdH);
                if (s < ns) { s1 = dot2(xp[i], ONES, s1); s2 = dot2(xp[i], xp[i], s2); }
            }
            if (s < ns) gst(rs, xp, vx, WS_X1 + ((14 * g + 4 * w + 2 * s) * 2048 + 32 * ti) * 16);
        }
        s1 = hsum(s1); s2 = hsum(s2);
        if (hh == 0) gst8(rs, (u32x2){__float_as_uint(s1), __float_as_uint(s2)}, r32 * 8, WS_STX + ((g * 4 + w) * 2048 + 32 * ti) * 8);
        WAIT_BAR(0);
        if (tid == 0) set_flag(P.ws, flag_off(4, ti * 4 + g), e);
        STAMP(10);
    }
    const int j7 = 4 * xcd + (slot >> 3), g7 = slot & 7;
    const unsigned wvoff4 = [&] { const int sub = lane >= 28 ? 1 : 0, ll = lane - 28 * sub;
        return (unsigned)(ll < 16 ? (4 * sub + (ll >> 2)) * 896 + (ll & 3) * 16 : (4 * sub + (ll - 16) / 3) * 896 + 64 + ((ll - 16) % 3) * 16); }();
    {
        const unsigned trA6 = L0 + trq * 64 + gq * 8 + 2 * hh * 896, trB6 = L0 + 768 + trq * 32 + (gq & 3) * 8 + 2 * hh * 896;
        {
            float c = 0.f;
            const unsigned a = w < 3 ? trA6 + w * 256 : trB6;
#pragma unroll 4
            for (int k = 0; k < 28; ++k) { const u32x4 f = wfrag(a + 4 * k * 896, a + (4 * k + 1) * 896);
#pragma unroll
                for (int i = 0; i < 4; ++i) c = dot2(f[i], ONES, c); }
            c = hsum(c);
            if (hh == 0) m2[w * 32 + r32] = c;
        }
        LBAR();
        wait_flags(P.ws, flag_off(4, (4 * m + w) * 4), 4, e, lane);
        const int tk = 128 * m + 32 * w;
        u32x4 xf[28];
#pragma unroll
        for (int k = 0; k < 28; ++k) xf[k] = gld(rs, vx, WS_X1 + (2 * k * 2048 + tk) * 16);
        float t1 = 0.f, t2 = 0.f;
        { u32x2 sv[8];
#pragma unroll
          for (int q = 0; q < 8; ++q) sv[q] = gld8(rs, (8 * hh * 2048 + r32) * 8, WS_STX + (q * 2048 + tk) * 8);
#pragma unroll
          for (int q = 0; q < 8; ++q) { t1 += __uint_as_float(sv[q].x); t2 += __uint_as_float(sv[q].y); } }
        STAMP(11);
        t1 = hsum(t1); t2 = hsum(t2);
        const float mean = t1 * (1.f / D), var = fmaxf(t2 * (1.f / D) - mean * mean, 0.f), rstd = __builtin_amdgcn_rsqf(var + EPS);
#pragma unroll 1
        for (int t = 0; t < 4; ++t) {
            f32x16 acc = {};
            const unsigned a = t < 3 ? trA6 + t * 256 : trB6;
#pragma unroll
            for (int k = 0; k < 28; ++k) acc = mfma(wfrag(a + 4 * k * 896, a + (4 * k + 1) * 896), xf[k], acc);
#pragma unroll
            for (int q = 0; q < 4; ++q) {
                const f32x4 c = *(const LAS f32x4*)(m2 + t * 32 + 8 * q + 4 * hh);
#pragma unroll
                for (int i = 0; i < 4; ++i) acc[4 * q + i] = gelu_t((acc[4 * q + i] - mean * c[i]) * rstd);
            }
            const unsigned so = WS_U + ((14 * s16 + 4 * t) * 2048 + tk) * 16;
            gst(rs, pack8(acc, 0), vx, so);
            if (t < 3) gst(rs, pack8(acc, 1), vx, so + 2 * 2048 * 16);
        }
        LBAR();
        if (lane < 56) dma_run((const char*)P.w4 + (size_t)(448 * w) * 896 + g7 * 112, 8 * 896, wvoff4, L0 + w * L_WST, 896, 28);
        WAIT_BAR(0);
        if (tid == 0) set_flag(P.ws, flag_off(5, m * 16 + s16), e);
        STAMP(12);
    }
    {
        const unsigned tA = L0 + w * L_WST + trq * 64 + gq * 8 + 2 * hh * 448;
        const unsigned tB = L0 + w * L_WST + 256 + trq * 48 + (gq > 5 ? gq - 2 : gq) * 8 + 2 * hh * 448;
        u32x4 wr[14][2];
#pragma unroll
        for (int k = 0; k < 14; ++k) { wr[k][0] = wfrag(tA + 4 * k * 448, tA + (4 * k + 1) * 448); wr[k][1] = wfrag(tB + 4 * k * 448, tB + (4 * k + 1) * 448); }
        asm volatile("s_waitcnt lgkmcnt(0)" ::: "memory");
        wait_flags(P.ws, flag_off(5, (j7 >> 1) * 16 + 4 * w), 4, e, lane);
        const int tk = 64 * j7;
        u32x4 xa[14][2];
#pragma unroll
        for (int k = 0; k < 14; ++k) { xa[k][0] = gld(rs, vx, WS_U + ((56 * w + 2 * k) * 2048 + tk) * 16); xa[k][1] = gld(rs, vx, WS_U + ((56 * w + 2 * k) * 2048 + tk + 32) * 16); }
        if (lane < 56) dma_run((const char*)P.w4 + (size_t)(448 * w + 224) * 896 + g7 * 112, 8 * 896, wvoff4, L0 + w * L_WST, 896, 28);
        u32x4 xb[14][2];
#pragma unroll
        for (int k = 0; k < 14; ++k) { xb[k][0] = gld(rs, vx, WS_U + ((56 * w + 28 + 2 * k) * 2048 + tk) * 16); xb[k][1] = gld(rs, vx, WS_U + ((56 * w + 28 + 2 * k) * 2048 + tk + 32) * 16); }
        const int tt = w >> 1, nt = w & 1;
        u32x4 rh[2], rx[2];
#pragma unroll
        for (int s = 0; s < 2; ++s) {
            const int cc = 4 * nt + 2 * s + hh;
            const unsigned vo = ((cc < 7 ? hh : 0) * 2048 + r32) * 16, so = ((7 * g7 + 4 * nt + 2 * s) * 2048 + tk + 32 * tt) * 16;
            rh[s] = gld(rs, vo, WS_H + so); rx[s] = gld(rs, vo, WS_X1 + so);
        }
        STAMP(13);
        f32x16 acc[4] = {};
#pragma unroll
        for (int k = 0; k < 14; ++k) {
            acc[0] = mfma(wr[k][0], xa[k][0], acc[0]); acc[1] = mfma(wr[k][1], xa[k][0], acc[1]);
            acc[2] = mfma(wr[k][0], xa[k][1], acc[2]); acc[3] = mfma(wr[k][1], xa[k][1], acc[3]);
        }
        WAIT_VM(0);
#pragma unroll
        for (int k = 0; k < 14; ++k) {
            const u32x4 w0 = wfrag(tA + 4 * k * 448, tA + (4 * k + 1) * 448), w1 = wfrag(tB + 4 * k * 448, tB + (4 * k + 1) * 448);
            acc[0] = mfma(w0, xb[k][0], acc[0]); acc[1] = mfma(w1, xb[k][0], acc[1]);
            acc[2] = mfma(w0, xb[k][1], acc[2]); acc[3] = mfma(w1, xb[k][1], acc[3]);
        }
#pragma unroll
        for (int t = 0; t < 4; ++t) {
            if (t != w) {
                const unsigned a = L0 + L_RED + t * 12288 + (w < t ? w : w - 1) * 4096 + lane * 16;
#pragma unroll
                for (int q = 0; q < 4; ++q) stsf4(a + q * 1024, (f32x4){acc[t][4 * q], acc[t][4 * q + 1], acc[t][4 * q + 2], acc[t][4 * q + 3]});
            }
        }
        LBAR();
        f32x16 r = w == 0 ? acc[0] : w == 1 ? acc[1] : w == 2 ? acc[2] : acc[3];
        const unsigned a = L0 + L_RED + w * 12288 + lane * 16;
#pragma unroll
        for (int j = 0; j < 3; ++j)
#pragma unroll
            for (int q = 0; q < 4; ++q) { const f32x4 v = ldsf4(a + j * 4096 + q * 1024); r[4 * q] += v[0]; r[4 * q + 1] += v[1]; r[4 * q + 2] += v[2]; r[4 * q + 3] += v[3]; }
        char* op = (char*)P.out + (size_t)(tk + 32 * tt + r32) * 896 + 112 * g7 + 64 * nt + 16 * hh;
#pragma unroll
        for (int s = 0; s < 2; ++s) {
            u32x4 ov;
#pragma unroll
            for (int i = 0; i < 4; ++i)
                ov[i] = pk(r[8 * s + 2 * i] + blo(rx[s][i]) + blo(rh[s][i]), r[8 * s + 2 * i + 1] + bhi(rx[s][i]) + bhi(rh[s][i]));
            if (4 * nt + 2 * s + hh < 7) *(GAS u32x4*)(op + 32 * s) = ov;
        }
        STAMP(14);
    }
    if (b == 0 && w == 0) {
        for (int r = 0; r < 4; ++r) {
            const GAS unsigned* p = (const GAS unsigned*)(P.ws + flag_off(0, r * 64 + lane));
            for (unsigned sp = 0; sp < SPIN_CAP; ++sp) { unsigned v = __hip_atomic_load(p, __ATOMIC_RELAXED, __HIP_MEMORY_SCOPE_AGENT); if (__all(v == e)) break; }
        }
        if (lane == 0) __hip_atomic_store((GAS unsigned*)(P.ws + WS_EP), e, __ATOMIC_RELAXED, __HIP_MEMORY_SCOPE_AGENT);
    }
}
}

extern "C" void kernel_launch(void* const* d_in, const int* in_sizes, int n_in, void* d_out, int out_size, void* d_ws, size_t ws_size,
                              hipStream_t stream) {
    mk::Params P;
    P.x = (const u16*)d_in[0]; P.wqkv = (const u16*)d_in[1]; P.wo = (const u16*)d_in[2];
    P.w1 = (const u16*)d_in[4]; P.w2 = (const u16*)d_in[5]; P.w3 = (const u16*)d_in[6]; P.w4 = (const u16*)d_in[7];
    P.out = (u16*)d_out; P.ws = (char*)d_ws;
    mk::fused_fwd<<<256, 256, 0, stream>>>(P);
}
```

```cpp
#include <hip/hip_runtime.h>
#include <hip/hip_bf16.h>
#include <stdint.h>
#include <type_traits>
#include <utility>

typedef unsigned short u16;
typedef __attribute__((ext_vector_type(8))) short bf16x8;
typedef __attribute__((ext_vector_type(4))) short s16x4;
typedef __attribute__((ext_vector_type(16))) float f32x16;
typedef __attribute__((ext_vector_type(4))) float f32x4;
typedef __attribute__((ext_vector_type(2))) float f32x2;
typedef __attribute__((ext_vector_type(4))) unsigned u32x4;
typedef __attribute__((ext_vector_type(2))) unsigned u32x2;
typedef __attribute__((ext_vector_type(2))) __bf16 bf2;
#define LAS __attribute__((address_space(3)))
#define GAS __attribute__((address_space(1)))
#define DEVI __device__ __forceinline__

#define MK_STAMP 0

#if defined(__HIP_DEVICE_COMPILE__)
#pragma clang attribute push (__attribute__((target("no-packed-fp32-ops"))), apply_to = function)
#endif
namespace mk {
constexpr int S = 2048, D = 448;
constexpr unsigned WS_EP = 0;
constexpr unsigned WS_FLAG = 4096;
constexpr unsigned WS_KV = 262144;
constexpr unsigned WS_O = WS_KV + 4194304;
constexpr unsigned WS_H = WS_O + 2097152;
constexpr unsigned WS_HH = WS_H + 1835008;
constexpr unsigned WS_X1 = WS_HH + 1835008;
constexpr unsigned WS_U = WS_X1 + 1835008;
constexpr unsigned WS_STH = WS_U + 7340032;
constexpr unsigned WS_STX = WS_STH + 262144;
constexpr unsigned WS_ST1 = WS_STX + 262144;
constexpr unsigned WS_STAMP = WS_ST1 + 32768;
constexpr unsigned WS_END = WS_STAMP + 256 * 32 * 8;
constexpr unsigned L_SLOT = 28672;
constexpr unsigned L_MISC = 143360;
constexpr unsigned L_KV = 114688;
constexpr unsigned L_OWNKV = L_KV + 8 * 4096;
constexpr unsigned L_WST = 28672;
constexpr unsigned L_X = 114688;
constexpr unsigned L_M2 = 147456;
constexpr float EPS = 1e-5f;
constexpr unsigned SPIN_CAP = 1u << 18;

struct Params {
    const u16* x; const u16* wqkv; const u16* wo; const u16* w1; const u16* w2; const u16* w3; const u16* w4;
    u16* out; char* ws;
};

DEVI unsigned f2u(float f) { return __builtin_bit_cast(unsigned, f); }
DEVI float u2f(unsigned u) { return __builtin_bit_cast(float, u); }
DEVI bool wall(bool p) { return __builtin_amdgcn_ballot_w64(!p) == 0; }
template <int X> DEVI float xor32(float v) { return u2f((unsigned)__builtin_amdgcn_ds_swizzle((int)f2u(v), (X << 10) | 0x1f)); }
DEVI unsigned pk(float lo, float hi) { bf2 v = {(__bf16)lo, (__bf16)hi}; return __builtin_bit_cast(unsigned, v); }
DEVI float blo(unsigned u) { return u2f(u << 16); }
DEVI float bhi(unsigned u) { return u2f(u & 0xffff0000u); }
DEVI float dot2(unsigned a, unsigned b, float c) { return __builtin_amdgcn_fdot2_f32_bf16(__builtin_bit_cast(bf2, a), __builtin_bit_cast(bf2, b), c, false); }
constexpr unsigned ONES = 0x3f803f80u;
DEVI float hsum(float v) { auto r = __builtin_amdgcn_permlane32_swap(f2u(v), f2u(v), false, false); return u2f(r[0]) + u2f(r[1]); }
DEVI float hmax(float v) { auto r = __builtin_amdgcn_permlane32_swap(f2u(v), f2u(v), false, false); return __builtin_fmaxf(u2f(r[0]), u2f(r[1])); }
DEVI f32x16 mfma(u32x4 a, u32x4 b, f32x16 c) { return __builtin_amdgcn_mfma_f32_32x32x16_bf16(__builtin_bit_cast(bf16x8, a), __builtin_bit_cast(bf16x8, b), c, 0, 0, 0); }
DEVI u32x4 lds128(unsigned a) { return *(const LAS u32x4*)(uintptr_t)a; }
DEVI f32x4 ldsf4(unsigned a) { return *(const LAS f32x4*)(uintptr_t)a; }
DEVI void sts128(unsigned a, u32x4 v) { *(LAS u32x4*)(uintptr_t)a = v; }
DEVI void stsf4(unsigned a, f32x4 v) { *(LAS f32x4*)(uintptr_t)a = v; }
DEVI u32x2 ldstr(unsigned a) { return __builtin_bit_cast(u32x2, __builtin_amdgcn_ds_read_tr16_b64_v4i16((LAS s16x4*)(uintptr_t)a)); }
DEVI u32x4 wfrag(unsigned a0, unsigned a1) { u32x2 p = ldstr(a0), q = ldstr(a1); return (u32x4){p.x, p.y, q.x, q.y}; }
DEVI u32x4 pack8(const f32x16& a, int s) { return (u32x4){pk(a[8 * s], a[8 * s + 1]), pk(a[8 * s + 2], a[8 * s + 3]), pk(a[8 * s + 4], a[8 * s + 5]), pk(a[8 * s + 6], a[8 * s + 7])}; }
constexpr int SC1 = 16;
DEVI u32x4 gld(__amdgpu_buffer_rsrc_t rs, unsigned voff, unsigned soff) { return __builtin_amdgcn_raw_buffer_load_b128(rs, voff, soff, SC1); }
DEVI u32x2 gld8(__amdgpu_buffer_rsrc_t rs, unsigned voff, unsigned soff) { return __builtin_amdgcn_raw_buffer_load_b64(rs, voff, soff, SC1); }
DEVI void gst(__amdgpu_buffer_rsrc_t rs, u32x4 v, unsigned voff, unsigned soff) { __builtin_amdgcn_raw_buffer_store_b128(v, rs, voff + soff, 0, SC1); }
DEVI void gst8(__amdgpu_buffer_rsrc_t rs, u32x2 v, unsigned voff, unsigned soff) { __builtin_amdgcn_raw_buffer_store_b64(v, rs, voff, soff, SC1); }
#define WAIT_VM(N) asm volatile("s_waitcnt vmcnt(" #N ")" ::: "memory")
#define WAIT_LGKM() asm volatile("s_waitcnt lgkmcnt(0)" ::: "memory")
#define WAIT_BAR(N) asm volatile("s_waitcnt vmcnt(" #N ") lgkmcnt(0)\n\ts_barrier" ::: "memory")
#define LBAR() asm volatile("s_waitcnt lgkmcnt(0)\n\ts_barrier" ::: "memory")
#define SB() __builtin_amdgcn_sched_barrier(0)
#define PIN(x) asm volatile("" : "+v"(x))
DEVI float fma1(float a, float b, float c) { return __builtin_fmaf(a, b, c); }
DEVI float mul1(float a, float b) { return a * b; }
DEVI float max3(float a, float b, float c) { return __builtin_fmaxf(__builtin_fmaxf(a, b), c); }
template <int... Is, class F> DEVI void sfor_impl(std::integer_sequence<int, Is...>, F&& f) { (f(std::integral_constant<int, Is>{}), ...); }
template <int N, class F> DEVI void sfor(F&& f) { sfor_impl(std::make_integer_sequence<int, N>{}, f); }
#define AI __attribute__((always_inline))
#define IC(n) std::integral_constant<int, n>{}

DEVI void dma1(const char* sbase, unsigned voff, unsigned lds) {
    asm volatile("s_mov_b32 m0, %2\n\ts_nop 0\n\tglobal_load_lds_dwordx4 %0, %1" :: "v"(voff), "s"(sbase), "s"(lds) : "memory");
}
struct WSrc { const char* base; unsigned step, voff; };
DEVI void wpiece(const WSrc& s, unsigned lds, int n) { dma1(s.base + (size_t)n * s.step, s.voff, lds + n * 1024); }
DEVI WSrc wsrc_a(const char* base, unsigned rs, int nt, int lane) { const int t = (lane >> 4) < nt ? (lane >> 4) : nt - 1; return WSrc{base, 4 * rs, ((lane >> 2) & 3) * rs + t * 64 + (lane & 3) * 16}; }
DEVI WSrc wsrc_b(const char* base, unsigned rs, int lane) { return WSrc{base, 8 * rs, (4 * (lane >> 5) + ((lane >> 2) & 3)) * rs + ((lane >> 4) & 1) * 64 + (lane & 3) * 16}; }

DEVI void wait_flags(const char* ws, unsigned foff, int n, unsigned e, int lane) {
    const GAS unsigned* p = (const GAS unsigned*)(ws + foff) + (lane < n ? lane : 0) * 32;
    for (unsigned sp = 0; sp < SPIN_CAP; ++sp) {
        unsigned v = __hip_atomic_load(p, __ATOMIC_RELAXED, __HIP_MEMORY_SCOPE_AGENT);
        if (wall(v == e)) break;
    }
    __builtin_amdgcn_fence(__ATOMIC_ACQUIRE, "wavefront");
}
DEVI void set_flag(char* ws, unsigned foff, unsigned e) { __hip_atomic_store((GAS unsigned*)(ws + foff), e, __ATOMIC_RELAXED, __HIP_MEMORY_SCOPE_AGENT); }
DEVI unsigned flag_off(int ph, int idx) { return WS_FLAG + (ph * 256 + idx) * 128; }

constexpr float GC0 = -2.f * 0.7978845608028654f * 1.4426950408889634f, GC1 = GC0 * 0.044715f;
DEVI float gelu_t(float x) {
    float z = x * __builtin_fmaf(x * x, GC1, GC0);
    return x * __builtin_amdgcn_rcpf(1.f + __builtin_amdgcn_exp2f(z));
}
struct GeluK { float nmean, k1, k0, irs; };
DEVI void gelu_step(int o, float& y, float& z, float c, const GeluK& g) {
    if (o == 0) y = fma1(g.nmean, c, y);
    else if (o == 1) z = mul1(y, y);
    else if (o == 2) z = fma1(z, g.k1, g.k0);
    else if (o == 3) z = mul1(y, z);
    else if (o == 4) z = __builtin_amdgcn_exp2f(z);
    else if (o == 5) z = fma1(z, g.irs, g.irs);
    else if (o == 6) z = __builtin_amdgcn_rcpf(z);
    else y = mul1(y, z);
}

#define STAMP(k) do {} while (0)
#define STAMP_DECL
#define STAMP_FLUSH() do {} while (0)

__global__ __launch_bounds__(256, 1) void fused_fwd(Params P) {
    __shared__ __attribute__((aligned(16))) char smem[163840];
    const unsigned L0 = (unsigned)(uintptr_t)(LAS char*)smem;
    const int tid = threadIdx.x, lane = tid & 63, w = __builtin_amdgcn_readfirstlane(tid >> 6);
    const int r32 = lane & 31, hh = lane >> 5;
    const int b = blockIdx.x, xcd = b & 7, slot = b >> 3;
    const unsigned e = *(const unsigned*)(P.ws + WS_EP) + 1u;
    const __amdgpu_buffer_rsrc_t rs = __builtin_amdgcn_make_buffer_rsrc((void*)P.ws, 0, WS_END, 0x00020000);
    const int trq = (lane & 15) >> 2, trQ = ((lane >> 4) & 1) * 4 + (lane & 3);
    const int gq = (trQ & 4) | ((trQ & 1) << 1) | ((trQ >> 1) & 1);
    STAMP_DECL
    STAMP(0);

    const int m = 2 * xcd + (slot >> 4), h = slot & 15;
    u32x4 qf0, qf1;
    {
        const unsigned xoffE = (lane >> 3) * 896 + (((lane & 7) ^ (lane >> 4)) << 4);
        const unsigned xoffO = (lane >> 3) * 896 + (((lane & 7) ^ (4 + (lane >> 4))) << 4);
        const unsigned colb = (56 * h) & ~15u, shift = (56 * h) & 15u;
        unsigned woff[3];
#pragma unroll
        for (int j = 0; j < 3; ++j) {
            const int bl = 4 * j + (lane >> 4), kql = bl / 3, tile = bl % 3;
            woff[j] = (4 * kql + ((lane >> 2) & 3)) * 2688 + tile * 896 + colb + (lane & 3) * 16;
        }
        const char* xb = (const char*)P.x + (size_t)(128 * m + 32 * w) * 896;
        const char* wb = (const char*)P.wqkv + (size_t)(16 * w) * 2688;
        auto piece = [&](int i, int kc, unsigned sl) AI {
            const unsigned base = L0 + sl * L_SLOT;
            if (i < 4) dma1(xb + kc * 128 + i * 8 * 896, (i & 1) ? xoffO : xoffE, base + (4 * w + i) * 1024);
            else dma1(wb + (size_t)kc * 64 * 2688, woff[i - 4], base + 16384 + (3 * w + i - 4) * 1024);
        };
        const int srow = 128 * m + 8 * h + 2 * w + hh;
        const char* sp = (const char*)P.x + (size_t)srow * 896 + r32 * 16;
        u32x4 sa, sb;
        asm volatile("global_load_dwordx4 %0, %2, off\n\tglobal_load_dwordx4 %1, %3, off" : "=&v"(sa), "=&v"(sb) : "v"(sp), "v"(sp + (r32 < 24 ? 512 : 0)) : "memory");
        SB();
#pragma unroll 1
        for (int c = 0; c < 4; ++c) {
#pragma unroll
            for (int i = 0; i < 7; ++i) piece(i, c, c);
        }
        STAMP(15);
        {
            asm volatile("s_waitcnt vmcnt(28)" : "+v"(sa), "+v"(sb) :: "memory");
            if (r32 >= 24) sb = (u32x4){0u, 0u, 0u, 0u};
            float t1 = 0.f, t2 = 0.f;
#pragma unroll
            for (int i = 0; i < 4; ++i) { t1 = dot2(sa[i], ONES, t1); t2 = dot2(sa[i], sa[i], t2); t1 = dot2(sb[i], ONES, t1); t2 = dot2(sb[i], sb[i], t2); }
            t1 += xor32<1>(t1); t2 += xor32<1>(t2); t1 += xor32<2>(t1); t2 += xor32<2>(t2); t1 += xor32<4>(t1); t2 += xor32<4>(t2);
            t1 += xor32<8>(t1); t2 += xor32<8>(t2); t1 += xor32<16>(t1); t2 += xor32<16>(t2);
            const float mean = t1 * (1.f / D), var = __builtin_fmaxf(t2 * (1.f / D) - mean * mean, 0.f);
            if (r32 == 0) gst(rs, (u32x4){f2u(mean), e, f2u(__builtin_amdgcn_rsqf(var + EPS)), e}, srow * 16, WS_ST1);
        }
        f32x16 aq = {}, ak = {}, av = {};
        float csq = 0.f, csk = 0.f, csv = 0.f;
        const unsigned xrd = (32 * w + r32) * 128 + ((hh ^ ((r32 >> 1) & 7)) << 4);
        const unsigned wrd = 16384 + hh * 6 * 256 + trq * 64 + ((shift + gq * 8) & 63);
        unsigned xa[4], wa[4];
#pragma unroll
        for (int j = 0; j < 4; ++j) { const int ks = (j + w) & 3; xa[j] = xrd ^ (ks << 5); wa[j] = wrd + ks * 3072; }
        struct Fr { u32x4 x, q, k, v; };
        Fr F[4];
        auto ldx = [&](unsigned base, int j) AI { F[j].x = lds128(base + xa[j]); const unsigned a = base + wa[j]; F[j].q = wfrag(a, a + 768); };
        auto ldk = [&](unsigned base, int j) AI { const unsigned a = base + wa[j]; F[j].k = wfrag(a + 256, a + 1024); };
        auto ldv = [&](unsigned base, int j) AI { const unsigned a = base + wa[j]; F[j].v = wfrag(a + 512, a + 1280); };
        WAIT_BAR(21);
        STAMP(16);
#pragma unroll
        for (int j = 0; j < 2; ++j) { ldx(L0, j); ldk(L0, j); ldv(L0, j); }
        SB();
#pragma unroll 1
        for (int c = 0; c < 7; ++c) {
            const unsigned base = L0 + (c >= 5 ? c - 5 : c) * L_SLOT, nbase = L0 + (c >= 4 ? c - 4 : c + 1) * L_SLOT;
            const bool dm = c < 3;
            const unsigned dsl = c >= 1 ? c - 1 : 4;
            sfor<4>([&](auto jc) AI {
                constexpr int j = decltype(jc)::value, jn = (j + 2) & 3;
                if constexpr (j == 2) {
                    if (c < 3) WAIT_BAR(21); else if (c == 3) WAIT_BAR(14); else if (c == 4) WAIT_BAR(7); else WAIT_BAR(0);
                    SB();
                }
                const unsigned lb = j < 2 ? base : nbase;
                aq = mfma(F[j].q, F[j].x, aq);
                ldx(lb, jn);
                if constexpr (j == 0) { csq = dot2(F[0].q[0], ONES, csq); csq = dot2(F[0].q[1], ONES, csq); csq = dot2(F[0].q[2], ONES, csq); csq = dot2(F[0].q[3], ONES, csq); PIN(csq); }
                if constexpr (j < 2) { if (dm) piece(3 * j, c + 4, dsl); }
                SB();
                ak = mfma(F[j].k, F[j].x, ak);
                ldk(lb, jn);
                if constexpr (j == 0) { csk = dot2(F[0].k[0], ONES, csk); csk = dot2(F[0].k[1], ONES, csk); csk = dot2(F[0].k[2], ONES, csk); csk = dot2(F[0].k[3], ONES, csk); PIN(csk); }
                if constexpr (j < 2) { if (dm) piece(3 * j + 1, c + 4, dsl); }
                SB();
                av = mfma(F[j].x, F[j].v, av);
                ldv(lb, jn);
                if constexpr (j == 0) { csv = dot2(F[0].v[0], ONES, csv); csv = dot2(F[0].v[1], ONES, csv); csv = dot2(F[0].v[2], ONES, csv); csv = dot2(F[0].v[3], ONES, csv); PIN(csv); }
                if constexpr (j < 2) { if (dm) piece(3 * j + 2, c + 4, dsl); }
                if constexpr (j == 1) { if (dm) piece(6, c + 4, dsl); }
                SB();
            });
        }
        STAMP(1);
        float mean, rstd;
        {
            const unsigned go = (128 * m + 32 * w + r32) * 16;
            u32x4 gv;
            for (unsigned sp2 = 0; sp2 < SPIN_CAP; ++sp2) { gv = gld(rs, go, WS_ST1); if (wall(gv.y == e && gv.w == e)) break; }
            mean = u2f(gv.x); rstd = u2f(gv.z);
        }
        csq = hsum(csq); csk = hsum(csk); csv = hsum(csv);
        LAS float* csp = (LAS float*)(uintptr_t)(L0 + L_MISC);
        LAS float* csw = (LAS float*)(uintptr_t)(L0 + L_MISC + 1536 + w * 384);
        LAS float* stw = (LAS float*)(uintptr_t)(L0 + L_MISC + 3072 + w * 256);
        if (hh == 0) { csp[w * 96 + r32] = csq; csp[w * 96 + 32 + r32] = csk; csp[w * 96 + 64 + r32] = csv; stw[r32] = mean * rstd; stw[32 + r32] = rstd; }
        LBAR();
        float cq = 0.f, ck = 0.f, cv = 0.f;
#pragma unroll
        for (int ww = 0; ww < 4; ++ww) { cq += csp[ww * 96 + r32]; ck += csp[ww * 96 + 32 + r32]; cv += csp[ww * 96 + 64 + r32]; }
        if (hh == 0) { csw[r32] = cq; csw[32 + r32] = ck; }
        WAIT_LGKM();
        const float qs = rstd * (0.18898223650461362f * 1.4426950408889634f);
#pragma unroll
        for (int g = 0; g < 4; ++g) {
            const f32x4 c1 = *(const LAS f32x4*)(csw + 8 * g + 4 * hh), c2 = *(const LAS f32x4*)(csw + 32 + 8 * g + 4 * hh);
            const f32x4 mr = *(const LAS f32x4*)(stw + 8 * g + 4 * hh), rr = *(const LAS f32x4*)(stw + 32 + 8 * g + 4 * hh);
#pragma unroll
            for (int i = 0; i < 4; ++i) {
                aq[4 * g + i] = (aq[4 * g + i] - mean * c1[i]) * qs;
                ak[4 * g + i] = (ak[4 * g + i] - mean * c2[i]) * rstd;
                av[4 * g + i] = av[4 * g + i] * rr[i] - mr[i] * cv;
            }
        }
        if (hh == 1) { aq[12] = 1.f; aq[13] = 0.f; aq[14] = 0.f; aq[15] = 0.f; ak[12] = 0.f; ak[13] = 0.f; ak[14] = 0.f; ak[15] = 0.f; }
        if (r32 >= 28) {
            const float pv = r32 == 28 ? 1.f : 0.f;
#pragma unroll
            for (int i = 0; i < 16; ++i) av[i] = pv;
        }
        qf0 = pack8(aq, 0); qf1 = pack8(aq, 1);
        const u32x4 k0 = pack8(ak, 0), k1 = pack8(ak, 1), v0 = pack8(av, 0), v1 = pack8(av, 1);
        const unsigned go = WS_KV + (h * 64 + 4 * m + w) * 4096;
        gst(rs, k0, lane * 16, go); gst(rs, k1, lane * 16, go + 1024); gst(rs, v0, lane * 16, go + 2048); gst(rs, v1, lane * 16, go + 3072);
        const unsigned lo = L0 + L_OWNKV + w * 4096 + lane * 16;
        sts128(lo, k0); sts128(lo + 1024, k1); sts128(lo + 2048, v0); sts128(lo + 3072, v1);
        WAIT_BAR(0);
        if (tid == 0) set_flag(P.ws, flag_off(0, m * 16 + h), e);
        STAMP(2);
    }
    const int g = slot & 3, nt = g < 2 ? 4 : 3, tb = g < 2 ? 4 * g : 3 * g + 2;
    const unsigned wst = L0 + w * L_WST;
    {
        const int mt = m - 2 + (w >> 1);
        u32x4 kv[8];
        if (mt >= 0) {
            wait_flags(P.ws, flag_off(0, mt * 16 + h), 1, e, lane);
            const unsigned go = WS_KV + (h * 64 + 4 * m - 8 + 2 * w) * 4096;
#pragma unroll
            for (int i = 0; i < 8; ++i) kv[i] = gld(rs, lane * 16, go + i * 1024);
        } else {
#pragma unroll
            for (int i = 0; i < 8; ++i) kv[i] = (u32x4){0u, 0u, (i & 3) == 1 && hh == 1 ? 0xF149u : 0u, 0u};
        }
        f32x16 cdiag, cold;
#pragma unroll
        for (int i = 0; i < 16; ++i) { const int kr = (i & 3) + 8 * (i >> 2) + 4 * hh; cdiag[i] = kr <= r32 ? 0.f : -1e30f; cold[i] = kr > r32 ? 0.f : -1e30f; }
#pragma unroll
        for (int i = 0; i < 8; ++i) sts128(L0 + L_KV + 2 * w * 4096 + i * 1024 + lane * 16, kv[i]);
        LBAR();
        STAMP(3);
        const WSrc wos = wsrc_a((const char*)P.wo + (size_t)(112 * w) * 896 + tb * 64, 896, nt, lane);
        const unsigned kvb = L0 + L_KV + w * 4096 + lane * 16;
        u32x4 kf[9][2], vf[9][2];
        const f32x16 zero16 = {};
        auto smax = [&](const f32x16& s, float a) AI {
#pragma unroll
            for (int i = 0; i < 16; i += 2) a = max3(a, s[i], s[i + 1]);
            return a; };
#pragma unroll
        for (int i = 0; i < 2; ++i) { kf[i][0] = lds128(kvb + i * 4096); kf[i][1] = lds128(kvb + i * 4096 + 1024); }
        float mx = -3e38f;
        f32x16 s[2];
#pragma unroll
        for (int i = 0; i < 9; ++i) {
            if (i + 2 < 9) { kf[i + 2][0] = lds128(kvb + (i + 2) * 4096); kf[i + 2][1] = lds128(kvb + (i + 2) * 4096 + 1024); }
            vf[i][0] = lds128(kvb + i * 4096 + 2048); vf[i][1] = lds128(kvb + i * 4096 + 3072);
            s[i & 1] = mfma(kf[i][0], qf0, i == 0 ? cold : i == 8 ? cdiag : zero16);
            wpiece(wos, wst, i);
            s[i & 1] = mfma(kf[i][1], qf1, s[i & 1]);
            if (i > 0) mx = smax(s[(i - 1) & 1], mx);
            SB();
        }
        mx = hmax(smax(s[0], mx));
        STAMP(25);
        f32x16 o = {}, cm;
#pragma unroll
        for (int i = 0; i < 16; ++i) { cm[i] = -mx; cdiag[i] -= mx; cold[i] -= mx; }
        s[0] = mfma(kf[0][0], qf0, cold); s[0] = mfma(kf[0][1], qf1, s[0]);
        SB();
#pragma unroll
        for (int i = 0; i < 9; ++i) {
            f32x16& c = s[i & 1];
            f32x16& n = s[(i + 1) & 1];
            if (i + 1 < 9) n = mfma(kf[i + 1][0], qf0, i + 1 == 8 ? cdiag : cm);
#pragma unroll
            for (int j = 0; j < 8; ++j) c[j] = __builtin_amdgcn_exp2f(c[j]);
            const u32x4 p0 = pack8(c, 0);
            wpiece(wos, wst, 9 + 2 * i);
            SB();
            if (i + 1 < 9) n = mfma(kf[i + 1][1], qf1, n);
            o = mfma(vf[i][0], p0, o);
#pragma unroll
            for (int j = 8; j < 16; ++j) c[j] = __builtin_amdgcn_exp2f(c[j]);
            const u32x4 p1 = pack8(c, 1);
            wpiece(wos, wst, 10 + 2 * i);
            o = mfma(vf[i][1], p1, o);
            SB();
        }
        wpiece(wos, wst, 27);
        STAMP(26);
        auto lr = __builtin_amdgcn_permlane32_swap(f2u(o[12]), f2u(o[12]), false, false);
        const float inv = __builtin_amdgcn_rcpf(u2f(lr[1]));
#pragma unroll
        for (int i = 0; i < 16; ++i) o[i] *= inv;
        if (hh == 1) { o[12] = 0.f; o[13] = 0.f; o[14] = 0.f; o[15] = 0.f; }
        const unsigned vo = (hh * 2048 + r32) * 16, so = WS_O + ((4 * h) * 2048 + 128 * m + 32 * w) * 16;
        gst(rs, pack8(o, 0), vo, so); gst(rs, pack8(o, 1), vo, so + 2 * 2048 * 16);
        WAIT_BAR(0);
        if (tid == 0) set_flag(P.ws, flag_off(1, m * 16 + h), e);
        STAMP(4);
    }
    const int ti = 8 * xcd + (slot >> 2);
    const unsigned trW = L0 + (w < nt ? w : nt - 1) * 256 + trq * 64 + gq * 8 + 2 * hh * 1024;
    const unsigned vx = (hh * 2048 + r32) * 16;
    const unsigned xl = L0 + L_X + lane * 16;
    const bool act = w < nt;
    const int tw = tb + w;
    LAS float* m2 = (LAS float*)(uintptr_t)(L0 + L_M2 + w * 256);
    auto gemm = [&](auto nk, const u32x4* wr, const WSrc& nx) AI -> f32x16 {
        constexpr int NK = decltype(nk)::value, PD = 4;
        f32x16 acc = {};
        u32x4 xf[NK];
#pragma unroll
        for (int k = 0; k < PD; ++k) xf[k] = lds128(xl + k * 1024);
        SB();
        sfor<NK>([&](auto kc) AI {
            constexpr int k = decltype(kc)::value;
            if constexpr (k + PD < NK) xf[k + PD] = lds128(xl + (k + PD) * 1024);
            acc = mfma(wr[k], xf[k], acc);
            if constexpr ((k & 1) == 0 && k / 2 < 14) wpiece(nx, wst, 14 + k / 2);
            SB();
        });
        return acc;
    };
    u32x4 hp[2];
    float meanH, rstdH;
    {
        u32x4 xr[2];
        const char* xp = (const char*)P.x + (size_t)(32 * ti + r32) * 896 + 64 * (act ? tw : tb) + 16 * hh;
        xr[0] = *(const GAS u32x4*)xp; xr[1] = *(const GAS u32x4*)(xp + 32);
        u32x4 wr[32];
#pragma unroll
        for (int hd = 0; hd < 16; ++hd)
#pragma unroll
            for (int s = 0; s < 2; ++s) {
                const unsigned o0 = (7 * hd + 4 * s) * 1024;
                const unsigned o1 = (hd == 15 && s == 1) ? (hh ? o0 : o0 + 1024) : o0 + 1024;
                wr[2 * hd + s] = wfrag(trW + o0, trW + o1);
            }
        PIN(xr[0]); PIN(xr[1]);
        LBAR();
        const WSrc nx = wsrc_a((const char*)P.w1 + (size_t)(112 * w) * 896 + tb * 64, 896, nt, lane);
#pragma unroll
        for (int n = 0; n < 14; ++n) wpiece(nx, wst, n);
        wait_flags(P.ws, flag_off(1, (ti >> 2) * 16 + 4 * w), 4, e, lane);
        u32x4 xf[8];
#pragma unroll
        for (int k = 0; k < 8; ++k) xf[k] = gld(rs, vx, WS_O + ((16 * w + 2 * k) * 2048 + 32 * ti) * 16);
#pragma unroll
        for (int k = 0; k < 8; ++k) sts128(xl + (8 * w + k) * 1024, xf[k]);
        LBAR();
        STAMP(5);
        f32x16 r = gemm(IC(32), wr, nx);
        float s1 = 0.f, s2 = 0.f;
#pragma unroll
        for (int s = 0; s < 2; ++s) {
#pragma unroll
            for (int i = 0; i < 4; ++i) {
                hp[s][i] = pk(r[8 * s + 2 * i] + blo(xr[s][i]), r[8 * s + 2 * i + 1] + bhi(xr[s][i]));
                s1 = dot2(hp[s][i], ONES, s1); s2 = dot2(hp[s][i], hp[s][i], s2);
            }
            if (act) gst(rs, hp[s], vx, WS_H + ((4 * tw + 2 * s) * 2048 + 32 * ti) * 16);
        }
        s1 = hsum(s1); s2 = hsum(s2);
        if (act && hh == 0) gst8(rs, (u32x2){f2u(s1), f2u(s2)}, r32 * 8, WS_STH + (tw * 2048 + 32 * ti) * 8);
        WAIT_BAR(0);
        if (tid == 0) set_flag(P.ws, flag_off(2, ti * 4 + g), e);
        STAMP(6);
    }
    auto load_wr = [&](u32x4 (&wr)[28]) AI {
#pragma unroll
        for (int k = 0; k < 28; ++k) wr[k] = wfrag(trW + 4 * k * 1024, trW + (4 * k + 1) * 1024);
    };
    const int fs0 = w == 0 ? 0 : w - 1 + (w == 1 ? 0 : 0), fsn = w == 0 ? 1 : 2;
    const int fsl = w == 0 ? 0 : w == 1 ? 0 : w == 2 ? 1 : 2;
    (void)fs0;
    {
        u32x4 wr[28];
        load_wr(wr);
        LBAR();
        const WSrc nx = wsrc_a((const char*)P.w2 + (size_t)(112 * w) * 896 + tb * 64, 896, nt, lane);
#pragma unroll
        for (int n = 0; n < 14; ++n) wpiece(nx, wst, n);
        {
            float cs[4] = {0.f, 0.f, 0.f, 0.f};
#pragma unroll
            for (int k = 0; k < 28; ++k)
#pragma unroll
                for (int i = 0; i < 4; ++i) cs[i] = dot2(wr[k][i], ONES, cs[i]);
            const float c = hsum((cs[0] + cs[1]) + (cs[2] + cs[3]));
            if (hh == 0) m2[r32] = c;
        }
        wait_flags(P.ws, flag_off(2, ti * 4 + fsl), fsn, e, lane);
        u32x4 xf[7];
#pragma unroll
        for (int k = 0; k < 7; ++k) xf[k] = gld(rs, vx, WS_H + ((14 * w + 2 * k) * 2048 + 32 * ti) * 16);
#pragma unroll
        for (int k = 0; k < 7; ++k) sts128(xl + (7 * w + k) * 1024, xf[k]);
        LBAR();
        u32x2 sv[7];
#pragma unroll
        for (int q = 0; q < 7; ++q) sv[q] = gld8(rs, (7 * hh * 2048 + r32) * 8, WS_STH + (q * 2048 + 32 * ti) * 8);
        f32x4 c[4];
#pragma unroll
        for (int q = 0; q < 4; ++q) c[q] = *(const LAS f32x4*)(m2 + 8 * q + 4 * hh);
#pragma unroll
        for (int q = 0; q < 7; ++q) PIN(sv[q]);
        STAMP(7);
        f32x16 r = gemm(IC(28), wr, nx);
        float t1 = 0.f, t2 = 0.f;
#pragma unroll
        for (int q = 0; q < 7; ++q) { t1 += u2f(sv[q].x); t2 += u2f(sv[q].y); }
        t1 = hsum(t1); t2 = hsum(t2);
        meanH = t1 * (1.f / D);
        const float var = __builtin_fmaxf(t2 * (1.f / D) - meanH * meanH, 0.f);
        rstdH = __builtin_amdgcn_rsqf(var + EPS);
        const float sc = rstdH * __builtin_amdgcn_rsqf(var * rstdH * rstdH + EPS);
#pragma unroll
        for (int q = 0; q < 4; ++q)
#pragma unroll
            for (int i = 0; i < 4; ++i) r[4 * q + i] = gelu_t((r[4 * q + i] - meanH * c[q][i]) * sc);
#pragma unroll
        for (int s = 0; s < 2; ++s) if (act) gst(rs, pack8(r, s), vx, WS_HH + ((4 * tw + 2 * s) * 2048 + 32 * ti) * 16);
        WAIT_BAR(0);
        if (tid == 0) set_flag(P.ws, flag_off(3, ti * 4 + g), e);
        STAMP(8);
    }
    const int s16 = slot & 15, pr = s16 >> 1;
    const int nt6 = ((s16 ^ pr) & 1) ? 3 : 4, tb6 = 7 * pr + ((s16 & 1) ? (pr & 1 ? 3 : 4) : 0);
    {
        u32x4 wr[28];
        load_wr(wr);
        LBAR();
        const WSrc nx = wsrc_a((const char*)P.w3 + (size_t)(112 * w) * 3584 + tb6 * 64, 3584, nt6, lane);
#pragma unroll
        for (int n = 0; n < 14; ++n) wpiece(nx, wst, n);
        const int sh = w == 0 ? -1 : w == 1 ? 1 : w == 2 ? -2 : 2;
        const int tn = ti + (sh < 0 ? -1 : 1);
        {
            const bool nb = tn >= 0 && tn < 64;
            const int li = lane & 3;
            const GAS unsigned* p = (const GAS unsigned*)(P.ws + flag_off(3, (((li & 2) && nb) ? tn : ti) * 4 + fsl + ((li & 1) && fsn == 2 ? 1 : 0)));
            for (unsigned sp = 0; sp < SPIN_CAP; ++sp) { unsigned v = __hip_atomic_load(p, __ATOMIC_RELAXED, __HIP_MEMORY_SCOPE_AGENT); if (wall(v == e)) break; }
            __builtin_amdgcn_fence(__ATOMIC_ACQUIRE, "wavefront");
        }
        const int tok = 32 * ti + r32 + sh;
        const bool ok = tok >= 0 && tok < S;
        const unsigned vxs = ok ? (hh * 2048 + tok) * 16 : 0x7ffffff0u;
        u32x4 xf[7];
#pragma unroll
        for (int k = 0; k < 7; ++k) { xf[k] = gld(rs, vxs, WS_HH + (14 * w + 2 * k) * 2048 * 16); }
#pragma unroll
        for (int k = 0; k < 7; ++k) sts128(xl + (7 * w + k) * 1024, xf[k]);
        LBAR();
        STAMP(9);
        f32x16 r = gemm(IC(28), wr, nx);
        float s1 = 0.f, s2 = 0.f;
#pragma unroll
        for (int s = 0; s < 2; ++s) {
            u32x4 xp;
#pragma unroll
            for (int i = 0; i < 4; ++i) {
                xp[i] = pk(r[8 * s + 2 * i] + (blo(hp[s][i]) - meanH) * rstdH, r[8 * s + 2 * i + 1] + (bhi(hp[s][i]) - meanH) * rstdH);
                s1 = dot2(xp[i], ONES, s1); s2 = dot2(xp[i], xp[i], s2);
            }
            if (act) gst(rs, xp, vx, WS_X1 + ((4 * tw + 2 * s) * 2048 + 32 * ti) * 16);
        }
        s1 = hsum(s1); s2 = hsum(s2);
        if (act && hh == 0) gst8(rs, (u32x2){f2u(s1), f2u(s2)}, r32 * 8, WS_STX + (tw * 2048 + 32 * ti) * 8);
        WAIT_BAR(0);
        if (tid == 0) set_flag(P.ws, flag_off(4, ti * 4 + g), e);
        STAMP(10);
    }
    const bool act7 = slot < 28;
    const int j7 = 4 * xcd + (act7 ? slot / 7 : 0), g7 = act7 ? slot % 7 : 0;
    const WSrc w4a = wsrc_b((const char*)P.w4 + (size_t)(448 * w) * 896 + g7 * 128, 896, lane);
    {
        unsigned tr6[4];
#pragma unroll
        for (int q = 0; q < 4; ++q) { tr6[q] = L0 + q * L_WST + trq * 64 + gq * 8 + 2 * hh * 1024; PIN(tr6[q]); }
        auto wf6 = [&](int t, int k) AI { const unsigned a = tr6[k / 7] + t * 256 + 4 * (k % 7) * 1024; return wfrag(a, a + 1024); };
        LAS float* cs6 = (LAS float*)(uintptr_t)(L0 + L_M2);
        {
            float c[4] = {0.f, 0.f, 0.f, 0.f};
#pragma unroll
            for (int k = 0; k < 28; ++k) { const unsigned a = tr6[k / 7] + w * 256 + 4 * (k % 7) * 1024; const u32x4 f = wfrag(a, a + 1024);
#pragma unroll
                for (int i = 0; i < 4; ++i) c[i] = dot2(f[i], ONES, c[i]); }
            const float cc = hsum((c[0] + c[1]) + (c[2] + c[3]));
            if (hh == 0) cs6[w * 32 + r32] = cc;
        }
        LBAR();
        wait_flags(P.ws, flag_off(4, (4 * m + w) * 4), 4, e, lane);
        const int tk = 128 * m + 32 * w;
        constexpr int PX = 10, PW = 3;
        u32x4 xf[28];
#pragma unroll
        for (int k = 0; k < PX; ++k) xf[k] = gld(rs, vx, WS_X1 + (2 * k * 2048 + tk) * 16);
        u32x2 sv[7];
#pragma unroll
        for (int q = 0; q < 7; ++q) sv[q] = gld8(rs, (7 * hh * 2048 + r32) * 8, WS_STX + (q * 2048 + tk) * 8);
        STAMP(11);
        f32x16 a0 = {}, a1 = {};
        {
            u32x4 wA[28], wB[28];
#pragma unroll
            for (int k = 0; k < PW; ++k) { wA[k] = wf6(0, k); wB[k] = wf6(1, k); }
            SB();
            sfor<28>([&](auto kc) AI {
                constexpr int k = decltype(kc)::value;
                if constexpr (k + PW < 28) { wA[k + PW] = wf6(0, k + PW); wB[k + PW] = wf6(1, k + PW); }
                a0 = mfma(wA[k], xf[k], a0);
                if constexpr (k + PX < 28) xf[k + PX] = gld(rs, vx, WS_X1 + (2 * (k + PX) * 2048 + tk) * 16);
                a1 = mfma(wB[k], xf[k], a1);
                SB();
            });
        }
        float t1 = 0.f, t2 = 0.f;
#pragma unroll
        for (int q = 0; q < 7; ++q) { t1 += u2f(sv[q].x); t2 += u2f(sv[q].y); }
        t1 = hsum(t1); t2 = hsum(t2);
        const float mean = t1 * (1.f / D), var = __builtin_fmaxf(t2 * (1.f / D) - mean * mean, 0.f) + EPS, rstd = __builtin_amdgcn_rsqf(var);
        const GeluK gk = {-mean, GC1 * rstd * rstd * rstd, GC0 * rstd, var * rstd};
        const unsigned uo = WS_U + ((4 * tb6) * 2048 + tk) * 16;
        auto tile_epi = [&](auto tmc, f32x16& an, auto tec, f32x16& y) AI {
            constexpr int tm = decltype(tmc)::value, te = decltype(tec)::value, PD = 4;
            f32x4 c[4];
#pragma unroll
            for (int q = 0; q < 4; ++q) c[q] = *(const LAS f32x4*)(cs6 + te * 32 + 8 * q + 4 * hh);
            float z[16];
            u32x4 wf[28];
#pragma unroll
            for (int k = 0; k < PD; ++k) wf[k] = wf6(tm, k);
            SB();
            sfor<28>([&](auto kc) AI {
                constexpr int k = decltype(kc)::value;
                if constexpr (k + PD < 28) wf[k + PD] = wf6(tm, k + PD);
                an = mfma(wf[k], xf[k], an);
                sfor<16>([&](auto ec) AI {
                    constexpr int el = decltype(ec)::value, o = k - (el * 5) / 4;
                    if constexpr (o >= 0 && o < 8) { float yy = y[el]; gelu_step(o, yy, z[el], c[el >> 2][el & 3], gk); if (o == 0 || o == 7) PIN(yy); else PIN(z[el]); y[el] = yy; }
                });
                if constexpr (k == 26) gst(rs, pack8(y, 0), vx, uo + 4 * te * 2048 * 16);
                if constexpr (k == 27) gst(rs, pack8(y, 1), vx, uo + (4 * te + 2) * 2048 * 16);
                SB();
            });
        };
        STAMP(23);
        f32x16 a2 = {}, a3 = {};
        tile_epi(IC(2), a2, IC(0), a0);
        tile_epi(IC(3), a3, IC(1), a1);
        STAMP(24);
        LBAR();
        auto epi = [&](f32x16& y, int te, int p0) AI {
#pragma unroll
            for (int q = 0; q < 4; ++q) {
                const f32x4 c = *(const LAS f32x4*)(cs6 + te * 32 + 8 * q + 4 * hh);
#pragma unroll
                for (int i = 0; i < 4; ++i) y[4 * q + i] = gelu_t((y[4 * q + i] - mean * c[i]) * rstd);
#pragma unroll
                for (int i = 0; i < 4; ++i) if (p0 + 4 * q + i < 28 && act7) wpiece(w4a, wst, p0 + 4 * q + i);
                SB();
            }
            gst(rs, pack8(y, 0), vx, uo + 4 * te * 2048 * 16); gst(rs, pack8(y, 1), vx, uo + (4 * te + 2) * 2048 * 16);
        };
        epi(a2, 2, 0);
        if (nt6 == 4) epi(a3, 3, 16);
        else if (act7) {
#pragma unroll
            for (int n = 16; n < 28; ++n) wpiece(w4a, wst, n);
        }
        WAIT_BAR(0);
        if (tid == 0) set_flag(P.ws, flag_off(5, m * 16 + s16), e);
        STAMP(12);
    }
    if (act7) {
        const unsigned tA = wst + trq * 64 + gq * 8 + 2 * hh * 512;
        u32x4 wr[28][2];
#pragma unroll
        for (int k = 0; k < 14; ++k) { wr[k][0] = wfrag(tA + 4 * k * 512, tA + (4 * k + 1) * 512); wr[k][1] = wfrag(tA + 4 * k * 512 + 256, tA + (4 * k + 1) * 512 + 256); }
        WAIT_LGKM(); SB();
        { WSrc w4h = w4a; w4h.base += (size_t)224 * 896;
#pragma unroll
          for (int n = 0; n < 28; ++n) wpiece(w4h, wst, n); }
        const int tk = 64 * j7;
        const int tt = w >> 1, nt7 = w & 1;
        wait_flags(P.ws, flag_off(5, (j7 >> 1) * 16 + 4 * w), 4, e, lane);
        constexpr int PX = 6;
        u32x4 xa[28][2];
#pragma unroll
        for (int k = 0; k < PX; ++k) { xa[k][0] = gld(rs, vx, WS_U + ((56 * w + 2 * k) * 2048 + tk) * 16); xa[k][1] = gld(rs, vx, WS_U + ((56 * w + 2 * k) * 2048 + tk + 32) * 16); }
        STAMP(13);
        SB();
        f32x16 acc[4] = {};
        u32x4 rh[2], rx[2];
        sfor<28>([&](auto kc) AI {
            constexpr int k = decltype(kc)::value;
            if constexpr (k + 3 >= 14 && k + 3 < 28) { constexpr int kk = k + 3 - 14; wr[k + 3][0] = wfrag(tA + 4 * kk * 512, tA + (4 * kk + 1) * 512); wr[k + 3][1] = wfrag(tA + 4 * kk * 512 + 256, tA + (4 * kk + 1) * 512 + 256); }
            acc[0] = mfma(wr[k][0], xa[k][0], acc[0]); acc[1] = mfma(wr[k][1], xa[k][0], acc[1]);
            if constexpr (k + PX < 28) xa[k + PX][0] = gld(rs, vx, WS_U + ((56 * w + 2 * (k + PX)) * 2048 + tk) * 16);
            acc[2] = mfma(wr[k][0], xa[k][1], acc[2]); acc[3] = mfma(wr[k][1], xa[k][1], acc[3]);
            if constexpr (k + PX < 28) xa[k + PX][1] = gld(rs, vx, WS_U + ((56 * w + 2 * (k + PX)) * 2048 + tk + 32) * 16);
            if constexpr (k == 22 || k == 23) {
                constexpr int s = k - 22;
                const unsigned so = ((8 * g7 + 4 * nt7 + 2 * s) * 2048 + tk + 32 * tt) * 16;
                rh[s] = gld(rs, vx, WS_H + so); rx[s] = gld(rs, vx, WS_X1 + so);
            }
            SB();
        });
        STAMP(27);
#pragma unroll
        for (int t = 0; t < 4; ++t) {
            if (t != w) {
                const unsigned a = L0 + L_X + t * 12288 + (w < t ? w : w - 1) * 4096 + lane * 16;
#pragma unroll
                for (int q = 0; q < 4; ++q) stsf4(a + q * 1024, (f32x4){acc[t][4 * q], acc[t][4 * q + 1], acc[t][4 * q + 2], acc[t][4 * q + 3]});
            }
        }
        LBAR();
        f32x16 r = w == 0 ? acc[0] : w == 1 ? acc[1] : w == 2 ? acc[2] : acc[3];
        const unsigned a = L0 + L_X + w * 12288 + lane * 16;
#pragma unroll
        for (int j = 0; j < 3; ++j)
#pragma unroll
            for (int q = 0; q < 4; ++q) { const f32x4 v = ldsf4(a + j * 4096 + q * 1024); r[4 * q] += v[0]; r[4 * q + 1] += v[1]; r[4 * q + 2] += v[2]; r[4 * q + 3] += v[3]; }
        char* op = (char*)P.out + (size_t)(tk + 32 * tt + r32) * 896 + 128 * g7 + 64 * nt7 + 16 * hh;
#pragma unroll
        for (int s = 0; s < 2; ++s) {
            u32x4 ov;
#pragma unroll
            for (int i = 0; i < 4; ++i)
                ov[i] = pk(r[8 * s + 2 * i] + blo(rx[s][i]) + blo(rh[s][i]), r[8 * s + 2 * i + 1] + bhi(rx[s][i]) + bhi(rh[s][i]));
            *(GAS u32x4*)(op + 32 * s) = ov;
        }
        STAMP(14);
    }
    STAMP_FLUSH();
    if (b == 0 && w == 0) {
        for (int r = 0; r < 4; ++r) {
            const GAS unsigned* p = (const GAS unsigned*)(P.ws + flag_off(0, r * 64 + lane));
            for (unsigned sp = 0; sp < SPIN_CAP; ++sp) { unsigned v = __hip_atomic_load(p, __ATOMIC_RELAXED, __HIP_MEMORY_SCOPE_AGENT); if (wall(v == e)) break; }
        }
        if (lane == 0) __hip_atomic_store((GAS unsigned*)(P.ws + WS_EP), e, __ATOMIC_RELAXED, __HIP_MEMORY_SCOPE_AGENT);
    }
}
}
#if defined(__HIP_DEVICE_COMPILE__)
#pragma clang attribute pop
#endif

extern "C" void kernel_launch(void* const* d_in, const int* in_sizes, int n_in, void* d_out, int out_size, void* d_ws, size_t ws_size,
                              hipStream_t stream) {
    mk::Params P;
    P.x = (const u16*)d_in[0]; P.wqkv = (const u16*)d_in[1]; P.wo = (const u16*)d_in[2];
    P.w1 = (const u16*)d_in[4]; P.w2 = (const u16*)d_in[5]; P.w3 = (const u16*)d_in[6]; P.w4 = (const u16*)d_in[7];
    P.out = (u16*)d_out; P.ws = (char*)d_ws;
    mk::fused_fwd<<<256, 256, 0, stream>>>(P);
}
```

```cpp
#define TOUCH_CODE 1
#define TOUCH_W 1
#include <hip/hip_runtime.h>
#include <hip/hip_bf16.h>
#include <stdint.h>
#include <type_traits>
#include <utility>

typedef unsigned short u16;
typedef __attribute__((ext_vector_type(8))) short bf16x8;
typedef __attribute__((ext_vector_type(4))) short s16x4;
typedef __attribute__((ext_vector_type(16))) float f32x16;
typedef __attribute__((ext_vector_type(4))) float f32x4;
typedef __attribute__((ext_vector_type(2))) float f32x2;
typedef __attribute__((ext_vector_type(4))) unsigned u32x4;
typedef __attribute__((ext_vector_type(2))) unsigned u32x2;
typedef __attribute__((ext_vector_type(2))) __bf16 bf2;
#define LAS __attribute__((address_space(3)))
#define GAS __attribute__((address_space(1)))
#define DEVI __device__ __forceinline__

#define MK_STAMP 0

#if defined(__HIP_DEVICE_COMPILE__)
#pragma clang attribute push (__attribute__((target("no-packed-fp32-ops"))), apply_to = function)
#endif
namespace mk {
constexpr int S = 2048, D = 448;
constexpr unsigned WS_EP = 0;
constexpr unsigned WS_FLAG = 4096;
constexpr unsigned WS_KV = 262144;
constexpr unsigned WS_O = WS_KV + 4194304;
constexpr unsigned WS_H = WS_O + 2097152;
constexpr unsigned WS_HH = WS_H + 1835008;
constexpr unsigned WS_X1 = WS_HH + 1835008;
constexpr unsigned WS_U = WS_X1 + 1835008;
constexpr unsigned WS_STH = WS_U + 7340032;
constexpr unsigned WS_STX = WS_STH + 262144;
constexpr unsigned WS_ST1 = WS_STX + 262144;
constexpr unsigned WS_STAMP = WS_ST1 + 32768;
constexpr unsigned WS_END = WS_STAMP + 256 * 32 * 8;
constexpr unsigned L_SLOT = 28672;
constexpr unsigned L_MISC = 143360;
constexpr unsigned L_KV = 114688;
constexpr unsigned L_OWNKV = L_KV + 8 * 4096;
constexpr unsigned L_WST = 28672;
constexpr unsigned L_X = 114688;
constexpr unsigned L_M2 = 147456;
constexpr float EPS = 1e-5f;
constexpr unsigned SPIN_CAP = 1u << 18;

struct Params {
    const u16* x; const u16* wqkv; const u16* wo; const u16* w1; const u16* w2; const u16* w3; const u16* w4;
    u16* out; char* ws;
};

DEVI unsigned f2u(float f) { return __builtin_bit_cast(unsigned, f); }
DEVI float u2f(unsigned u) { return __builtin_bit_cast(float, u); }
DEVI bool wall(bool p) { return __builtin_amdgcn_ballot_w64(!p) == 0; }
template <int X> DEVI float xor32(float v) { return u2f((unsigned)__builtin_amdgcn_ds_swizzle((int)f2u(v), (X << 10) | 0x1f)); }
DEVI unsigned pk(float lo, float hi) { bf2 v = {(__bf16)lo, (__bf16)hi}; return __builtin_bit_cast(unsigned, v); }
DEVI float blo(unsigned u) { return u2f(u << 16); }
DEVI float bhi(unsigned u) { return u2f(u & 0xffff0000u); }
DEVI float dot2(unsigned a, unsigned b, float c) { return __builtin_amdgcn_fdot2_f32_bf16(__builtin_bit_cast(bf2, a), __builtin_bit_cast(bf2, b), c, false); }
constexpr unsigned ONES = 0x3f803f80u;
DEVI float hsum(float v) { auto r = __builtin_amdgcn_permlane32_swap(f2u(v), f2u(v), false, false); return u2f(r[0]) + u2f(r[1]); }
DEVI float hmax(float v) { auto r = __builtin_amdgcn_permlane32_swap(f2u(v), f2u(v), false, false); return __builtin_fmaxf(u2f(r[0]), u2f(r[1])); }
DEVI f32x16 mfma(u32x4 a, u32x4 b, f32x16 c) { return __builtin_amdgcn_mfma_f32_32x32x16_bf16(__builtin_bit_cast(bf16x8, a), __builtin_bit_cast(bf16x8, b), c, 0, 0, 0); }
DEVI u32x4 lds128(unsigned a) { return *(const LAS u32x4*)(uintptr_t)a; }
DEVI f32x4 ldsf4(unsigned a) { return *(const LAS f32x4*)(uintptr_t)a; }
DEVI void sts128(unsigned a, u32x4 v) { *(LAS u32x4*)(uintptr_t)a = v; }
DEVI void stsf4(unsigned a, f32x4 v) { *(LAS f32x4*)(uintptr_t)a = v; }
DEVI u32x2 ldstr(unsigned a) { return __builtin_bit_cast(u32x2, __builtin_amdgcn_ds_read_tr16_b64_v4i16((LAS s16x4*)(uintptr_t)a)); }
DEVI u32x4 wfrag(unsigned a0, unsigned a1) { u32x2 p = ldstr(a0), q = ldstr(a1); return (u32x4){p.x, p.y, q.x, q.y}; }
DEVI u32x4 pack8(const f32x16& a, int s) { return (u32x4){pk(a[8 * s], a[8 * s + 1]), pk(a[8 * s + 2], a[8 * s + 3]), pk(a[8 * s + 4], a[8 * s + 5]), pk(a[8 * s + 6], a[8 * s + 7])}; }
constexpr int SC1 = 16;
DEVI u32x4 gld(__amdgpu_buffer_rsrc_t rs, unsigned voff, unsigned soff) { return __builtin_amdgcn_raw_buffer_load_b128(rs, voff, soff, SC1); }
DEVI u32x2 gld8(__amdgpu_buffer_rsrc_t rs, unsigned voff, unsigned soff) { return __builtin_amdgcn_raw_buffer_load_b64(rs, voff, soff, SC1); }
DEVI void gst(__amdgpu_buffer_rsrc_t rs, u32x4 v, unsigned voff, unsigned soff) { __builtin_amdgcn_raw_buffer_store_b128(v, rs, voff + soff, 0, SC1); }
DEVI void gst8(__amdgpu_buffer_rsrc_t rs, u32x2 v, unsigned voff, unsigned soff) { __builtin_amdgcn_raw_buffer_store_b64(v, rs, voff, soff, SC1); }
#define WAIT_VM(N) asm volatile("s_waitcnt vmcnt(" #N ")" ::: "memory")
#define WAIT_LGKM() asm volatile("s_waitcnt lgkmcnt(0)" ::: "memory")
#define WAIT_BAR(N) asm volatile("s_waitcnt vmcnt(" #N ") lgkmcnt(0)\n\ts_barrier" ::: "memory")
#define LBAR() asm volatile("s_waitcnt lgkmcnt(0)\n\ts_barrier" ::: "memory")
#define SB() __builtin_amdgcn_sched_barrier(0)
#define PIN(x) asm volatile("" : "+v"(x))
DEVI float fma1(float a, float b, float c) { return __builtin_fmaf(a, b, c); }
DEVI float mul1(float a, float b) { return a * b; }
DEVI float max3(float a, float b, float c) { return __builtin_fmaxf(__builtin_fmaxf(a, b), c); }
template <int... Is, class F> DEVI void sfor_impl(std::integer_sequence<int, Is...>, F&& f) { (f(std::integral_constant<int, Is>{}), ...); }
template <int N, class F> DEVI void sfor(F&& f) { sfor_impl(std::make_integer_sequence<int, N>{}, f); }
#define AI __attribute__((always_inline))
#define IC(n) std::integral_constant<int, n>{}

DEVI void dma1(const char* sbase, unsigned voff, unsigned lds) {
    asm volatile("s_mov_b32 m0, %2\n\ts_nop 0\n\tglobal_load_lds_dwordx4 %0, %1" :: "v"(voff), "s"(sbase), "s"(lds) : "memory");
}
DEVI void touch(const char* sbase, unsigned voff, unsigned lds) {
    asm volatile("s_mov_b32 m0, %2\n\ts_nop 0\n\tglobal_load_lds_dword %0, %1" :: "v"(voff), "s"(sbase), "s"(lds) : "memory");
}
struct WSrc { const char* base; unsigned step, voff; };
DEVI void wpiece(const WSrc& s, unsigned lds, int n) { dma1(s.base + (size_t)n * s.step, s.voff, lds + n * 1024); }
DEVI WSrc wsrc_a(const char* base, unsigned rs, int nt, int lane) { const int t = (lane >> 4) < nt ? (lane >> 4) : nt - 1; return WSrc{base, 4 * rs, ((lane >> 2) & 3) * rs + t * 64 + (lane & 3) * 16}; }
DEVI WSrc wsrc_b(const char* base, unsigned rs, int lane) { return WSrc{base, 8 * rs, (4 * (lane >> 5) + ((lane >> 2) & 3)) * rs + ((lane >> 4) & 1) * 64 + (lane & 3) * 16}; }

DEVI void wait_flags(const char* ws, unsigned foff, int n, unsigned e, int lane) {
    const GAS unsigned* p = (const GAS unsigned*)(ws + foff) + (lane < n ? lane : 0) * 32;
    for (unsigned sp = 0; sp < SPIN_CAP; ++sp) {
        unsigned v = __hip_atomic_load(p, __ATOMIC_RELAXED, __HIP_MEMORY_SCOPE_AGENT);
        if (wall(v == e)) break;
    }
    __builtin_amdgcn_fence(__ATOMIC_ACQUIRE, "wavefront");
}
DEVI void set_flag(char* ws, unsigned foff, unsigned e) { __hip_atomic_store((GAS unsigned*)(ws + foff), e, __ATOMIC_RELAXED, __HIP_MEMORY_SCOPE_AGENT); }
DEVI unsigned flag_off(int ph, int idx) { return WS_FLAG + (ph * 256 + idx) * 128; }

constexpr float GC0 = -2.f * 0.7978845608028654f * 1.4426950408889634f, GC1 = GC0 * 0.044715f;
DEVI float gelu_t(float x) {
    float z = x * __builtin_fmaf(x * x, GC1, GC0);
    return x * __builtin_amdgcn_rcpf(1.f + __builtin_amdgcn_exp2f(z));
}
struct GeluK { float nmean, k1, k0, irs; };
DEVI void gelu_step(int o, float& y, float& z, float c, const GeluK& g) {
    if (o == 0) y = fma1(g.nmean, c, y);
    else if (o == 1) z = mul1(y, y);
    else if (o == 2) z = fma1(z, g.k1, g.k0);
    else if (o == 3) z = mul1(y, z);
    else if (o == 4) z = __builtin_amdgcn_exp2f(z);
    else if (o == 5) z = fma1(z, g.irs, g.irs);
    else if (o == 6) z = __builtin_amdgcn_rcpf(z);
    else y = mul1(y, z);
}

#define STAMP(k) do {} while (0)
#define STAMP_DECL
#define STAMP_FLUSH() do {} while (0)

__global__ __launch_bounds__(256, 1) void fused_fwd(Params P) {
    __shared__ __attribute__((aligned(16))) char smem[163840];
    const unsigned L0 = (unsigned)(uintptr_t)(LAS char*)smem;
    const int tid = threadIdx.x, lane = tid & 63, w = __builtin_amdgcn_readfirstlane(tid >> 6);
    const int r32 = lane & 31, hh = lane >> 5;
    const int b = blockIdx.x, xcd = b & 7, slot = b >> 3;
    const unsigned e = *(const unsigned*)(P.ws + WS_EP) + 1u;
    const __amdgpu_buffer_rsrc_t rs = __builtin_amdgcn_make_buffer_rsrc((void*)P.ws, 0, WS_END, 0x00020000);
    const int trq = (lane & 15) >> 2, trQ = ((lane >> 4) & 1) * 4 + (lane & 3);
    const int gq = (trQ & 4) | ((trQ & 1) << 1) | ((trQ >> 1) & 1);
    STAMP_DECL
    STAMP(0);

    if (w == 3) {
        unsigned long long pc; asm volatile("s_getpc_b64 %0" : "=s"(pc));
        touch((const char*)(pc & ~255ull), (slot * 12 + (lane & 15) % 12) * 128, L0 + L_MISC + 3840);
    }
    if (w == 2) {
        const unsigned o = (b * 64 + lane) * 128;
        touch((const char*)P.w3, o < 1605632 ? o : 0, L0 + L_MISC + 3840);
        touch((const char*)P.w4, o < 1605632 ? o : 0, L0 + L_MISC + 3840);
        const unsigned o2 = (b * 16 + (lane & 15)) * 128;
        const char* wp = lane < 16 ? (const char*)P.wo : lane < 32 ? (const char*)P.w1 : (const char*)P.w2;
        if (lane < 48) { asm volatile("s_mov_b32 m0, %1\n\ts_nop 0\n\tglobal_load_lds_dword %0, off" :: "v"(wp + (o2 < 401408 ? o2 : 0)), "s"(L0 + L_MISC + 3840) : "memory"); }
    }
    const int m = 2 * xcd + (slot >> 4), h = slot & 15;
    u32x4 qf0, qf1;
    {
        const unsigned xoffE = (lane >> 3) * 896 + (((lane & 7) ^ (lane >> 4)) << 4);
        const unsigned xoffO = (lane >> 3) * 896 + (((lane & 7) ^ (4 + (lane >> 4))) << 4);
        const unsigned colb = (56 * h) & ~15u, shift = (56 * h) & 15u;
        unsigned woff[3];
#pragma unroll
        for (int j = 0; j < 3; ++j) {
            const int bl = 4 * j + (lane >> 4), kql = bl / 3, tile = bl % 3;
            woff[j] = (4 * kql + ((lane >> 2) & 3)) * 2688 + tile * 896 + colb + (lane & 3) * 16;
        }
        const char* xb = (const char*)P.x + (size_t)(128 * m + 32 * w) * 896;
        const char* wb = (const char*)P.wqkv + (size_t)(16 * w) * 2688;
        auto piece = [&](int i, int kc, unsigned sl) AI {
            const unsigned base = L0 + sl * L_SLOT;
            if (i < 4) dma1(xb + kc * 128 + i * 8 * 896, (i & 1) ? xoffO : xoffE, base + (4 * w + i) * 1024);
            else dma1(wb + (size_t)kc * 64 * 2688, woff[i - 4], base + 16384 + (3 * w + i - 4) * 1024);
        };
        const int srow = 128 * m + 8 * h + 2 * w + hh;
        const char* sp = (const char*)P.x + (size_t)srow * 896 + r32 * 16;
        u32x4 sa, sb;
        asm volatile("global_load_dwordx4 %0, %2, off\n\tglobal_load_dwordx4 %1, %3, off" : "=&v"(sa), "=&v"(sb) : "v"(sp), "v"(sp + (r32 < 24 ? 512 : 0)) : "memory");
        SB();
#pragma unroll 1
        for (int c = 0; c < 4; ++c) {
#pragma unroll
            for (int i = 0; i < 7; ++i) piece(i, c, c);
        }
        STAMP(15);
        {
            asm volatile("s_waitcnt vmcnt(28)" : "+v"(sa), "+v"(sb) :: "memory");
            if (r32 >= 24) sb = (u32x4){0u, 0u, 0u, 0u};
            float t1 = 0.f, t2 = 0.f;
#pragma unroll
            for (int i = 0; i < 4; ++i) { t1 = dot2(sa[i], ONES, t1); t2 = dot2(sa[i], sa[i], t2); t1 = dot2(sb[i], ONES, t1); t2 = dot2(sb[i], sb[i], t2); }
            t1 += xor32<1>(t1); t2 += xor32<1>(t2); t1 += xor32<2>(t1); t2 += xor32<2>(t2); t1 += xor32<4>(t1); t2 += xor32<4>(t2);
            t1 += xor32<8>(t1); t2 += xor32<8>(t2); t1 += xor32<16>(t1); t2 += xor32<16>(t2);
            const float mean = t1 * (1.f / D), var = __builtin_fmaxf(t2 * (1.f / D) - mean * mean, 0.f);
            if (r32 == 0) gst(rs, (u32x4){f2u(mean), e, f2u(__builtin_amdgcn_rsqf(var + EPS)), e}, srow * 16, WS_ST1);
        }
        f32x16 aq = {}, ak = {}, av = {};
        float csq = 0.f, csk = 0.f, csv = 0.f;
        const unsigned xrd = (32 * w + r32) * 128 + ((hh ^ ((r32 >> 1) & 7)) << 4);
        const unsigned wrd = 16384 + hh * 6 * 256 + trq * 64 + ((shift + gq * 8) & 63);
        unsigned xa[4], wa[4];
#pragma unroll
        for (int j = 0; j < 4; ++j) { const int ks = (j + w) & 3; xa[j] = xrd ^ (ks << 5); wa[j] = wrd + ks * 3072; }
        struct Fr { u32x4 x, q, k, v; };
        Fr F[4];
        auto ldx = [&](unsigned base, int j) AI { F[j].x = lds128(base + xa[j]); const unsigned a = base + wa[j]; F[j].q = wfrag(a, a + 768); };
        auto ldk = [&](unsigned base, int j) AI { const unsigned a = base + wa[j]; F[j].k = wfrag(a + 256, a + 1024); };
        auto ldv = [&](unsigned base, int j) AI { const unsigned a = base + wa[j]; F[j].v = wfrag(a + 512, a + 1280); };
        WAIT_BAR(21);
        STAMP(16);
#pragma unroll
        for (int j = 0; j < 2; ++j) { ldx(L0, j); ldk(L0, j); ldv(L0, j); }
        SB();
#pragma unroll 1
        for (int c = 0; c < 7; ++c) {
            const unsigned base = L0 + (c >= 5 ? c - 5 : c) * L_SLOT, nbase = L0 + (c >= 4 ? c - 4 : c + 1) * L_SLOT;
            const bool dm = c < 3;
            const unsigned dsl = c >= 1 ? c - 1 : 4;
            sfor<4>([&](auto jc) AI {
                constexpr int j = decltype(jc)::value, jn = (j + 2) & 3;
                if constexpr (j == 2) {
                    if (c < 3) WAIT_BAR(21); else if (c == 3) WAIT_BAR(14); else if (c == 4) WAIT_BAR(7); else WAIT_BAR(0);
                    SB();
                }
                const unsigned lb = j < 2 ? base : nbase;
                aq = mfma(F[j].q, F[j].x, aq);
                ldx(lb, jn);
                if constexpr (j == 0) { csq = dot2(F[0].q[0], ONES, csq); csq = dot2(F[0].q[1], ONES, csq); csq = dot2(F[0].q[2], ONES, csq); csq = dot2(F[0].q[3], ONES, csq); PIN(csq); }
                if constexpr (j < 2) { if (dm) piece(3 * j, c + 4, dsl); }
                SB();
                ak = mfma(F[j].k, F[j].x, ak);
                ldk(lb, jn);
                if constexpr (j == 0) { csk = dot2(F[0].k[0], ONES, csk); csk = dot2(F[0].k[1], ONES, csk); csk = dot2(F[0].k[2], ONES, csk); csk = dot2(F[0].k[3], ONES, csk); PIN(csk); }
                if constexpr (j < 2) { if (dm) piece(3 * j + 1, c + 4, dsl); }
                SB();
                av = mfma(F[j].x, F[j].v, av);
                ldv(lb, jn);
                if constexpr (j == 0) { csv = dot2(F[0].v[0], ONES, csv); csv = dot2(F[0].v[1], ONES, csv); csv = dot2(F[0].v[2], ONES, csv); csv = dot2(F[0].v[3], ONES, csv); PIN(csv); }
                if constexpr (j < 2) { if (dm) piece(3 * j + 2, c + 4, dsl); }
                if constexpr (j == 1) { if (dm) piece(6, c + 4, dsl); }
                SB();
            });
        }
        STAMP(1);
        float mean, rstd;
        {
            const unsigned go = (128 * m + 32 * w + r32) * 16;
            u32x4 gv;
            for (unsigned sp2 = 0; sp2 < SPIN_CAP; ++sp2) { gv = gld(rs, go, WS_ST1); if (wall(gv.y == e && gv.w == e)) break; }
            mean = u2f(gv.x); rstd = u2f(gv.z);
        }
        csq = hsum(csq); csk = hsum(csk); csv = hsum(csv);
        LAS float* csp = (LAS float*)(uintptr_t)(L0 + L_MISC);
        LAS float* csw = (LAS float*)(uintptr_t)(L0 + L_MISC + 1536 + w * 384);
        LAS float* stw = (LAS float*)(uintptr_t)(L0 + L_MISC + 3072 + w * 256);
        if (hh == 0) { csp[w * 96 + r32] = csq; csp[w * 96 + 32 + r32] = csk; csp[w * 96 + 64 + r32] = csv; stw[r32] = mean * rstd; stw[32 + r32] = rstd; }
        LBAR();
        float cq = 0.f, ck = 0.f, cv = 0.f;
#pragma unroll
        for (int ww = 0; ww < 4; ++ww) { cq += csp[ww * 96 + r32]; ck += csp[ww * 96 + 32 + r32]; cv += csp[ww * 96 + 64 + r32]; }
        if (hh == 0) { csw[r32] = cq; csw[32 + r32] = ck; }
        WAIT_LGKM();
        const float qs = rstd * (0.18898223650461362f * 1.4426950408889634f);
#pragma unroll
        for (int g = 0; g < 4; ++g) {
            const f32x4 c1 = *(const LAS f32x4*)(csw + 8 * g + 4 * hh), c2 = *(const LAS f32x4*)(csw + 32 + 8 * g + 4 * hh);
            const f32x4 mr = *(const LAS f32x4*)(stw + 8 * g + 4 * hh), rr = *(const LAS f32x4*)(stw + 32 + 8 * g + 4 * hh);
#pragma unroll
            for (int i = 0; i < 4; ++i) {
                aq[4 * g + i] = (aq[4 * g + i] - mean * c1[i]) * qs;
                ak[4 * g + i] = (ak[4 * g + i] - mean * c2[i]) * rstd;
                av[4 * g + i] = av[4 * g + i] * rr[i] - mr[i] * cv;
            }
        }
        if (hh == 1) { aq[12] = 1.f; aq[13] = 0.f; aq[14] = 0.f; aq[15] = 0.f; ak[12] = 0.f; ak[13] = 0.f; ak[14] = 0.f; ak[15] = 0.f; }
        if (r32 >= 28) {
            const float pv = r32 == 28 ? 1.f : 0.f;
#pragma unroll
            for (int i = 0; i < 16; ++i) av[i] = pv;
        }
        qf0 = pack8(aq, 0); qf1 = pack8(aq, 1);
        const u32x4 k0 = pack8(ak, 0), k1 = pack8(ak, 1), v0 = pack8(av, 0), v1 = pack8(av, 1);
        const unsigned go = WS_KV + (h * 64 + 4 * m + w) * 4096;
        gst(rs, k0, lane * 16, go); gst(rs, k1, lane * 16, go + 1024); gst(rs, v0, lane * 16, go + 2048); gst(rs, v1, lane * 16, go + 3072);
        const unsigned lo = L0 + L_OWNKV + w * 4096 + lane * 16;
        sts128(lo, k0); sts128(lo + 1024, k1); sts128(lo + 2048, v0); sts128(lo + 3072, v1);
        WAIT_BAR(0);
        if (tid == 0) set_flag(P.ws, flag_off(0, m * 16 + h), e);
        STAMP(2);
    }
    const int g = slot & 3, nt = g < 2 ? 4 : 3, tb = g < 2 ? 4 * g : 3 * g + 2;
    const unsigned wst = L0 + w * L_WST;
    {
        const int mt = m - 2 + (w >> 1);
        u32x4 kv[8];
        if (mt >= 0) {
            wait_flags(P.ws, flag_off(0, mt * 16 + h), 1, e, lane);
            const unsigned go = WS_KV + (h * 64 + 4 * m - 8 + 2 * w) * 4096;
#pragma unroll
            for (int i = 0; i < 8; ++i) kv[i] = gld(rs, lane * 16, go + i * 1024);
        } else {
#pragma unroll
            for (int i = 0; i < 8; ++i) kv[i] = (u32x4){0u, 0u, (i & 3) == 1 && hh == 1 ? 0xF149u : 0u, 0u};
        }
        f32x16 cdiag, cold;
#pragma unroll
        for (int i = 0; i < 16; ++i) { const int kr = (i & 3) + 8 * (i >> 2) + 4 * hh; cdiag[i] = kr <= r32 ? 0.f : -1e30f; cold[i] = kr > r32 ? 0.f : -1e30f; }
#pragma unroll
        for (int i = 0; i < 8; ++i) sts128(L0 + L_KV + 2 * w * 4096 + i * 1024 + lane * 16, kv[i]);
        LBAR();
        STAMP(3);
        const WSrc wos = wsrc_a((const char*)P.wo + (size_t)(112 * w) * 896 + tb * 64, 896, nt, lane);
        const unsigned kvb = L0 + L_KV + w * 4096 + lane * 16;
        u32x4 kf[9][2], vf[9][2];
        const f32x16 zero16 = {};
        auto smax = [&](const f32x16& s, float a) AI {
#pragma unroll
            for (int i = 0; i < 16; i += 2) a = max3(a, s[i], s[i + 1]);
            return a; };
#pragma unroll
        for (int i = 0; i < 2; ++i) { kf[i][0] = lds128(kvb + i * 4096); kf[i][1] = lds128(kvb + i * 4096 + 1024); }
        float mx = -3e38f;
        f32x16 s[2];
#pragma unroll
        for (int i = 0; i < 9; ++i) {
            if (i + 2 < 9) { kf[i + 2][0] = lds128(kvb + (i + 2) * 4096); kf[i + 2][1] = lds128(kvb + (i + 2) * 4096 + 1024); }
            vf[i][0] = lds128(kvb + i * 4096 + 2048); vf[i][1] = lds128(kvb + i * 4096 + 3072);
            s[i & 1] = mfma(kf[i][0], qf0, i == 0 ? cold : i == 8 ? cdiag : zero16);
            wpiece(wos, wst, i);
            s[i & 1] = mfma(kf[i][1], qf1, s[i & 1]);
            if (i > 0) mx = smax(s[(i - 1) & 1], mx);
            SB();
        }
        mx = hmax(smax(s[0], mx));
        STAMP(25);
        f32x16 o = {}, cm;
#pragma unroll
        for (int i = 0; i < 16; ++i) { cm[i] = -mx; cdiag[i] -= mx; cold[i] -= mx; }
        s[0] = mfma(kf[0][0], qf0, cold); s[0] = mfma(kf[0][1], qf1, s[0]);
        SB();
#pragma unroll
        for (int i = 0; i < 9; ++i) {
            f32x16& c = s[i & 1];
            f32x16& n = s[(i + 1) & 1];
            if (i + 1 < 9) n = mfma(kf[i + 1][0], qf0, i + 1 == 8 ? cdiag : cm);
#pragma unroll
            for (int j = 0; j < 8; ++j) c[j] = __builtin_amdgcn_exp2f(c[j]);
            const u32x4 p0 = pack8(c, 0);
            wpiece(wos, wst, 9 + 2 * i);
            SB();
            if (i + 1 < 9) n = mfma(kf[i + 1][1], qf1, n);
            o = mfma(vf[i][0], p0, o);
#pragma unroll
            for (int j = 8; j < 16; ++j) c[j] = __builtin_amdgcn_exp2f(c[j]);
            const u32x4 p1 = pack8(c, 1);
            wpiece(wos, wst, 10 + 2 * i);
            o = mfma(vf[i][1], p1, o);
            SB();
        }
        wpiece(wos, wst, 27);
        STAMP(26);
        auto lr = __builtin_amdgcn_permlane32_swap(f2u(o[12]), f2u(o[12]), false, false);
        const float inv = __builtin_amdgcn_rcpf(u2f(lr[1]));
#pragma unroll
        for (int i = 0; i < 16; ++i) o[i] *= inv;
        if (hh == 1) { o[12] = 0.f; o[13] = 0.f; o[14] = 0.f; o[15] = 0.f; }
        const unsigned vo = (hh * 2048 + r32) * 16, so = WS_O + ((4 * h) * 2048 + 128 * m + 32 * w) * 16;
        gst(rs, pack8(o, 0), vo, so); gst(rs, pack8(o, 1), vo, so + 2 * 2048 * 16);
        WAIT_BAR(0);
        if (tid == 0) set_flag(P.ws, flag_off(1, m * 16 + h), e);
        STAMP(4);
    }
    const int ti = 8 * xcd + (slot >> 2);
    const unsigned trW = L0 + (w < nt ? w : nt - 1) * 256 + trq * 64 + gq * 8 + 2 * hh * 1024;
    const unsigned vx = (hh * 2048 + r32) * 16;
    const unsigned xl = L0 + L_X + lane * 16;
    const bool act = w < nt;
    const int tw = tb + w;
    LAS float* m2 = (LAS float*)(uintptr_t)(L0 + L_M2 + w * 256);
    auto gemm = [&](auto nk, const u32x4* wr, const WSrc& nx) AI -> f32x16 {
        constexpr int NK = decltype(nk)::value, PD = 4;
        f32x16 acc = {};
        u32x4 xf[NK];
#pragma unroll
        for (int k = 0; k < PD; ++k) xf[k] = lds128(xl + k * 1024);
        SB();
        sfor<NK>([&](auto kc) AI {
            constexpr int k = decltype(kc)::value;
            if constexpr (k + PD < NK) xf[k + PD] = lds128(xl + (k + PD) * 1024);
            acc = mfma(wr[k], xf[k], acc);
            if constexpr ((k & 1) == 0 && k / 2 < 14) wpiece(nx, wst, 14 + k / 2);
            SB();
        });
        return acc;
    };
    u32x4 hp[2];
    float meanH, rstdH;
    {
        u32x4 xr[2];
        const char* xp = (const char*)P.x + (size_t)(32 * ti + r32) * 896 + 64 * (act ? tw : tb) + 16 * hh;
        xr[0] = *(const GAS u32x4*)xp; xr[1] = *(const GAS u32x4*)(xp + 32);
        u32x4 wr[32];
#pragma unroll
        for (int hd = 0; hd < 16; ++hd)
#pragma unroll
            for (int s = 0; s < 2; ++s) {
                const unsigned o0 = (7 * hd + 4 * s) * 1024;
                const unsigned o1 = (hd == 15 && s == 1) ? (hh ? o0 : o0 + 1024) : o0 + 1024;
                wr[2 * hd + s] = wfrag(trW + o0, trW + o1);
            }
        PIN(xr[0]); PIN(xr[1]);
        LBAR();
        const WSrc nx = wsrc_a((const char*)P.w1 + (size_t)(112 * w) * 896 + tb * 64, 896, nt, lane);
#pragma unroll
        for (int n = 0; n < 14; ++n) wpiece(nx, wst, n);
        wait_flags(P.ws, flag_off(1, (ti >> 2) * 16 + 4 * w), 4, e, lane);
        u32x4 xf[8];
#pragma unroll
        for (int k = 0; k < 8; ++k) xf[k] = gld(rs, vx, WS_O + ((16 * w + 2 * k) * 2048 + 32 * ti) * 16);
#pragma unroll
        for (int k = 0; k < 8; ++k) sts128(xl + (8 * w + k) * 1024, xf[k]);
        LBAR();
        STAMP(5);
        f32x16 r = gemm(IC(32), wr, nx);
        float s1 = 0.f, s2 = 0.f;
#pragma unroll
        for (int s = 0; s < 2; ++s) {
#pragma unroll
            for (int i = 0; i < 4; ++i) {
                hp[s][i] = pk(r[8 * s + 2 * i] + blo(xr[s][i]), r[8 * s + 2 * i + 1] + bhi(xr[s][i]));
                s1 = dot2(hp[s][i], ONES, s1); s2 = dot2(hp[s][i], hp[s][i], s2);
            }
            if (act) gst(rs, hp[s], vx, WS_H + ((4 * tw + 2 * s) * 2048 + 32 * ti) * 16);
        }
        s1 = hsum(s1); s2 = hsum(s2);
        if (act && hh == 0) gst8(rs, (u32x2){f2u(s1), f2u(s2)}, r32 * 8, WS_STH + (tw * 2048 + 32 * ti) * 8);
        WAIT_BAR(0);
        if (tid == 0) set_flag(P.ws, flag_off(2, ti * 4 + g), e);
        STAMP(6);
    }
    auto load_wr = [&](u32x4 (&wr)[28]) AI {
#pragma unroll
        for (int k = 0; k < 28; ++k) wr[k] = wfrag(trW + 4 * k * 1024, trW + (4 * k + 1) * 1024);
    };
    const int fs0 = w == 0 ? 0 : w - 1 + (w == 1 ? 0 : 0), fsn = w == 0 ? 1 : 2;
    const int fsl = w == 0 ? 0 : w == 1 ? 0 : w == 2 ? 1 : 2;
    (void)fs0;
    {
        u32x4 wr[28];
        load_wr(wr);
        LBAR();
        const WSrc nx = wsrc_a((const char*)P.w2 + (size_t)(112 * w) * 896 + tb * 64, 896, nt, lane);
#pragma unroll
        for (int n = 0; n < 14; ++n) wpiece(nx, wst, n);
        {
            float cs[4] = {0.f, 0.f, 0.f, 0.f};
#pragma unroll
            for (int k = 0; k < 28; ++k)
#pragma unroll
                for (int i = 0; i < 4; ++i) cs[i] = dot2(wr[k][i], ONES, cs[i]);
            const float c = hsum((cs[0] + cs[1]) + (cs[2] + cs[3]));
            if (hh == 0) m2[r32] = c;
        }
        wait_flags(P.ws, flag_off(2, ti * 4 + fsl), fsn, e, lane);
        u32x4 xf[7];
#pragma unroll
        for (int k = 0; k < 7; ++k) xf[k] = gld(rs, vx, WS_H + ((14 * w + 2 * k) * 2048 + 32 * ti) * 16);
#pragma unroll
        for (int k = 0; k < 7; ++k) sts128(xl + (7 * w + k) * 1024, xf[k]);
        LBAR();
        u32x2 sv[7];
#pragma unroll
        for (int q = 0; q < 7; ++q) sv[q] = gld8(rs, (7 * hh * 2048 + r32) * 8, WS_STH + (q * 2048 + 32 * ti) * 8);
        f32x4 c[4];
#pragma unroll
        for (int q = 0; q < 4; ++q) c[q] = *(const LAS f32x4*)(m2 + 8 * q + 4 * hh);
#pragma unroll
        for (int q = 0; q < 7; ++q) PIN(sv[q]);
        STAMP(7);
        f32x16 r = gemm(IC(28), wr, nx);
        float t1 = 0.f, t2 = 0.f;
#pragma unroll
        for (int q = 0; q < 7; ++q) { t1 += u2f(sv[q].x); t2 += u2f(sv[q].y); }
        t1 = hsum(t1); t2 = hsum(t2);
        meanH = t1 * (1.f / D);
        const float var = __builtin_fmaxf(t2 * (1.f / D) - meanH * meanH, 0.f);
        rstdH = __builtin_amdgcn_rsqf(var + EPS);
        const float sc = rstdH * __builtin_amdgcn_rsqf(var * rstdH * rstdH + EPS);
#pragma unroll
        for (int q = 0; q < 4; ++q)
#pragma unroll
            for (int i = 0; i < 4; ++i) r[4 * q + i] = gelu_t((r[4 * q + i] - meanH * c[q][i]) * sc);
#pragma unroll
        for (int s = 0; s < 2; ++s) if (act) gst(rs, pack8(r, s), vx, WS_HH + ((4 * tw + 2 * s) * 2048 + 32 * ti) * 16);
        WAIT_BAR(0);
        if (tid == 0) set_flag(P.ws, flag_off(3, ti * 4 + g), e);
        STAMP(8);
    }
    const int s16 = slot & 15, pr = s16 >> 1;
    const int nt6 = ((s16 ^ pr) & 1) ? 3 : 4, tb6 = 7 * pr + ((s16 & 1) ? (pr & 1 ? 3 : 4) : 0);
    {
        u32x4 wr[28];
        load_wr(wr);
        LBAR();
        const WSrc nx = wsrc_a((const char*)P.w3 + (size_t)(112 * w) * 3584 + tb6 * 64, 3584, nt6, lane);
#pragma unroll
        for (int n = 0; n < 14; ++n) wpiece(nx, wst, n);
        const int sh = w == 0 ? -1 : w == 1 ? 1 : w == 2 ? -2 : 2;
        const int tn = ti + (sh < 0 ? -1 : 1);
        {
            const bool nb = tn >= 0 && tn < 64;
            const int li = lane & 3;
            const GAS unsigned* p = (const GAS unsigned*)(P.ws + flag_off(3, (((li & 2) && nb) ? tn : ti) * 4 + fsl + ((li & 1) && fsn == 2 ? 1 : 0)));
            for (unsigned sp = 0; sp < SPIN_CAP; ++sp) { unsigned v = __hip_atomic_load(p, __ATOMIC_RELAXED, __HIP_MEMORY_SCOPE_AGENT); if (wall(v == e)) break; }
            __builtin_amdgcn_fence(__ATOMIC_ACQUIRE, "wavefront");
        }
        const int tok = 32 * ti + r32 + sh;
        const bool ok = tok >= 0 && tok < S;
        const unsigned vxs = ok ? (hh * 2048 + tok) * 16 : 0x7ffffff0u;
        u32x4 xf[7];
#pragma unroll
        for (int k = 0; k < 7; ++k) { xf[k] = gld(rs, vxs, WS_HH + (14 * w + 2 * k) * 2048 * 16); }
#pragma unroll
        for (int k = 0; k < 7; ++k) sts128(xl + (7 * w + k) * 1024, xf[k]);
        LBAR();
        STAMP(9);
        f32x16 r = gemm(IC(28), wr, nx);
        float s1 = 0.f, s2 = 0.f;
#pragma unroll
        for (int s = 0; s < 2; ++s) {
            u32x4 xp;
#pragma unroll
            for (int i = 0; i < 4; ++i) {
                xp[i] = pk(r[8 * s + 2 * i] + (blo(hp[s][i]) - meanH) * rstdH, r[8 * s + 2 * i + 1] + (bhi(hp[s][i]) - meanH) * rstdH);
                s1 = dot2(xp[i], ONES, s1); s2 = dot2(xp[i], xp[i], s2);
            }
            if (act) gst(rs, xp, vx, WS_X1 + ((4 * tw + 2 * s) * 2048 + 32 * ti) * 16);
        }
        s1 = hsum(s1); s2 = hsum(s2);
        if (act && hh == 0) gst8(rs, (u32x2){f2u(s1), f2u(s2)}, r32 * 8, WS_STX + (tw * 2048 + 32 * ti) * 8);
        WAIT_BAR(0);
        if (tid == 0) set_flag(P.ws, flag_off(4, ti * 4 + g), e);
        STAMP(10);
    }
    const bool act7 = slot < 28;
    const int j7 = 4 * xcd + (act7 ? slot / 7 : 0), g7 = act7 ? slot % 7 : 0;
    const WSrc w4a = wsrc_b((const char*)P.w4 + (size_t)(448 * w) * 896 + g7 * 128, 896, lane);
    {
        unsigned tr6[4];
#pragma unroll
        for (int q = 0; q < 4; ++q) { tr6[q] = L0 + q * L_WST + trq * 64 + gq * 8 + 2 * hh * 1024; PIN(tr6[q]); }
        auto wf6 = [&](int t, int k) AI { const unsigned a = tr6[k / 7] + t * 256 + 4 * (k % 7) * 1024; return wfrag(a, a + 1024); };
        LAS float* cs6 = (LAS float*)(uintptr_t)(L0 + L_M2);
        {
            float c[4] = {0.f, 0.f, 0.f, 0.f};
#pragma unroll
            for (int k = 0; k < 28; ++k) { const unsigned a = tr6[k / 7] + w * 256 + 4 * (k % 7) * 1024; const u32x4 f = wfrag(a, a + 1024);
#pragma unroll
                for (int i = 0; i < 4; ++i) c[i] = dot2(f[i], ONES, c[i]); }
            const float cc = hsum((c[0] + c[1]) + (c[2] + c[3]));
            if (hh == 0) cs6[w * 32 + r32] = cc;
        }
        LBAR();
        wait_flags(P.ws, flag_off(4, (4 * m + w) * 4), 4, e, lane);
        const int tk = 128 * m + 32 * w;
        constexpr int PX = 10, PW = 3;
        u32x4 xf[28];
#pragma unroll
        for (int k = 0; k < PX; ++k) xf[k] = gld(rs, vx, WS_X1 + (2 * k * 2048 + tk) * 16);
        u32x2 sv[7];
#pragma unroll
        for (int q = 0; q < 7; ++q) sv[q] = gld8(rs, (7 * hh * 2048 + r32) * 8, WS_STX + (q * 2048 + tk) * 8);
        STAMP(11);
        f32x16 a0 = {}, a1 = {};
        {
            u32x4 wA[28], wB[28];
#pragma unroll
            for (int k = 0; k < PW; ++k) { wA[k] = wf6(0, k); wB[k] = wf6(1, k); }
            SB();
            sfor<28>([&](auto kc) AI {
                constexpr int k = decltype(kc)::value;
                if constexpr (k + PW < 28) { wA[k + PW] = wf6(0, k + PW); wB[k + PW] = wf6(1, k + PW); }
                a0 = mfma(wA[k], xf[k], a0);
                if constexpr (k + PX < 28) xf[k + PX] = gld(rs, vx, WS_X1 + (2 * (k + PX) * 2048 + tk) * 16);
                a1 = mfma(wB[k], xf[k], a1);
                SB();
            });
        }
        float t1 = 0.f, t2 = 0.f;
#pragma unroll
        for (int q = 0; q < 7; ++q) { t1 += u2f(sv[q].x); t2 += u2f(sv[q].y); }
        t1 = hsum(t1); t2 = hsum(t2);
        const float mean = t1 * (1.f / D), var = __builtin_fmaxf(t2 * (1.f / D) - mean * mean, 0.f) + EPS, rstd = __builtin_amdgcn_rsqf(var);
        const GeluK gk = {-mean, GC1 * rstd * rstd * rstd, GC0 * rstd, var * rstd};
        const unsigned uo = WS_U + ((4 * tb6) * 2048 + tk) * 16;
        auto tile_epi = [&](auto tmc, f32x16& an, auto tec, f32x16& y) AI {
            constexpr int tm = decltype(tmc)::value, te = decltype(tec)::value, PD = 4;
            f32x4 c[4];
#pragma unroll
            for (int q = 0; q < 4; ++q) c[q] = *(const LAS f32x4*)(cs6 + te * 32 + 8 * q + 4 * hh);
            float z[16];
            u32x4 wf[28];
#pragma unroll
            for (int k = 0; k < PD; ++k) wf[k] = wf6(tm, k);
            SB();
            sfor<28>([&](auto kc) AI {
                constexpr int k = decltype(kc)::value;
                if constexpr (k + PD < 28) wf[k + PD] = wf6(tm, k + PD);
                an = mfma(wf[k], xf[k], an);
                sfor<16>([&](auto ec) AI {
                    constexpr int el = decltype(ec)::value, o = k - (el * 5) / 4;
                    if constexpr (o >= 0 && o < 8) { float yy = y[el]; gelu_step(o, yy, z[el], c[el >> 2][el & 3], gk); if (o == 0 || o == 7) PIN(yy); else PIN(z[el]); y[el] = yy; }
                });
                if constexpr (k == 26) gst(rs, pack8(y, 0), vx, uo + 4 * te * 2048 * 16);
                if constexpr (k == 27) gst(rs, pack8(y, 1), vx, uo + (4 * te + 2) * 2048 * 16);
                SB();
            });
        };
        STAMP(23);
        f32x16 a2 = {}, a3 = {};
        tile_epi(IC(2), a2, IC(0), a0);
        tile_epi(IC(3), a3, IC(1), a1);
        STAMP(24);
        LBAR();
        auto epi = [&](f32x16& y, int te, int p0) AI {
#pragma unroll
            for (int q = 0; q < 4; ++q) {
                const f32x4 c = *(const LAS f32x4*)(cs6 + te * 32 + 8 * q + 4 * hh);
#pragma unroll
                for (int i = 0; i < 4; ++i) y[4 * q + i] = gelu_t((y[4 * q + i] - mean * c[i]) * rstd);
#pragma unroll
                for (int i = 0; i < 4; ++i) if (p0 + 4 * q + i < 28 && act7) wpiece(w4a, wst, p0 + 4 * q + i);
                SB();
            }
            gst(rs, pack8(y, 0), vx, uo + 4 * te * 2048 * 16); gst(rs, pack8(y, 1), vx, uo + (4 * te + 2) * 2048 * 16);
        };
        epi(a2, 2, 0);
        if (nt6 == 4) epi(a3, 3, 16);
        else if (act7) {
#pragma unroll
            for (int n = 16; n < 28; ++n) wpiece(w4a, wst, n);
        }
        WAIT_BAR(0);
        if (tid == 0) set_flag(P.ws, flag_off(5, m * 16 + s16), e);
        STAMP(12);
    }
    if (act7) {
        const unsigned tA = wst + trq * 64 + gq * 8 + 2 * hh * 512;
        u32x4 wr[28][2];
#pragma unroll
        for (int k = 0; k < 14; ++k) { wr[k][0] = wfrag(tA + 4 * k * 512, tA + (4 * k + 1) * 512); wr[k][1] = wfrag(tA + 4 * k * 512 + 256, tA + (4 * k + 1) * 512 + 256); }
        WAIT_LGKM(); SB();
        { WSrc w4h = w4a; w4h.base += (size_t)224 * 896;
#pragma unroll
          for (int n = 0; n < 28; ++n) wpiece(w4h, wst, n); }
        const int tk = 64 * j7;
        const int tt = w >> 1, nt7 = w & 1;
        wait_flags(P.ws, flag_off(5, (j7 >> 1) * 16 + 4 * w), 4, e, lane);
        constexpr int PX = 6;
        u32x4 xa[28][2];
#pragma unroll
        for (int k = 0; k < PX; ++k) { xa[k][0] = gld(rs, vx, WS_U + ((56 * w + 2 * k) * 2048 + tk) * 16); xa[k][1] = gld(rs, vx, WS_U + ((56 * w + 2 * k) * 2048 + tk + 32) * 16); }
        STAMP(13);
        SB();
        f32x16 acc[4] = {};
        u32x4 rh[2], rx[2];
        sfor<28>([&](auto kc) AI {
            constexpr int k = decltype(kc)::value;
            if constexpr (k + 3 >= 14 && k + 3 < 28) { constexpr int kk = k + 3 - 14; wr[k + 3][0] = wfrag(tA + 4 * kk * 512, tA + (4 * kk + 1) * 512); wr[k + 3][1] = wfrag(tA + 4 * kk * 512 + 256, tA + (4 * kk + 1) * 512 + 256); }
            acc[0] = mfma(wr[k][0], xa[k][0], acc[0]); acc[1] = mfma(wr[k][1], xa[k][0], acc[1]);
            if constexpr (k + PX < 28) xa[k + PX][0] = gld(rs, vx, WS_U + ((56 * w + 2 * (k + PX)) * 2048 + tk) * 16);
            acc[2] = mfma(wr[k][0], xa[k][1], acc[2]); acc[3] = mfma(wr[k][1], xa[k][1], acc[3]);
            if constexpr (k + PX < 28) xa[k + PX][1] = gld(rs, vx, WS_U + ((56 * w + 2 * (k + PX)) * 2048 + tk + 32) * 16);
            if constexpr (k == 22 || k == 23) {
                constexpr int s = k - 22;
                const unsigned so = ((8 * g7 + 4 * nt7 + 2 * s) * 2048 + tk + 32 * tt) * 16;
                rh[s] = gld(rs, vx, WS_H + so); rx[s] = gld(rs, vx, WS_X1 + so);
            }
            SB();
        });
        STAMP(27);
#pragma unroll
        for (int t = 0; t < 4; ++t) {
            if (t != w) {
                const unsigned a = L0 + L_X + t * 12288 + (w < t ? w : w - 1) * 4096 + lane * 16;
#pragma unroll
                for (int q = 0; q < 4; ++q) stsf4(a + q * 1024, (f32x4){acc[t][4 * q], acc[t][4 * q + 1], acc[t][4 * q + 2], acc[t][4 * q + 3]});
            }
        }
        LBAR();
        f32x16 r = w == 0 ? acc[0] : w == 1 ? acc[1] : w == 2 ? acc[2] : acc[3];
        const unsigned a = L0 + L_X + w * 12288 + lane * 16;
#pragma unroll
        for (int j = 0; j < 3; ++j)
#pragma unroll
            for (int q = 0; q < 4; ++q) { const f32x4 v = ldsf4(a + j * 4096 + q * 1024); r[4 * q] += v[0]; r[4 * q + 1] += v[1]; r[4 * q + 2] += v[2]; r[4 * q + 3] += v[3]; }
        char* op = (char*)P.out + (size_t)(tk + 32 * tt + r32) * 896 + 128 * g7 + 64 * nt7 + 16 * hh;
#pragma unroll
        for (int s = 0; s < 2; ++s) {
            u32x4 ov;
#pragma unroll
            for (int i = 0; i < 4; ++i)
                ov[i] = pk(r[8 * s + 2 * i] + blo(rx[s][i]) + blo(rh[s][i]), r[8 * s + 2 * i + 1] + bhi(rx[s][i]) + bhi(rh[s][i]));
            *(GAS u32x4*)(op + 32 * s) = ov;
        }
        STAMP(14);
    }
    STAMP_FLUSH();
    if (b == 0 && w == 0) {
        for (int r = 0; r < 4; ++r) {
            const GAS unsigned* p = (const GAS unsigned*)(P.ws + flag_off(0, r * 64 + lane));
            for (unsigned sp = 0; sp < SPIN_CAP; ++sp) { unsigned v = __hip_atomic_load(p, __ATOMIC_RELAXED, __HIP_MEMORY_SCOPE_AGENT); if (wall(v == e)) break; }
        }
        if (lane == 0) __hip_atomic_store((GAS unsigned*)(P.ws + WS_EP), e, __ATOMIC_RELAXED, __HIP_MEMORY_SCOPE_AGENT);
    }
}
}
#if defined(__HIP_DEVICE_COMPILE__)
#pragma clang attribute pop
#endif

extern "C" void kernel_launch(void* const* d_in, const int* in_sizes, int n_in, void* d_out, int out_size, void* d_ws, size_t ws_size,
                              hipStream_t stream) {
    mk::Params P;
    P.x = (const u16*)d_in[0]; P.wqkv = (const u16*)d_in[1]; P.wo = (const u16*)d_in[2];
    P.w1 = (const u16*)d_in[4]; P.w2 = (const u16*)d_in[5]; P.w3 = (const u16*)d_in[6]; P.w4 = (const u16*)d_in[7];
    P.out = (u16*)d_out; P.ws = (char*)d_ws;
    mk::fused_fwd<<<256, 256, 0, stream>>>(P);
}
```

```cpp
#include <hip/hip_runtime.h>
#include <hip/hip_bf16.h>
#include <stdint.h>
#include <type_traits>
#include <utility>

typedef unsigned short u16;
typedef __attribute__((ext_vector_type(8))) short bf16x8;
typedef __attribute__((ext_vector_type(4))) short s16x4;
typedef __attribute__((ext_vector_type(16))) float f32x16;
typedef __attribute__((ext_vector_type(4))) float f32x4;
typedef __attribute__((ext_vector_type(2))) float f32x2;
typedef __attribute__((ext_vector_type(4))) unsigned u32x4;
typedef __attribute__((ext_vector_type(2))) unsigned u32x2;
typedef __attribute__((ext_vector_type(2))) __bf16 bf2;
#define LAS __attribute__((address_space(3)))
#define GAS __attribute__((address_space(1)))
#define DEVI __device__ __forceinline__

#define MK_STAMP 0

#if defined(__HIP_DEVICE_COMPILE__)
#pragma clang attribute push (__attribute__((target("no-packed-fp32-ops"))), apply_to = function)
#endif
namespace mk {
constexpr int S = 2048, D = 448;
constexpr unsigned WS_EP = 0;
constexpr unsigned WS_FLAG = 4096;
constexpr unsigned WS_KV = 262144;
constexpr unsigned WS_O = WS_KV + 4194304;
constexpr unsigned WS_H = WS_O + 2097152;
constexpr unsigned WS_HH = WS_H + 1835008;
constexpr unsigned WS_X1 = WS_HH + 1835008;
constexpr unsigned WS_U = WS_X1 + 1835008;
constexpr unsigned WS_STH = WS_U + 7340032;
constexpr unsigned WS_STX = WS_STH + 262144;
constexpr unsigned WS_ST1 = WS_STX + 262144;
constexpr unsigned WS_STAMP = WS_ST1 + 32768;
constexpr unsigned WS_END = WS_STAMP + 256 * 48 * 8;
constexpr unsigned L_SLOT = 28672;
constexpr unsigned L_MISC = 143360;
constexpr unsigned L_KV = 114688;
constexpr unsigned L_OWNKV = L_KV + 8 * 4096;
constexpr unsigned L_WST = 28672;
constexpr unsigned L_X = 114688;
constexpr unsigned L_M2 = 147456;
constexpr unsigned CODE_TOUCH_BYTES = 32768;
constexpr float EPS = 1e-5f;
constexpr unsigned SPIN_CAP = 1u << 18;

struct Params {
    const u16* x; const u16* wqkv; const u16* wo; const u16* w1; const u16* w2; const u16* w3; const u16* w4;
    u16* out; char* ws;
};

DEVI unsigned f2u(float f) { return __builtin_bit_cast(unsigned, f); }
DEVI float u2f(unsigned u) { return __builtin_bit_cast(float, u); }
DEVI bool wall(bool p) { return __builtin_amdgcn_ballot_w64(!p) == 0; }
template <int X> DEVI float xor32(float v) { return u2f((unsigned)__builtin_amdgcn_ds_swizzle((int)f2u(v), (X << 10) | 0x1f)); }
DEVI unsigned pk(float lo, float hi) { bf2 v = {(__bf16)lo, (__bf16)hi}; return __builtin_bit_cast(unsigned, v); }
DEVI float blo(unsigned u) { return u2f(u << 16); }
DEVI float bhi(unsigned u) { return u2f(u & 0xffff0000u); }
DEVI float dot2(unsigned a, unsigned b, float c) { return __builtin_amdgcn_fdot2_f32_bf16(__builtin_bit_cast(bf2, a), __builtin_bit_cast(bf2, b), c, false); }
constexpr unsigned ONES = 0x3f803f80u;
DEVI float hsum(float v) { auto r = __builtin_amdgcn_permlane32_swap(f2u(v), f2u(v), false, false); return u2f(r[0]) + u2f(r[1]); }
DEVI float hmax(float v) { auto r = __builtin_amdgcn_permlane32_swap(f2u(v), f2u(v), false, false); return __builtin_fmaxf(u2f(r[0]), u2f(r[1])); }
DEVI f32x16 mfma(u32x4 a, u32x4 b, f32x16 c) { return __builtin_amdgcn_mfma_f32_32x32x16_bf16(__builtin_bit_cast(bf16x8, a), __builtin_bit_cast(bf16x8, b), c, 0, 0, 0); }
#define MFMA(acc, a, b, c) do { acc = mfma(a, b, c); asm volatile("" : "+v"(acc)); } while (0)
DEVI u32x4 lds128(unsigned a) { return *(const LAS u32x4*)(uintptr_t)a; }
DEVI f32x4 ldsf4(unsigned a) { return *(const LAS f32x4*)(uintptr_t)a; }
DEVI void sts128(unsigned a, u32x4 v) { *(LAS u32x4*)(uintptr_t)a = v; }
DEVI void stsf4(unsigned a, f32x4 v) { *(LAS f32x4*)(uintptr_t)a = v; }
DEVI u32x2 ldstr(unsigned a) { return __builtin_bit_cast(u32x2, __builtin_amdgcn_ds_read_tr16_b64_v4i16((LAS s16x4*)(uintptr_t)a)); }
DEVI u32x4 wfrag(unsigned a0, unsigned a1) { u32x2 p = ldstr(a0), q = ldstr(a1); return (u32x4){p.x, p.y, q.x, q.y}; }
DEVI u32x4 pack8(const f32x16& a, int s) { return (u32x4){pk(a[8 * s], a[8 * s + 1]), pk(a[8 * s + 2], a[8 * s + 3]), pk(a[8 * s + 4], a[8 * s + 5]), pk(a[8 * s + 6], a[8 * s + 7])}; }
constexpr int SC1 = 16;
DEVI u32x4 gld(__amdgpu_buffer_rsrc_t rs, unsigned voff, unsigned soff) { return __builtin_amdgcn_raw_buffer_load_b128(rs, voff, soff, SC1); }
DEVI u32x2 gld8(__amdgpu_buffer_rsrc_t rs, unsigned voff, unsigned soff) { return __builtin_amdgcn_raw_buffer_load_b64(rs, voff, soff, SC1); }
DEVI void gst(__amdgpu_buffer_rsrc_t rs, u32x4 v, unsigned voff, unsigned soff) { __builtin_amdgcn_raw_buffer_store_b128(v, rs, voff + soff, 0, SC1); }
DEVI void gst8(__amdgpu_buffer_rsrc_t rs, u32x2 v, unsigned voff, unsigned soff) { __builtin_amdgcn_raw_buffer_store_b64(v, rs, voff, soff, SC1); }
#define WAIT_VM(N) asm volatile("s_waitcnt vmcnt(" #N ")" ::: "memory")
#define WAIT_LGKM() asm volatile("s_waitcnt lgkmcnt(0)" ::: "memory")
#define WAIT_BAR(N) asm volatile("s_waitcnt vmcnt(" #N ") lgkmcnt(0)\n\ts_barrier" ::: "memory")
#define LBAR() asm volatile("s_waitcnt lgkmcnt(0)\n\ts_barrier" ::: "memory")
#define SB() __builtin_amdgcn_sched_barrier(0)
#define PIN(x) asm volatile("" : "+v"(x))
DEVI float fma1(float a, float b, float c) { return __builtin_fmaf(a, b, c); }
DEVI float mul1(float a, float b) { return a * b; }
DEVI float max3(float a, float b, float c) { return __builtin_fmaxf(__builtin_fmaxf(a, b), c); }
template <int... Is, class F> DEVI void sfor_impl(std::integer_sequence<int, Is...>, F&& f) { (f(std::integral_constant<int, Is>{}), ...); }
template <int N, class F> DEVI void sfor(F&& f) { sfor_impl(std::make_integer_sequence<int, N>{}, f); }
#define AI __attribute__((always_inline))
#define IC(n) std::integral_constant<int, n>{}

DEVI void dma1(const char* sbase, unsigned voff, unsigned lds) {
    asm volatile("s_mov_b32 m0, %2\n\ts_nop 0\n\tglobal_load_lds_dwordx4 %0, %1" :: "v"(voff), "s"(sbase), "s"(lds) : "memory");
}
DEVI void touch(const char* sbase, unsigned voff, unsigned lds) {
    asm volatile("s_mov_b32 m0, %2\n\ts_nop 0\n\tglobal_load_lds_dword %0, %1" :: "v"(voff), "s"(sbase), "s"(lds) : "memory");
}
struct WSrc { const char* base; unsigned step, voff; };
DEVI void wpiece(const WSrc& s, unsigned lds, int n) { dma1(s.base + (size_t)n * s.step, s.voff, lds + n * 1024); }
DEVI WSrc wsrc_a(const char* base, unsigned rs, int nt, int lane) { const int t = (lane >> 4) < nt ? (lane >> 4) : nt - 1; return WSrc{base, 4 * rs, ((lane >> 2) & 3) * rs + t * 64 + (lane & 3) * 16}; }
DEVI WSrc wsrc_b(const char* base, unsigned rs, int lane) { return WSrc{base, 8 * rs, (4 * (lane >> 5) + ((lane >> 2) & 3)) * rs + ((lane >> 4) & 1) * 64 + (lane & 3) * 16}; }

DEVI void wait_flags(const char* ws, unsigned foff, int n, unsigned e, int lane) {
    const GAS unsigned* p = (const GAS unsigned*)(ws + foff) + (lane < n ? lane : 0) * 32;
#pragma nounroll
    for (unsigned sp = 0; sp < SPIN_CAP; ++sp) {
        unsigned v = __hip_atomic_load(p, __ATOMIC_RELAXED, __HIP_MEMORY_SCOPE_AGENT);
        if (wall(v == e)) break;
    }
    __builtin_amdgcn_fence(__ATOMIC_ACQUIRE, "wavefront");
}
DEVI void set_flag(char* ws, unsigned foff, unsigned e) { __hip_atomic_store((GAS unsigned*)(ws + foff), e, __ATOMIC_RELAXED, __HIP_MEMORY_SCOPE_AGENT); }
DEVI unsigned flag_off(int ph, int idx) { return WS_FLAG + (ph * 256 + idx) * 128; }

constexpr float GC0 = -2.f * 0.7978845608028654f * 1.4426950408889634f, GC1 = GC0 * 0.044715f;
DEVI float gelu_t(float x) {
    float z = x * __builtin_fmaf(x * x, GC1, GC0);
    return x * __builtin_amdgcn_rcpf(1.f + __builtin_amdgcn_exp2f(z));
}
struct GeluK { float nmean, k1, k0, irs; };
DEVI void gelu_step(int o, float& y, float& z, float c, const GeluK& g) {
    if (o == 0) y = fma1(g.nmean, c, y);
    else if (o == 1) z = mul1(y, y);
    else if (o == 2) z = fma1(z, g.k1, g.k0);
    else if (o == 3) z = mul1(y, z);
    else if (o == 4) z = __builtin_amdgcn_exp2f(z);
    else if (o == 5) z = fma1(z, g.irs, g.irs);
    else if (o == 6) z = __builtin_amdgcn_rcpf(z);
    else y = mul1(y, z);
}

#define STAMP(k) do {} while (0)
#define STAMP_DECL
#define STAMP_FLUSH() do {} while (0)

#define LANE_GEOM() int lane_ = threadIdx.x & 63; asm volatile("" : "+v"(lane_)); const int lane = lane_, r32 = lane & 31, hh = lane >> 5; \
    const int trq = (lane & 15) >> 2, trQ_ = ((lane >> 4) & 1) * 4 + (lane & 3), gq = (trQ_ & 4) | ((trQ_ & 1) << 1) | ((trQ_ >> 1) & 1); \
    const unsigned vx = (hh * 2048 + r32) * 16; (void)trq; (void)gq; (void)vx; (void)r32; (void)hh
constexpr unsigned L_FLG = L_MISC + 1920, F_RDY = 0, F_DONE = 16;
DEVI unsigned ldsw_ld(unsigned i) { return *(volatile LAS unsigned*)(uintptr_t)(L_FLG + 4 * i); }
DEVI u32x2 ldsw_ld2(unsigned i) { return *(volatile LAS u32x2*)(uintptr_t)(L_FLG + 4 * i); }
DEVI void ldsw_st(unsigned i, unsigned v) { *(volatile LAS unsigned*)(uintptr_t)(L_FLG + 4 * i) = v; }
DEVI void ring_wait(int c) {
#pragma nounroll
    for (unsigned sp = 0; sp < SPIN_CAP * 16u; ++sp) { const u32x2 v = ldsw_ld2(F_RDY + 2 * c); if (__builtin_amdgcn_readfirstlane(v.x & v.y)) break; __builtin_amdgcn_s_sleep(1); }
}
DEVI void slot_wait(unsigned n, int lane) {
#pragma nounroll
    for (unsigned sp = 0; sp < SPIN_CAP * 16u; ++sp) { const unsigned v = ldsw_ld(F_DONE + (lane < 6 ? lane : 0)); if (wall(v >= n)) break; __builtin_amdgcn_s_sleep(1); }
}
#define LB(N) asm volatile("s_waitcnt vmcnt(" #N ")\n\ts_barrier" ::: "memory")
#define BAR() asm volatile("s_barrier" ::: "memory")

__global__ __launch_bounds__(512, 2) void fused_fwd(Params P) {
    __shared__ __attribute__((aligned(16))) char smem[163840];
    const unsigned L0 = (unsigned)(uintptr_t)(LAS char*)smem;
    const int tid = threadIdx.x, wv = __builtin_amdgcn_readfirstlane(tid >> 6), w = wv & 3;
    const int b = blockIdx.x, xcd = b & 7, slot = b >> 3;
    const int m = 2 * xcd + (slot >> 4), h = slot & 15;
    const int g = slot & 3, nt = g < 2 ? 4 : 3, tb = g < 2 ? 4 * g : 3 * g + 2;
    const int ti = 8 * xcd + (slot >> 2);
    const int s16 = slot & 15, pr = s16 >> 1;
    const int nt6 = ((s16 ^ pr) & 1) ? 3 : 4, tb6 = 7 * pr + ((s16 & 1) ? (pr & 1 ? 3 : 4) : 0);
    const bool act7 = slot < 28;
    const int j7 = 4 * xcd + (act7 ? slot / 7 : 0), g7 = act7 ? slot % 7 : 0;
    const unsigned wst = L0 + w * L_WST;
    const unsigned shift = (56 * h) & 15u;
    STAMP_DECL

    if (wv >= 4) {
        LANE_GEOM();
        const unsigned wrd = 16384 + hh * 6 * 256 + trq * 64 + ((shift + gq * 8) & 63);
        LAS float* csp = (LAS float*)(uintptr_t)(L0 + L_MISC);
        if (w < 2) {
            const unsigned xoffE = (lane >> 3) * 896 + (((lane & 7) ^ (lane >> 4)) << 4);
            const unsigned xoffO = (lane >> 3) * 896 + (((lane & 7) ^ (4 + (lane >> 4))) << 4);
            const unsigned colb = (56 * h) & ~15u;
            unsigned woff[3];
#pragma unroll
            for (int j = 0; j < 3; ++j) {
                const int bl = 4 * j + (lane >> 4), kql = bl / 3, tile = bl % 3;
                woff[j] = (4 * kql + ((lane >> 2) & 3)) * 2688 + tile * 896 + colb + (lane & 3) * 16;
            }
            const char* xb = (const char*)P.x + (size_t)(128 * m + 64 * w) * 896;
            const char* wb = (const char*)P.wqkv + (size_t)(32 * w) * 2688;
            LBAR();
#pragma unroll 1
            for (int kc = 0; kc < 7; ++kc) {
                if (kc >= 5) slot_wait(kc - 4, lane);
                const unsigned base = L0 + (kc >= 5 ? kc - 5 : kc) * L_SLOT;
#pragma unroll
                for (int i = 0; i < 6; ++i) dma1(wb + (size_t)kc * 64 * 2688 + (i / 3) * 16 * 2688, woff[i % 3], base + 16384 + (6 * w + i) * 1024);
#pragma unroll
                for (int i = 0; i < 8; ++i) dma1(xb + kc * 128 + i * 8 * 896, (i & 1) ? xoffO : xoffE, base + (8 * w + i) * 1024);
                if (kc > 0) { WAIT_VM(14); ldsw_st(F_RDY + 2 * (kc - 1) + w, 1); }
            }
            WAIT_VM(0); ldsw_st(F_RDY + 12 + w, 1);
        } else {
            const int srow = 128 * m + 8 * h + 4 * (w - 2) + (lane >> 4);
            const char* sp = (const char*)P.x + (size_t)srow * 896 + (lane & 15) * 16;
            u32x4 sr[4];
            asm volatile("global_load_dwordx4 %0, %4, off\n\tglobal_load_dwordx4 %1, %4, off offset:256\n\tglobal_load_dwordx4 %2, %4, off offset:512\n\tglobal_load_dwordx4 %3, %5, off"
                         : "=&v"(sr[0]), "=&v"(sr[1]), "=&v"(sr[2]), "=&v"(sr[3]) : "v"(sp), "v"(sp + ((lane & 15) < 8 ? 768 : 0)) : "memory");
            {
                unsigned long long pc; unsigned back;
                asm volatile("s_getpc_b64 %0\nmk_code_here:\n\ts_mov_b32 %1, mk_code_here-_ZN2mk9fused_fwdENS_6ParamsE" : "=s"(pc), "=s"(back));
                constexpr unsigned len = CODE_TOUCH_BYTES;
                const unsigned oc = (slot * 16 + (lane & 15)) * 128, o3 = (b * 64 + lane) * 128, o2 = (b * 16 + (lane & 15)) * 128;
                const char* t0 = w == 2 ? (const char*)(pc - back) + (oc < len ? oc : 0) : (const char*)P.w3 + (o3 < 1605632 ? o3 : 0);
                const char* t1 = w == 2 ? (lane < 16 ? (const char*)P.wo : lane < 32 ? (const char*)P.w1 : (const char*)P.w2) + (o2 < 401408 ? o2 : 0) : (const char*)P.w4 + (o3 < 1605632 ? o3 : 0);
                asm volatile("s_mov_b32 m0, %2\n\ts_nop 0\n\tglobal_load_lds_dword %0, off\n\tglobal_load_lds_dword %1, off" :: "v"(t0), "v"(t1), "s"(L0 + L_MISC + 2048) : "memory");
            }
            if (w == 2 && lane < 32) ldsw_st(lane, 0);
            LBAR();
            {
                asm volatile("s_waitcnt vmcnt(2)" : "+v"(sr[0]), "+v"(sr[1]), "+v"(sr[2]), "+v"(sr[3]) :: "memory");
                if ((lane & 15) >= 8) sr[3] = (u32x4){0u, 0u, 0u, 0u};
                float t1 = 0.f, t2 = 0.f;
#pragma unroll
                for (int q = 0; q < 4; ++q)
#pragma unroll
                    for (int i = 0; i < 4; ++i) { t1 = dot2(sr[q][i], ONES, t1); t2 = dot2(sr[q][i], sr[q][i], t2); }
                t1 += xor32<1>(t1); t2 += xor32<1>(t2); t1 += xor32<2>(t1); t2 += xor32<2>(t2); t1 += xor32<4>(t1); t2 += xor32<4>(t2); t1 += xor32<8>(t1); t2 += xor32<8>(t2);
                const float mean = t1 * (1.f / D), var = __builtin_fmaxf(t2 * (1.f / D) - mean * mean, 0.f);
                const unsigned e = *(const unsigned*)(P.ws + WS_EP) + 1u;
                const __amdgpu_buffer_rsrc_t rs = __builtin_amdgcn_make_buffer_rsrc((void*)P.ws, 0, WS_END, 0x00020000);
                if ((lane & 15) == 0) gst(rs, (u32x4){f2u(mean), e, f2u(__builtin_amdgcn_rsqf(var + EPS)), e}, srow * 16, WS_ST1);
            }
            float cs[3][4] = {};
            const unsigned wrd2 = wrd + (w - 2) * 2 * 3072;
#pragma unroll 1
            for (int c = 0; c < 7; ++c) {
                ring_wait(c);
                const unsigned a = L0 + (c >= 5 ? c - 5 : c) * L_SLOT + wrd2;
#pragma unroll
                for (int ks = 0; ks < 2; ++ks)
#pragma unroll
                    for (int t = 0; t < 3; ++t) { const u32x4 f = wfrag(a + ks * 3072 + t * 256, a + ks * 3072 + 768 + t * 256);
#pragma unroll
                        for (int i = 0; i < 4; ++i) cs[t][i] = dot2(f[i], ONES, cs[t][i]); }
                WAIT_LGKM();
                ldsw_st(F_DONE + 2 + w, c + 1);
            }
#pragma unroll
            for (int t = 0; t < 3; ++t) { const float v = hsum((cs[t][0] + cs[t][1]) + (cs[t][2] + cs[t][3])); if (hh == 0) csp[(w - 2) * 96 + t * 32 + r32] = v; }
            WAIT_LGKM();
            ldsw_st(F_DONE + 8 + w, 1);
            if (w == 2) {
#pragma nounroll
                for (unsigned sp2 = 0; sp2 < SPIN_CAP * 16u && __builtin_amdgcn_readfirstlane(ldsw_ld(F_DONE + 11)) == 0; ++sp2) __builtin_amdgcn_s_sleep(1);
                if (hh == 0) {
#pragma unroll
                    for (int t = 0; t < 3; ++t) csp[384 + t * 32 + r32] = csp[t * 32 + r32] + csp[96 + t * 32 + r32];
                }
            }
        }
        LBAR();
        auto stage = [&](const WSrc& s, unsigned lds, int n0, int n1) AI {
#pragma unroll 1
            for (int n = n0; n < n1; ++n) wpiece(s, lds, n);
        };
        stage(wsrc_a((const char*)P.wo + (size_t)(112 * w) * 896 + tb * 64, 896, nt, lane), wst, 0, 28);
        BAR();
        BAR();
        LB(0);
        BAR(); stage(wsrc_a((const char*)P.w1 + (size_t)(112 * w) * 896 + tb * 64, 896, nt, lane), wst, 0, 28); BAR(); LB(0);
        BAR(); stage(wsrc_a((const char*)P.w2 + (size_t)(112 * w) * 896 + tb * 64, 896, nt, lane), wst, 0, 28); BAR(); LB(0);
        BAR(); stage(wsrc_a((const char*)P.w3 + (size_t)(112 * w) * 3584 + tb6 * 64, 3584, nt6, lane), wst, 0, 28); BAR(); LB(0);
        const WSrc w4s = wsrc_b((const char*)P.w4 + (size_t)(448 * w) * 896 + g7 * 128, 896, lane);
        if (act7) stage(w4s, L0 + L_X + w * 8192, 0, 8);
        {
            float c[4] = {0.f, 0.f, 0.f, 0.f};
            const unsigned a0 = L0 + trq * 64 + gq * 8 + 2 * hh * 1024 + w * 256;
#pragma unroll 4
            for (int k = 0; k < 28; ++k) { const unsigned a = a0 + k * 4096; const u32x4 f = wfrag(a, a + 1024);
#pragma unroll
                for (int i = 0; i < 4; ++i) c[i] = dot2(f[i], ONES, c[i]); }
            const float cc = hsum((c[0] + c[1]) + (c[2] + c[3]));
            if (hh == 0) ((LAS float*)(uintptr_t)(L0 + L_M2 + 1024))[w * 32 + r32] = cc;
        }
        LBAR();
        BAR();
        if (act7) stage(w4s, wst - 8 * 1024, 8, 36);
        LB(0);
        if (act7) {
            BAR();
            stage(w4s, wst - 36 * 1024, 36, 56);
            LB(0);
            BAR();
        }
        STAMP_FLUSH();
        return;
    }

    const unsigned e = *(const unsigned*)(P.ws + WS_EP) + 1u;
    const __amdgpu_buffer_rsrc_t rs = __builtin_amdgcn_make_buffer_rsrc((void*)P.ws, 0, WS_END, 0x00020000);
    u32x4 qf0, qf1;
    {
        LANE_GEOM();
        const unsigned wrd = 16384 + hh * 6 * 256 + trq * 64 + ((shift + gq * 8) & 63);
        STAMP(15);
        f32x16 aq = {}, ak = {}, av = {};
        const unsigned xrd = (32 * w + r32) * 128 + ((hh ^ ((r32 >> 1) & 7)) << 4);
        struct Fr { u32x4 x, q, k, v; };
        Fr F[4];
        auto ldx = [&](unsigned base, int j) AI { F[j].x = lds128((base + xrd) ^ (j << 5)); const unsigned a = base + wrd + j * 3072; F[j].q = wfrag(a, a + 768); };
        auto ldk = [&](unsigned base, int j) AI { const unsigned a = base + wrd + j * 3072; F[j].k = wfrag(a + 256, a + 1024); };
        auto ldv = [&](unsigned base, int j) AI { const unsigned a = base + wrd + j * 3072; F[j].v = wfrag(a + 512, a + 1280); };
        LAS float* stw = (LAS float*)(uintptr_t)(L0 + L_MISC + 3072 + w * 256);
        BAR();
        ring_wait(0);
        STAMP(16);
#pragma unroll
        for (int j = 0; j < 2; ++j) { ldx(L0, j); ldk(L0, j); ldv(L0, j); }
        SB();
        u32x4 gv = {0u, 0u, 0u, 0u};
        const unsigned gvo = (128 * m + 32 * w + r32) * 16;
#pragma unroll 1
        for (int c = 0; c < 7; ++c) {
            const unsigned base = L0 + (c >= 5 ? c - 5 : c) * L_SLOT, nbase = L0 + (c >= 4 ? c - 4 : c + 1) * L_SLOT;
            ldsw_st(F_DONE + w, c);
            const u32x2 rdy = ldsw_ld2(F_RDY + 2 * (c < 6 ? c + 1 : 6));
            if (c == 3) gv = gld(rs, gvo, WS_ST1);
            if (c == 5) {
#pragma nounroll
                for (unsigned sp2 = 0; sp2 < SPIN_CAP && !wall(gv.y == e && gv.w == e); ++sp2) gv = gld(rs, gvo, WS_ST1);
                if (hh == 0) { stw[r32] = u2f(gv.x) * u2f(gv.z); stw[32 + r32] = u2f(gv.z); }
            }
            sfor<4>([&](auto jc) AI {
                constexpr int j = decltype(jc)::value, jn = (j + 2) & 3;
                if constexpr (j == 2) { if (__builtin_amdgcn_readfirstlane(rdy.x & rdy.y) == 0) ring_wait(c + 1); SB(); }
                const unsigned lb = j < 2 ? base : nbase;
                MFMA(aq, F[j].q, F[j].x, aq);
                ldx(lb, jn);
                SB();
                MFMA(ak, F[j].k, F[j].x, ak);
                ldk(lb, jn);
                SB();
                MFMA(av, F[j].x, F[j].v, av);
                ldv(lb, jn);
                SB();
            });
        }
        STAMP(1);
        const float mean = u2f(gv.x), rstd = u2f(gv.z);
        LBAR();
        const LAS float* csf = (const LAS float*)(uintptr_t)(L0 + L_MISC + 1536);
        const float cv = csf[64 + r32];
        const float qs = rstd * (0.18898223650461362f * 1.4426950408889634f);
#pragma unroll
        for (int q = 0; q < 4; ++q) {
            const f32x4 c1 = *(const LAS f32x4*)(csf + 8 * q + 4 * hh), c2 = *(const LAS f32x4*)(csf + 32 + 8 * q + 4 * hh);
            const f32x4 mr = *(const LAS f32x4*)(stw + 8 * q + 4 * hh), rr = *(const LAS f32x4*)(stw + 32 + 8 * q + 4 * hh);
#pragma unroll
            for (int i = 0; i < 4; ++i) {
                aq[4 * q + i] = (aq[4 * q + i] - mean * c1[i]) * qs;
                ak[4 * q + i] = (ak[4 * q + i] - mean * c2[i]) * rstd;
                av[4 * q + i] = av[4 * q + i] * rr[i] - mr[i] * cv;
            }
        }
        if (hh == 1) { aq[12] = 1.f; aq[13] = 0.f; aq[14] = 0.f; aq[15] = 0.f; ak[12] = 0.f; ak[13] = 0.f; ak[14] = 0.f; ak[15] = 0.f; }
        if (r32 >= 28) {
            const float pv = r32 == 28 ? 1.f : 0.f;
#pragma unroll
            for (int i = 0; i < 16; ++i) av[i] = pv;
        }
        const u32x4 k0 = pack8(ak, 0), k1 = pack8(ak, 1), v0 = pack8(av, 0), v1 = pack8(av, 1);
        const unsigned go = WS_KV + (h * 64 + 4 * m + w) * 4096;
        gst(rs, k0, lane * 16, go); gst(rs, k1, lane * 16, go + 1024); gst(rs, v0, lane * 16, go + 2048); gst(rs, v1, lane * 16, go + 3072);
        const unsigned lo = L0 + L_OWNKV + w * 4096 + lane * 16;
        sts128(lo, k0); sts128(lo + 1024, k1); sts128(lo + 2048, v0); sts128(lo + 3072, v1);
        qf0 = pack8(aq, 0); qf1 = pack8(aq, 1);
        WAIT_BAR(0);
        if (tid == 0) set_flag(P.ws, flag_off(0, m * 16 + h), e);
        STAMP(2);
    }
    {
        LANE_GEOM();
        const int mt = m - 2 + (w >> 1);
        u32x4 kv[8];
        if (mt >= 0) {
            wait_flags(P.ws, flag_off(0, mt * 16 + h), 1, e, lane);
            const unsigned go = WS_KV + (h * 64 + 4 * m - 8 + 2 * w) * 4096;
#pragma unroll
            for (int i = 0; i < 8; ++i) kv[i] = gld(rs, lane * 16, go + i * 1024);
        } else {
#pragma unroll
            for (int i = 0; i < 8; ++i) kv[i] = (u32x4){0u, 0u, (i & 3) == 1 && hh == 1 ? 0xF149u : 0u, 0u};
        }
        f32x16 cdiag, cold;
#pragma unroll
        for (int i = 0; i < 16; ++i) { const int kr = (i & 3) + 8 * (i >> 2) + 4 * hh; cdiag[i] = kr <= r32 ? 0.f : -1e30f; cold[i] = kr > r32 ? 0.f : -1e30f; }
#pragma unroll
        for (int i = 0; i < 8; ++i) sts128(L0 + L_KV + 2 * w * 4096 + i * 1024 + lane * 16, kv[i]);
        LBAR();
        STAMP(3);
        unsigned kvb = L0 + L_KV + w * 4096 + lane * 16;
        PIN(kvb);
        u32x4 kf[9][2], vf[9][2];
        const f32x16 zero16 = {};
        auto ldK = [&](int i) AI { kf[i][0] = lds128(kvb + i * 4096); kf[i][1] = lds128(kvb + i * 4096 + 1024); };
        auto ldV = [&](int i) AI { vf[i][0] = lds128(kvb + i * 4096 + 2048); vf[i][1] = lds128(kvb + i * 4096 + 3072); };
        auto smax = [&](const f32x16& s, float a) AI {
#pragma unroll
            for (int i = 0; i < 16; i += 2) a = max3(a, s[i], s[i + 1]);
            return a; };
        ldK(0); ldK(1);
        float mx = -3e38f;
        f32x16 s[2];
        auto smax8 = [&](const f32x16& v, int o, float a) AI {
#pragma unroll
            for (int i = 0; i < 8; i += 2) a = max3(a, v[o + i], v[o + i + 1]);
            return a; };
        sfor<9>([&](auto ic) AI {
            constexpr int i = decltype(ic)::value;
            if constexpr (i + 2 < 9) ldK(i + 2);
            MFMA(s[i & 1], kf[i][0], qf0, i == 0 ? cold : i == 8 ? cdiag : zero16);
            SB();
            MFMA(s[i & 1], kf[i][1], qf1, s[i & 1]);
            SB();
            if constexpr (i > 0) { mx = smax8(s[(i - 1) & 1], 0, mx); mx = smax8(s[(i - 1) & 1], 8, mx); PIN(mx); }
            SB();
        });
        ldK(0); ldK(1); ldV(0);
        mx = hmax(smax(s[0], mx));
        STAMP(25);
        f32x16 o = {}, cm;
#pragma unroll
        for (int i = 0; i < 16; ++i) { cm[i] = -mx; cdiag[i] -= mx; cold[i] -= mx; }
        MFMA(s[0], kf[0][0], qf0, cold); MFMA(s[0], kf[0][1], qf1, s[0]);
        SB();
        sfor<9>([&](auto ic) AI {
            constexpr int i = decltype(ic)::value;
            f32x16& c = s[i & 1];
            f32x16& n = s[(i + 1) & 1];
            if constexpr (i + 2 < 9) ldK(i + 2);
            if constexpr (i + 1 < 9) ldV(i + 1);
            if constexpr (i + 1 < 9) MFMA(n, kf[i + 1][0], qf0, i + 1 == 8 ? cdiag : cm);
#pragma unroll
            for (int j = 0; j < 8; ++j) c[j] = __builtin_amdgcn_exp2f(c[j]);
            u32x4 p0 = pack8(c, 0);
            PIN(p0);
            SB();
            if constexpr (i + 1 < 9) MFMA(n, kf[i + 1][1], qf1, n);
            MFMA(o, vf[i][0], p0, o);
#pragma unroll
            for (int j = 8; j < 16; ++j) c[j] = __builtin_amdgcn_exp2f(c[j]);
            const u32x4 p1 = pack8(c, 1);
            MFMA(o, vf[i][1], p1, o);
            SB();
        });
        STAMP(26);
        auto lr = __builtin_amdgcn_permlane32_swap(f2u(o[12]), f2u(o[12]), false, false);
        const float inv = __builtin_amdgcn_rcpf(u2f(lr[1]));
#pragma unroll
        for (int i = 0; i < 16; ++i) o[i] *= inv;
        if (hh == 1) { o[12] = 0.f; o[13] = 0.f; o[14] = 0.f; o[15] = 0.f; }
        const unsigned vo = (hh * 2048 + r32) * 16, so = WS_O + ((4 * h) * 2048 + 128 * m + 32 * w) * 16;
        gst(rs, pack8(o, 0), vo, so); gst(rs, pack8(o, 1), vo, so + 2 * 2048 * 16);
        WAIT_BAR(0);
        if (tid == 0) set_flag(P.ws, flag_off(1, m * 16 + h), e);
        STAMP(4);
    }
    const bool act = w < nt;
    const int tw = tb + w;
#define GEMM_GEOM() LANE_GEOM(); \
    unsigned trW[2]; trW[0] = L0 + (w < nt ? w : nt - 1) * 256 + trq * 64 + gq * 8 + 2 * hh * 1024; trW[1] = trW[0] + 57344; PIN(trW[0]); PIN(trW[1]); \
    auto wimg = [&](int kq0, int kq1) AI { return wfrag(kq0 < 56 ? trW[0] + kq0 * 1024 : trW[1] + (kq0 - 56) * 1024, kq1 < 56 ? trW[0] + kq1 * 1024 : trW[1] + (kq1 - 56) * 1024); }; \
    unsigned xl = L0 + L_X + lane * 16; PIN(xl); \
    LAS float* m2 = (LAS float*)(uintptr_t)(L0 + L_M2 + w * 256); (void)m2
    auto gemm = [&](auto nk, const u32x4* wr, unsigned xl) AI -> f32x16 {
        constexpr int NK = decltype(nk)::value, PD = 4;
        f32x16 acc = {};
        u32x4 xf[NK];
#pragma unroll
        for (int k = 0; k < PD; ++k) xf[k] = lds128(xl + k * 1024);
        SB();
        sfor<NK>([&](auto kc) AI {
            constexpr int k = decltype(kc)::value;
            if constexpr (k + PD < NK) xf[k + PD] = lds128(xl + (k + PD) * 1024);
            MFMA(acc, wr[k], xf[k], acc);
            SB();
        });
        return acc;
    };
    u32x4 hp[2];
    float meanH, rstdH;
    {
        GEMM_GEOM();
        u32x4 xr[2];
        const char* xp = (const char*)P.x + (size_t)(32 * ti + r32) * 896 + 64 * (act ? tw : tb) + 16 * hh;
        xr[0] = *(const GAS u32x4*)xp; xr[1] = *(const GAS u32x4*)(xp + 32);
        u32x4 wr[32];
        sfor<32>([&](auto kc) AI { constexpr int k = decltype(kc)::value, kq = 7 * (k >> 1) + 4 * (k & 1);
            if constexpr (k < 31) wr[k] = wimg(kq, kq + 1);
            else { const u32x2 p = ldstr(trW[1] + (kq - 56) * 1024), q = ldstr(trW[1] + (kq - 56) * 1024 + (hh ? 0 : 1024)); wr[k] = (u32x4){p.x, p.y, q.x, q.y}; } });
        LBAR();
        wait_flags(P.ws, flag_off(1, (ti >> 2) * 16 + 4 * w), 4, e, lane);
        u32x4 xf[8];
#pragma unroll
        for (int k = 0; k < 8; ++k) xf[k] = gld(rs, vx, WS_O + ((16 * w + 2 * k) * 2048 + 32 * ti) * 16);
#pragma unroll
        for (int k = 0; k < 8; ++k) sts128(xl + (8 * w + k) * 1024, xf[k]);
        LBAR();
        STAMP(5);
        f32x16 r = gemm(IC(32), wr, xl);
        float s1 = 0.f, s2 = 0.f;
#pragma unroll
        for (int s = 0; s < 2; ++s) {
#pragma unroll
            for (int i = 0; i < 4; ++i) {
                hp[s][i] = pk(r[8 * s + 2 * i] + blo(xr[s][i]), r[8 * s + 2 * i + 1] + bhi(xr[s][i]));
                s1 = dot2(hp[s][i], ONES, s1); s2 = dot2(hp[s][i], hp[s][i], s2);
            }
            if (act) gst(rs, hp[s], vx, WS_H + ((4 * tw + 2 * s) * 2048 + 32 * ti) * 16);
        }
        s1 = hsum(s1); s2 = hsum(s2);
        if (act && hh == 0) gst8(rs, (u32x2){f2u(s1), f2u(s2)}, r32 * 8, WS_STH + (tw * 2048 + 32 * ti) * 8);
        WAIT_BAR(0);
        if (tid == 0) set_flag(P.ws, flag_off(2, ti * 4 + g), e);
        STAMP(6);
    }
#define LOAD_WR() u32x4 wr[28]; sfor<28>([&](auto kc) AI { constexpr int k = decltype(kc)::value; wr[k] = wimg(4 * k, 4 * k + 1); })
    const int fsn = w == 0 ? 1 : 2, fsl = w <= 1 ? 0 : w - 1;
    {
        GEMM_GEOM();
        LOAD_WR();
        LBAR();
        {
            float cs[4] = {0.f, 0.f, 0.f, 0.f};
#pragma unroll
            for (int k = 0; k < 28; ++k)
#pragma unroll
                for (int i = 0; i < 4; ++i) cs[i] = dot2(wr[k][i], ONES, cs[i]);
            const float c = hsum((cs[0] + cs[1]) + (cs[2] + cs[3]));
            if (hh == 0) m2[r32] = c;
        }
        wait_flags(P.ws, flag_off(2, ti * 4), 4, e, lane);
        u32x4 xf[7];
#pragma unroll
        for (int k = 0; k < 7; ++k) xf[k] = gld(rs, vx, WS_H + ((14 * w + 2 * k) * 2048 + 32 * ti) * 16);
        u32x2 sv[7];
#pragma unroll
        for (int q = 0; q < 7; ++q) sv[q] = gld8(rs, (7 * hh * 2048 + r32) * 8, WS_STH + (q * 2048 + 32 * ti) * 8);
#pragma unroll
        for (int k = 0; k < 7; ++k) sts128(xl + (7 * w + k) * 1024, xf[k]);
        LBAR();
        STAMP(7);
        f32x16 r = gemm(IC(28), wr, xl);
        float t1 = 0.f, t2 = 0.f;
#pragma unroll
        for (int q = 0; q < 7; ++q) { t1 += u2f(sv[q].x); t2 += u2f(sv[q].y); }
        t1 = hsum(t1); t2 = hsum(t2);
        meanH = t1 * (1.f / D);
        const float var = __builtin_fmaxf(t2 * (1.f / D) - meanH * meanH, 0.f);
        rstdH = __builtin_amdgcn_rsqf(var + EPS);
        const float sc = rstdH * __builtin_amdgcn_rsqf(var * rstdH * rstdH + EPS);
#pragma unroll
        for (int q = 0; q < 4; ++q) {
            const f32x4 c = *(const LAS f32x4*)(m2 + 8 * q + 4 * hh);
#pragma unroll
            for (int i = 0; i < 4; ++i) r[4 * q + i] = gelu_t((r[4 * q + i] - meanH * c[i]) * sc);
        }
#pragma unroll
        for (int s = 0; s < 2; ++s) if (act) gst(rs, pack8(r, s), vx, WS_HH + ((4 * tw + 2 * s) * 2048 + 32 * ti) * 16);
        WAIT_BAR(0);
        if (tid == 0) set_flag(P.ws, flag_off(3, ti * 4 + g), e);
        STAMP(8);
    }
    {
        GEMM_GEOM();
        LOAD_WR();
        LBAR();
        const int sh = w == 0 ? -1 : w == 1 ? 1 : w == 2 ? -2 : 2;
        const int tn = ti + (sh < 0 ? -1 : 1);
        {
            const bool nb = tn >= 0 && tn < 64;
            const int li = lane & 3;
            const GAS unsigned* p = (const GAS unsigned*)(P.ws + flag_off(3, (((li & 2) && nb) ? tn : ti) * 4 + fsl + ((li & 1) && fsn == 2 ? 1 : 0)));
#pragma nounroll
            for (unsigned sp = 0; sp < SPIN_CAP; ++sp) { unsigned v = __hip_atomic_load(p, __ATOMIC_RELAXED, __HIP_MEMORY_SCOPE_AGENT); if (wall(v == e)) break; }
            __builtin_amdgcn_fence(__ATOMIC_ACQUIRE, "wavefront");
        }
        const int tok = 32 * ti + r32 + sh;
        const bool ok = tok >= 0 && tok < S;
        const unsigned vxs = ok ? (hh * 2048 + tok) * 16 : 0x7ffffff0u;
        u32x4 xf[7];
#pragma unroll
        for (int k = 0; k < 7; ++k) { xf[k] = gld(rs, vxs, WS_HH + (14 * w + 2 * k) * 2048 * 16); }
#pragma unroll
        for (int k = 0; k < 7; ++k) sts128(xl + (7 * w + k) * 1024, xf[k]);
        LBAR();
        STAMP(9);
        f32x16 r = gemm(IC(28), wr, xl);
        float s1 = 0.f, s2 = 0.f;
#pragma unroll
        for (int s = 0; s < 2; ++s) {
            u32x4 xp;
#pragma unroll
            for (int i = 0; i < 4; ++i) {
                xp[i] = pk(r[8 * s + 2 * i] + (blo(hp[s][i]) - meanH) * rstdH, r[8 * s + 2 * i + 1] + (bhi(hp[s][i]) - meanH) * rstdH);
                s1 = dot2(xp[i], ONES, s1); s2 = dot2(xp[i], xp[i], s2);
            }
            if (act) gst(rs, xp, vx, WS_X1 + ((4 * tw + 2 * s) * 2048 + 32 * ti) * 16);
        }
        s1 = hsum(s1); s2 = hsum(s2);
        if (act && hh == 0) gst8(rs, (u32x2){f2u(s1), f2u(s2)}, r32 * 8, WS_STX + (tw * 2048 + 32 * ti) * 8);
        WAIT_BAR(0);
        if (tid == 0) set_flag(P.ws, flag_off(4, ti * 4 + g), e);
        STAMP(10);
    }
    {
        LANE_GEOM();
        unsigned tr6[2];
        tr6[0] = L0 + trq * 64 + gq * 8 + 2 * hh * 1024; tr6[1] = tr6[0] + 57344; PIN(tr6[0]); PIN(tr6[1]);
        auto wf6 = [&](int t, int k) AI { const unsigned a = (k < 14 ? tr6[0] + k * 4096 : tr6[1] + (k - 14) * 4096) + t * 256; return wfrag(a, a + 1024); };
        const LAS float* cs6 = (const LAS float*)(uintptr_t)(L0 + L_M2 + 1024);
        BAR();
        wait_flags(P.ws, flag_off(4, (4 * m + w) * 4), 4, e, lane);
        const int tk = 128 * m + 32 * w;
        auto ldx1 = [&](int k) AI { return gld(rs, vx, WS_X1 + (2 * k * 2048 + tk) * 16); };
        unsigned vx2 = vx; PIN(vx2);
        auto ldx2 = [&](int k) AI { return gld(rs, vx2, WS_X1 + (2 * k * 2048 + tk) * 16); };
        constexpr int PX = 10, PW = 3;
        f32x16 a0 = {}, a1 = {};
        u32x2 sv[7];
        u32x4 xg[28];
        constexpr int PG = 6;
        {
            u32x4 xf[28], wA[28], wB[28];
#pragma unroll
            for (int k = 0; k < PX; ++k) xf[k] = ldx1(k);
#pragma unroll
            for (int q = 0; q < 7; ++q) sv[q] = gld8(rs, (7 * hh * 2048 + r32) * 8, WS_STX + (q * 2048 + tk) * 8);
            STAMP(11);
#pragma unroll
            for (int k = 0; k < PW; ++k) { wA[k] = wf6(0, k); wB[k] = wf6(1, k); }
            SB();
            sfor<28>([&](auto kc) AI {
                constexpr int k = decltype(kc)::value;
                if constexpr (k + PW < 28) { wA[k + PW] = wf6(0, k + PW); wB[k + PW] = wf6(1, k + PW); }
                MFMA(a0, wA[k], xf[k], a0);
                if constexpr (k + PX < 28) xf[k + PX] = ldx1(k + PX);
                else if constexpr (k + PX - 28 < PG) xg[k + PX - 28] = ldx2(k + PX - 28);
                MFMA(a1, wB[k], xf[k], a1);
                SB();
            });
        }
        float t1 = 0.f, t2 = 0.f;
#pragma unroll
        for (int q = 0; q < 7; ++q) { t1 += u2f(sv[q].x); t2 += u2f(sv[q].y); }
        t1 = hsum(t1); t2 = hsum(t2);
        const float mean = t1 * (1.f / D), var = __builtin_fmaxf(t2 * (1.f / D) - mean * mean, 0.f) + EPS, rstd = __builtin_amdgcn_rsqf(var);
        const GeluK gk = {-mean, GC1 * rstd * rstd * rstd, GC0 * rstd, var * rstd};
        const unsigned uo = WS_U + ((4 * tb6) * 2048 + tk) * 16;
        auto epi2 = [&](auto nsc, auto&& pre, f32x16& ya, int ta, f32x16& yb, int tb_, bool storeb) AI {
            constexpr int NS = decltype(nsc)::value, OPS = NS >= 28 ? 1 : 2, SK = NS >= 28 ? 5 : 2;
            f32x4 c[8];
            float z[32];
            sfor<NS>([&](auto kc) AI {
                constexpr int k = decltype(kc)::value;
                pre(kc);
                sfor<8>([&](auto qc) AI { constexpr int q = decltype(qc)::value, st = (4 * q * SK) / 8;
                    if constexpr (k == (st >= 2 ? st - 2 : 0)) c[q] = *(const LAS f32x4*)(cs6 + (q < 4 ? ta : tb_) * 32 + 8 * (q & 3) + 4 * hh); });
                sfor<32>([&](auto ec) AI {
                    constexpr int el = decltype(ec)::value, st = (el * SK) / 8;
                    sfor<8>([&](auto oc) AI { constexpr int o = decltype(oc)::value;
                        if constexpr (o / OPS == k - st) { f32x16& y = el < 16 ? ya : yb; float yy = y[el & 15]; gelu_step(o, yy, z[el], c[el >> 2][el & 3], gk); if (o == 0 || o == 7) PIN(yy); else PIN(z[el]); y[el & 15] = yy; } });
                    if constexpr (el == 15 && k == st + 8 / OPS) { gst(rs, pack8(ya, 0), vx, uo + 4 * ta * 2048 * 16); gst(rs, pack8(ya, 1), vx, uo + (4 * ta + 2) * 2048 * 16); }
                });
                SB();
            });
            if (storeb) { gst(rs, pack8(yb, 0), vx, uo + 4 * tb_ * 2048 * 16); gst(rs, pack8(yb, 1), vx, uo + (4 * tb_ + 2) * 2048 * 16); }
        };
        STAMP(23);
        f32x16 a2 = {}, a3 = {};
        {
            u32x4 wA[28], wB[28];
#pragma unroll
            for (int k = 0; k < PW; ++k) { wA[k] = wf6(2, k); wB[k] = wf6(3, k); }
            SB();
            epi2(IC(28), [&](auto kc) AI {
                constexpr int k = decltype(kc)::value;
                if constexpr (k + PW < 28) { wA[k + PW] = wf6(2, k + PW); wB[k + PW] = wf6(3, k + PW); }
                MFMA(a2, wA[k], xg[k], a2);
                if constexpr (k + PG < 28) xg[k + PG] = ldx2(k + PG);
                MFMA(a3, wB[k], xg[k], a3);
            }, a0, 0, a1, 1, true);
        }
        STAMP(24);
        LBAR();
        epi2(IC(14), [&](auto) AI {}, a2, 2, a3, 3, nt6 == 4);
        WAIT_BAR(0);
        if (tid == 0) set_flag(P.ws, flag_off(5, m * 16 + s16), e);
        STAMP(12);
    }
    if (act7) {
        LANE_GEOM();
        const unsigned lanep = trq * 64 + gq * 8 + 2 * hh * 512;
        unsigned tA = L0 + L_X + w * 8192 + lanep, tB = wst + lanep; PIN(tA); PIN(tB);
        auto w4f = [&](int k, int t) AI { const unsigned a = (k < 4 ? tA + k * 2048 : k < 18 ? tB + (k - 4) * 2048 : tB + (k - 18) * 2048) + t * 256; return wfrag(a, a + 512); };
        u32x4 wr[28][2];
#pragma unroll
        for (int k = 0; k < 14; ++k) { wr[k][0] = w4f(k, 0); wr[k][1] = w4f(k, 1); }
        LBAR();
        const int tk = 64 * j7;
        const int tt = w >> 1, nt7 = w & 1;
        wait_flags(P.ws, flag_off(5, (j7 >> 1) * 16 + 4 * w), 4, e, lane);
        constexpr int PX = 5;
        u32x4 xa[28][2];
#pragma unroll
        for (int k = 0; k < PX; ++k) { xa[k][0] = gld(rs, vx, WS_U + ((56 * w + 2 * k) * 2048 + tk) * 16); xa[k][1] = gld(rs, vx, WS_U + ((56 * w + 2 * k) * 2048 + tk + 32) * 16); }
        STAMP(13);
        SB();
        f32x16 acc[4] = {};
        u32x4 rh[2], rx[2];
        sfor<28>([&](auto kc) AI {
            constexpr int k = decltype(kc)::value;
            if constexpr (k == 13) { BAR(); SB(); }
            if constexpr (k + 3 >= 14 && k + 3 < 28) { wr[k + 3][0] = w4f(k + 3, 0); wr[k + 3][1] = w4f(k + 3, 1); }
            MFMA(acc[0], wr[k][0], xa[k][0], acc[0]); MFMA(acc[1], wr[k][1], xa[k][0], acc[1]);
            if constexpr (k + PX < 28) xa[k + PX][0] = gld(rs, vx, WS_U + ((56 * w + 2 * (k + PX)) * 2048 + tk) * 16);
            MFMA(acc[2], wr[k][0], xa[k][1], acc[2]); MFMA(acc[3], wr[k][1], xa[k][1], acc[3]);
            if constexpr (k + PX < 28) xa[k + PX][1] = gld(rs, vx, WS_U + ((56 * w + 2 * (k + PX)) * 2048 + tk + 32) * 16);
            if constexpr (k == 24 || k == 25) {
                constexpr int s = k - 24;
                const unsigned so = ((8 * g7 + 4 * nt7 + 2 * s) * 2048 + tk + 32 * tt) * 16;
                rh[s] = gld(rs, vx, WS_H + so); rx[s] = gld(rs, vx, WS_X1 + so);
            }
            SB();
        });
        STAMP(27);
#pragma unroll
        for (int t = 0; t < 4; ++t) {
            if (t != w) {
                const unsigned a = L0 + L_X + t * 12288 + (w < t ? w : w - 1) * 4096 + lane * 16;
#pragma unroll
                for (int q = 0; q < 4; ++q) stsf4(a + q * 1024, (f32x4){acc[t][4 * q], acc[t][4 * q + 1], acc[t][4 * q + 2], acc[t][4 * q + 3]});
            }
        }
        LBAR();
        f32x16 r = w == 0 ? acc[0] : w == 1 ? acc[1] : w == 2 ? acc[2] : acc[3];
        const unsigned a = L0 + L_X + w * 12288 + lane * 16;
#pragma unroll
        for (int j = 0; j < 3; ++j)
#pragma unroll
            for (int q = 0; q < 4; ++q) { const f32x4 v = ldsf4(a + j * 4096 + q * 1024); r[4 * q] += v[0]; r[4 * q + 1] += v[1]; r[4 * q + 2] += v[2]; r[4 * q + 3] += v[3]; }
        char* op = (char*)P.out + (size_t)(tk + 32 * tt + r32) * 896 + 128 * g7 + 64 * nt7 + 16 * hh;
#pragma unroll
        for (int s = 0; s < 2; ++s) {
            u32x4 ov;
#pragma unroll
            for (int i = 0; i < 4; ++i)
                ov[i] = pk(r[8 * s + 2 * i] + blo(rx[s][i]) + blo(rh[s][i]), r[8 * s + 2 * i + 1] + bhi(rx[s][i]) + bhi(rh[s][i]));
            *(GAS u32x4*)(op + 32 * s) = ov;
        }
        STAMP(14);
    }
    STAMP_FLUSH();
    if (b == 0 && w == 0) {
        const int lane = tid & 63;
        for (int r = 0; r < 4; ++r) {
            const GAS unsigned* p = (const GAS unsigned*)(P.ws + flag_off(0, r * 64 + lane));
#pragma nounroll
            for (unsigned sp = 0; sp < SPIN_CAP; ++sp) { unsigned v = __hip_atomic_load(p, __ATOMIC_RELAXED, __HIP_MEMORY_SCOPE_AGENT); if (wall(v == e)) break; }
        }
        if (lane == 0) __hip_atomic_store((GAS unsigned*)(P.ws + WS_EP), e, __ATOMIC_RELAXED, __HIP_MEMORY_SCOPE_AGENT);
    }
}
}
#if defined(__HIP_DEVICE_COMPILE__)
#pragma clang attribute pop
#endif

extern "C" void kernel_launch(void* const* d_in, const int* in_sizes, int n_in, void* d_out, int out_size, void* d_ws, size_t ws_size,
                              hipStream_t stream) {
    mk::Params P;
    P.x = (const u16*)d_in[0]; P.wqkv = (const u16*)d_in[1]; P.wo = (const u16*)d_in[2];
    P.w1 = (const u16*)d_in[4]; P.w2 = (const u16*)d_in[5]; P.w3 = (const u16*)d_in[6]; P.w4 = (const u16*)d_in[7];
    P.out = (u16*)d_out; P.ws = (char*)d_ws;
    mk::fused_fwd<<<256, 512, 0, stream>>>(P);
}
```

```cpp
#define TOUCH_TLB 1
#include <hip/hip_runtime.h>
#include <hip/hip_bf16.h>
#include <stdint.h>
#include <type_traits>
#include <utility>

typedef unsigned short u16;
typedef __attribute__((ext_vector_type(8))) short bf16x8;
typedef __attribute__((ext_vector_type(4))) short s16x4;
typedef __attribute__((ext_vector_type(16))) float f32x16;
typedef __attribute__((ext_vector_type(4))) float f32x4;
typedef __attribute__((ext_vector_type(2))) float f32x2;
typedef __attribute__((ext_vector_type(4))) unsigned u32x4;
typedef __attribute__((ext_vector_type(2))) unsigned u32x2;
typedef __attribute__((ext_vector_type(2))) __bf16 bf2;
#define LAS __attribute__((address_space(3)))
#define GAS __attribute__((address_space(1)))
#define DEVI __device__ __forceinline__

#define MK_STAMP 0

#if defined(__HIP_DEVICE_COMPILE__)
#pragma clang attribute push (__attribute__((target("no-packed-fp32-ops"))), apply_to = function)
#endif
namespace mk {
constexpr int S = 2048, D = 448;
constexpr unsigned XR = 2056, XROW = XR * 16;
constexpr unsigned KVH = 64 * 4096 + 128;
constexpr unsigned WS_EP = 0;
constexpr unsigned WS_FLAG = 4096;
constexpr unsigned WS_KV = 262144;
constexpr unsigned WS_O = WS_KV + 16 * KVH;
constexpr unsigned WS_H = WS_O + 64 * XROW;
constexpr unsigned WS_HH = WS_H + 56 * XROW;
constexpr unsigned WS_X1 = WS_HH + 56 * XROW;
constexpr unsigned WS_U = WS_X1 + 56 * XROW;
constexpr unsigned WS_STH = WS_U + 224 * XROW;
constexpr unsigned WS_STX = WS_STH + 262144;
constexpr unsigned WS_ST1 = WS_STX + 262144;
constexpr unsigned WS_SPARE = WS_ST1 + 32768;
constexpr unsigned WS_STAMP = WS_SPARE + 128;
constexpr unsigned WS_END = WS_STAMP + 256 * 48 * 8;
constexpr unsigned L_SLOT = 28672;
constexpr unsigned L_MISC = 143360;
constexpr unsigned L_KV = 114688;
constexpr unsigned L_OWNKV = L_KV + 8 * 4096;
constexpr unsigned L_WST = 28672;
constexpr unsigned L_X = 114688;
constexpr unsigned L_M2 = 147456;
constexpr unsigned CODE_TOUCH_BYTES = 32768;
constexpr float EPS = 1e-5f;
constexpr unsigned SPIN_CAP = 1u << 18;

struct Params {
    const u16* x; const u16* wqkv; const u16* wo; const u16* w1; const u16* w2; const u16* w3; const u16* w4;
    u16* out; char* ws;
};

DEVI unsigned f2u(float f) { return __builtin_bit_cast(unsigned, f); }
DEVI float u2f(unsigned u) { return __builtin_bit_cast(float, u); }
DEVI bool wall(bool p) { return __builtin_amdgcn_ballot_w64(!p) == 0; }
template <int X> DEVI float xor32(float v) { return u2f((unsigned)__builtin_amdgcn_ds_swizzle((int)f2u(v), (X << 10) | 0x1f)); }
DEVI unsigned pk(float lo, float hi) { bf2 v = {(__bf16)lo, (__bf16)hi}; return __builtin_bit_cast(unsigned, v); }
DEVI float blo(unsigned u) { return u2f(u << 16); }
DEVI float bhi(unsigned u) { return u2f(u & 0xffff0000u); }
DEVI float dot2(unsigned a, unsigned b, float c) { return __builtin_amdgcn_fdot2_f32_bf16(__builtin_bit_cast(bf2, a), __builtin_bit_cast(bf2, b), c, false); }
constexpr unsigned ONES = 0x3f803f80u;
DEVI float hsum(float v) { auto r = __builtin_amdgcn_permlane32_swap(f2u(v), f2u(v), false, false); return u2f(r[0]) + u2f(r[1]); }
DEVI float hmax(float v) { auto r = __builtin_amdgcn_permlane32_swap(f2u(v), f2u(v), false, false); return __builtin_fmaxf(u2f(r[0]), u2f(r[1])); }
DEVI f32x16 mfma(u32x4 a, u32x4 b, f32x16 c) { return __builtin_amdgcn_mfma_f32_32x32x16_bf16(__builtin_bit_cast(bf16x8, a), __builtin_bit_cast(bf16x8, b), c, 0, 0, 0); }
#define MFMA(acc, a, b, c) do { acc = mfma(a, b, c); asm volatile("" : "+v"(acc)); } while (0)
DEVI u32x4 lds128(unsigned a) { return *(const LAS u32x4*)(uintptr_t)a; }
DEVI f32x4 ldsf4(unsigned a) { return *(const LAS f32x4*)(uintptr_t)a; }
DEVI void sts128(unsigned a, u32x4 v) { *(LAS u32x4*)(uintptr_t)a = v; }
DEVI void stsf4(unsigned a, f32x4 v) { *(LAS f32x4*)(uintptr_t)a = v; }
DEVI u32x2 ldstr(unsigned a) { return __builtin_bit_cast(u32x2, __builtin_amdgcn_ds_read_tr16_b64_v4i16((LAS s16x4*)(uintptr_t)a)); }
DEVI u32x4 wfrag(unsigned a0, unsigned a1) { u32x2 p = ldstr(a0), q = ldstr(a1); return (u32x4){p.x, p.y, q.x, q.y}; }
DEVI u32x4 pack8(const f32x16& a, int s) { return (u32x4){pk(a[8 * s], a[8 * s + 1]), pk(a[8 * s + 2], a[8 * s + 3]), pk(a[8 * s + 4], a[8 * s + 5]), pk(a[8 * s + 6], a[8 * s + 7])}; }
constexpr int SC1 = 16;
DEVI u32x4 gld(__amdgpu_buffer_rsrc_t rs, unsigned voff, unsigned soff) { return __builtin_amdgcn_raw_buffer_load_b128(rs, voff, soff, SC1); }
DEVI u32x2 gld8(__amdgpu_buffer_rsrc_t rs, unsigned voff, unsigned soff) { return __builtin_amdgcn_raw_buffer_load_b64(rs, voff, soff, SC1); }
DEVI void gst(__amdgpu_buffer_rsrc_t rs, u32x4 v, unsigned voff, unsigned soff) { __builtin_amdgcn_raw_buffer_store_b128(v, rs, voff + soff, 0, SC1); }
DEVI void gst8(__amdgpu_buffer_rsrc_t rs, u32x2 v, unsigned voff, unsigned soff) { __builtin_amdgcn_raw_buffer_store_b64(v, rs, voff, soff, SC1); }
#define WAIT_VM(N) asm volatile("s_waitcnt vmcnt(" #N ")" ::: "memory")
#define WAIT_LGKM() asm volatile("s_waitcnt lgkmcnt(0)" ::: "memory")
#define WAIT_BAR(N) asm volatile("s_waitcnt vmcnt(" #N ") lgkmcnt(0)\n\ts_barrier" ::: "memory")
#define LBAR() asm volatile("s_waitcnt lgkmcnt(0)\n\ts_barrier" ::: "memory")
#define SB() __builtin_amdgcn_sched_barrier(0)
#define PIN(x) asm volatile("" : "+v"(x))
DEVI float fma1(float a, float b, float c) { return __builtin_fmaf(a, b, c); }
DEVI float mul1(float a, float b) { return a * b; }
DEVI float max3(float a, float b, float c) { return __builtin_fmaxf(__builtin_fmaxf(a, b), c); }
template <int... Is, class F> DEVI void sfor_impl(std::integer_sequence<int, Is...>, F&& f) { (f(std::integral_constant<int, Is>{}), ...); }
template <int N, class F> DEVI void sfor(F&& f) { sfor_impl(std::make_integer_sequence<int, N>{}, f); }
#define AI __attribute__((always_inline))
#define IC(n) std::integral_constant<int, n>{}

DEVI void dma1(const char* sbase, unsigned voff, unsigned lds) {
    asm volatile("s_mov_b32 m0, %2\n\ts_nop 0\n\tglobal_load_lds_dwordx4 %0, %1" :: "v"(voff), "s"(sbase), "s"(lds) : "memory");
}
DEVI void touch(const char* sbase, unsigned voff, unsigned lds) {
    asm volatile("s_mov_b32 m0, %2\n\ts_nop 0\n\tglobal_load_lds_dword %0, %1" :: "v"(voff), "s"(sbase), "s"(lds) : "memory");
}
struct WSrc { const char* base; unsigned step, voff; };
DEVI void wpiece(const WSrc& s, unsigned lds, int n) { dma1(s.base + (size_t)n * s.step, s.voff, lds + n * 1024); }
DEVI WSrc wsrc_a(const char* base, unsigned rs, int nt, int lane) { const int t = (lane >> 4) < nt ? (lane >> 4) : nt - 1; return WSrc{base, 4 * rs, ((lane >> 2) & 3) * rs + t * 64 + (lane & 3) * 16}; }
DEVI WSrc wsrc_b(const char* base, unsigned rs, int lane) { return WSrc{base, 8 * rs, (4 * (lane >> 5) + ((lane >> 2) & 3)) * rs + ((lane >> 4) & 1) * 64 + (lane & 3) * 16}; }

DEVI void wait_flags(const char* ws, unsigned foff, int n, unsigned e, int lane) {
    const GAS unsigned* p = (const GAS unsigned*)(ws + foff) + (lane < n ? lane : 0) * 32;
#pragma nounroll
    for (unsigned sp = 0; sp < SPIN_CAP; ++sp) {
        unsigned v = __hip_atomic_load(p, __ATOMIC_RELAXED, __HIP_MEMORY_SCOPE_AGENT);
        if (wall(v == e)) break;
    }
    __builtin_amdgcn_fence(__ATOMIC_ACQUIRE, "wavefront");
}
DEVI void set_flag(char* ws, unsigned foff, unsigned e) { __hip_atomic_store((GAS unsigned*)(ws + foff), e, __ATOMIC_RELAXED, __HIP_MEMORY_SCOPE_AGENT); }
DEVI unsigned flag_off(int ph, int idx) { return WS_FLAG + (ph * 256 + idx) * 128; }

constexpr float GC0 = -2.f * 0.7978845608028654f * 1.4426950408889634f, GC1 = GC0 * 0.044715f;
DEVI float gelu_t(float x) {
    float z = x * __builtin_fmaf(x * x, GC1, GC0);
    return x * __builtin_amdgcn_rcpf(1.f + __builtin_amdgcn_exp2f(z));
}
struct GeluK { float nmean, k1, k0, irs; };
DEVI void gelu_step(int o, float& y, float& z, float c, const GeluK& g) {
    if (o == 0) y = fma1(g.nmean, c, y);
    else if (o == 1) z = mul1(y, y);
    else if (o == 2) z = fma1(z, g.k1, g.k0);
    else if (o == 3) z = mul1(y, z);
    else if (o == 4) z = __builtin_amdgcn_exp2f(z);
    else if (o == 5) z = fma1(z, g.irs, g.irs);
    else if (o == 6) z = __builtin_amdgcn_rcpf(z);
    else y = mul1(y, z);
}

#define STAMP(k) do {} while (0)
#define STAMP_DECL
#define STAMP_FLUSH() do {} while (0)

#define LANE_GEOM() int lane_ = threadIdx.x & 63; asm volatile("" : "+v"(lane_)); const int lane = lane_, r32 = lane & 31, hh = lane >> 5; \
    const int trq = (lane & 15) >> 2, trQ_ = ((lane >> 4) & 1) * 4 + (lane & 3), gq = (trQ_ & 4) | ((trQ_ & 1) << 1) | ((trQ_ >> 1) & 1); \
    const unsigned vx = (hh * XR + r32) * 16; (void)trq; (void)gq; (void)vx; (void)r32; (void)hh
constexpr unsigned L_FLG = L_MISC + 1920, F_RDY = 0, F_DONE = 16;
DEVI unsigned ldsw_ld(unsigned i) { return *(volatile LAS unsigned*)(uintptr_t)(L_FLG + 4 * i); }
DEVI u32x2 ldsw_ld2(unsigned i) { return *(volatile LAS u32x2*)(uintptr_t)(L_FLG + 4 * i); }
DEVI void ldsw_st(unsigned i, unsigned v) { *(volatile LAS unsigned*)(uintptr_t)(L_FLG + 4 * i) = v; }
DEVI void ring_wait(int c) {
#pragma nounroll
    for (unsigned sp = 0; sp < SPIN_CAP * 16u; ++sp) { const u32x2 v = ldsw_ld2(F_RDY + 2 * c); if (__builtin_amdgcn_readfirstlane(v.x & v.y)) break; __builtin_amdgcn_s_sleep(1); }
}
DEVI void slot_wait(unsigned n, int lane) {
#pragma nounroll
    for (unsigned sp = 0; sp < SPIN_CAP * 16u; ++sp) { const unsigned v = ldsw_ld(F_DONE + (lane < 6 ? lane : 0)); if (wall(v >= n)) break; __builtin_amdgcn_s_sleep(1); }
}
#define LB(N) asm volatile("s_waitcnt vmcnt(" #N ")\n\ts_barrier" ::: "memory")
#define BAR() asm volatile("s_barrier" ::: "memory")

__global__ __launch_bounds__(512, 2) void fused_fwd(Params P) {
    __shared__ __attribute__((aligned(16))) char smem[163840];
    const unsigned L0 = (unsigned)(uintptr_t)(LAS char*)smem;
    const int tid = threadIdx.x, wv = __builtin_amdgcn_readfirstlane(tid >> 6), w = wv & 3;
    const int b = blockIdx.x, xcd = b & 7, slot = b >> 3;
    const int m = 2 * xcd + (slot >> 4), h = slot & 15;
    const int g = slot & 3, nt = g < 2 ? 4 : 3, tb = g < 2 ? 4 * g : 3 * g + 2;
    const int ti = 8 * xcd + (slot >> 2);
    const int s16 = slot & 15, pr = s16 >> 1;
    const int nt6 = ((s16 ^ pr) & 1) ? 3 : 4, tb6 = 7 * pr + ((s16 & 1) ? (pr & 1 ? 3 : 4) : 0);
    const bool act7 = slot < 28;
    const int j7 = 4 * xcd + (act7 ? slot / 7 : 0), g7 = act7 ? slot % 7 : 0;
    const unsigned wst = L0 + w * L_WST;
    const unsigned shift = (56 * h) & 15u;
    STAMP_DECL

    if (wv >= 4) {
        LANE_GEOM();
        const unsigned wrd = 16384 + hh * 6 * 256 + trq * 64 + ((shift + gq * 8) & 63);
        LAS float* csp = (LAS float*)(uintptr_t)(L0 + L_MISC);
        if (w < 2) {
            const unsigned xoffE = (lane >> 3) * 896 + (((lane & 7) ^ (lane >> 4)) << 4);
            const unsigned xoffO = (lane >> 3) * 896 + (((lane & 7) ^ (4 + (lane >> 4))) << 4);
            const unsigned colb = (56 * h) & ~15u;
            unsigned woff[3];
#pragma unroll
            for (int j = 0; j < 3; ++j) {
                const int bl = 4 * j + (lane >> 4), kql = bl / 3, tile = bl % 3;
                woff[j] = (4 * kql + ((lane >> 2) & 3)) * 2688 + tile * 896 + colb + (lane & 3) * 16;
            }
            const char* xb = (const char*)P.x + (size_t)(128 * m + 64 * w) * 896;
            const char* wb = (const char*)P.wqkv + (size_t)(32 * w) * 2688;
            LBAR();
#pragma unroll 1
            for (int kc = 0; kc < 7; ++kc) {
                if (kc >= 5) slot_wait(kc - 4, lane);
                const unsigned base = L0 + (kc >= 5 ? kc - 5 : kc) * L_SLOT;
#pragma unroll
                for (int i = 0; i < 6; ++i) dma1(wb + (size_t)kc * 64 * 2688 + (i / 3) * 16 * 2688, woff[i % 3], base + 16384 + (6 * w + i) * 1024);
#pragma unroll
                for (int i = 0; i < 8; ++i) dma1(xb + kc * 128 + i * 8 * 896, (i & 1) ? xoffO : xoffE, base + (8 * w + i) * 1024);
                if (kc > 0) { WAIT_VM(14); ldsw_st(F_RDY + 2 * (kc - 1) + w, 1); }
            }
            WAIT_VM(0); ldsw_st(F_RDY + 12 + w, 1);
        } else {
            const int srow = 128 * m + 8 * h + 4 * (w - 2) + (lane >> 4);
            const char* sp = (const char*)P.x + (size_t)srow * 896 + (lane & 15) * 16;
            u32x4 sr[4];
            asm volatile("global_load_dwordx4 %0, %4, off\n\tglobal_load_dwordx4 %1, %4, off offset:256\n\tglobal_load_dwordx4 %2, %4, off offset:512\n\tglobal_load_dwordx4 %3, %5, off"
                         : "=&v"(sr[0]), "=&v"(sr[1]), "=&v"(sr[2]), "=&v"(sr[3]) : "v"(sp), "v"(sp + ((lane & 15) < 8 ? 768 : 0)) : "memory");
            {
                unsigned long long pc; unsigned back;
                asm volatile("s_getpc_b64 %0\nmk_code_here:\n\ts_mov_b32 %1, mk_code_here-_ZN2mk9fused_fwdENS_6ParamsE" : "=s"(pc), "=s"(back));
                constexpr unsigned len = CODE_TOUCH_BYTES;
                const unsigned oc = (slot * 16 + (lane & 15)) * 128, o3 = (b * 64 + lane) * 128, o2 = (b * 16 + (lane & 15)) * 128;
                const char* t0 = w == 2 ? (const char*)(pc - back) + (oc < len ? oc : 0) : (const char*)P.w3 + (o3 < 1605632 ? o3 : 0);
                const char* t1 = w == 2 ? (lane < 16 ? (const char*)P.wo : lane < 32 ? (const char*)P.w1 : (const char*)P.w2) + (o2 < 401408 ? o2 : 0) : (const char*)P.w4 + (o3 < 1605632 ? o3 : 0);
                const unsigned li = slot * 16 + (lane & 15);
                const unsigned wo_ = li < 64 ? WS_O + li * XROW + 32768 : li < 456 ? WS_H + (li - 64) * XROW + 32768 : li < 472 ? WS_KV + (li - 456) * KVH + 262144 : WS_SPARE;
                const char* t2 = P.ws + (TOUCH_TLB ? wo_ : WS_SPARE);
                asm volatile("s_mov_b32 m0, %3\n\ts_nop 0\n\tglobal_load_lds_dword %0, off\n\tglobal_load_lds_dword %1, off\n\tglobal_load_lds_dword %2, off" :: "v"(t0), "v"(t1), "v"(t2), "s"(L0 + L_MISC + 2048) : "memory");
            }
            if (w == 2 && lane < 32) ldsw_st(lane, 0);
            LBAR();
            {
                asm volatile("s_waitcnt vmcnt(3)" : "+v"(sr[0]), "+v"(sr[1]), "+v"(sr[2]), "+v"(sr[3]) :: "memory");
                if ((lane & 15) >= 8) sr[3] = (u32x4){0u, 0u, 0u, 0u};
                float t1 = 0.f, t2 = 0.f;
#pragma unroll
                for (int q = 0; q < 4; ++q)
#pragma unroll
                    for (int i = 0; i < 4; ++i) { t1 = dot2(sr[q][i], ONES, t1); t2 = dot2(sr[q][i], sr[q][i], t2); }
                t1 += xor32<1>(t1); t2 += xor32<1>(t2); t1 += xor32<2>(t1); t2 += xor32<2>(t2); t1 += xor32<4>(t1); t2 += xor32<4>(t2); t1 += xor32<8>(t1); t2 += xor32<8>(t2);
                const float mean = t1 * (1.f / D), var = __builtin_fmaxf(t2 * (1.f / D) - mean * mean, 0.f);
                const unsigned e = *(const unsigned*)(P.ws + WS_EP) + 1u;
                const __amdgpu_buffer_rsrc_t rs = __builtin_amdgcn_make_buffer_rsrc((void*)P.ws, 0, WS_END, 0x00020000);
                if ((lane & 15) == 0) gst(rs, (u32x4){f2u(mean), e, f2u(__builtin_amdgcn_rsqf(var + EPS)), e}, srow * 16, WS_ST1);
            }
            float cs[3][4] = {};
            const unsigned wrd2 = wrd + (w - 2) * 2 * 3072;
#pragma unroll 1
            for (int c = 0; c < 7; ++c) {
                ring_wait(c);
                const unsigned a = L0 + (c >= 5 ? c - 5 : c) * L_SLOT + wrd2;
#pragma unroll
                for (int ks = 0; ks < 2; ++ks)
#pragma unroll
                    for (int t = 0; t < 3; ++t) { const u32x4 f = wfrag(a + ks * 3072 + t * 256, a + ks * 3072 + 768 + t * 256);
#pragma unroll
                        for (int i = 0; i < 4; ++i) cs[t][i] = dot2(f[i], ONES, cs[t][i]); }
                WAIT_LGKM();
                ldsw_st(F_DONE + 2 + w, c + 1);
            }
#pragma unroll
            for (int t = 0; t < 3; ++t) { const float v = hsum((cs[t][0] + cs[t][1]) + (cs[t][2] + cs[t][3])); if (hh == 0) csp[(w - 2) * 96 + t * 32 + r32] = v; }
            WAIT_LGKM();
            ldsw_st(F_DONE + 8 + w, 1);
            if (w == 2) {
#pragma nounroll
                for (unsigned sp2 = 0; sp2 < SPIN_CAP * 16u && __builtin_amdgcn_readfirstlane(ldsw_ld(F_DONE + 11)) == 0; ++sp2) __builtin_amdgcn_s_sleep(1);
                if (hh == 0) {
#pragma unroll
                    for (int t = 0; t < 3; ++t) csp[384 + t * 32 + r32] = csp[t * 32 + r32] + csp[96 + t * 32 + r32];
                }
            }
        }
        LBAR();
        auto stage = [&](const WSrc& s, unsigned lds, int n0, int n1) AI {
#pragma unroll 1
            for (int n = n0; n < n1; ++n) wpiece(s, lds, n);
        };
        stage(wsrc_a((const char*)P.wo + (size_t)(112 * w) * 896 + tb * 64, 896, nt, lane), wst, 0, 28);
        BAR();
        BAR();
        LB(0);
        BAR(); stage(wsrc_a((const char*)P.w1 + (size_t)(112 * w) * 896 + tb * 64, 896, nt, lane), wst, 0, 28); BAR(); LB(0);
        BAR(); stage(wsrc_a((const char*)P.w2 + (size_t)(112 * w) * 896 + tb * 64, 896, nt, lane), wst, 0, 28); BAR(); LB(0);
        BAR(); stage(wsrc_a((const char*)P.w3 + (size_t)(112 * w) * 3584 + tb6 * 64, 3584, nt6, lane), wst, 0, 28); BAR(); LB(0);
        const WSrc w4s = wsrc_b((const char*)P.w4 + (size_t)(448 * w) * 896 + g7 * 128, 896, lane);
        if (act7) stage(w4s, L0 + L_X + w * 8192, 0, 8);
        {
            float c[4] = {0.f, 0.f, 0.f, 0.f};
            const unsigned a0 = L0 + trq * 64 + gq * 8 + 2 * hh * 1024 + w * 256;
#pragma unroll 4
            for (int k = 0; k < 28; ++k) { const unsigned a = a0 + k * 4096; const u32x4 f = wfrag(a, a + 1024);
#pragma unroll
                for (int i = 0; i < 4; ++i) c[i] = dot2(f[i], ONES, c[i]); }
            const float cc = hsum((c[0] + c[1]) + (c[2] + c[3]));
            if (hh == 0) ((LAS float*)(uintptr_t)(L0 + L_M2 + 1024))[w * 32 + r32] = cc;
        }
        LBAR();
        BAR();
        if (act7) stage(w4s, wst - 8 * 1024, 8, 36);
        LB(0);
        if (act7) {
            BAR();
            stage(w4s, wst - 36 * 1024, 36, 56);
            LB(0);
            BAR();
        }
        STAMP_FLUSH();
        return;
    }

    const unsigned e = *(const unsigned*)(P.ws + WS_EP) + 1u;
    const __amdgpu_buffer_rsrc_t rs = __builtin_amdgcn_make_buffer_rsrc((void*)P.ws, 0, WS_END, 0x00020000);
    u32x4 qf0, qf1;
    {
        LANE_GEOM();
        const unsigned wrd = 16384 + hh * 6 * 256 + trq * 64 + ((shift + gq * 8) & 63);
        STAMP(15);
        f32x16 aq = {}, ak = {}, av = {};
        const unsigned xrd = (32 * w + r32) * 128 + ((hh ^ ((r32 >> 1) & 7)) << 4);
        struct Fr { u32x4 x, q, k, v; };
        Fr F[4];
        auto ldx = [&](unsigned base, int j) AI { F[j].x = lds128((base + xrd) ^ (j << 5)); const unsigned a = base + wrd + j * 3072; F[j].q = wfrag(a, a + 768); };
        auto ldk = [&](unsigned base, int j) AI { const unsigned a = base + wrd + j * 3072; F[j].k = wfrag(a + 256, a + 1024); };
        auto ldv = [&](unsigned base, int j) AI { const unsigned a = base + wrd + j * 3072; F[j].v = wfrag(a + 512, a + 1280); };
        LAS float* stw = (LAS float*)(uintptr_t)(L0 + L_MISC + 3072 + w * 256);
        BAR();
        ring_wait(0);
        STAMP(16);
#pragma unroll
        for (int j = 0; j < 2; ++j) { ldx(L0, j); ldk(L0, j); ldv(L0, j); }
        SB();
        u32x4 gv = {0u, 0u, 0u, 0u};
        const unsigned gvo = (128 * m + 32 * w + r32) * 16;
#pragma unroll 1
        for (int c = 0; c < 7; ++c) {
            const unsigned base = L0 + (c >= 5 ? c - 5 : c) * L_SLOT, nbase = L0 + (c >= 4 ? c - 4 : c + 1) * L_SLOT;
            ldsw_st(F_DONE + w, c);
            const u32x2 rdy = ldsw_ld2(F_RDY + 2 * (c < 6 ? c + 1 : 6));
            if (c == 3) gv = gld(rs, gvo, WS_ST1);
            if (c == 5) {
#pragma nounroll
                for (unsigned sp2 = 0; sp2 < SPIN_CAP && !wall(gv.y == e && gv.w == e); ++sp2) gv = gld(rs, gvo, WS_ST1);
                if (hh == 0) { stw[r32] = u2f(gv.x) * u2f(gv.z); stw[32 + r32] = u2f(gv.z); }
            }
            sfor<4>([&](auto jc) AI {
                constexpr int j = decltype(jc)::value, jn = (j + 2) & 3;
                if constexpr (j == 2) { if (__builtin_amdgcn_readfirstlane(rdy.x & rdy.y) == 0) ring_wait(c + 1); SB(); }
                const unsigned lb = j < 2 ? base : nbase;
                MFMA(aq, F[j].q, F[j].x, aq);
                ldx(lb, jn);
                SB();
                MFMA(ak, F[j].k, F[j].x, ak);
                ldk(lb, jn);
                SB();
                MFMA(av, F[j].x, F[j].v, av);
                ldv(lb, jn);
                SB();
            });
        }
        STAMP(1);
        const float mean = u2f(gv.x), rstd = u2f(gv.z);
        LBAR();
        const LAS float* csf = (const LAS float*)(uintptr_t)(L0 + L_MISC + 1536);
        const float cv = csf[64 + r32];
        const float qs = rstd * (0.18898223650461362f * 1.4426950408889634f);
#pragma unroll
        for (int q = 0; q < 4; ++q) {
            const f32x4 c1 = *(const LAS f32x4*)(csf + 8 * q + 4 * hh), c2 = *(const LAS f32x4*)(csf + 32 + 8 * q + 4 * hh);
            const f32x4 mr = *(const LAS f32x4*)(stw + 8 * q + 4 * hh), rr = *(const LAS f32x4*)(stw + 32 + 8 * q + 4 * hh);
#pragma unroll
            for (int i = 0; i < 4; ++i) {
                aq[4 * q + i] = (aq[4 * q + i] - mean * c1[i]) * qs;
                ak[4 * q + i] = (ak[4 * q + i] - mean * c2[i]) * rstd;
                av[4 * q + i] = av[4 * q + i] * rr[i] - mr[i] * cv;
            }
        }
        if (hh == 1) { aq[12] = 1.f; aq[13] = 0.f; aq[14] = 0.f; aq[15] = 0.f; ak[12] = 0.f; ak[13] = 0.f; ak[14] = 0.f; ak[15] = 0.f; }
        if (r32 >= 28) {
            const float pv = r32 == 28 ? 1.f : 0.f;
#pragma unroll
            for (int i = 0; i < 16; ++i) av[i] = pv;
        }
        const u32x4 k0 = pack8(ak, 0), k1 = pack8(ak, 1), v0 = pack8(av, 0), v1 = pack8(av, 1);
        const unsigned go = WS_KV + h * KVH + (4 * m + w) * 4096;
        gst(rs, k0, lane * 16, go); gst(rs, k1, lane * 16, go + 1024); gst(rs, v0, lane * 16, go + 2048); gst(rs, v1, lane * 16, go + 3072);
        const unsigned lo = L0 + L_OWNKV + w * 4096 + lane * 16;
        sts128(lo, k0); sts128(lo + 1024, k1); sts128(lo + 2048, v0); sts128(lo + 3072, v1);
        qf0 = pack8(aq, 0); qf1 = pack8(aq, 1);
        WAIT_BAR(0);
        if (tid == 0) set_flag(P.ws, flag_off(0, m * 16 + h), e);
        STAMP(2);
    }
    {
        LANE_GEOM();
        const int mt = m - 2 + (w >> 1);
        u32x4 kv[8];
        if (mt >= 0) {
            wait_flags(P.ws, flag_off(0, mt * 16 + h), 1, e, lane);
            const unsigned go = WS_KV + h * KVH + (4 * m - 8 + 2 * w) * 4096;
#pragma unroll
            for (int i = 0; i < 8; ++i) kv[i] = gld(rs, lane * 16, go + i * 1024);
        } else {
#pragma unroll
            for (int i = 0; i < 8; ++i) kv[i] = (u32x4){0u, 0u, (i & 3) == 1 && hh == 1 ? 0xF149u : 0u, 0u};
        }
        f32x16 cdiag, cold;
#pragma unroll
        for (int i = 0; i < 16; ++i) { const int kr = (i & 3) + 8 * (i >> 2) + 4 * hh; cdiag[i] = kr <= r32 ? 0.f : -1e30f; cold[i] = kr > r32 ? 0.f : -1e30f; }
#pragma unroll
        for (int i = 0; i < 8; ++i) sts128(L0 + L_KV + 2 * w * 4096 + i * 1024 + lane * 16, kv[i]);
        LBAR();
        STAMP(3);
        unsigned kvb = L0 + L_KV + w * 4096 + lane * 16;
        PIN(kvb);
        u32x4 kf[9][2], vf[9][2];
        const f32x16 zero16 = {};
        auto ldK = [&](int i) AI { kf[i][0] = lds128(kvb + i * 4096); kf[i][1] = lds128(kvb + i * 4096 + 1024); };
        auto ldV = [&](int i) AI { vf[i][0] = lds128(kvb + i * 4096 + 2048); vf[i][1] = lds128(kvb + i * 4096 + 3072); };
        auto smax = [&](const f32x16& s, float a) AI {
#pragma unroll
            for (int i = 0; i < 16; i += 2) a = max3(a, s[i], s[i + 1]);
            return a; };
        ldK(0); ldK(1);
        float mx = -3e38f;
        f32x16 s[2];
        auto smax8 = [&](const f32x16& v, int o, float a) AI {
#pragma unroll
            for (int i = 0; i < 8; i += 2) a = max3(a, v[o + i], v[o + i + 1]);
            return a; };
        sfor<9>([&](auto ic) AI {
            constexpr int i = decltype(ic)::value;
            if constexpr (i + 2 < 9) ldK(i + 2);
            MFMA(s[i & 1], kf[i][0], qf0, i == 0 ? cold : i == 8 ? cdiag : zero16);
            SB();
            MFMA(s[i & 1], kf[i][1], qf1, s[i & 1]);
            SB();
            if constexpr (i > 0) { mx = smax8(s[(i - 1) & 1], 0, mx); mx = smax8(s[(i - 1) & 1], 8, mx); PIN(mx); }
            SB();
        });
        ldK(0); ldK(1); ldV(0);
        mx = hmax(smax(s[0], mx));
        STAMP(25);
        f32x16 o = {}, cm;
#pragma unroll
        for (int i = 0; i < 16; ++i) { cm[i] = -mx; cdiag[i] -= mx; cold[i] -= mx; }
        MFMA(s[0], kf[0][0], qf0, cold); MFMA(s[0], kf[0][1], qf1, s[0]);
        SB();
        sfor<9>([&](auto ic) AI {
            constexpr int i = decltype(ic)::value;
            f32x16& c = s[i & 1];
            f32x16& n = s[(i + 1) & 1];
            if constexpr (i + 2 < 9) ldK(i + 2);
            if constexpr (i + 1 < 9) ldV(i + 1);
            if constexpr (i + 1 < 9) MFMA(n, kf[i + 1][0], qf0, i + 1 == 8 ? cdiag : cm);
#pragma unroll
            for (int j = 0; j < 8; ++j) c[j] = __builtin_amdgcn_exp2f(c[j]);
            u32x4 p0 = pack8(c, 0);
            PIN(p0);
            SB();
            if constexpr (i + 1 < 9) MFMA(n, kf[i + 1][1], qf1, n);
            MFMA(o, vf[i][0], p0, o);
#pragma unroll
            for (int j = 8; j < 16; ++j) c[j] = __builtin_amdgcn_exp2f(c[j]);
            const u32x4 p1 = pack8(c, 1);
            MFMA(o, vf[i][1], p1, o);
            SB();
        });
        STAMP(26);
        auto lr = __builtin_amdgcn_permlane32_swap(f2u(o[12]), f2u(o[12]), false, false);
        const float inv = __builtin_amdgcn_rcpf(u2f(lr[1]));
#pragma unroll
        for (int i = 0; i < 16; ++i) o[i] *= inv;
        if (hh == 1) { o[12] = 0.f; o[13] = 0.f; o[14] = 0.f; o[15] = 0.f; }
        const unsigned vo = (hh * XR + r32) * 16, so = WS_O + ((4 * h) * XR + 128 * m + 32 * w) * 16;
        gst(rs, pack8(o, 0), vo, so); gst(rs, pack8(o, 1), vo, so + 2 * XROW);
        WAIT_BAR(0);
        if (tid == 0) set_flag(P.ws, flag_off(1, m * 16 + h), e);
        STAMP(4);
    }
    const bool act = w < nt;
    const int tw = tb + w;
#define GEMM_GEOM() LANE_GEOM(); \
    unsigned trW[2]; trW[0] = L0 + (w < nt ? w : nt - 1) * 256 + trq * 64 + gq * 8 + 2 * hh * 1024; trW[1] = trW[0] + 57344; PIN(trW[0]); PIN(trW[1]); \
    auto wimg = [&](int kq0, int kq1) AI { return wfrag(kq0 < 56 ? trW[0] + kq0 * 1024 : trW[1] + (kq0 - 56) * 1024, kq1 < 56 ? trW[0] + kq1 * 1024 : trW[1] + (kq1 - 56) * 1024); }; \
    unsigned xl = L0 + L_X + lane * 16; PIN(xl); \
    LAS float* m2 = (LAS float*)(uintptr_t)(L0 + L_M2 + w * 256); (void)m2
    auto gemm = [&](auto nk, const u32x4* wr, unsigned xl) AI -> f32x16 {
        constexpr int NK = decltype(nk)::value, PD = 4;
        f32x16 acc = {};
        u32x4 xf[NK];
#pragma unroll
        for (int k = 0; k < PD; ++k) xf[k] = lds128(xl + k * 1024);
        SB();
        sfor<NK>([&](auto kc) AI {
            constexpr int k = decltype(kc)::value;
            if constexpr (k + PD < NK) xf[k + PD] = lds128(xl + (k + PD) * 1024);
            MFMA(acc, wr[k], xf[k], acc);
            SB();
        });
        return acc;
    };
    u32x4 hp[2];
    float meanH, rstdH;
    {
        GEMM_GEOM();
        u32x4 xr[2];
        const char* xp = (const char*)P.x + (size_t)(32 * ti + r32) * 896 + 64 * (act ? tw : tb) + 16 * hh;
        xr[0] = *(const GAS u32x4*)xp; xr[1] = *(const GAS u32x4*)(xp + 32);
        u32x4 wr[32];
        sfor<32>([&](auto kc) AI { constexpr int k = decltype(kc)::value, kq = 7 * (k >> 1) + 4 * (k & 1);
            if constexpr (k < 31) wr[k] = wimg(kq, kq + 1);
            else { const u32x2 p = ldstr(trW[1] + (kq - 56) * 1024), q = ldstr(trW[1] + (kq - 56) * 1024 + (hh ? 0 : 1024)); wr[k] = (u32x4){p.x, p.y, q.x, q.y}; } });
        LBAR();
        wait_flags(P.ws, flag_off(1, (ti >> 2) * 16 + 4 * w), 4, e, lane);
        u32x4 xf[8];
#pragma unroll
        for (int k = 0; k < 8; ++k) xf[k] = gld(rs, vx, WS_O + ((16 * w + 2 * k) * XR + 32 * ti) * 16);
#pragma unroll
        for (int k = 0; k < 8; ++k) sts128(xl + (8 * w + k) * 1024, xf[k]);
        LBAR();
        STAMP(5);
        f32x16 r = gemm(IC(32), wr, xl);
        float s1 = 0.f, s2 = 0.f;
#pragma unroll
        for (int s = 0; s < 2; ++s) {
#pragma unroll
            for (int i = 0; i < 4; ++i) {
                hp[s][i] = pk(r[8 * s + 2 * i] + blo(xr[s][i]), r[8 * s + 2 * i + 1] + bhi(xr[s][i]));
                s1 = dot2(hp[s][i], ONES, s1); s2 = dot2(hp[s][i], hp[s][i], s2);
            }
            if (act) gst(rs, hp[s], vx, WS_H + ((4 * tw + 2 * s) * XR + 32 * ti) * 16);
        }
        s1 = hsum(s1); s2 = hsum(s2);
        if (act && hh == 0) gst8(rs, (u32x2){f2u(s1), f2u(s2)}, r32 * 8, WS_STH + (tw * 2048 + 32 * ti) * 8);
        WAIT_BAR(0);
        if (tid == 0) set_flag(P.ws, flag_off(2, ti * 4 + g), e);
        STAMP(6);
    }
#define LOAD_WR() u32x4 wr[28]; sfor<28>([&](auto kc) AI { constexpr int k = decltype(kc)::value; wr[k] = wimg(4 * k, 4 * k + 1); })
    const int fsn = w == 0 ? 1 : 2, fsl = w <= 1 ? 0 : w - 1;
    {
        GEMM_GEOM();
        LOAD_WR();
        LBAR();
        {
            float cs[4] = {0.f, 0.f, 0.f, 0.f};
#pragma unroll
            for (int k = 0; k < 28; ++k)
#pragma unroll
                for (int i = 0; i < 4; ++i) cs[i] = dot2(wr[k][i], ONES, cs[i]);
            const float c = hsum((cs[0] + cs[1]) + (cs[2] + cs[3]));
            if (hh == 0) m2[r32] = c;
        }
        wait_flags(P.ws, flag_off(2, ti * 4), 4, e, lane);
        u32x4 xf[7];
#pragma unroll
        for (int k = 0; k < 7; ++k) xf[k] = gld(rs, vx, WS_H + ((14 * w + 2 * k) * XR + 32 * ti) * 16);
        u32x2 sv[7];
#pragma unroll
        for (int q = 0; q < 7; ++q) sv[q] = gld8(rs, (7 * hh * 2048 + r32) * 8, WS_STH + (q * 2048 + 32 * ti) * 8);
#pragma unroll
        for (int k = 0; k < 7; ++k) sts128(xl + (7 * w + k) * 1024, xf[k]);
        LBAR();
        STAMP(7);
        f32x16 r = gemm(IC(28), wr, xl);
        float t1 = 0.f, t2 = 0.f;
#pragma unroll
        for (int q = 0; q < 7; ++q) { t1 += u2f(sv[q].x); t2 += u2f(sv[q].y); }
        t1 = hsum(t1); t2 = hsum(t2);
        meanH = t1 * (1.f / D);
        const float var = __builtin_fmaxf(t2 * (1.f / D) - meanH * meanH, 0.f);
        rstdH = __builtin_amdgcn_rsqf(var + EPS);
        const float sc = rstdH * __builtin_amdgcn_rsqf(var * rstdH * rstdH + EPS);
#pragma unroll
        for (int q = 0; q < 4; ++q) {
            const f32x4 c = *(const LAS f32x4*)(m2 + 8 * q + 4 * hh);
#pragma unroll
            for (int i = 0; i < 4; ++i) r[4 * q + i] = gelu_t((r[4 * q + i] - meanH * c[i]) * sc);
        }
#pragma unroll
        for (int s = 0; s < 2; ++s) if (act) gst(rs, pack8(r, s), vx, WS_HH + ((4 * tw + 2 * s) * XR + 32 * ti) * 16);
        WAIT_BAR(0);
        if (tid == 0) set_flag(P.ws, flag_off(3, ti * 4 + g), e);
        STAMP(8);
    }
    {
        GEMM_GEOM();
        LOAD_WR();
        LBAR();
        const int sh = w == 0 ? -1 : w == 1 ? 1 : w == 2 ? -2 : 2;
        const int tn = ti + (sh < 0 ? -1 : 1);
        {
            const bool nb = tn >= 0 && tn < 64;
            const int li = lane & 3;
            const GAS unsigned* p = (const GAS unsigned*)(P.ws + flag_off(3, (((li & 2) && nb) ? tn : ti) * 4 + fsl + ((li & 1) && fsn == 2 ? 1 : 0)));
#pragma nounroll
            for (unsigned sp = 0; sp < SPIN_CAP; ++sp) { unsigned v = __hip_atomic_load(p, __ATOMIC_RELAXED, __HIP_MEMORY_SCOPE_AGENT); if (wall(v == e)) break; }
            __builtin_amdgcn_fence(__ATOMIC_ACQUIRE, "wavefront");
        }
        const int tok = 32 * ti + r32 + sh;
        const bool ok = tok >= 0 && tok < S;
        const unsigned vxs = ok ? (hh * XR + tok) * 16 : 0x7ffffff0u;
        u32x4 xf[7];
#pragma unroll
        for (int k = 0; k < 7; ++k) { xf[k] = gld(rs, vxs, WS_HH + (14 * w + 2 * k) * XROW); }
#pragma unroll
        for (int k = 0; k < 7; ++k) sts128(xl + (7 * w + k) * 1024, xf[k]);
        LBAR();
        STAMP(9);
        f32x16 r = gemm(IC(28), wr, xl);
        float s1 = 0.f, s2 = 0.f;
#pragma unroll
        for (int s = 0; s < 2; ++s) {
            u32x4 xp;
#pragma unroll
            for (int i = 0; i < 4; ++i) {
                xp[i] = pk(r[8 * s + 2 * i] + (blo(hp[s][i]) - meanH) * rstdH, r[8 * s + 2 * i + 1] + (bhi(hp[s][i]) - meanH) * rstdH);
                s1 = dot2(xp[i], ONES, s1); s2 = dot2(xp[i], xp[i], s2);
            }
            if (act) gst(rs, xp, vx, WS_X1 + ((4 * tw + 2 * s) * XR + 32 * ti) * 16);
        }
        s1 = hsum(s1); s2 = hsum(s2);
        if (act && hh == 0) gst8(rs, (u32x2){f2u(s1), f2u(s2)}, r32 * 8, WS_STX + (tw * 2048 + 32 * ti) * 8);
        WAIT_BAR(0);
        if (tid == 0) set_flag(P.ws, flag_off(4, ti * 4 + g), e);
        STAMP(10);
    }
    {
        LANE_GEOM();
        unsigned tr6[2];
        tr6[0] = L0 + trq * 64 + gq * 8 + 2 * hh * 1024; tr6[1] = tr6[0] + 57344; PIN(tr6[0]); PIN(tr6[1]);
        auto wf6 = [&](int t, int k) AI { const unsigned a = (k < 14 ? tr6[0] + k * 4096 : tr6[1] + (k - 14) * 4096) + t * 256; return wfrag(a, a + 1024); };
        const LAS float* cs6 = (const LAS float*)(uintptr_t)(L0 + L_M2 + 1024);
        BAR();
        wait_flags(P.ws, flag_off(4, (4 * m + w) * 4), 4, e, lane);
        const int tk = 128 * m + 32 * w;
        auto ldx1 = [&](int k) AI { return gld(rs, vx, WS_X1 + (2 * k * XR + tk) * 16); };
        unsigned vx2 = vx; PIN(vx2);
        auto ldx2 = [&](int k) AI { return gld(rs, vx2, WS_X1 + (2 * k * XR + tk) * 16); };
        constexpr int PX = 10, PW = 3;
        f32x16 a0 = {}, a1 = {};
        u32x2 sv[7];
        u32x4 xg[28];
        constexpr int PG = 6;
        {
            u32x4 xf[28], wA[28], wB[28];
#pragma unroll
            for (int k = 0; k < PX; ++k) xf[k] = ldx1(k);
#pragma unroll
            for (int q = 0; q < 7; ++q) sv[q] = gld8(rs, (7 * hh * 2048 + r32) * 8, WS_STX + (q * 2048 + tk) * 8);
            STAMP(11);
#pragma unroll
            for (int k = 0; k < PW; ++k) { wA[k] = wf6(0, k); wB[k] = wf6(1, k); }
            SB();
            sfor<28>([&](auto kc) AI {
                constexpr int k = decltype(kc)::value;
                if constexpr (k + PW < 28) { wA[k + PW] = wf6(0, k + PW); wB[k + PW] = wf6(1, k + PW); }
                MFMA(a0, wA[k], xf[k], a0);
                if constexpr (k + PX < 28) xf[k + PX] = ldx1(k + PX);
                else if constexpr (k + PX - 28 < PG) xg[k + PX - 28] = ldx2(k + PX - 28);
                MFMA(a1, wB[k], xf[k], a1);
                SB();
            });
        }
        float t1 = 0.f, t2 = 0.f;
#pragma unroll
        for (int q = 0; q < 7; ++q) { t1 += u2f(sv[q].x); t2 += u2f(sv[q].y); }
        t1 = hsum(t1); t2 = hsum(t2);
        const float mean = t1 * (1.f / D), var = __builtin_fmaxf(t2 * (1.f / D) - mean * mean, 0.f) + EPS, rstd = __builtin_amdgcn_rsqf(var);
        const GeluK gk = {-mean, GC1 * rstd * rstd * rstd, GC0 * rstd, var * rstd};
        const unsigned uo = WS_U + ((4 * tb6) * XR + tk) * 16;
        auto epi2 = [&](auto nsc, auto&& pre, f32x16& ya, int ta, f32x16& yb, int tb_, bool storeb) AI {
            constexpr int NS = decltype(nsc)::value, OPS = NS >= 28 ? 1 : 2, SK = NS >= 28 ? 5 : 2;
            f32x4 c[8];
            float z[32];
            sfor<NS>([&](auto kc) AI {
                constexpr int k = decltype(kc)::value;
                pre(kc);
                sfor<8>([&](auto qc) AI { constexpr int q = decltype(qc)::value, st = (4 * q * SK) / 8;
                    if constexpr (k == (st >= 2 ? st - 2 : 0)) c[q] = *(const LAS f32x4*)(cs6 + (q < 4 ? ta : tb_) * 32 + 8 * (q & 3) + 4 * hh); });
                sfor<32>([&](auto ec) AI {
                    constexpr int el = decltype(ec)::value, st = (el * SK) / 8;
                    sfor<8>([&](auto oc) AI { constexpr int o = decltype(oc)::value;
                        if constexpr (o / OPS == k - st) { f32x16& y = el < 16 ? ya : yb; float yy = y[el & 15]; gelu_step(o, yy, z[el], c[el >> 2][el & 3], gk); if (o == 0 || o == 7) PIN(yy); else PIN(z[el]); y[el & 15] = yy; } });
                    if constexpr (el == 15 && k == st + 8 / OPS) { gst(rs, pack8(ya, 0), vx, uo + 4 * ta * XROW); gst(rs, pack8(ya, 1), vx, uo + (4 * ta + 2) * XROW); }
                });
                SB();
            });
            if (storeb) { gst(rs, pack8(yb, 0), vx, uo + 4 * tb_ * XROW); gst(rs, pack8(yb, 1), vx, uo + (4 * tb_ + 2) * XROW); }
        };
        STAMP(23);
        f32x16 a2 = {}, a3 = {};
        {
            u32x4 wA[28], wB[28];
#pragma unroll
            for (int k = 0; k < PW; ++k) { wA[k] = wf6(2, k); wB[k] = wf6(3, k); }
            SB();
            epi2(IC(28), [&](auto kc) AI {
                constexpr int k = decltype(kc)::value;
                if constexpr (k + PW < 28) { wA[k + PW] = wf6(2, k + PW); wB[k + PW] = wf6(3, k + PW); }
                MFMA(a2, wA[k], xg[k], a2);
                if constexpr (k + PG < 28) xg[k + PG] = ldx2(k + PG);
                MFMA(a3, wB[k], xg[k], a3);
            }, a0, 0, a1, 1, true);
        }
        STAMP(24);
        LBAR();
        epi2(IC(14), [&](auto) AI {}, a2, 2, a3, 3, nt6 == 4);
        WAIT_BAR(0);
        if (tid == 0) set_flag(P.ws, flag_off(5, m * 16 + s16), e);
        STAMP(12);
    }
    if (act7) {
        LANE_GEOM();
        const unsigned lanep = trq * 64 + gq * 8 + 2 * hh * 512;
        unsigned tA = L0 + L_X + w * 8192 + lanep, tB = wst + lanep; PIN(tA); PIN(tB);
        auto w4f = [&](int k, int t) AI { const unsigned a = (k < 4 ? tA + k * 2048 : k < 18 ? tB + (k - 4) * 2048 : tB + (k - 18) * 2048) + t * 256; return wfrag(a, a + 512); };
        u32x4 wr[28][2];
#pragma unroll
        for (int k = 0; k < 14; ++k) { wr[k][0] = w4f(k, 0); wr[k][1] = w4f(k, 1); }
        LBAR();
        const int tk = 64 * j7;
        const int tt = w >> 1, nt7 = w & 1;
        wait_flags(P.ws, flag_off(5, (j7 >> 1) * 16 + 4 * w), 4, e, lane);
        constexpr int PX = 5;
        u32x4 xa[28][2];
#pragma unroll
        for (int k = 0; k < PX; ++k) { xa[k][0] = gld(rs, vx, WS_U + ((56 * w + 2 * k) * XR + tk) * 16); xa[k][1] = gld(rs, vx, WS_U + ((56 * w + 2 * k) * XR + tk + 32) * 16); }
        STAMP(13);
        SB();
        f32x16 acc[4] = {};
        u32x4 rh[2], rx[2];
        sfor<28>([&](auto kc) AI {
            constexpr int k = decltype(kc)::value;
            if constexpr (k == 13) { BAR(); SB(); }
            if constexpr (k + 3 >= 14 && k + 3 < 28) { wr[k + 3][0] = w4f(k + 3, 0); wr[k + 3][1] = w4f(k + 3, 1); }
            MFMA(acc[0], wr[k][0], xa[k][0], acc[0]); MFMA(acc[1], wr[k][1], xa[k][0], acc[1]);
            if constexpr (k + PX < 28) xa[k + PX][0] = gld(rs, vx, WS_U + ((56 * w + 2 * (k + PX)) * XR + tk) * 16);
            MFMA(acc[2], wr[k][0], xa[k][1], acc[2]); MFMA(acc[3], wr[k][1], xa[k][1], acc[3]);
            if constexpr (k + PX < 28) xa[k + PX][1] = gld(rs, vx, WS_U + ((56 * w + 2 * (k + PX)) * XR + tk + 32) * 16);
            if constexpr (k == 24 || k == 25) {
                constexpr int s = k - 24;
                const unsigned so = ((8 * g7 + 4 * nt7 + 2 * s) * XR + tk + 32 * tt) * 16;
                rh[s] = gld(rs, vx, WS_H + so); rx[s] = gld(rs, vx, WS_X1 + so);
            }
            SB();
        });
        STAMP(27);
#pragma unroll
        for (int t = 0; t < 4; ++t) {
            if (t != w) {
                const unsigned a = L0 + L_X + t * 12288 + (w < t ? w : w - 1) * 4096 + lane * 16;
#pragma unroll
                for (int q = 0; q < 4; ++q) stsf4(a + q * 1024, (f32x4){acc[t][4 * q], acc[t][4 * q + 1], acc[t][4 * q + 2], acc[t][4 * q + 3]});
            }
        }
        LBAR();
        f32x16 r = w == 0 ? acc[0] : w == 1 ? acc[1] : w == 2 ? acc[2] : acc[3];
        const unsigned a = L0 + L_X + w * 12288 + lane * 16;
#pragma unroll
        for (int j = 0; j < 3; ++j)
#pragma unroll
            for (int q = 0; q < 4; ++q) { const f32x4 v = ldsf4(a + j * 4096 + q * 1024); r[4 * q] += v[0]; r[4 * q + 1] += v[1]; r[4 * q + 2] += v[2]; r[4 * q + 3] += v[3]; }
        char* op = (char*)P.out + (size_t)(tk + 32 * tt + r32) * 896 + 128 * g7 + 64 * nt7 + 16 * hh;
#pragma unroll
        for (int s = 0; s < 2; ++s) {
            u32x4 ov;
#pragma unroll
            for (int i = 0; i < 4; ++i)
                ov[i] = pk(r[8 * s + 2 * i] + blo(rx[s][i]) + blo(rh[s][i]), r[8 * s + 2 * i + 1] + bhi(rx[s][i]) + bhi(rh[s][i]));
            *(GAS u32x4*)(op + 32 * s) = ov;
        }
        STAMP(14);
    }
    STAMP_FLUSH();
    if (b == 0 && w == 0) {
        const int lane = tid & 63;
        for (int r = 0; r < 4; ++r) {
            const GAS unsigned* p = (const GAS unsigned*)(P.ws + flag_off(0, r * 64 + lane));
#pragma nounroll
            for (unsigned sp = 0; sp < SPIN_CAP; ++sp) { unsigned v = __hip_atomic_load(p, __ATOMIC_RELAXED, __HIP_MEMORY_SCOPE_AGENT); if (wall(v == e)) break; }
        }
        if (lane == 0) __hip_atomic_store((GAS unsigned*)(P.ws + WS_EP), e, __ATOMIC_RELAXED, __HIP_MEMORY_SCOPE_AGENT);
    }
}
}
#if defined(__HIP_DEVICE_COMPILE__)
#pragma clang attribute pop
#endif

extern "C" void kernel_launch(void* const* d_in, const int* in_sizes, int n_in, void* d_out, int out_size, void* d_ws, size_t ws_size,
                              hipStream_t stream) {
    mk::Params P;
    P.x = (const u16*)d_in[0]; P.wqkv = (const u16*)d_in[1]; P.wo = (const u16*)d_in[2];
    P.w1 = (const u16*)d_in[4]; P.w2 = (const u16*)d_in[5]; P.w3 = (const u16*)d_in[6]; P.w4 = (const u16*)d_in[7];
    P.out = (u16*)d_out; P.ws = (char*)d_ws;
    mk::fused_fwd<<<256, 512, 0, stream>>>(P);
}
```

```cpp
#define LATE_TOUCH 3
#include <hip/hip_runtime.h>
#include <hip/hip_bf16.h>
#include <stdint.h>
#include <type_traits>
#include <utility>

typedef unsigned short u16;
typedef __attribute__((ext_vector_type(8))) short bf16x8;
typedef __attribute__((ext_vector_type(4))) short s16x4;
typedef __attribute__((ext_vector_type(16))) float f32x16;
typedef __attribute__((ext_vector_type(4))) float f32x4;
typedef __attribute__((ext_vector_type(2))) float f32x2;
typedef __attribute__((ext_vector_type(4))) unsigned u32x4;
typedef __attribute__((ext_vector_type(2))) unsigned u32x2;
typedef __attribute__((ext_vector_type(2))) __bf16 bf2;
#define LAS __attribute__((address_space(3)))
#define GAS __attribute__((address_space(1)))
#define DEVI __device__ __forceinline__

#define MK_STAMP 0

namespace mk {
constexpr int S = 2048, D = 448;
constexpr unsigned XR = 2056, XROW = XR * 16;
constexpr unsigned KVH = 64 * 4096 + 128;
constexpr unsigned WS_EP = 0;
constexpr unsigned WS_FLAG = 4096;
constexpr unsigned WS_KV = 262144;
constexpr unsigned WS_O = WS_KV + 16 * KVH;
constexpr unsigned WS_H = WS_O + 64 * XROW;
constexpr unsigned WS_HH = WS_H + 56 * XROW;
constexpr unsigned WS_X1 = WS_HH + 56 * XROW;
constexpr unsigned WS_U = WS_X1 + 56 * XROW;
constexpr unsigned WS_STH = WS_U + 224 * XROW;
constexpr unsigned WS_STX = WS_STH + 262144;
constexpr unsigned WS_ST1 = WS_STX + 262144;
constexpr unsigned WS_SPARE = WS_ST1 + 32768;
constexpr unsigned WS_STAMP = WS_SPARE + 128;
constexpr unsigned WS_END = WS_STAMP + 256 * 48 * 8;
constexpr unsigned L_SLOT = 28672;
constexpr unsigned L_MISC = 143360;
constexpr unsigned L_KV = 114688;
constexpr unsigned L_OWNKV = L_KV + 8 * 4096;
constexpr unsigned L_WST = 28672;
constexpr unsigned L_X = 114688;
constexpr unsigned L_M2 = 147456;
constexpr unsigned CODE_TOUCH_BYTES = 36000;
constexpr float EPS = 1e-5f;
constexpr unsigned SPIN_CAP = 1u << 18;

struct Params {
    const u16* x; const u16* wqkv; const u16* wo; const u16* w1; const u16* w2; const u16* w3; const u16* w4;
    u16* out; char* ws;
};

DEVI unsigned f2u(float f) { return __builtin_bit_cast(unsigned, f); }
DEVI float u2f(unsigned u) { return __builtin_bit_cast(float, u); }
DEVI bool wall(bool p) { return __builtin_amdgcn_ballot_w64(!p) == 0; }
template <int X> DEVI float xor32(float v) { return u2f((unsigned)__builtin_amdgcn_ds_swizzle((int)f2u(v), (X << 10) | 0x1f)); }
DEVI unsigned pk(float lo, float hi) { bf2 v = {(__bf16)lo, (__bf16)hi}; return __builtin_bit_cast(unsigned, v); }
DEVI float blo(unsigned u) { return u2f(u << 16); }
DEVI float bhi(unsigned u) { return u2f(u & 0xffff0000u); }
DEVI float dot2(unsigned a, unsigned b, float c) { return __builtin_amdgcn_fdot2_f32_bf16(__builtin_bit_cast(bf2, a), __builtin_bit_cast(bf2, b), c, false); }
constexpr unsigned ONES = 0x3f803f80u;
DEVI float hsum(float v) { auto r = __builtin_amdgcn_permlane32_swap(f2u(v), f2u(v), false, false); return u2f(r[0]) + u2f(r[1]); }
DEVI float hmax(float v) { auto r = __builtin_amdgcn_permlane32_swap(f2u(v), f2u(v), false, false); return __builtin_fmaxf(u2f(r[0]), u2f(r[1])); }
DEVI f32x16 mfma(u32x4 a, u32x4 b, f32x16 c) { return __builtin_amdgcn_mfma_f32_32x32x16_bf16(__builtin_bit_cast(bf16x8, a), __builtin_bit_cast(bf16x8, b), c, 0, 0, 0); }
#define MFMA(acc, a, b, c) do { acc = mfma(a, b, c); asm volatile("" : "+v"(acc)); } while (0)
DEVI u32x4 lds128(unsigned a) { return *(const LAS u32x4*)(uintptr_t)a; }
DEVI f32x4 ldsf4(unsigned a) { return *(const LAS f32x4*)(uintptr_t)a; }
DEVI void sts128(unsigned a, u32x4 v) { *(LAS u32x4*)(uintptr_t)a = v; }
DEVI void stsf4(unsigned a, f32x4 v) { *(LAS f32x4*)(uintptr_t)a = v; }
DEVI u32x2 ldstr(unsigned a) { return __builtin_bit_cast(u32x2, __builtin_amdgcn_ds_read_tr16_b64_v4i16((LAS s16x4*)(uintptr_t)a)); }
DEVI u32x4 wfrag(unsigned a0, unsigned a1) { u32x2 p = ldstr(a0), q = ldstr(a1); return (u32x4){p.x, p.y, q.x, q.y}; }
DEVI u32x4 pack8(const f32x16& a, int s) { return (u32x4){pk(a[8 * s], a[8 * s + 1]), pk(a[8 * s + 2], a[8 * s + 3]), pk(a[8 * s + 4], a[8 * s + 5]), pk(a[8 * s + 6], a[8 * s + 7])}; }
constexpr int SC1 = 16;
DEVI u32x4 gld(__amdgpu_buffer_rsrc_t rs, unsigned voff, unsigned soff) { return __builtin_amdgcn_raw_buffer_load_b128(rs, voff, soff, SC1); }
DEVI u32x2 gld8(__amdgpu_buffer_rsrc_t rs, unsigned voff, unsigned soff) { return __builtin_amdgcn_raw_buffer_load_b64(rs, voff, soff, SC1); }
DEVI void gst(__amdgpu_buffer_rsrc_t rs, u32x4 v, unsigned voff, unsigned soff) { __builtin_amdgcn_raw_buffer_store_b128(v, rs, voff + soff, 0, SC1); }
DEVI void gst8(__amdgpu_buffer_rsrc_t rs, u32x2 v, unsigned voff, unsigned soff) { __builtin_amdgcn_raw_buffer_store_b64(v, rs, voff, soff, SC1); }
#define WAIT_VM(N) asm volatile("s_waitcnt vmcnt(" #N ")" ::: "memory")
#define WAIT_LGKM() asm volatile("s_waitcnt lgkmcnt(0)" ::: "memory")
#define WAIT_BAR(N) asm volatile("s_waitcnt vmcnt(" #N ") lgkmcnt(0)\n\ts_barrier" ::: "memory")
#define LBAR() asm volatile("s_waitcnt lgkmcnt(0)\n\ts_barrier" ::: "memory")
#define SB() __builtin_amdgcn_sched_barrier(0)
#define PIN(x) asm volatile("" : "+v"(x))
DEVI float fma1(float a, float b, float c) { return __builtin_fmaf(a, b, c); }
DEVI float mul1(float a, float b) { return a * b; }
DEVI float max3(float a, float b, float c) { return __builtin_fmaxf(__builtin_fmaxf(a, b), c); }
template <int... Is, class F> DEVI void sfor_impl(std::integer_sequence<int, Is...>, F&& f) { (f(std::integral_constant<int, Is>{}), ...); }
template <int N, class F> DEVI void sfor(F&& f) { sfor_impl(std::make_integer_sequence<int, N>{}, f); }
#define AI __attribute__((always_inline))
#define IC(n) std::integral_constant<int, n>{}

DEVI void dma1(const char* sbase, unsigned voff, unsigned lds) {
    asm volatile("s_mov_b32 m0, %2\n\ts_nop 0\n\tglobal_load_lds_dwordx4 %0, %1" :: "v"(voff), "s"(sbase), "s"(lds) : "memory");
}
DEVI void touch(const char* sbase, unsigned voff, unsigned lds) {
    asm volatile("s_mov_b32 m0, %2\n\ts_nop 0\n\tglobal_load_lds_dword %0, %1" :: "v"(voff), "s"(sbase), "s"(lds) : "memory");
}
struct WSrc { const char* base; unsigned step, voff; };
DEVI void wpiece(const WSrc& s, unsigned lds, int n) { dma1(s.base + (size_t)n * s.step, s.voff, lds + n * 1024); }
DEVI WSrc wsrc_a(const char* base, unsigned rs, int nt, int lane) { const int t = (lane >> 4) < nt ? (lane >> 4) : nt - 1; return WSrc{base, 4 * rs, ((lane >> 2) & 3) * rs + t * 64 + (lane & 3) * 16}; }
DEVI WSrc wsrc_b(const char* base, unsigned rs, int lane) { return WSrc{base, 8 * rs, (4 * (lane >> 5) + ((lane >> 2) & 3)) * rs + ((lane >> 4) & 1) * 64 + (lane & 3) * 16}; }

DEVI void wait_flags(const char* ws, unsigned foff, int n, unsigned e, int lane) {
    const GAS unsigned* p = (const GAS unsigned*)(ws + foff) + (lane < n ? lane : 0) * 32;
#pragma nounroll
    for (unsigned sp = 0; sp < SPIN_CAP; ++sp) {
        unsigned v = __hip_atomic_load(p, __ATOMIC_RELAXED, __HIP_MEMORY_SCOPE_AGENT);
        if (wall(v == e)) break;
    }
    __builtin_amdgcn_fence(__ATOMIC_ACQUIRE, "wavefront");
}
DEVI void set_flag(char* ws, unsigned foff, unsigned e) { __hip_atomic_store((GAS unsigned*)(ws + foff), e, __ATOMIC_RELAXED, __HIP_MEMORY_SCOPE_AGENT); }
DEVI unsigned flag_off(int ph, int idx) { return WS_FLAG + (ph * 256 + idx) * 128; }

constexpr float GC0 = -2.f * 0.7978845608028654f * 1.4426950408889634f, GC1 = GC0 * 0.044715f;
DEVI float gelu_t(float x) {
    float z = x * __builtin_fmaf(x * x, GC1, GC0);
    return x * __builtin_amdgcn_rcpf(1.f + __builtin_amdgcn_exp2f(z));
}
struct GeluK { float nmean, k1, k0, irs; };
DEVI void gelu_step(int o, float& y, float& z, float c, const GeluK& g) {
    if (o == 0) y = fma1(g.nmean, c, y);
    else if (o == 1) z = mul1(y, y);
    else if (o == 2) z = fma1(z, g.k1, g.k0);
    else if (o == 3) z = mul1(y, z);
    else if (o == 4) z = __builtin_amdgcn_exp2f(z);
    else if (o == 5) z = fma1(z, g.irs, g.irs);
    else if (o == 6) z = __builtin_amdgcn_rcpf(z);
    else y = mul1(y, z);
}

#define STAMP(k) do {} while (0)
#define STAMP_DECL
#define STAMP_FLUSH() do {} while (0)

#define LANE_GEOM() int lane_ = threadIdx.x & 63; asm volatile("" : "+v"(lane_)); const int lane = lane_, r32 = lane & 31, hh = lane >> 5; \
    const int trq = (lane & 15) >> 2, trQ_ = ((lane >> 4) & 1) * 4 + (lane & 3), gq = (trQ_ & 4) | ((trQ_ & 1) << 1) | ((trQ_ >> 1) & 1); \
    const unsigned vx = (hh * XR + r32) * 16; (void)trq; (void)gq; (void)vx; (void)r32; (void)hh
constexpr unsigned L_FLG = L_MISC + 1920, F_RDY = 0, F_DONE = 16;
DEVI unsigned ldsw_ld(unsigned i) { return *(volatile LAS unsigned*)(uintptr_t)(L_FLG + 4 * i); }
DEVI u32x2 ldsw_ld2(unsigned i) { return *(volatile LAS u32x2*)(uintptr_t)(L_FLG + 4 * i); }
DEVI void ldsw_st(unsigned i, unsigned v) { *(volatile LAS unsigned*)(uintptr_t)(L_FLG + 4 * i) = v; }
DEVI void ring_wait(int c) {
#pragma nounroll
    for (unsigned sp = 0; sp < SPIN_CAP * 16u; ++sp) { const u32x2 v = ldsw_ld2(F_RDY + 2 * c); if (__builtin_amdgcn_readfirstlane(v.x & v.y)) break; __builtin_amdgcn_s_sleep(1); }
}
DEVI void slot_wait(unsigned n, int lane) {
#pragma nounroll
    for (unsigned sp = 0; sp < SPIN_CAP * 16u; ++sp) { const unsigned v = ldsw_ld(F_DONE + (lane < 6 ? lane : 0)); if (wall(v >= n)) break; __builtin_amdgcn_s_sleep(1); }
}
#define LB(N) asm volatile("s_waitcnt vmcnt(" #N ")\n\ts_barrier" ::: "memory")
#define BAR() asm volatile("s_barrier" ::: "memory")

__global__ __launch_bounds__(512, 2) void fused_fwd(Params P) {
    __shared__ __attribute__((aligned(16))) char smem[163840];
    const unsigned L0 = (unsigned)(uintptr_t)(LAS char*)smem;
    const int tid = threadIdx.x, wv = __builtin_amdgcn_readfirstlane(tid >> 6), w = wv & 3;
    const int b = blockIdx.x, xcd = b & 7, slot = b >> 3;
    const int m = 2 * xcd + (slot >> 4), h = slot & 15;
    const int g = slot & 3, nt = g < 2 ? 4 : 3, tb = g < 2 ? 4 * g : 3 * g + 2;
    const int ti = 8 * xcd + (slot >> 2);
    const int s16 = slot & 15, pr = s16 >> 1;
    const int nt6 = ((s16 ^ pr) & 1) ? 3 : 4, tb6 = 7 * pr + ((s16 & 1) ? (pr & 1 ? 3 : 4) : 0);
    const bool act7 = slot < 28;
    const int j7 = 4 * xcd + (act7 ? slot / 7 : 0), g7 = act7 ? slot % 7 : 0;
    const unsigned wst = L0 + w * L_WST;
    const unsigned shift = (56 * h) & 15u;
    STAMP_DECL

    if (wv >= 4) {
        LANE_GEOM();
        const unsigned wrd = 16384 + hh * 6 * 256 + trq * 64 + ((shift + gq * 8) & 63);
        LAS float* csp = (LAS float*)(uintptr_t)(L0 + L_MISC);
        if (w < 2) {
            const unsigned xoffE = (lane >> 3) * 896 + (((lane & 7) ^ (lane >> 4)) << 4);
            const unsigned xoffO = (lane >> 3) * 896 + (((lane & 7) ^ (4 + (lane >> 4))) << 4);
            const unsigned colb = (56 * h) & ~15u;
            unsigned woff[3];
#pragma unroll
            for (int j = 0; j < 3; ++j) {
                const int bl = 4 * j + (lane >> 4), kql = bl / 3, tile = bl % 3;
                woff[j] = (4 * kql + ((lane >> 2) & 3)) * 2688 + tile * 896 + colb + (lane & 3) * 16;
            }
            const char* xb = (const char*)P.x + (size_t)(128 * m + 64 * w) * 896;
            const char* wb = (const char*)P.wqkv + (size_t)(32 * w) * 2688;
            LBAR();
#pragma unroll 1
            for (int kc = 0; kc < 7; ++kc) {
                if (kc >= 5) slot_wait(kc - 4, lane);
                const unsigned base = L0 + (kc >= 5 ? kc - 5 : kc) * L_SLOT;
#pragma unroll
                for (int i = 0; i < 6; ++i) dma1(wb + (size_t)kc * 64 * 2688 + (i / 3) * 16 * 2688, woff[i % 3], base + 16384 + (6 * w + i) * 1024);
#pragma unroll
                for (int i = 0; i < 8; ++i) dma1(xb + kc * 128 + i * 8 * 896, (i & 1) ? xoffO : xoffE, base + (8 * w + i) * 1024);
                if (kc > 0) { WAIT_VM(14); ldsw_st(F_RDY + 2 * (kc - 1) + w, 1); }
            }
            WAIT_VM(0); ldsw_st(F_RDY + 12 + w, 1);
        } else {
            const int srow = 128 * m + 8 * h + 4 * (w - 2) + (lane >> 4);
            const char* sp = (const char*)P.x + (size_t)srow * 896 + (lane & 15) * 16;
            u32x4 sr[4];
            asm volatile("global_load_dwordx4 %0, %4, off\n\tglobal_load_dwordx4 %1, %4, off offset:256\n\tglobal_load_dwordx4 %2, %4, off offset:512\n\tglobal_load_dwordx4 %3, %5, off"
                         : "=&v"(sr[0]), "=&v"(sr[1]), "=&v"(sr[2]), "=&v"(sr[3]) : "v"(sp), "v"(sp + ((lane & 15) < 8 ? 768 : 0)) : "memory");
            if (w == 2 && lane < 32) ldsw_st(lane, 0);
            LBAR();
            {
                asm volatile("s_waitcnt vmcnt(0)" : "+v"(sr[0]), "+v"(sr[1]), "+v"(sr[2]), "+v"(sr[3]) :: "memory");
                if ((lane & 15) >= 8) sr[3] = (u32x4){0u, 0u, 0u, 0u};
                float t1 = 0.f, t2 = 0.f;
#pragma unroll
                for (int q = 0; q < 4; ++q)
#pragma unroll
                    for (int i = 0; i < 4; ++i) { t1 = dot2(sr[q][i], ONES, t1); t2 = dot2(sr[q][i], sr[q][i], t2); }
                t1 += xor32<1>(t1); t2 += xor32<1>(t2); t1 += xor32<2>(t1); t2 += xor32<2>(t2); t1 += xor32<4>(t1); t2 += xor32<4>(t2); t1 += xor32<8>(t1); t2 += xor32<8>(t2);
                const float mean = t1 * (1.f / D), var = __builtin_fmaxf(t2 * (1.f / D) - mean * mean, 0.f);
                const unsigned e = *(const unsigned*)(P.ws + WS_EP) + 1u;
                const __amdgpu_buffer_rsrc_t rs = __builtin_amdgcn_make_buffer_rsrc((void*)P.ws, 0, WS_END, 0x00020000);
                if ((lane & 15) == 0) gst(rs, (u32x4){f2u(mean), e, f2u(__builtin_amdgcn_rsqf(var + EPS)), e}, srow * 16, WS_ST1);
            }
            float cs[3][4] = {};
            const unsigned wrd2 = wrd + (w - 2) * 2 * 3072;
#pragma unroll 1
            for (int c = 0; c < 7; ++c) {
                ring_wait(c);
                if (c == LATE_TOUCH) {
                    unsigned long long pc; unsigned back;
                    asm volatile("s_getpc_b64 %0\nmk_code_here:\n\ts_mov_b32 %1, mk_code_here-_ZN2mk9fused_fwdENS_6ParamsE" : "=s"(pc), "=s"(back));
                    constexpr unsigned len = CODE_TOUCH_BYTES;
                    const unsigned oc = (slot * 18 + (lane < 18 ? lane : 0)) * 128, o3 = (b * 64 + lane) * 128, o2 = (b * 16 + (lane & 15)) * 128;
                    const char* t0 = w == 2 ? (const char*)(pc - back) + (oc < len ? oc : 0) : (const char*)P.w3 + (o3 < 1605632 ? o3 : 0);
                    const char* t1 = w == 2 ? (lane < 16 ? (const char*)P.wo : lane < 32 ? (const char*)P.w1 : (const char*)P.w2) + (o2 < 401408 ? o2 : 0) : (const char*)P.w4 + (o3 < 1605632 ? o3 : 0);
                    asm volatile("s_mov_b32 m0, %2\n\ts_nop 0\n\tglobal_load_lds_dword %0, off\n\tglobal_load_lds_dword %1, off" :: "v"(t0), "v"(t1), "s"(L0 + L_MISC + 2048) : "memory");
                }
                const unsigned a = L0 + (c >= 5 ? c - 5 : c) * L_SLOT + wrd2;
#pragma unroll
                for (int ks = 0; ks < 2; ++ks)
#pragma unroll
                    for (int t = 0; t < 3; ++t) { const u32x4 f = wfrag(a + ks * 3072 + t * 256, a + ks * 3072 + 768 + t * 256);
#pragma unroll
                        for (int i = 0; i < 4; ++i) cs[t][i] = dot2(f[i], ONES, cs[t][i]); }
                WAIT_LGKM();
                ldsw_st(F_DONE + 2 + w, c + 1);
            }
#pragma unroll
            for (int t = 0; t < 3; ++t) { const float v = hsum((cs[t][0] + cs[t][1]) + (cs[t][2] + cs[t][3])); if (hh == 0) csp[(w - 2) * 96 + t * 32 + r32] = v; }
            WAIT_VM(0);
            WAIT_LGKM();
            ldsw_st(F_DONE + 8 + w, 1);
            if (w == 2) {
#pragma nounroll
                for (unsigned sp2 = 0; sp2 < SPIN_CAP * 16u && __builtin_amdgcn_readfirstlane(ldsw_ld(F_DONE + 11)) == 0; ++sp2) __builtin_amdgcn_s_sleep(1);
                if (hh == 0) {
#pragma unroll
                    for (int t = 0; t < 3; ++t) csp[384 + t * 32 + r32] = csp[t * 32 + r32] + csp[96 + t * 32 + r32];
                }
            }
        }
        LBAR();
        auto stage = [&](const WSrc& s, unsigned lds, int n0, int n1) AI {
#pragma unroll 1
            for (int n = n0; n < n1; ++n) wpiece(s, lds, n);
        };
        stage(wsrc_a((const char*)P.wo + (size_t)(112 * w) * 896 + tb * 64, 896, nt, lane), wst, 0, 28);
        BAR();
        BAR();
        LB(0);
        BAR(); stage(wsrc_a((const char*)P.w1 + (size_t)(112 * w) * 896 + tb * 64, 896, nt, lane), wst, 0, 28); BAR(); LB(0);
        BAR(); stage(wsrc_a((const char*)P.w2 + (size_t)(112 * w) * 896 + tb * 64, 896, nt, lane), wst, 0, 28); BAR(); LB(0);
        BAR(); stage(wsrc_a((const char*)P.w3 + (size_t)(112 * w) * 3584 + tb6 * 64, 3584, nt6, lane), wst, 0, 28); BAR(); LB(0);
        const WSrc w4s = wsrc_b((const char*)P.w4 + (size_t)(448 * w) * 896 + g7 * 128, 896, lane);
        if (act7) stage(w4s, L0 + L_X + w * 8192, 0, 8);
        {
            float c[4] = {0.f, 0.f, 0.f, 0.f};
            const unsigned a0 = L0 + trq * 64 + gq * 8 + 2 * hh * 1024 + w * 256;
#pragma unroll 4
            for (int k = 0; k < 28; ++k) { const unsigned a = a0 + k * 4096; const u32x4 f = wfrag(a, a + 1024);
#pragma unroll
                for (int i = 0; i < 4; ++i) c[i] = dot2(f[i], ONES, c[i]); }
            const float cc = hsum((c[0] + c[1]) + (c[2] + c[3]));
            if (hh == 0) ((LAS float*)(uintptr_t)(L0 + L_M2 + 1024))[w * 32 + r32] = cc;
        }
        LBAR();
        BAR();
        if (act7) stage(w4s, wst - 8 * 1024, 8, 36);
        LB(0);
        if (act7) {
            BAR();
            stage(w4s, wst - 36 * 1024, 36, 56);
            LB(0);
            BAR();
        }
        STAMP_FLUSH();
        return;
    }

    const unsigned e = *(const unsigned*)(P.ws + WS_EP) + 1u;
    const __amdgpu_buffer_rsrc_t rs = __builtin_amdgcn_make_buffer_rsrc((void*)P.ws, 0, WS_END, 0x00020000);
    u32x4 qf0, qf1;
    {
        LANE_GEOM();
        const unsigned wrd = 16384 + hh * 6 * 256 + trq * 64 + ((shift + gq * 8) & 63);
        STAMP(15);
        f32x16 aq = {}, ak = {}, av = {};
        const unsigned xrd = (32 * w + r32) * 128 + ((hh ^ ((r32 >> 1) & 7)) << 4);
        struct Fr { u32x4 x, q, k, v; };
        Fr F[4];
        unsigned xb[3][4], wb3[3];
#pragma unroll
        for (int gsl = 0; gsl < 3; ++gsl) {
#pragma unroll
            for (int j = 0; j < 4; ++j) { xb[gsl][j] = L0 + (xrd ^ (j << 5)) + gsl * 2 * L_SLOT; PIN(xb[gsl][j]); }
            wb3[gsl] = L0 + wrd + gsl * 2 * L_SLOT; PIN(wb3[gsl]);
        }
        auto ldx = [&](int sl, int j) AI { F[j].x = lds128(xb[sl / 2][j] + (sl & 1) * L_SLOT); const unsigned a = wb3[sl / 2] + (sl & 1) * L_SLOT + j * 3072; F[j].q = wfrag(a, a + 768); };
        auto ldk = [&](int sl, int j) AI { const unsigned a = wb3[sl / 2] + (sl & 1) * L_SLOT + j * 3072; F[j].k = wfrag(a + 256, a + 1024); };
        auto ldv = [&](int sl, int j) AI { const unsigned a = wb3[sl / 2] + (sl & 1) * L_SLOT + j * 3072; F[j].v = wfrag(a + 512, a + 1280); };
        LAS float* stw = (LAS float*)(uintptr_t)(L0 + L_MISC + 3072 + w * 256);
        BAR();
        ring_wait(0);
        STAMP(16);
#pragma unroll
        for (int j = 0; j < 2; ++j) { ldx(0, j); ldk(0, j); ldv(0, j); }
        SB();
        u32x4 gv = {0u, 0u, 0u, 0u};
        const unsigned gvo = (128 * m + 32 * w + r32) * 16;
        sfor<7>([&](auto cc) AI {
            constexpr int c = decltype(cc)::value, sl = c >= 5 ? c - 5 : c, nsl = c >= 4 ? c - 4 : c + 1;
            u32x2 rdy;
            sfor<4>([&](auto jc) AI {
                constexpr int j = decltype(jc)::value, jn = (j + 2) & 3, lb = j < 2 ? sl : nsl;
                constexpr bool ld = j < 2 || c < 6;
                if constexpr (j == 2 && c < 6) { if (__builtin_amdgcn_readfirstlane(rdy.x & rdy.y) == 0) ring_wait(c + 1); SB(); }
                MFMA(aq, F[j].q, F[j].x, aq);
                if constexpr (ld) ldx(lb, jn);
                SB();
                MFMA(ak, F[j].k, F[j].x, ak);
                if constexpr (ld) ldk(lb, jn);
                if constexpr (j == 0 && c >= 1 && c <= 2) ldsw_st(F_DONE + w, c);
                if constexpr (j == 0 && c == 4) gv = gld(rs, gvo, WS_ST1);
                SB();
                MFMA(av, F[j].x, F[j].v, av);
                if constexpr (ld) ldv(lb, jn);
                if constexpr (j == 0 && c < 6) rdy = ldsw_ld2(F_RDY + 2 * (c + 1));
                SB();
            });
        });
#pragma nounroll
        for (unsigned sp2 = 0; sp2 < SPIN_CAP && !wall(gv.y == e && gv.w == e); ++sp2) gv = gld(rs, gvo, WS_ST1);
        if (hh == 0) { stw[r32] = u2f(gv.x) * u2f(gv.z); stw[32 + r32] = u2f(gv.z); }
        STAMP(1);
        const float mean = u2f(gv.x), rstd = u2f(gv.z);
        LBAR();
        const LAS float* csf = (const LAS float*)(uintptr_t)(L0 + L_MISC + 1536);
        const float cv = csf[64 + r32];
        const float qs = rstd * (0.18898223650461362f * 1.4426950408889634f);
#pragma unroll
        for (int q = 0; q < 4; ++q) {
            const f32x4 c1 = *(const LAS f32x4*)(csf + 8 * q + 4 * hh), c2 = *(const LAS f32x4*)(csf + 32 + 8 * q + 4 * hh);
            const f32x4 mr = *(const LAS f32x4*)(stw + 8 * q + 4 * hh), rr = *(const LAS f32x4*)(stw + 32 + 8 * q + 4 * hh);
#pragma unroll
            for (int i = 0; i < 4; ++i) {
                aq[4 * q + i] = (aq[4 * q + i] - mean * c1[i]) * qs;
                ak[4 * q + i] = (ak[4 * q + i] - mean * c2[i]) * rstd;
                av[4 * q + i] = av[4 * q + i] * rr[i] - mr[i] * cv;
            }
        }
        if (hh == 1) { aq[12] = 1.f; aq[13] = 0.f; aq[14] = 0.f; aq[15] = 0.f; ak[12] = 0.f; ak[13] = 0.f; ak[14] = 0.f; ak[15] = 0.f; }
        if (r32 >= 28) {
            const float pv = r32 == 28 ? 1.f : 0.f;
#pragma unroll
            for (int i = 0; i < 16; ++i) av[i] = pv;
        }
        const u32x4 k0 = pack8(ak, 0), k1 = pack8(ak, 1), v0 = pack8(av, 0), v1 = pack8(av, 1);
        const unsigned go = WS_KV + h * KVH + (4 * m + w) * 4096;
        gst(rs, k0, lane * 16, go); gst(rs, k1, lane * 16, go + 1024); gst(rs, v0, lane * 16, go + 2048); gst(rs, v1, lane * 16, go + 3072);
        const unsigned lo = L0 + L_OWNKV + w * 4096 + lane * 16;
        sts128(lo, k0); sts128(lo + 1024, k1); sts128(lo + 2048, v0); sts128(lo + 3072, v1);
        qf0 = pack8(aq, 0); qf1 = pack8(aq, 1);
        WAIT_BAR(0);
        if (tid == 0) set_flag(P.ws, flag_off(0, m * 16 + h), e);
        STAMP(2);
    }
    {
        LANE_GEOM();
        const int mt = m - 2 + (w >> 1);
        u32x4 kv[8];
        if (mt >= 0) {
            wait_flags(P.ws, flag_off(0, mt * 16 + h), 1, e, lane);
            const unsigned go = WS_KV + h * KVH + (4 * m - 8 + 2 * w) * 4096;
#pragma unroll
            for (int i = 0; i < 8; ++i) kv[i] = gld(rs, lane * 16, go + i * 1024);
        } else {
#pragma unroll
            for (int i = 0; i < 8; ++i) kv[i] = (u32x4){0u, 0u, (i & 3) == 1 && hh == 1 ? 0xF149u : 0u, 0u};
        }
        f32x16 cdiag, cold;
#pragma unroll
        for (int i = 0; i < 16; ++i) { const int kr = (i & 3) + 8 * (i >> 2) + 4 * hh; cdiag[i] = kr <= r32 ? 0.f : -1e30f; cold[i] = kr > r32 ? 0.f : -1e30f; }
#pragma unroll
        for (int i = 0; i < 8; ++i) sts128(L0 + L_KV + 2 * w * 4096 + i * 1024 + lane * 16, kv[i]);
        LBAR();
        STAMP(3);
        unsigned kvb = L0 + L_KV + w * 4096 + lane * 16;
        PIN(kvb);
        u32x4 kf[9][2], vf[9][2];
        const f32x16 zero16 = {};
        auto ldK = [&](int i) AI { kf[i][0] = lds128(kvb + i * 4096); kf[i][1] = lds128(kvb + i * 4096 + 1024); };
        auto ldV = [&](int i) AI { vf[i][0] = lds128(kvb + i * 4096 + 2048); vf[i][1] = lds128(kvb + i * 4096 + 3072); };
        auto smax = [&](const f32x16& s, float a) AI {
#pragma unroll
            for (int i = 0; i < 16; i += 2) a = max3(a, s[i], s[i + 1]);
            return a; };
        ldK(0); ldK(1);
        float mx = -3e38f;
        f32x16 s[2];
        auto smax8 = [&](const f32x16& v, int o, float a) AI {
#pragma unroll
            for (int i = 0; i < 8; i += 2) a = max3(a, v[o + i], v[o + i + 1]);
            return a; };
        sfor<9>([&](auto ic) AI {
            constexpr int i = decltype(ic)::value;
            if constexpr (i + 2 < 9) ldK(i + 2);
            MFMA(s[i & 1], kf[i][0], qf0, i == 0 ? cold : i == 8 ? cdiag : zero16);
            SB();
            MFMA(s[i & 1], kf[i][1], qf1, s[i & 1]);
            SB();
            if constexpr (i > 0) { mx = smax8(s[(i - 1) & 1], 0, mx); mx = smax8(s[(i - 1) & 1], 8, mx); PIN(mx); }
            SB();
        });
        ldK(0); ldK(1); ldV(0);
        mx = hmax(smax(s[0], mx));
        STAMP(25);
        f32x16 o = {}, cm;
#pragma unroll
        for (int i = 0; i < 16; ++i) { cm[i] = -mx; cdiag[i] -= mx; cold[i] -= mx; }
        MFMA(s[0], kf[0][0], qf0, cold); MFMA(s[0], kf[0][1], qf1, s[0]);
        SB();
        sfor<9>([&](auto ic) AI {
            constexpr int i = decltype(ic)::value;
            f32x16& c = s[i & 1];
            f32x16& n = s[(i + 1) & 1];
            if constexpr (i + 2 < 9) ldK(i + 2);
            if constexpr (i + 1 < 9) ldV(i + 1);
            if constexpr (i + 1 < 9) MFMA(n, kf[i + 1][0], qf0, i + 1 == 8 ? cdiag : cm);
#pragma unroll
            for (int j = 0; j < 8; ++j) c[j] = __builtin_amdgcn_exp2f(c[j]);
            u32x4 p0 = pack8(c, 0);
            PIN(p0);
            SB();
            if constexpr (i + 1 < 9) MFMA(n, kf[i + 1][1], qf1, n);
            MFMA(o, vf[i][0], p0, o);
#pragma unroll
            for (int j = 8; j < 16; ++j) c[j] = __builtin_amdgcn_exp2f(c[j]);
            const u32x4 p1 = pack8(c, 1);
            MFMA(o, vf[i][1], p1, o);
            SB();
        });
        STAMP(26);
        auto lr = __builtin_amdgcn_permlane32_swap(f2u(o[12]), f2u(o[12]), false, false);
        const float inv = __builtin_amdgcn_rcpf(u2f(lr[1]));
#pragma unroll
        for (int i = 0; i < 16; ++i) o[i] *= inv;
        if (hh == 1) { o[12] = 0.f; o[13] = 0.f; o[14] = 0.f; o[15] = 0.f; }
        const unsigned vo = (hh * XR + r32) * 16, so = WS_O + ((4 * h) * XR + 128 * m + 32 * w) * 16;
        gst(rs, pack8(o, 0), vo, so); gst(rs, pack8(o, 1), vo, so + 2 * XROW);
        WAIT_BAR(0);
        if (tid == 0) set_flag(P.ws, flag_off(1, m * 16 + h), e);
        STAMP(4);
    }
    const bool act = w < nt;
    const int tw = tb + w;
#define GEMM_GEOM() LANE_GEOM(); \
    unsigned trW[2]; trW[0] = L0 + (w < nt ? w : nt - 1) * 256 + trq * 64 + gq * 8 + 2 * hh * 1024; trW[1] = trW[0] + 57344; PIN(trW[0]); PIN(trW[1]); \
    auto wimg = [&](int kq0, int kq1) AI { return wfrag(kq0 < 56 ? trW[0] + kq0 * 1024 : trW[1] + (kq0 - 56) * 1024, kq1 < 56 ? trW[0] + kq1 * 1024 : trW[1] + (kq1 - 56) * 1024); }; \
    unsigned xl = L0 + L_X + lane * 16; PIN(xl); \
    LAS float* m2 = (LAS float*)(uintptr_t)(L0 + L_M2 + w * 256); (void)m2
    auto gemm = [&](auto nk, const u32x4* wr, unsigned xl) AI -> f32x16 {
        constexpr int NK = decltype(nk)::value, PD = 4;
        f32x16 acc = {};
        u32x4 xf[NK];
#pragma unroll
        for (int k = 0; k < PD; ++k) xf[k] = lds128(xl + k * 1024);
        SB();
        sfor<NK>([&](auto kc) AI {
            constexpr int k = decltype(kc)::value;
            if constexpr (k + PD < NK) xf[k + PD] = lds128(xl + (k + PD) * 1024);
            MFMA(acc, wr[k], xf[k], acc);
            SB();
        });
        return acc;
    };
    u32x4 hp[2];
    float meanH, rstdH;
    {
        GEMM_GEOM();
        u32x4 xr[2];
        const char* xp = (const char*)P.x + (size_t)(32 * ti + r32) * 896 + 64 * (act ? tw : tb) + 16 * hh;
        xr[0] = *(const GAS u32x4*)xp; xr[1] = *(const GAS u32x4*)(xp + 32);
        u32x4 wr[32];
        sfor<32>([&](auto kc) AI { constexpr int k = decltype(kc)::value, kq = 7 * (k >> 1) + 4 * (k & 1);
            if constexpr (k < 31) wr[k] = wimg(kq, kq + 1);
            else { const u32x2 p = ldstr(trW[1] + (kq - 56) * 1024), q = ldstr(trW[1] + (kq - 56) * 1024 + (hh ? 0 : 1024)); wr[k] = (u32x4){p.x, p.y, q.x, q.y}; } });
        LBAR();
        wait_flags(P.ws, flag_off(1, (ti >> 2) * 16 + 4 * w), 4, e, lane);
        u32x4 xf[8];
#pragma unroll
        for (int k = 0; k < 8; ++k) xf[k] = gld(rs, vx, WS_O + ((16 * w + 2 * k) * XR + 32 * ti) * 16);
#pragma unroll
        for (int k = 0; k < 8; ++k) sts128(xl + (8 * w + k) * 1024, xf[k]);
        LBAR();
        STAMP(5);
        f32x16 r = gemm(IC(32), wr, xl);
        float s1 = 0.f, s2 = 0.f;
#pragma unroll
        for (int s = 0; s < 2; ++s) {
#pragma unroll
            for (int i = 0; i < 4; ++i) {
                hp[s][i] = pk(r[8 * s + 2 * i] + blo(xr[s][i]), r[8 * s + 2 * i + 1] + bhi(xr[s][i]));
                s1 = dot2(hp[s][i], ONES, s1); s2 = dot2(hp[s][i], hp[s][i], s2);
            }
            if (act) gst(rs, hp[s], vx, WS_H + ((4 * tw + 2 * s) * XR + 32 * ti) * 16);
        }
        s1 = hsum(s1); s2 = hsum(s2);
        if (act && hh == 0) gst8(rs, (u32x2){f2u(s1), f2u(s2)}, r32 * 8, WS_STH + (tw * 2048 + 32 * ti) * 8);
        WAIT_BAR(0);
        if (tid == 0) set_flag(P.ws, flag_off(2, ti * 4 + g), e);
        STAMP(6);
    }
#define LOAD_WR() u32x4 wr[28]; sfor<28>([&](auto kc) AI { constexpr int k = decltype(kc)::value; wr[k] = wimg(4 * k, 4 * k + 1); })
    const int fsn = w == 0 ? 1 : 2, fsl = w <= 1 ? 0 : w - 1;
    {
        GEMM_GEOM();
        LOAD_WR();
        LBAR();
        {
            float cs[4] = {0.f, 0.f, 0.f, 0.f};
#pragma unroll
            for (int k = 0; k < 28; ++k)
#pragma unroll
                for (int i = 0; i < 4; ++i) cs[i] = dot2(wr[k][i], ONES, cs[i]);
            const float c = hsum((cs[0] + cs[1]) + (cs[2] + cs[3]));
            if (hh == 0) m2[r32] = c;
        }
        wait_flags(P.ws, flag_off(2, ti * 4), 4, e, lane);
        u32x4 xf[7];
#pragma unroll
        for (int k = 0; k < 7; ++k) xf[k] = gld(rs, vx, WS_H + ((14 * w + 2 * k) * XR + 32 * ti) * 16);
        u32x2 sv[7];
#pragma unroll
        for (int q = 0; q < 7; ++q) sv[q] = gld8(rs, (7 * hh * 2048 + r32) * 8, WS_STH + (q * 2048 + 32 * ti) * 8);
#pragma unroll
        for (int k = 0; k < 7; ++k) sts128(xl + (7 * w + k) * 1024, xf[k]);
        LBAR();
        STAMP(7);
        f32x16 r = gemm(IC(28), wr, xl);
        float t1 = 0.f, t2 = 0.f;
#pragma unroll
        for (int q = 0; q < 7; ++q) { t1 += u2f(sv[q].x); t2 += u2f(sv[q].y); }
        t1 = hsum(t1); t2 = hsum(t2);
        meanH = t1 * (1.f / D);
        const float var = __builtin_fmaxf(t2 * (1.f / D) - meanH * meanH, 0.f);
        rstdH = __builtin_amdgcn_rsqf(var + EPS);
        const float sc = rstdH * __builtin_amdgcn_rsqf(var * rstdH * rstdH + EPS);
#pragma unroll
        for (int q = 0; q < 4; ++q) {
            const f32x4 c = *(const LAS f32x4*)(m2 + 8 * q + 4 * hh);
#pragma unroll
            for (int i = 0; i < 4; ++i) r[4 * q + i] = gelu_t((r[4 * q + i] - meanH * c[i]) * sc);
        }
#pragma unroll
        for (int s = 0; s < 2; ++s) if (act) gst(rs, pack8(r, s), vx, WS_HH + ((4 * tw + 2 * s) * XR + 32 * ti) * 16);
        WAIT_BAR(0);
        if (tid == 0) set_flag(P.ws, flag_off(3, ti * 4 + g), e);
        STAMP(8);
    }
    {
        GEMM_GEOM();
        LOAD_WR();
        LBAR();
        const int sh = w == 0 ? -1 : w == 1 ? 1 : w == 2 ? -2 : 2;
        const int tn = ti + (sh < 0 ? -1 : 1);
        {
            const bool nb = tn >= 0 && tn < 64;
            const int li = lane & 3;
            const GAS unsigned* p = (const GAS unsigned*)(P.ws + flag_off(3, (((li & 2) && nb) ? tn : ti) * 4 + fsl + ((li & 1) && fsn == 2 ? 1 : 0)));
#pragma nounroll
            for (unsigned sp = 0; sp < SPIN_CAP; ++sp) { unsigned v = __hip_atomic_load(p, __ATOMIC_RELAXED, __HIP_MEMORY_SCOPE_AGENT); if (wall(v == e)) break; }
            __builtin_amdgcn_fence(__ATOMIC_ACQUIRE, "wavefront");
        }
        const int tok = 32 * ti + r32 + sh;
        const bool ok = tok >= 0 && tok < S;
        const unsigned vxs = ok ? (hh * XR + tok) * 16 : 0x7ffffff0u;
        u32x4 xf[7];
#pragma unroll
        for (int k = 0; k < 7; ++k) { xf[k] = gld(rs, vxs, WS_HH + (14 * w + 2 * k) * XROW); }
#pragma unroll
        for (int k = 0; k < 7; ++k) sts128(xl + (7 * w + k) * 1024, xf[k]);
        LBAR();
        STAMP(9);
        f32x16 r = gemm(IC(28), wr, xl);
        float s1 = 0.f, s2 = 0.f;
#pragma unroll
        for (int s = 0; s < 2; ++s) {
            u32x4 xp;
#pragma unroll
            for (int i = 0; i < 4; ++i) {
                xp[i] = pk(r[8 * s + 2 * i] + (blo(hp[s][i]) - meanH) * rstdH, r[8 * s + 2 * i + 1] + (bhi(hp[s][i]) - meanH) * rstdH);
                s1 = dot2(xp[i], ONES, s1); s2 = dot2(xp[i], xp[i], s2);
            }
            if (act) gst(rs, xp, vx, WS_X1 + ((4 * tw + 2 * s) * XR + 32 * ti) * 16);
        }
        s1 = hsum(s1); s2 = hsum(s2);
        if (act && hh == 0) gst8(rs, (u32x2){f2u(s1), f2u(s2)}, r32 * 8, WS_STX + (tw * 2048 + 32 * ti) * 8);
        WAIT_BAR(0);
        if (tid == 0) set_flag(P.ws, flag_off(4, ti * 4 + g), e);
        STAMP(10);
    }
    {
        LANE_GEOM();
        unsigned tr6[2];
        tr6[0] = L0 + trq * 64 + gq * 8 + 2 * hh * 1024; tr6[1] = tr6[0] + 57344; PIN(tr6[0]); PIN(tr6[1]);
        auto wf6 = [&](int t, int k) AI { const unsigned a = (k < 14 ? tr6[0] + k * 4096 : tr6[1] + (k - 14) * 4096) + t * 256; return wfrag(a, a + 1024); };
        const LAS float* cs6 = (const LAS float*)(uintptr_t)(L0 + L_M2 + 1024);
        BAR();
        wait_flags(P.ws, flag_off(4, (4 * m + w) * 4), 4, e, lane);
        const int tk = 128 * m + 32 * w;
        auto ldx1 = [&](int k) AI { return gld(rs, vx, WS_X1 + (2 * k * XR + tk) * 16); };
        unsigned vx2 = vx; PIN(vx2);
        auto ldx2 = [&](int k) AI { return gld(rs, vx2, WS_X1 + (2 * k * XR + tk) * 16); };
        constexpr int PX = 10, PW = 3;
        f32x16 a0 = {}, a1 = {};
        u32x2 sv[7];
        u32x4 xg[28];
        constexpr int PG = 6;
        {
            u32x4 xf[28], wA[28], wB[28];
#pragma unroll
            for (int k = 0; k < PX; ++k) xf[k] = ldx1(k);
#pragma unroll
            for (int q = 0; q < 7; ++q) sv[q] = gld8(rs, (7 * hh * 2048 + r32) * 8, WS_STX + (q * 2048 + tk) * 8);
            STAMP(11);
#pragma unroll
            for (int k = 0; k < PW; ++k) { wA[k] = wf6(0, k); wB[k] = wf6(1, k); }
            SB();
            sfor<28>([&](auto kc) AI {
                constexpr int k = decltype(kc)::value;
                if constexpr (k + PW < 28) { wA[k + PW] = wf6(0, k + PW); wB[k + PW] = wf6(1, k + PW); }
                MFMA(a0, wA[k], xf[k], a0);
                if constexpr (k + PX < 28) xf[k + PX] = ldx1(k + PX);
                else if constexpr (k + PX - 28 < PG) xg[k + PX - 28] = ldx2(k + PX - 28);
                MFMA(a1, wB[k], xf[k], a1);
                SB();
            });
        }
        float t1 = 0.f, t2 = 0.f;
#pragma unroll
        for (int q = 0; q < 7; ++q) { t1 += u2f(sv[q].x); t2 += u2f(sv[q].y); }
        t1 = hsum(t1); t2 = hsum(t2);
        const float mean = t1 * (1.f / D), var = __builtin_fmaxf(t2 * (1.f / D) - mean * mean, 0.f) + EPS, rstd = __builtin_amdgcn_rsqf(var);
        const GeluK gk = {-mean, GC1 * rstd * rstd * rstd, GC0 * rstd, var * rstd};
        const unsigned uo = WS_U + ((4 * tb6) * XR + tk) * 16;
        auto epi2 = [&](auto nsc, auto&& pre, f32x16& ya, int ta, f32x16& yb, int tb_, bool storeb) AI {
            constexpr int NS = decltype(nsc)::value, OPS = NS >= 28 ? 1 : 2, SK = NS >= 28 ? 5 : 2;
            f32x4 c[8];
            float z[32];
            sfor<NS>([&](auto kc) AI {
                constexpr int k = decltype(kc)::value;
                pre(kc);
                sfor<8>([&](auto qc) AI { constexpr int q = decltype(qc)::value, st = (4 * q * SK) / 8;
                    if constexpr (k == (st >= 2 ? st - 2 : 0)) c[q] = *(const LAS f32x4*)(cs6 + (q < 4 ? ta : tb_) * 32 + 8 * (q & 3) + 4 * hh); });
                sfor<32>([&](auto ec) AI {
                    constexpr int el = decltype(ec)::value, st = (el * SK) / 8;
                    sfor<8>([&](auto oc) AI { constexpr int o = decltype(oc)::value;
                        if constexpr (o / OPS == k - st) { f32x16& y = el < 16 ? ya : yb; float yy = y[el & 15]; gelu_step(o, yy, z[el], c[el >> 2][el & 3], gk); y[el & 15] = yy; } });
                });
                sfor<32>([&](auto ec) AI {
                    constexpr int el = decltype(ec)::value, st = (el * SK) / 8, o = k - st;
                    if constexpr (o >= 0 && o < 8) { if constexpr (o == 0 || o == 7) { f32x16& y = el < 16 ? ya : yb; float yy = y[el & 15]; PIN(yy); y[el & 15] = yy; } else PIN(z[el]); }
                    if constexpr (el == 15 && k == st + 8) { gst(rs, pack8(ya, 0), vx, uo + 4 * ta * XROW); gst(rs, pack8(ya, 1), vx, uo + (4 * ta + 2) * XROW); }
                });
                SB();
            });
            if (storeb) { gst(rs, pack8(yb, 0), vx, uo + 4 * tb_ * XROW); gst(rs, pack8(yb, 1), vx, uo + (4 * tb_ + 2) * XROW); }
        };
        STAMP(23);
        f32x16 a2 = {}, a3 = {};
        {
            u32x4 wA[28], wB[28];
#pragma unroll
            for (int k = 0; k < PW; ++k) { wA[k] = wf6(2, k); wB[k] = wf6(3, k); }
            SB();
            epi2(IC(28), [&](auto kc) AI {
                constexpr int k = decltype(kc)::value;
                if constexpr (k + PW < 28) { wA[k + PW] = wf6(2, k + PW); wB[k + PW] = wf6(3, k + PW); }
                MFMA(a2, wA[k], xg[k], a2);
                if constexpr (k + PG < 28) xg[k + PG] = ldx2(k + PG);
                MFMA(a3, wB[k], xg[k], a3);
            }, a0, 0, a1, 1, true);
        }
        STAMP(24);
        LBAR();
        auto epi1 = [&](f32x16& y, int te) AI {
#pragma unroll
            for (int q = 0; q < 4; ++q) {
                const f32x4 c = *(const LAS f32x4*)(cs6 + te * 32 + 8 * q + 4 * hh);
#pragma unroll
                for (int i = 0; i < 4; ++i) {
                    const float v = __builtin_fmaf(gk.nmean, c[i], y[4 * q + i]);
                    const float z = v * __builtin_fmaf(v * v, gk.k1, gk.k0);
                    y[4 * q + i] = v * __builtin_amdgcn_rcpf(__builtin_fmaf(__builtin_amdgcn_exp2f(z), gk.irs, gk.irs));
                }
            }
            gst(rs, pack8(y, 0), vx, uo + 4 * te * XROW); gst(rs, pack8(y, 1), vx, uo + (4 * te + 2) * XROW);
        };
        epi1(a2, 2);
        if (nt6 == 4) epi1(a3, 3);
        WAIT_BAR(0);
        if (tid == 0) set_flag(P.ws, flag_off(5, m * 16 + s16), e);
        STAMP(12);
    }
    if (act7) {
        LANE_GEOM();
        const unsigned lanep = trq * 64 + gq * 8 + 2 * hh * 512;
        unsigned tA = L0 + L_X + w * 8192 + lanep, tB = wst + lanep; PIN(tA); PIN(tB);
        auto w4f = [&](int k, int t) AI { const unsigned a = (k < 4 ? tA + k * 2048 : k < 18 ? tB + (k - 4) * 2048 : tB + (k - 18) * 2048) + t * 256; return wfrag(a, a + 512); };
        u32x4 wr[28][2];
#pragma unroll
        for (int k = 0; k < 14; ++k) { wr[k][0] = w4f(k, 0); wr[k][1] = w4f(k, 1); }
        LBAR();
        const int tk = 64 * j7;
        const int tt = w >> 1, nt7 = w & 1;
        wait_flags(P.ws, flag_off(5, (j7 >> 1) * 16 + 4 * w), 4, e, lane);
        constexpr int PX = 5;
        u32x4 xa[28][2];
#pragma unroll
        for (int k = 0; k < PX; ++k) { xa[k][0] = gld(rs, vx, WS_U + ((56 * w + 2 * k) * XR + tk) * 16); xa[k][1] = gld(rs, vx, WS_U + ((56 * w + 2 * k) * XR + tk + 32) * 16); }
        STAMP(13);
        SB();
        f32x16 acc[4] = {};
        u32x4 rh[2], rx[2];
        sfor<28>([&](auto kc) AI {
            constexpr int k = decltype(kc)::value;
            if constexpr (k == 13) { BAR(); SB(); }
            if constexpr (k + 3 >= 14 && k + 3 < 28) { wr[k + 3][0] = w4f(k + 3, 0); wr[k + 3][1] = w4f(k + 3, 1); }
            MFMA(acc[0], wr[k][0], xa[k][0], acc[0]); MFMA(acc[1], wr[k][1], xa[k][0], acc[1]);
            if constexpr (k + PX < 28) xa[k + PX][0] = gld(rs, vx, WS_U + ((56 * w + 2 * (k + PX)) * XR + tk) * 16);
            MFMA(acc[2], wr[k][0], xa[k][1], acc[2]); MFMA(acc[3], wr[k][1], xa[k][1], acc[3]);
            if constexpr (k + PX < 28) xa[k + PX][1] = gld(rs, vx, WS_U + ((56 * w + 2 * (k + PX)) * XR + tk + 32) * 16);
            if constexpr (k == 24 || k == 25) {
                constexpr int s = k - 24;
                const unsigned so = ((8 * g7 + 4 * nt7 + 2 * s) * XR + tk + 32 * tt) * 16;
                rh[s] = gld(rs, vx, WS_H + so); rx[s] = gld(rs, vx, WS_X1 + so);
            }
            SB();
        });
        STAMP(27);
#pragma unroll
        for (int t = 0; t < 4; ++t) {
            if (t != w) {
                const unsigned a = L0 + L_X + t * 12288 + (w < t ? w : w - 1) * 4096 + lane * 16;
#pragma unroll
                for (int q = 0; q < 4; ++q) stsf4(a + q * 1024, (f32x4){acc[t][4 * q], acc[t][4 * q + 1], acc[t][4 * q + 2], acc[t][4 * q + 3]});
            }
        }
        LBAR();
        f32x16 r = w == 0 ? acc[0] : w == 1 ? acc[1] : w == 2 ? acc[2] : acc[3];
        const unsigned a = L0 + L_X + w * 12288 + lane * 16;
#pragma unroll
        for (int j = 0; j < 3; ++j)
#pragma unroll
            for (int q = 0; q < 4; ++q) { const f32x4 v = ldsf4(a + j * 4096 + q * 1024); r[4 * q] += v[0]; r[4 * q + 1] += v[1]; r[4 * q + 2] += v[2]; r[4 * q + 3] += v[3]; }
        char* op = (char*)P.out + (size_t)(tk + 32 * tt + r32) * 896 + 128 * g7 + 64 * nt7 + 16 * hh;
#pragma unroll
        for (int s = 0; s < 2; ++s) {
            u32x4 ov;
#pragma unroll
            for (int i = 0; i < 4; ++i)
                ov[i] = pk(r[8 * s + 2 * i] + blo(rx[s][i]) + blo(rh[s][i]), r[8 * s + 2 * i + 1] + bhi(rx[s][i]) + bhi(rh[s][i]));
            *(GAS u32x4*)(op + 32 * s) = ov;
        }
        STAMP(14);
    }
    STAMP_FLUSH();
    if (b == 0 && w == 0) {
        const int lane = tid & 63;
        for (int r = 0; r < 4; ++r) {
            const GAS unsigned* p = (const GAS unsigned*)(P.ws + flag_off(0, r * 64 + lane));
#pragma nounroll
            for (unsigned sp = 0; sp < SPIN_CAP; ++sp) { unsigned v = __hip_atomic_load(p, __ATOMIC_RELAXED, __HIP_MEMORY_SCOPE_AGENT); if (wall(v == e)) break; }
        }
        if (lane == 0) __hip_atomic_store((GAS unsigned*)(P.ws + WS_EP), e, __ATOMIC_RELAXED, __HIP_MEMORY_SCOPE_AGENT);
    }
}
}

extern "C" void kernel_launch(void* const* d_in, const int* in_sizes, int n_in, void* d_out, int out_size, void* d_ws, size_t ws_size,
                              hipStream_t stream) {
    mk::Params P;
    P.x = (const u16*)d_in[0]; P.wqkv = (const u16*)d_in[1]; P.wo = (const u16*)d_in[2];
    P.w1 = (const u16*)d_in[4]; P.w2 = (const u16*)d_in[5]; P.w3 = (const u16*)d_in[6]; P.w4 = (const u16*)d_in[7];
    P.out = (u16*)d_out; P.ws = (char*)d_ws;
    mk::fused_fwd<<<256, 512, 0, stream>>>(P);
}
```

```cpp
#define LATE_TOUCH 0
#define TOUCH_OUT 1
#define TOUCH_WS 1
#include <hip/hip_runtime.h>
#include <hip/hip_bf16.h>
#include <stdint.h>
#include <type_traits>
#include <utility>

typedef unsigned short u16;
typedef __attribute__((ext_vector_type(8))) short bf16x8;
typedef __attribute__((ext_vector_type(4))) short s16x4;
typedef __attribute__((ext_vector_type(16))) float f32x16;
typedef __attribute__((ext_vector_type(4))) float f32x4;
typedef __attribute__((ext_vector_type(2))) float f32x2;
typedef __attribute__((ext_vector_type(4))) unsigned u32x4;
typedef __attribute__((ext_vector_type(2))) unsigned u32x2;
typedef __attribute__((ext_vector_type(2))) __bf16 bf2;
#define LAS __attribute__((address_space(3)))
#define GAS __attribute__((address_space(1)))
#define DEVI __device__ __forceinline__

#define MK_STAMP 0

namespace mk {
constexpr int S = 2048, D = 448;
constexpr unsigned XR = 2056, XROW = XR * 16;
constexpr unsigned KVH = 64 * 4096 + 128;
constexpr unsigned WS_EP = 0;
constexpr unsigned WS_FLAG = 4096;
constexpr unsigned WS_KV = 262144;
constexpr unsigned WS_O = WS_KV + 16 * KVH;
constexpr unsigned WS_H = WS_O + 64 * XROW;
constexpr unsigned WS_HH = WS_H + 56 * XROW;
constexpr unsigned WS_X1 = WS_HH + 56 * XROW;
constexpr unsigned WS_U = WS_X1 + 56 * XROW;
constexpr unsigned WS_STH = WS_U + 224 * XROW;
constexpr unsigned WS_STX = WS_STH + 262144;
constexpr unsigned WS_ST1 = WS_STX + 262144;
constexpr unsigned WS_SPARE = WS_ST1 + 32768;
constexpr unsigned WS_STAMP = WS_SPARE + 128;
constexpr unsigned WS_END = WS_STAMP + 16 * 512 * 8;
constexpr unsigned L_SLOT = 28672;
constexpr unsigned L_MISC = 143360;
constexpr unsigned L_KV = 114688;
constexpr unsigned L_OWNKV = L_KV + 8 * 4096;
constexpr unsigned L_WST = 28672;
constexpr unsigned L_X = 114688;
constexpr unsigned L_M2 = 147456;
constexpr unsigned CODE_TOUCH_BYTES = 36000;
constexpr float EPS = 1e-5f;
constexpr unsigned SPIN_CAP = 1u << 18;

struct Params {
    const u16* x; const u16* wqkv; const u16* wo; const u16* w1; const u16* w2; const u16* w3; const u16* w4;
    u16* out; char* ws;
};

DEVI unsigned f2u(float f) { return __builtin_bit_cast(unsigned, f); }
DEVI float u2f(unsigned u) { return __builtin_bit_cast(float, u); }
DEVI bool wall(bool p) { return __builtin_amdgcn_ballot_w64(!p) == 0; }
template <int X> DEVI float xor32(float v) { return u2f((unsigned)__builtin_amdgcn_ds_swizzle((int)f2u(v), (X << 10) | 0x1f)); }
DEVI unsigned pk(float lo, float hi) { bf2 v = {(__bf16)lo, (__bf16)hi}; return __builtin_bit_cast(unsigned, v); }
DEVI float blo(unsigned u) { return u2f(u << 16); }
DEVI float bhi(unsigned u) { return u2f(u & 0xffff0000u); }
DEVI float dot2(unsigned a, unsigned b, float c) { return __builtin_amdgcn_fdot2_f32_bf16(__builtin_bit_cast(bf2, a), __builtin_bit_cast(bf2, b), c, false); }
constexpr unsigned ONES = 0x3f803f80u;
DEVI float hsum(float v) { auto r = __builtin_amdgcn_permlane32_swap(f2u(v), f2u(v), false, false); return u2f(r[0]) + u2f(r[1]); }
DEVI float hmax(float v) { auto r = __builtin_amdgcn_permlane32_swap(f2u(v), f2u(v), false, false); return __builtin_fmaxf(u2f(r[0]), u2f(r[1])); }
DEVI f32x16 mfma(u32x4 a, u32x4 b, f32x16 c) { return __builtin_amdgcn_mfma_f32_32x32x16_bf16(__builtin_bit_cast(bf16x8, a), __builtin_bit_cast(bf16x8, b), c, 0, 0, 0); }
#define MFMA(acc, a, b, c) do { acc = mfma(a, b, c); asm volatile("" : "+v"(acc)); } while (0)
DEVI u32x4 lds128(unsigned a) { return *(const LAS u32x4*)(uintptr_t)a; }
DEVI f32x4 ldsf4(unsigned a) { return *(const LAS f32x4*)(uintptr_t)a; }
DEVI void sts128(unsigned a, u32x4 v) { *(LAS u32x4*)(uintptr_t)a = v; }
DEVI void stsf4(unsigned a, f32x4 v) { *(LAS f32x4*)(uintptr_t)a = v; }
DEVI u32x2 ldstr(unsigned a) { return __builtin_bit_cast(u32x2, __builtin_amdgcn_ds_read_tr16_b64_v4i16((LAS s16x4*)(uintptr_t)a)); }
DEVI u32x4 wfrag(unsigned a0, unsigned a1) { u32x2 p = ldstr(a0), q = ldstr(a1); return (u32x4){p.x, p.y, q.x, q.y}; }
DEVI u32x4 pack8(const f32x16& a, int s) { return (u32x4){pk(a[8 * s], a[8 * s + 1]), pk(a[8 * s + 2], a[8 * s + 3]), pk(a[8 * s + 4], a[8 * s + 5]), pk(a[8 * s + 6], a[8 * s + 7])}; }
constexpr int SC1 = 16;
DEVI u32x4 gld(__amdgpu_buffer_rsrc_t rs, unsigned voff, unsigned soff) { return __builtin_amdgcn_raw_buffer_load_b128(rs, voff, soff, SC1); }
DEVI u32x2 gld8(__amdgpu_buffer_rsrc_t rs, unsigned voff, unsigned soff) { return __builtin_amdgcn_raw_buffer_load_b64(rs, voff, soff, SC1); }
DEVI void gst(__amdgpu_buffer_rsrc_t rs, u32x4 v, unsigned voff, unsigned soff) { __builtin_amdgcn_raw_buffer_store_b128(v, rs, voff + soff, 0, SC1); }
DEVI void gst8(__amdgpu_buffer_rsrc_t rs, u32x2 v, unsigned voff, unsigned soff) { __builtin_amdgcn_raw_buffer_store_b64(v, rs, voff, soff, SC1); }
#define WAIT_VM(N) asm volatile("s_waitcnt vmcnt(" #N ")" ::: "memory")
#define WAIT_LGKM() asm volatile("s_waitcnt lgkmcnt(0)" ::: "memory")
#define WAIT_BAR(N) asm volatile("s_waitcnt vmcnt(" #N ") lgkmcnt(0)\n\ts_barrier" ::: "memory")
#define LBAR() asm volatile("s_waitcnt lgkmcnt(0)\n\ts_barrier" ::: "memory")
#define SB() __builtin_amdgcn_sched_barrier(0)
#define PIN(x) asm volatile("" : "+v"(x))
DEVI float fma1(float a, float b, float c) { return __builtin_fmaf(a, b, c); }
DEVI float mul1(float a, float b) { return a * b; }
DEVI float max3(float a, float b, float c) { return __builtin_fmaxf(__builtin_fmaxf(a, b), c); }
template <int... Is, class F> DEVI void sfor_impl(std::integer_sequence<int, Is...>, F&& f) { (f(std::integral_constant<int, Is>{}), ...); }
template <int N, class F> DEVI void sfor(F&& f) { sfor_impl(std::make_integer_sequence<int, N>{}, f); }
#define AI __attribute__((always_inline))
#define IC(n) std::integral_constant<int, n>{}

DEVI void dma1(const char* sbase, unsigned voff, unsigned lds) {
    asm volatile("s_mov_b32 m0, %2\n\ts_nop 0\n\tglobal_load_lds_dwordx4 %0, %1" :: "v"(voff), "s"(sbase), "s"(lds) : "memory");
}
DEVI void touch(const char* sbase, unsigned voff, unsigned lds) {
    asm volatile("s_mov_b32 m0, %2\n\ts_nop 0\n\tglobal_load_lds_dword %0, %1" :: "v"(voff), "s"(sbase), "s"(lds) : "memory");
}
struct WSrc { const char* base; unsigned step, voff; };
DEVI void wpiece(const WSrc& s, unsigned lds, int n) { dma1(s.base + (size_t)n * s.step, s.voff, lds + n * 1024); }
DEVI WSrc wsrc_a(const char* base, unsigned rs, int nt, int lane) { const int t = (lane >> 4) < nt ? (lane >> 4) : nt - 1; return WSrc{base, 4 * rs, ((lane >> 2) & 3) * rs + t * 64 + (lane & 3) * 16}; }
DEVI WSrc wsrc_b(const char* base, unsigned rs, int lane) { return WSrc{base, 8 * rs, (4 * (lane >> 5) + ((lane >> 2) & 3)) * rs + ((lane >> 4) & 1) * 64 + (lane & 3) * 16}; }

DEVI void wait_flags(const char* ws, unsigned foff, int n, unsigned e, int lane) {
    const GAS unsigned* p = (const GAS unsigned*)(ws + foff) + (lane < n ? lane : 0) * 32;
#pragma nounroll
    for (unsigned sp = 0; sp < SPIN_CAP; ++sp) {
        unsigned v = __hip_atomic_load(p, __ATOMIC_RELAXED, __HIP_MEMORY_SCOPE_AGENT);
        if (wall(v == e)) break;
    }
    __builtin_amdgcn_fence(__ATOMIC_ACQUIRE, "wavefront");
}
DEVI void set_flag(char* ws, unsigned foff, unsigned e) { __hip_atomic_store((GAS unsigned*)(ws + foff), e, __ATOMIC_RELAXED, __HIP_MEMORY_SCOPE_AGENT); }
DEVI unsigned flag_off(int ph, int idx) { return WS_FLAG + (ph * 256 + idx) * 128; }

constexpr float GC0 = -2.f * 0.7978845608028654f * 1.4426950408889634f, GC1 = GC0 * 0.044715f;
DEVI float gelu_t(float x) {
    float z = x * __builtin_fmaf(x * x, GC1, GC0);
    return x * __builtin_amdgcn_rcpf(1.f + __builtin_amdgcn_exp2f(z));
}
struct GeluK { float nmean, k1, k0, irs; };
DEVI void gelu_step(int o, float& y, float& z, float c, const GeluK& g) {
    if (o == 0) y = fma1(g.nmean, c, y);
    else if (o == 1) z = mul1(y, y);
    else if (o == 2) z = fma1(z, g.k1, g.k0);
    else if (o == 3) z = mul1(y, z);
    else if (o == 4) z = __builtin_amdgcn_exp2f(z);
    else if (o == 5) z = fma1(z, g.irs, g.irs);
    else if (o == 6) z = __builtin_amdgcn_rcpf(z);
    else y = mul1(y, z);
}

#define STAMP(k) do {} while (0)
#define STAMP_DECL
#define STAMP_FLUSH() do {} while (0)

#define LANE_GEOM() int lane_ = threadIdx.x & 63; asm volatile("" : "+v"(lane_)); const int lane = lane_, r32 = lane & 31, hh = lane >> 5; \
    const int trq = (lane & 15) >> 2, trQ_ = ((lane >> 4) & 1) * 4 + (lane & 3), gq = (trQ_ & 4) | ((trQ_ & 1) << 1) | ((trQ_ >> 1) & 1); \
    const unsigned vx = (hh * XR + r32) * 16; (void)trq; (void)gq; (void)vx; (void)r32; (void)hh
constexpr unsigned L_FLG = L_MISC + 1920, F_RDY = 0, F_DONE = 16;
DEVI unsigned ldsw_ld(unsigned i) { return *(volatile LAS unsigned*)(uintptr_t)(L_FLG + 4 * i); }
DEVI u32x2 ldsw_ld2(unsigned i) { return *(volatile LAS u32x2*)(uintptr_t)(L_FLG + 4 * i); }
DEVI void ldsw_st(unsigned i, unsigned v) { *(volatile LAS unsigned*)(uintptr_t)(L_FLG + 4 * i) = v; }
DEVI void ring_wait(int c) {
#pragma nounroll
    for (unsigned sp = 0; sp < SPIN_CAP * 16u; ++sp) { const u32x2 v = ldsw_ld2(F_RDY + 2 * c); if (__builtin_amdgcn_readfirstlane(v.x & v.y)) break; __builtin_amdgcn_s_sleep(1); }
}
DEVI void slot_wait(unsigned n, int lane) {
#pragma nounroll
    for (unsigned sp = 0; sp < SPIN_CAP * 16u; ++sp) { const unsigned v = ldsw_ld(F_DONE + (lane < 6 ? lane : 0)); if (wall(v >= n)) break; __builtin_amdgcn_s_sleep(1); }
}
#define LB(N) asm volatile("s_waitcnt vmcnt(" #N ")\n\ts_barrier" ::: "memory")
#define BAR() asm volatile("s_barrier" ::: "memory")

__global__ __launch_bounds__(512, 2) void fused_fwd(Params P) {
    __shared__ __attribute__((aligned(16))) char smem[163840];
    const unsigned L0 = (unsigned)(uintptr_t)(LAS char*)smem;
    const int tid = threadIdx.x, wv = __builtin_amdgcn_readfirstlane(tid >> 6), w = wv & 3;
    const int b = blockIdx.x, xcd = b & 7, slot = b >> 3;
    const int m = 2 * xcd + (slot >> 4), h = slot & 15;
    const int g = slot & 3, nt = g < 2 ? 4 : 3, tb = g < 2 ? 4 * g : 3 * g + 2;
    const int ti = 8 * xcd + (slot >> 2);
    const int s16 = slot & 15, pr = s16 >> 1;
    const int nt6 = ((s16 ^ pr) & 1) ? 3 : 4, tb6 = 7 * pr + ((s16 & 1) ? (pr & 1 ? 3 : 4) : 0);
    const bool act7 = slot < 28;
    const int j7 = 4 * xcd + (act7 ? slot / 7 : 0), g7 = act7 ? slot % 7 : 0;
    const unsigned wst = L0 + w * L_WST;
    const unsigned shift = (56 * h) & 15u;
    STAMP_DECL

    if (wv >= 4) {
        LANE_GEOM();
        const unsigned wrd = 16384 + hh * 6 * 256 + trq * 64 + ((shift + gq * 8) & 63);
        LAS float* csp = (LAS float*)(uintptr_t)(L0 + L_MISC);
        if (w < 2) {
            const unsigned xoffE = (lane >> 3) * 896 + (((lane & 7) ^ (lane >> 4)) << 4);
            const unsigned xoffO = (lane >> 3) * 896 + (((lane & 7) ^ (4 + (lane >> 4))) << 4);
            const unsigned colb = (56 * h) & ~15u;
            unsigned woff[3];
#pragma unroll
            for (int j = 0; j < 3; ++j) {
                const int bl = 4 * j + (lane >> 4), kql = bl / 3, tile = bl % 3;
                woff[j] = (4 * kql + ((lane >> 2) & 3)) * 2688 + tile * 896 + colb + (lane & 3) * 16;
            }
            const char* xb = (const char*)P.x + (size_t)(128 * m + 64 * w) * 896;
            const char* wb = (const char*)P.wqkv + (size_t)(32 * w) * 2688;
            LBAR();
#pragma unroll 1
            for (int kc = 0; kc < 7; ++kc) {
                if (kc >= 5) slot_wait(kc - 4, lane);
                const unsigned base = L0 + (kc >= 5 ? kc - 5 : kc) * L_SLOT;
#pragma unroll
                for (int i = 0; i < 6; ++i) dma1(wb + (size_t)kc * 64 * 2688 + (i / 3) * 16 * 2688, woff[i % 3], base + 16384 + (6 * w + i) * 1024);
#pragma unroll
                for (int i = 0; i < 8; ++i) dma1(xb + kc * 128 + i * 8 * 896, (i & 1) ? xoffO : xoffE, base + (8 * w + i) * 1024);
                if (kc > 0) { WAIT_VM(14); ldsw_st(F_RDY + 2 * (kc - 1) + w, 1); }
            }
            WAIT_VM(0); ldsw_st(F_RDY + 12 + w, 1);
        } else {
            const int srow = 128 * m + 8 * h + 4 * (w - 2) + (lane >> 4);
            const char* sp = (const char*)P.x + (size_t)srow * 896 + (lane & 15) * 16;
            u32x4 sr[4];
            asm volatile("global_load_dwordx4 %0, %4, off\n\tglobal_load_dwordx4 %1, %4, off offset:256\n\tglobal_load_dwordx4 %2, %4, off offset:512\n\tglobal_load_dwordx4 %3, %5, off"
                         : "=&v"(sr[0]), "=&v"(sr[1]), "=&v"(sr[2]), "=&v"(sr[3]) : "v"(sp), "v"(sp + ((lane & 15) < 8 ? 768 : 0)) : "memory");
            if (w == 2 && lane < 32) ldsw_st(lane, 0);
            LBAR();
            {
                asm volatile("s_waitcnt vmcnt(0)" : "+v"(sr[0]), "+v"(sr[1]), "+v"(sr[2]), "+v"(sr[3]) :: "memory");
                if ((lane & 15) >= 8) sr[3] = (u32x4){0u, 0u, 0u, 0u};
                float t1 = 0.f, t2 = 0.f;
#pragma unroll
                for (int q = 0; q < 4; ++q)
#pragma unroll
                    for (int i = 0; i < 4; ++i) { t1 = dot2(sr[q][i], ONES, t1); t2 = dot2(sr[q][i], sr[q][i], t2); }
                t1 += xor32<1>(t1); t2 += xor32<1>(t2); t1 += xor32<2>(t1); t2 += xor32<2>(t2); t1 += xor32<4>(t1); t2 += xor32<4>(t2); t1 += xor32<8>(t1); t2 += xor32<8>(t2);
                const float mean = t1 * (1.f / D), var = __builtin_fmaxf(t2 * (1.f / D) - mean * mean, 0.f);
                const unsigned e = *(const unsigned*)(P.ws + WS_EP) + 1u;
                const __amdgpu_buffer_rsrc_t rs = __builtin_amdgcn_make_buffer_rsrc((void*)P.ws, 0, WS_END, 0x00020000);
                if ((lane & 15) == 0) gst(rs, (u32x4){f2u(mean), e, f2u(__builtin_amdgcn_rsqf(var + EPS)), e}, srow * 16, WS_ST1);
            }
            float cs[3][4] = {};
            const unsigned wrd2 = wrd + (w - 2) * 2 * 3072;
#pragma unroll 1
            for (int c = 0; c < 7; ++c) {
                ring_wait(c);
                if (c == LATE_TOUCH) {
                    unsigned long long pc; unsigned back;
                    asm volatile("s_getpc_b64 %0\nmk_code_here:\n\ts_mov_b32 %1, mk_code_here-_ZN2mk9fused_fwdENS_6ParamsE" : "=s"(pc), "=s"(back));
                    constexpr unsigned len = CODE_TOUCH_BYTES;
                    const unsigned oc = (slot * 18 + (lane < 18 ? lane : 0)) * 128, o3 = (b * 64 + lane) * 128, o2 = (b * 16 + (lane & 15)) * 128;
                    const char* t0 = w == 2 ? (const char*)(pc - back) + (oc < len ? oc : 0) : (const char*)P.w3 + (o3 < 1605632 ? o3 : 0);
                    const char* t1 = w == 2 ? (lane < 16 ? (const char*)P.wo : lane < 32 ? (const char*)P.w1 : (const char*)P.w2) + (o2 < 401408 ? o2 : 0) : (const char*)P.w4 + (o3 < 1605632 ? o3 : 0);
                    const unsigned oo = (slot * 14 + (lane < 14 ? lane : 0)) * 4096, li = slot * 16 + ((lane - 16) & 15);
                    const unsigned wo_ = li < 64 ? WS_O + li * XROW + 32768 : li < 456 ? WS_H + (li - 64) * XROW + 32768 : li < 472 ? WS_KV + (li - 456) * KVH + 262144 : WS_SPARE;
                    const char* t2 = (TOUCH_OUT && w == 2 && lane < 16) ? (const char*)P.out + oo : P.ws + ((TOUCH_WS && w == 2 && lane >= 16 && lane < 32) ? wo_ : WS_SPARE);
                    asm volatile("s_mov_b32 m0, %3\n\ts_nop 0\n\tglobal_load_lds_dword %0, off\n\tglobal_load_lds_dword %1, off\n\tglobal_load_lds_dword %2, off" :: "v"(t0), "v"(t1), "v"(t2), "s"(L0 + L_MISC + 2048) : "memory");
                }
                const unsigned a = L0 + (c >= 5 ? c - 5 : c) * L_SLOT + wrd2;
#pragma unroll
                for (int ks = 0; ks < 2; ++ks)
#pragma unroll
                    for (int t = 0; t < 3; ++t) { const u32x4 f = wfrag(a + ks * 3072 + t * 256, a + ks * 3072 + 768 + t * 256);
#pragma unroll
                        for (int i = 0; i < 4; ++i) cs[t][i] = dot2(f[i], ONES, cs[t][i]); }
                WAIT_LGKM();
                ldsw_st(F_DONE + 2 + w, c + 1);
            }
#pragma unroll
            for (int t = 0; t < 3; ++t) { const float v = hsum((cs[t][0] + cs[t][1]) + (cs[t][2] + cs[t][3])); if (hh == 0) csp[(w - 2) * 96 + t * 32 + r32] = v; }
            WAIT_VM(0);
            WAIT_LGKM();
            ldsw_st(F_DONE + 8 + w, 1);
            if (w == 2) {
#pragma nounroll
                for (unsigned sp2 = 0; sp2 < SPIN_CAP * 16u && __builtin_amdgcn_readfirstlane(ldsw_ld(F_DONE + 11)) == 0; ++sp2) __builtin_amdgcn_s_sleep(1);
                if (hh == 0) {
#pragma unroll
                    for (int t = 0; t < 3; ++t) csp[384 + t * 32 + r32] = csp[t * 32 + r32] + csp[96 + t * 32 + r32];
                }
            }
        }
        LBAR();
        auto stage = [&](const WSrc& s, unsigned lds, int n0, int n1) AI {
#pragma unroll 1
            for (int n = n0; n < n1; ++n) wpiece(s, lds, n);
        };
        stage(wsrc_a((const char*)P.wo + (size_t)(112 * w) * 896 + tb * 64, 896, nt, lane), wst, 0, 28);
        BAR();
        BAR();
        LB(0);
        BAR(); stage(wsrc_a((const char*)P.w1 + (size_t)(112 * w) * 896 + tb * 64, 896, nt, lane), wst, 0, 28); BAR(); LB(0);
        BAR(); stage(wsrc_a((const char*)P.w2 + (size_t)(112 * w) * 896 + tb * 64, 896, nt, lane), wst, 0, 28); BAR(); LB(0);
        BAR(); stage(wsrc_a((const char*)P.w3 + (size_t)(112 * w) * 3584 + tb6 * 64, 3584, nt6, lane), wst, 0, 28); BAR(); LB(0);
        const WSrc w4s = wsrc_b((const char*)P.w4 + (size_t)(448 * w) * 896 + g7 * 128, 896, lane);
        if (act7) stage(w4s, L0 + L_X + w * 8192, 0, 8);
        {
            float c[4] = {0.f, 0.f, 0.f, 0.f};
            const unsigned a0 = L0 + trq * 64 + gq * 8 + 2 * hh * 1024 + w * 256;
#pragma unroll 4
            for (int k = 0; k < 28; ++k) { const unsigned a = a0 + k * 4096; const u32x4 f = wfrag(a, a + 1024);
#pragma unroll
                for (int i = 0; i < 4; ++i) c[i] = dot2(f[i], ONES, c[i]); }
            const float cc = hsum((c[0] + c[1]) + (c[2] + c[3]));
            if (hh == 0) ((LAS float*)(uintptr_t)(L0 + L_M2 + 1024))[w * 32 + r32] = cc;
        }
        LBAR();
        if (b == 0 && w == 3) {
            const unsigned e = *(const volatile GAS unsigned*)(P.ws + WS_EP) + 1u;
            const GAS unsigned* p = (const GAS unsigned*)(P.ws + flag_off(0, lane));
#pragma nounroll
            for (unsigned sp = 0; sp < SPIN_CAP; ++sp) {
                const unsigned v0 = __hip_atomic_load(p, __ATOMIC_RELAXED, __HIP_MEMORY_SCOPE_AGENT), v1 = __hip_atomic_load(p + 64 * 32, __ATOMIC_RELAXED, __HIP_MEMORY_SCOPE_AGENT);
                const unsigned v2 = __hip_atomic_load(p + 128 * 32, __ATOMIC_RELAXED, __HIP_MEMORY_SCOPE_AGENT), v3 = __hip_atomic_load(p + 192 * 32, __ATOMIC_RELAXED, __HIP_MEMORY_SCOPE_AGENT);
                if (wall(v0 == e && v1 == e && v2 == e && v3 == e)) break;
            }
            if (lane == 0) __hip_atomic_store((GAS unsigned*)(P.ws + WS_EP), e, __ATOMIC_RELAXED, __HIP_MEMORY_SCOPE_AGENT);
        }
        BAR();
        if (act7) stage(w4s, wst - 8 * 1024, 8, 36);
        LB(0);
        if (act7) {
            BAR();
            stage(w4s, wst - 36 * 1024, 36, 56);
            LB(0);
            BAR();
        }
        STAMP_FLUSH();
        return;
    }

    const unsigned e = *(const unsigned*)(P.ws + WS_EP) + 1u;
    const __amdgpu_buffer_rsrc_t rs = __builtin_amdgcn_make_buffer_rsrc((void*)P.ws, 0, WS_END, 0x00020000);
    u32x4 qf0, qf1;
    {
        LANE_GEOM();
        const unsigned wrd = 16384 + hh * 6 * 256 + trq * 64 + ((shift + gq * 8) & 63);
        STAMP(15);
        f32x16 aq = {}, ak = {}, av = {};
        const unsigned xrd = (32 * w + r32) * 128 + ((hh ^ ((r32 >> 1) & 7)) << 4);
        struct Fr { u32x4 x, q, k, v; };
        Fr F[4];
        unsigned xb[3][4], wb3[3];
#pragma unroll
        for (int gsl = 0; gsl < 3; ++gsl) {
#pragma unroll
            for (int j = 0; j < 4; ++j) { xb[gsl][j] = L0 + (xrd ^ (j << 5)) + gsl * 2 * L_SLOT; PIN(xb[gsl][j]); }
            wb3[gsl] = L0 + wrd + gsl * 2 * L_SLOT; PIN(wb3[gsl]);
        }
        auto ldx = [&](int sl, int j) AI { F[j].x = lds128(xb[sl / 2][j] + (sl & 1) * L_SLOT); const unsigned a = wb3[sl / 2] + (sl & 1) * L_SLOT + j * 3072; F[j].q = wfrag(a, a + 768); };
        auto ldk = [&](int sl, int j) AI { const unsigned a = wb3[sl / 2] + (sl & 1) * L_SLOT + j * 3072; F[j].k = wfrag(a + 256, a + 1024); };
        auto ldv = [&](int sl, int j) AI { const unsigned a = wb3[sl / 2] + (sl & 1) * L_SLOT + j * 3072; F[j].v = wfrag(a + 512, a + 1280); };
        LAS float* stw = (LAS float*)(uintptr_t)(L0 + L_MISC + 3072 + w * 256);
        BAR();
        ring_wait(0);
        STAMP(16);
#pragma unroll
        for (int j = 0; j < 2; ++j) { ldx(0, j); ldk(0, j); ldv(0, j); }
        SB();
        u32x4 gv = {0u, 0u, 0u, 0u};
        const unsigned gvo = (128 * m + 32 * w + r32) * 16;
        sfor<7>([&](auto cc) AI {
            constexpr int c = decltype(cc)::value, sl = c >= 5 ? c - 5 : c, nsl = c >= 4 ? c - 4 : c + 1;
            u32x2 rdy;
            sfor<4>([&](auto jc) AI {
                constexpr int j = decltype(jc)::value, jn = (j + 2) & 3, lb = j < 2 ? sl : nsl;
                constexpr bool ld = j < 2 || c < 6;
                if constexpr (j == 2 && c < 6) { if (__builtin_amdgcn_readfirstlane(rdy.x & rdy.y) == 0) ring_wait(c + 1); SB(); }
                MFMA(aq, F[j].q, F[j].x, aq);
                if constexpr (ld) ldx(lb, jn);
                SB();
                MFMA(ak, F[j].k, F[j].x, ak);
                if constexpr (ld) ldk(lb, jn);
                if constexpr (j == 0 && c >= 1 && c <= 2) ldsw_st(F_DONE + w, c);
                if constexpr (j == 0 && c == 4) gv = gld(rs, gvo, WS_ST1);
                SB();
                MFMA(av, F[j].x, F[j].v, av);
                if constexpr (ld) ldv(lb, jn);
                if constexpr (j == 0 && c < 6) rdy = ldsw_ld2(F_RDY + 2 * (c + 1));
                SB();
            });
        });
#pragma nounroll
        for (unsigned sp2 = 0; sp2 < SPIN_CAP && !wall(gv.y == e && gv.w == e); ++sp2) gv = gld(rs, gvo, WS_ST1);
        if (hh == 0) { stw[r32] = u2f(gv.x) * u2f(gv.z); stw[32 + r32] = u2f(gv.z); }
        STAMP(1);
        const float mean = u2f(gv.x), rstd = u2f(gv.z);
        LBAR();
        const LAS float* csf = (const LAS float*)(uintptr_t)(L0 + L_MISC + 1536);
        const float cv = csf[64 + r32];
        const float qs = rstd * (0.18898223650461362f * 1.4426950408889634f);
#pragma unroll
        for (int q = 0; q < 4; ++q) {
            const f32x4 c1 = *(const LAS f32x4*)(csf + 8 * q + 4 * hh), c2 = *(const LAS f32x4*)(csf + 32 + 8 * q + 4 * hh);
            const f32x4 mr = *(const LAS f32x4*)(stw + 8 * q + 4 * hh), rr = *(const LAS f32x4*)(stw + 32 + 8 * q + 4 * hh);
#pragma unroll
            for (int i = 0; i < 4; ++i) {
                aq[4 * q + i] = (aq[4 * q + i] - mean * c1[i]) * qs;
                ak[4 * q + i] = (ak[4 * q + i] - mean * c2[i]) * rstd;
                av[4 * q + i] = av[4 * q + i] * rr[i] - mr[i] * cv;
            }
        }
        if (hh == 1) { aq[12] = 1.f; aq[13] = 0.f; aq[14] = 0.f; aq[15] = 0.f; ak[12] = 0.f; ak[13] = 0.f; ak[14] = 0.f; ak[15] = 0.f; }
        if (r32 >= 28) {
            const float pv = r32 == 28 ? 1.f : 0.f;
#pragma unroll
            for (int i = 0; i < 16; ++i) av[i] = pv;
        }
        const u32x4 k0 = pack8(ak, 0), k1 = pack8(ak, 1), v0 = pack8(av, 0), v1 = pack8(av, 1);
        const unsigned go = WS_KV + h * KVH + (4 * m + w) * 4096;
        gst(rs, k0, lane * 16, go); gst(rs, k1, lane * 16, go + 1024); gst(rs, v0, lane * 16, go + 2048); gst(rs, v1, lane * 16, go + 3072);
        const unsigned lo = L0 + L_OWNKV + w * 4096 + lane * 16;
        sts128(lo, k0); sts128(lo + 1024, k1); sts128(lo + 2048, v0); sts128(lo + 3072, v1);
        qf0 = pack8(aq, 0); qf1 = pack8(aq, 1);
        WAIT_BAR(0);
        if (tid == 0) set_flag(P.ws, flag_off(0, m * 16 + h), e);
        STAMP(2);
    }
    {
        LANE_GEOM();
        const int mt = m - 2 + (w >> 1);
        u32x4 kv[8];
        if (mt >= 0) {
            wait_flags(P.ws, flag_off(0, mt * 16 + h), 1, e, lane);
            const unsigned go = WS_KV + h * KVH + (4 * m - 8 + 2 * w) * 4096;
#pragma unroll
            for (int i = 0; i < 8; ++i) kv[i] = gld(rs, lane * 16, go + i * 1024);
        } else {
#pragma unroll
            for (int i = 0; i < 8; ++i) kv[i] = (u32x4){0u, 0u, (i & 3) == 1 && hh == 1 ? 0xF149u : 0u, 0u};
        }
        f32x16 cdiag, cold;
#pragma unroll
        for (int i = 0; i < 16; ++i) { const int kr = (i & 3) + 8 * (i >> 2) + 4 * hh; cdiag[i] = kr <= r32 ? 0.f : -1e30f; cold[i] = kr > r32 ? 0.f : -1e30f; }
#pragma unroll
        for (int i = 0; i < 8; ++i) sts128(L0 + L_KV + 2 * w * 4096 + i * 1024 + lane * 16, kv[i]);
        LBAR();
        STAMP(3);
        unsigned kvb = L0 + L_KV + w * 4096 + lane * 16;
        PIN(kvb);
        u32x4 kf[9][2], vf[9][2];
        const f32x16 zero16 = {};
        auto ldK = [&](int i) AI { kf[i][0] = lds128(kvb + i * 4096); kf[i][1] = lds128(kvb + i * 4096 + 1024); };
        auto ldV = [&](int i) AI { vf[i][0] = lds128(kvb + i * 4096 + 2048); vf[i][1] = lds128(kvb + i * 4096 + 3072); };
        auto smax = [&](const f32x16& s, float a) AI {
#pragma unroll
            for (int i = 0; i < 16; i += 2) a = max3(a, s[i], s[i + 1]);
            return a; };
        ldK(0); ldK(1);
        float mx = -3e38f;
        f32x16 s[2];
        auto smax8 = [&](const f32x16& v, int o, float a) AI {
#pragma unroll
            for (int i = 0; i < 8; i += 2) a = max3(a, v[o + i], v[o + i + 1]);
            return a; };
        sfor<9>([&](auto ic) AI {
            constexpr int i = decltype(ic)::value;
            if constexpr (i + 2 < 9) ldK(i + 2);
            MFMA(s[i & 1], kf[i][0], qf0, i == 0 ? cold : i == 8 ? cdiag : zero16);
            SB();
            MFMA(s[i & 1], kf[i][1], qf1, s[i & 1]);
            SB();
            if constexpr (i > 0) { mx = smax8(s[(i - 1) & 1], 0, mx); mx = smax8(s[(i - 1) & 1], 8, mx); PIN(mx); }
            SB();
        });
        ldK(0); ldK(1); ldV(0);
        mx = hmax(smax(s[0], mx));
        STAMP(25);
        f32x16 o = {}, cm;
#pragma unroll
        for (int i = 0; i < 16; ++i) { cm[i] = -mx; cdiag[i] -= mx; cold[i] -= mx; }
        MFMA(s[0], kf[0][0], qf0, cold); MFMA(s[0], kf[0][1], qf1, s[0]);
        SB();
        sfor<9>([&](auto ic) AI {
            constexpr int i = decltype(ic)::value;
            f32x16& c = s[i & 1];
            f32x16& n = s[(i + 1) & 1];
            if constexpr (i + 2 < 9) ldK(i + 2);
            if constexpr (i + 1 < 9) ldV(i + 1);
            if constexpr (i + 1 < 9) MFMA(n, kf[i + 1][0], qf0, i + 1 == 8 ? cdiag : cm);
#pragma unroll
            for (int j = 0; j < 8; ++j) c[j] = __builtin_amdgcn_exp2f(c[j]);
            u32x4 p0 = pack8(c, 0);
            PIN(p0);
            SB();
            if constexpr (i + 1 < 9) MFMA(n, kf[i + 1][1], qf1, n);
            MFMA(o, vf[i][0], p0, o);
#pragma unroll
            for (int j = 8; j < 16; ++j) c[j] = __builtin_amdgcn_exp2f(c[j]);
            const u32x4 p1 = pack8(c, 1);
            MFMA(o, vf[i][1], p1, o);
            SB();
        });
        STAMP(26);
        auto lr = __builtin_amdgcn_permlane32_swap(f2u(o[12]), f2u(o[12]), false, false);
        const float inv = __builtin_amdgcn_rcpf(u2f(lr[1]));
#pragma unroll
        for (int i = 0; i < 16; ++i) o[i] *= inv;
        if (hh == 1) { o[12] = 0.f; o[13] = 0.f; o[14] = 0.f; o[15] = 0.f; }
        const unsigned vo = (hh * XR + r32) * 16, so = WS_O + ((4 * h) * XR + 128 * m + 32 * w) * 16;
        gst(rs, pack8(o, 0), vo, so); gst(rs, pack8(o, 1), vo, so + 2 * XROW);
        WAIT_BAR(0);
        if (tid == 0) set_flag(P.ws, flag_off(1, m * 16 + h), e);
        STAMP(4);
    }
    const bool act = w < nt;
    const int tw = tb + w;
#define GEMM_GEOM() LANE_GEOM(); \
    unsigned trW[2]; trW[0] = L0 + (w < nt ? w : nt - 1) * 256 + trq * 64 + gq * 8 + 2 * hh * 1024; trW[1] = trW[0] + 57344; PIN(trW[0]); PIN(trW[1]); \
    auto wimg = [&](int kq0, int kq1) AI { return wfrag(kq0 < 56 ? trW[0] + kq0 * 1024 : trW[1] + (kq0 - 56) * 1024, kq1 < 56 ? trW[0] + kq1 * 1024 : trW[1] + (kq1 - 56) * 1024); }; \
    unsigned xl = L0 + L_X + lane * 16; PIN(xl); \
    LAS float* m2 = (LAS float*)(uintptr_t)(L0 + L_M2 + w * 256); (void)m2
    auto gemm = [&](auto nk, const u32x4* wr, unsigned xl) AI -> f32x16 {
        constexpr int NK = decltype(nk)::value, PD = 4;
        f32x16 acc = {};
        u32x4 xf[NK];
#pragma unroll
        for (int k = 0; k < PD; ++k) xf[k] = lds128(xl + k * 1024);
        SB();
        sfor<NK>([&](auto kc) AI {
            constexpr int k = decltype(kc)::value;
            if constexpr (k + PD < NK) xf[k + PD] = lds128(xl + (k + PD) * 1024);
            MFMA(acc, wr[k], xf[k], acc);
            SB();
        });
        return acc;
    };
    u32x4 hp[2];
    float meanH, rstdH;
    {
        GEMM_GEOM();
        u32x4 xr[2];
        const char* xp = (const char*)P.x + (size_t)(32 * ti + r32) * 896 + 64 * (act ? tw : tb) + 16 * hh;
        xr[0] = *(const GAS u32x4*)xp; xr[1] = *(const GAS u32x4*)(xp + 32);
        u32x4 wr[32];
        sfor<32>([&](auto kc) AI { constexpr int k = decltype(kc)::value, kq = 7 * (k >> 1) + 4 * (k & 1);
            if constexpr (k < 31) wr[k] = wimg(kq, kq + 1);
            else { const u32x2 p = ldstr(trW[1] + (kq - 56) * 1024), q = ldstr(trW[1] + (kq - 56) * 1024 + (hh ? 0 : 1024)); wr[k] = (u32x4){p.x, p.y, q.x, q.y}; } });
        LBAR();
        wait_flags(P.ws, flag_off(1, (ti >> 2) * 16 + 4 * w), 4, e, lane);
        u32x4 xf[8];
#pragma unroll
        for (int k = 0; k < 8; ++k) xf[k] = gld(rs, vx, WS_O + ((16 * w + 2 * k) * XR + 32 * ti) * 16);
#pragma unroll
        for (int k = 0; k < 8; ++k) sts128(xl + (8 * w + k) * 1024, xf[k]);
        LBAR();
        STAMP(5);
        f32x16 r = gemm(IC(32), wr, xl);
        float s1 = 0.f, s2 = 0.f;
#pragma unroll
        for (int s = 0; s < 2; ++s) {
#pragma unroll
            for (int i = 0; i < 4; ++i) {
                hp[s][i] = pk(r[8 * s + 2 * i] + blo(xr[s][i]), r[8 * s + 2 * i + 1] + bhi(xr[s][i]));
                s1 = dot2(hp[s][i], ONES, s1); s2 = dot2(hp[s][i], hp[s][i], s2);
            }
            if (act) gst(rs, hp[s], vx, WS_H + ((4 * tw + 2 * s) * XR + 32 * ti) * 16);
        }
        s1 = hsum(s1); s2 = hsum(s2);
        if (act && hh == 0) gst8(rs, (u32x2){f2u(s1), f2u(s2)}, r32 * 8, WS_STH + (tw * 2048 + 32 * ti) * 8);
        WAIT_BAR(0);
        if (tid == 0) set_flag(P.ws, flag_off(2, ti * 4 + g), e);
        STAMP(6);
    }
#define LOAD_WR() u32x4 wr[28]; sfor<28>([&](auto kc) AI { constexpr int k = decltype(kc)::value; wr[k] = wimg(4 * k, 4 * k + 1); })
    const int fsn = w == 0 ? 1 : 2, fsl = w <= 1 ? 0 : w - 1;
    {
        GEMM_GEOM();
        LOAD_WR();
        LBAR();
        {
            float cs[4] = {0.f, 0.f, 0.f, 0.f};
#pragma unroll
            for (int k = 0; k < 28; ++k)
#pragma unroll
                for (int i = 0; i < 4; ++i) cs[i] = dot2(wr[k][i], ONES, cs[i]);
            const float c = hsum((cs[0] + cs[1]) + (cs[2] + cs[3]));
            if (hh == 0) m2[r32] = c;
        }
        wait_flags(P.ws, flag_off(2, ti * 4), 4, e, lane);
        u32x4 xf[7];
#pragma unroll
        for (int k = 0; k < 7; ++k) xf[k] = gld(rs, vx, WS_H + ((14 * w + 2 * k) * XR + 32 * ti) * 16);
        u32x2 sv[7];
#pragma unroll
        for (int q = 0; q < 7; ++q) sv[q] = gld8(rs, (7 * hh * 2048 + r32) * 8, WS_STH + (q * 2048 + 32 * ti) * 8);
#pragma unroll
        for (int k = 0; k < 7; ++k) sts128(xl + (7 * w + k) * 1024, xf[k]);
        LBAR();
        STAMP(7);
        f32x16 r = gemm(IC(28), wr, xl);
        float t1 = 0.f, t2 = 0.f;
#pragma unroll
        for (int q = 0; q < 7; ++q) { t1 += u2f(sv[q].x); t2 += u2f(sv[q].y); }
        t1 = hsum(t1); t2 = hsum(t2);
        meanH = t1 * (1.f / D);
        const float var = __builtin_fmaxf(t2 * (1.f / D) - meanH * meanH, 0.f);
        rstdH = __builtin_amdgcn_rsqf(var + EPS);
        const float sc = rstdH * __builtin_amdgcn_rsqf(var * rstdH * rstdH + EPS);
#pragma unroll
        for (int q = 0; q < 4; ++q) {
            const f32x4 c = *(const LAS f32x4*)(m2 + 8 * q + 4 * hh);
#pragma unroll
            for (int i = 0; i < 4; ++i) r[4 * q + i] = gelu_t((r[4 * q + i] - meanH * c[i]) * sc);
        }
#pragma unroll
        for (int s = 0; s < 2; ++s) if (act) gst(rs, pack8(r, s), vx, WS_HH + ((4 * tw + 2 * s) * XR + 32 * ti) * 16);
        WAIT_BAR(0);
        if (tid == 0) set_flag(P.ws, flag_off(3, ti * 4 + g), e);
        STAMP(8);
    }
    {
        GEMM_GEOM();
        LOAD_WR();
        LBAR();
        const int sh = w == 0 ? -1 : w == 1 ? 1 : w == 2 ? -2 : 2;
        const int tn = ti + (sh < 0 ? -1 : 1);
        {
            const bool nb = tn >= 0 && tn < 64;
            const int li = lane & 3;
            const GAS unsigned* p = (const GAS unsigned*)(P.ws + flag_off(3, (((li & 2) && nb) ? tn : ti) * 4 + fsl + ((li & 1) && fsn == 2 ? 1 : 0)));
#pragma nounroll
            for (unsigned sp = 0; sp < SPIN_CAP; ++sp) { unsigned v = __hip_atomic_load(p, __ATOMIC_RELAXED, __HIP_MEMORY_SCOPE_AGENT); if (wall(v == e)) break; }
            __builtin_amdgcn_fence(__ATOMIC_ACQUIRE, "wavefront");
        }
        const int tok = 32 * ti + r32 + sh;
        const bool ok = tok >= 0 && tok < S;
        const unsigned vxs = ok ? (hh * XR + tok) * 16 : 0x7ffffff0u;
        u32x4 xf[7];
#pragma unroll
        for (int k = 0; k < 7; ++k) { xf[k] = gld(rs, vxs, WS_HH + (14 * w + 2 * k) * XROW); }
#pragma unroll
        for (int k = 0; k < 7; ++k) sts128(xl + (7 * w + k) * 1024, xf[k]);
        LBAR();
        STAMP(9);
        f32x16 r = gemm(IC(28), wr, xl);
        float s1 = 0.f, s2 = 0.f;
#pragma unroll
        for (int s = 0; s < 2; ++s) {
            u32x4 xp;
#pragma unroll
            for (int i = 0; i < 4; ++i) {
                xp[i] = pk(r[8 * s + 2 * i] + (blo(hp[s][i]) - meanH) * rstdH, r[8 * s + 2 * i + 1] + (bhi(hp[s][i]) - meanH) * rstdH);
                s1 = dot2(xp[i], ONES, s1); s2 = dot2(xp[i], xp[i], s2);
            }
            if (act) gst(rs, xp, vx, WS_X1 + ((4 * tw + 2 * s) * XR + 32 * ti) * 16);
        }
        s1 = hsum(s1); s2 = hsum(s2);
        if (act && hh == 0) gst8(rs, (u32x2){f2u(s1), f2u(s2)}, r32 * 8, WS_STX + (tw * 2048 + 32 * ti) * 8);
        WAIT_BAR(0);
        if (tid == 0) set_flag(P.ws, flag_off(4, ti * 4 + g), e);
        STAMP(10);
    }
    {
        LANE_GEOM();
        unsigned tr6[2];
        tr6[0] = L0 + trq * 64 + gq * 8 + 2 * hh * 1024; tr6[1] = tr6[0] + 57344; PIN(tr6[0]); PIN(tr6[1]);
        auto wf6 = [&](int t, int k) AI { const unsigned a = (k < 14 ? tr6[0] + k * 4096 : tr6[1] + (k - 14) * 4096) + t * 256; return wfrag(a, a + 1024); };
        const LAS float* cs6 = (const LAS float*)(uintptr_t)(L0 + L_M2 + 1024);
        BAR();
        wait_flags(P.ws, flag_off(4, (4 * m + w) * 4), 4, e, lane);
        const int tk = 128 * m + 32 * w;
        auto ldx1 = [&](int k) AI { return gld(rs, vx, WS_X1 + (2 * k * XR + tk) * 16); };
        unsigned vx2 = vx; PIN(vx2);
        auto ldx2 = [&](int k) AI { return gld(rs, vx2, WS_X1 + (2 * k * XR + tk) * 16); };
        constexpr int PX = 10, PW = 3;
        f32x16 a0 = {}, a1 = {};
        u32x2 sv[7];
        u32x4 xg[28];
        constexpr int PG = 6;
        {
            u32x4 xf[28], wA[28], wB[28];
#pragma unroll
            for (int k = 0; k < PX; ++k) xf[k] = ldx1(k);
#pragma unroll
            for (int q = 0; q < 7; ++q) sv[q] = gld8(rs, (7 * hh * 2048 + r32) * 8, WS_STX + (q * 2048 + tk) * 8);
            STAMP(11);
#pragma unroll
            for (int k = 0; k < PW; ++k) { wA[k] = wf6(0, k); wB[k] = wf6(1, k); }
            SB();
            sfor<28>([&](auto kc) AI {
                constexpr int k = decltype(kc)::value;
                if constexpr (k + PW < 28) { wA[k + PW] = wf6(0, k + PW); wB[k + PW] = wf6(1, k + PW); }
                MFMA(a0, wA[k], xf[k], a0);
                if constexpr (k + PX < 28) xf[k + PX] = ldx1(k + PX);
                else if constexpr (k + PX - 28 < PG) xg[k + PX - 28] = ldx2(k + PX - 28);
                MFMA(a1, wB[k], xf[k], a1);
                SB();
            });
        }
        float t1 = 0.f, t2 = 0.f;
#pragma unroll
        for (int q = 0; q < 7; ++q) { t1 += u2f(sv[q].x); t2 += u2f(sv[q].y); }
        t1 = hsum(t1); t2 = hsum(t2);
        const float mean = t1 * (1.f / D), var = __builtin_fmaxf(t2 * (1.f / D) - mean * mean, 0.f) + EPS, rstd = __builtin_amdgcn_rsqf(var);
        const GeluK gk = {-mean, GC1 * rstd * rstd * rstd, GC0 * rstd, var * rstd};
        const unsigned uo = WS_U + ((4 * tb6) * XR + tk) * 16;
        auto epi2 = [&](auto nsc, auto&& pre, f32x16& ya, int ta, f32x16& yb, int tb_, bool storeb) AI {
            constexpr int NS = decltype(nsc)::value, OPS = NS >= 28 ? 1 : 2, SK = NS >= 28 ? 5 : 2;
            f32x4 c[8];
            float z[32];
            sfor<NS>([&](auto kc) AI {
                constexpr int k = decltype(kc)::value;
                pre(kc);
                sfor<8>([&](auto qc) AI { constexpr int q = decltype(qc)::value, st = (4 * q * SK) / 8;
                    if constexpr (k == (st >= 2 ? st - 2 : 0)) c[q] = *(const LAS f32x4*)(cs6 + (q < 4 ? ta : tb_) * 32 + 8 * (q & 3) + 4 * hh); });
                sfor<32>([&](auto ec) AI {
                    constexpr int el = decltype(ec)::value, st = (el * SK) / 8;
                    sfor<8>([&](auto oc) AI { constexpr int o = decltype(oc)::value;
                        if constexpr (o / OPS == k - st) { f32x16& y = el < 16 ? ya : yb; float yy = y[el & 15]; gelu_step(o, yy, z[el], c[el >> 2][el & 3], gk); y[el & 15] = yy; } });
                });
                sfor<32>([&](auto ec) AI {
                    constexpr int el = decltype(ec)::value, st = (el * SK) / 8, o = k - st;
                    if constexpr (o >= 0 && o < 8) { if constexpr (o == 0 || o == 7) { f32x16& y = el < 16 ? ya : yb; float yy = y[el & 15]; PIN(yy); y[el & 15] = yy; } else PIN(z[el]); }
                    if constexpr (el == 15 && k == st + 8) { gst(rs, pack8(ya, 0), vx, uo + 4 * ta * XROW); gst(rs, pack8(ya, 1), vx, uo + (4 * ta + 2) * XROW); }
                });
                SB();
            });
            if (storeb) { gst(rs, pack8(yb, 0), vx, uo + 4 * tb_ * XROW); gst(rs, pack8(yb, 1), vx, uo + (4 * tb_ + 2) * XROW); }
        };
        STAMP(23);
        f32x16 a2 = {}, a3 = {};
        {
            u32x4 wA[28], wB[28];
#pragma unroll
            for (int k = 0; k < PW; ++k) { wA[k] = wf6(2, k); wB[k] = wf6(3, k); }
            SB();
            epi2(IC(28), [&](auto kc) AI {
                constexpr int k = decltype(kc)::value;
                if constexpr (k + PW < 28) { wA[k + PW] = wf6(2, k + PW); wB[k + PW] = wf6(3, k + PW); }
                MFMA(a2, wA[k], xg[k], a2);
                if constexpr (k + PG < 28) xg[k + PG] = ldx2(k + PG);
                MFMA(a3, wB[k], xg[k], a3);
            }, a0, 0, a1, 1, true);
        }
        STAMP(24);
        LBAR();
        auto epi1 = [&](f32x16& y, int te) AI {
#pragma unroll
            for (int q = 0; q < 4; ++q) {
                const f32x4 c = *(const LAS f32x4*)(cs6 + te * 32 + 8 * q + 4 * hh);
#pragma unroll
                for (int i = 0; i < 4; ++i) {
                    const float v = __builtin_fmaf(gk.nmean, c[i], y[4 * q + i]);
                    const float z = v * __builtin_fmaf(v * v, gk.k1, gk.k0);
                    y[4 * q + i] = v * __builtin_amdgcn_rcpf(__builtin_fmaf(__builtin_amdgcn_exp2f(z), gk.irs, gk.irs));
                }
            }
            gst(rs, pack8(y, 0), vx, uo + 4 * te * XROW); gst(rs, pack8(y, 1), vx, uo + (4 * te + 2) * XROW);
        };
        epi1(a2, 2);
        if (nt6 == 4) epi1(a3, 3);
        WAIT_BAR(0);
        if (tid == 0) set_flag(P.ws, flag_off(5, m * 16 + s16), e);
        STAMP(12);
    }
    if (act7) {
        LANE_GEOM();
        const unsigned lanep = trq * 64 + gq * 8 + 2 * hh * 512;
        unsigned tA = L0 + L_X + w * 8192 + lanep, tB = wst + lanep; PIN(tA); PIN(tB);
        auto w4f = [&](int k, int t) AI { const unsigned a = (k < 4 ? tA + k * 2048 : k < 18 ? tB + (k - 4) * 2048 : tB + (k - 18) * 2048) + t * 256; return wfrag(a, a + 512); };
        u32x4 wr[28][2];
#pragma unroll
        for (int k = 0; k < 14; ++k) { wr[k][0] = w4f(k, 0); wr[k][1] = w4f(k, 1); }
        LBAR();
        const int tk = 64 * j7;
        const int tt = w >> 1, nt7 = w & 1;
        wait_flags(P.ws, flag_off(5, (j7 >> 1) * 16 + 4 * w), 4, e, lane);
        constexpr int PX = 5;
        u32x4 xa[28][2];
#pragma unroll
        for (int k = 0; k < PX; ++k) { xa[k][0] = gld(rs, vx, WS_U + ((56 * w + 2 * k) * XR + tk) * 16); xa[k][1] = gld(rs, vx, WS_U + ((56 * w + 2 * k) * XR + tk + 32) * 16); }
        STAMP(13);
        SB();
        f32x16 acc[4] = {};
        u32x4 rh[2], rx[2];
        sfor<28>([&](auto kc) AI {
            constexpr int k = decltype(kc)::value;
            if constexpr (k == 13) { BAR(); SB(); }
            if constexpr (k + 3 >= 14 && k + 3 < 28) { wr[k + 3][0] = w4f(k + 3, 0); wr[k + 3][1] = w4f(k + 3, 1); }
            MFMA(acc[0], wr[k][0], xa[k][0], acc[0]); MFMA(acc[1], wr[k][1], xa[k][0], acc[1]);
            if constexpr (k + PX < 28) xa[k + PX][0] = gld(rs, vx, WS_U + ((56 * w + 2 * (k + PX)) * XR + tk) * 16);
            MFMA(acc[2], wr[k][0], xa[k][1], acc[2]); MFMA(acc[3], wr[k][1], xa[k][1], acc[3]);
            if constexpr (k + PX < 28) xa[k + PX][1] = gld(rs, vx, WS_U + ((56 * w + 2 * (k + PX)) * XR + tk + 32) * 16);
            if constexpr (k == 24 || k == 25) {
                constexpr int s = k - 24;
                const unsigned so = ((8 * g7 + 4 * nt7 + 2 * s) * XR + tk + 32 * tt) * 16;
                rh[s] = gld(rs, vx, WS_H + so); rx[s] = gld(rs, vx, WS_X1 + so);
            }
            SB();
        });
        STAMP(27);
#pragma unroll
        for (int t = 0; t < 4; ++t) {
            if (t != w) {
                const unsigned a = L0 + L_X + t * 12288 + (w < t ? w : w - 1) * 4096 + lane * 16;
#pragma unroll
                for (int q = 0; q < 4; ++q) stsf4(a + q * 1024, (f32x4){acc[t][4 * q], acc[t][4 * q + 1], acc[t][4 * q + 2], acc[t][4 * q + 3]});
            }
        }
        LBAR();
        f32x16 r = w == 0 ? acc[0] : w == 1 ? acc[1] : w == 2 ? acc[2] : acc[3];
        const unsigned a = L0 + L_X + w * 12288 + lane * 16;
#pragma unroll
        for (int j = 0; j < 3; ++j)
#pragma unroll
            for (int q = 0; q < 4; ++q) { const f32x4 v = ldsf4(a + j * 4096 + q * 1024); r[4 * q] += v[0]; r[4 * q + 1] += v[1]; r[4 * q + 2] += v[2]; r[4 * q + 3] += v[3]; }
        char* op = (char*)P.out + (size_t)(tk + 32 * tt + r32) * 896 + 128 * g7 + 64 * nt7 + 16 * hh;
#pragma unroll
        for (int s = 0; s < 2; ++s) {
            u32x4 ov;
#pragma unroll
            for (int i = 0; i < 4; ++i)
                ov[i] = pk(r[8 * s + 2 * i] + blo(rx[s][i]) + blo(rh[s][i]), r[8 * s + 2 * i + 1] + bhi(rx[s][i]) + bhi(rh[s][i]));
            *(GAS u32x4*)(op + 32 * s) = ov;
        }
        STAMP(14);
    }
    STAMP_FLUSH();
}
}

extern "C" void kernel_launch(void* const* d_in, const int* in_sizes, int n_in, void* d_out, int out_size, void* d_ws, size_t ws_size,
                              hipStream_t stream) {
    mk::Params P;
    P.x = (const u16*)d_in[0]; P.wqkv = (const u16*)d_in[1]; P.wo = (const u16*)d_in[2];
    P.w1 = (const u16*)d_in[4]; P.w2 = (const u16*)d_in[5]; P.w3 = (const u16*)d_in[6]; P.w4 = (const u16*)d_in[7];
    P.out = (u16*)d_out; P.ws = (char*)d_ws;
    mk::fused_fwd<<<256, 512, 0, stream>>>(P);
}
```

```cpp
#define TOUCH_OUT 1
#define TOUCH_WS 1
#define LATE_TOUCH 3
#include <hip/hip_runtime.h>
#include <hip/hip_bf16.h>
#include <stdint.h>
#include <type_traits>
#include <utility>

typedef unsigned short u16;
typedef __attribute__((ext_vector_type(8))) short bf16x8;
typedef __attribute__((ext_vector_type(4))) short s16x4;
typedef __attribute__((ext_vector_type(16))) float f32x16;
typedef __attribute__((ext_vector_type(4))) float f32x4;
typedef __attribute__((ext_vector_type(2))) float f32x2;
typedef __attribute__((ext_vector_type(4))) unsigned u32x4;
typedef __attribute__((ext_vector_type(2))) unsigned u32x2;
typedef __attribute__((ext_vector_type(2))) __bf16 bf2;
#define LAS __attribute__((address_space(3)))
#define GAS __attribute__((address_space(1)))
#define DEVI __device__ __forceinline__

#define MK_STAMP 0

namespace mk {
constexpr int S = 2048, D = 448;
constexpr unsigned XR = 2056, XROW = XR * 16;
constexpr unsigned KVH = 64 * 4096 + 128;
constexpr unsigned WS_EP = 0;
constexpr unsigned WS_FLAG = 4096;
constexpr unsigned WS_KV = 262144;
constexpr unsigned WS_O = WS_KV + 16 * KVH;
constexpr unsigned WS_H = WS_O + 64 * XROW;
constexpr unsigned WS_HH = WS_H + 56 * XROW;
constexpr unsigned WS_X1 = WS_HH + 56 * XROW;
constexpr unsigned WS_U = WS_X1 + 56 * XROW;
constexpr unsigned WS_STH = WS_U + 224 * XROW;
constexpr unsigned WS_STX = WS_STH + 262144;
constexpr unsigned WS_ST1 = WS_STX + 262144;
constexpr unsigned WS_SPARE = WS_ST1 + 32768;
constexpr unsigned WS_STAMP = WS_SPARE + 128;
constexpr unsigned WS_END = WS_STAMP + 16 * 512 * 8;
constexpr unsigned L_SLOT = 28672;
constexpr unsigned L_MISC = 143360;
constexpr unsigned L_KV = 114688;
constexpr unsigned L_OWNKV = L_KV + 8 * 4096;
constexpr unsigned L_WST = 28672;
constexpr unsigned L_X = 114688;
constexpr unsigned L_M2 = 147456;
constexpr unsigned CODE_TOUCH_BYTES = 36000;
constexpr float EPS = 1e-5f;
constexpr unsigned SPIN_CAP = 1u << 18;

struct Params {
    const u16* x; const u16* wqkv; const u16* wo; const u16* w1; const u16* w2; const u16* w3; const u16* w4;
    u16* out; char* ws;
};

DEVI unsigned f2u(float f) { return __builtin_bit_cast(unsigned, f); }
DEVI float u2f(unsigned u) { return __builtin_bit_cast(float, u); }
DEVI bool wall(bool p) { return __builtin_amdgcn_ballot_w64(!p) == 0; }
template <int X> DEVI float xor32(float v) { return u2f((unsigned)__builtin_amdgcn_ds_swizzle((int)f2u(v), (X << 10) | 0x1f)); }
DEVI unsigned pk(float lo, float hi) { bf2 v = {(__bf16)lo, (__bf16)hi}; return __builtin_bit_cast(unsigned, v); }
DEVI float blo(unsigned u) { return u2f(u << 16); }
DEVI float bhi(unsigned u) { return u2f(u & 0xffff0000u); }
DEVI float dot2(unsigned a, unsigned b, float c) { return __builtin_amdgcn_fdot2_f32_bf16(__builtin_bit_cast(bf2, a), __builtin_bit_cast(bf2, b), c, false); }
constexpr unsigned ONES = 0x3f803f80u;
DEVI float hsum(float v) { auto r = __builtin_amdgcn_permlane32_swap(f2u(v), f2u(v), false, false); return u2f(r[0]) + u2f(r[1]); }
DEVI float hmax(float v) { auto r = __builtin_amdgcn_permlane32_swap(f2u(v), f2u(v), false, false); return __builtin_fmaxf(u2f(r[0]), u2f(r[1])); }
DEVI f32x16 mfma(u32x4 a, u32x4 b, f32x16 c) { return __builtin_amdgcn_mfma_f32_32x32x16_bf16(__builtin_bit_cast(bf16x8, a), __builtin_bit_cast(bf16x8, b), c, 0, 0, 0); }
#define MFMA(acc, a, b, c) do { acc = mfma(a, b, c); asm volatile("" : "+v"(acc)); } while (0)
DEVI u32x4 lds128(unsigned a) { return *(const LAS u32x4*)(uintptr_t)a; }
DEVI f32x4 ldsf4(unsigned a) { return *(const LAS f32x4*)(uintptr_t)a; }
DEVI void sts128(unsigned a, u32x4 v) { *(LAS u32x4*)(uintptr_t)a = v; }
DEVI void stsf4(unsigned a, f32x4 v) { *(LAS f32x4*)(uintptr_t)a = v; }
DEVI u32x2 ldstr(unsigned a) { return __builtin_bit_cast(u32x2, __builtin_amdgcn_ds_read_tr16_b64_v4i16((LAS s16x4*)(uintptr_t)a)); }
DEVI u32x4 wfrag(unsigned a0, unsigned a1) { u32x2 p = ldstr(a0), q = ldstr(a1); return (u32x4){p.x, p.y, q.x, q.y}; }
DEVI u32x4 pack8(const f32x16& a, int s) { return (u32x4){pk(a[8 * s], a[8 * s + 1]), pk(a[8 * s + 2], a[8 * s + 3]), pk(a[8 * s + 4], a[8 * s + 5]), pk(a[8 * s + 6], a[8 * s + 7])}; }
constexpr int SC1 = 16;
DEVI u32x4 gld(__amdgpu_buffer_rsrc_t rs, unsigned voff, unsigned soff) { return __builtin_amdgcn_raw_buffer_load_b128(rs, voff, soff, SC1); }
DEVI u32x2 gld8(__amdgpu_buffer_rsrc_t rs, unsigned voff, unsigned soff) { return __builtin_amdgcn_raw_buffer_load_b64(rs, voff, soff, SC1); }
DEVI void gst(__amdgpu_buffer_rsrc_t rs, u32x4 v, unsigned voff, unsigned soff) { __builtin_amdgcn_raw_buffer_store_b128(v, rs, voff + soff, 0, SC1); }
DEVI void gst8(__amdgpu_buffer_rsrc_t rs, u32x2 v, unsigned voff, unsigned soff) { __builtin_amdgcn_raw_buffer_store_b64(v, rs, voff, soff, SC1); }
#define WAIT_VM(N) asm volatile("s_waitcnt vmcnt(" #N ")" ::: "memory")
#define WAIT_LGKM() asm volatile("s_waitcnt lgkmcnt(0)" ::: "memory")
#define WAIT_BAR(N) asm volatile("s_waitcnt vmcnt(" #N ") lgkmcnt(0)\n\ts_barrier" ::: "memory")
#define LBAR() asm volatile("s_waitcnt lgkmcnt(0)\n\ts_barrier" ::: "memory")
#define SB() __builtin_amdgcn_sched_barrier(0)
#define PIN(x) asm volatile("" : "+v"(x))
DEVI float fma1(float a, float b, float c) { return __builtin_fmaf(a, b, c); }
DEVI float mul1(float a, float b) { return a * b; }
DEVI float max3(float a, float b, float c) { return __builtin_fmaxf(__builtin_fmaxf(a, b), c); }
template <int... Is, class F> DEVI void sfor_impl(std::integer_sequence<int, Is...>, F&& f) { (f(std::integral_constant<int, Is>{}), ...); }
template <int N, class F> DEVI void sfor(F&& f) { sfor_impl(std::make_integer_sequence<int, N>{}, f); }
#define AI __attribute__((always_inline))
#define IC(n) std::integral_constant<int, n>{}

DEVI void dma1(const char* sbase, unsigned voff, unsigned lds) {
    asm volatile("s_mov_b32 m0, %2\n\ts_nop 0\n\tglobal_load_lds_dwordx4 %0, %1" :: "v"(voff), "s"(sbase), "s"(lds) : "memory");
}
DEVI void touch(const char* sbase, unsigned voff, unsigned lds) {
    asm volatile("s_mov_b32 m0, %2\n\ts_nop 0\n\tglobal_load_lds_dword %0, %1" :: "v"(voff), "s"(sbase), "s"(lds) : "memory");
}
struct WSrc { const char* base; unsigned step, voff; };
DEVI void wpiece(const WSrc& s, unsigned lds, int n) { dma1(s.base + (size_t)n * s.step, s.voff, lds + n * 1024); }
DEVI WSrc wsrc_a(const char* base, unsigned rs, int nt, int lane) { const int t = (lane >> 4) < nt ? (lane >> 4) : nt - 1; return WSrc{base, 4 * rs, ((lane >> 2) & 3) * rs + t * 64 + (lane & 3) * 16}; }
DEVI WSrc wsrc_b(const char* base, unsigned rs, int lane) { return WSrc{base, 8 * rs, (4 * (lane >> 5) + ((lane >> 2) & 3)) * rs + ((lane >> 4) & 1) * 64 + (lane & 3) * 16}; }

DEVI void wait_flags(const char* ws, unsigned foff, int n, unsigned e, int lane) {
    const GAS unsigned* p = (const GAS unsigned*)(ws + foff) + (lane < n ? lane : 0) * 32;
#pragma nounroll
    for (unsigned sp = 0; sp < SPIN_CAP; ++sp) {
        unsigned v = __hip_atomic_load(p, __ATOMIC_RELAXED, __HIP_MEMORY_SCOPE_AGENT);
        if (wall(v == e)) break;
    }
    __builtin_amdgcn_fence(__ATOMIC_ACQUIRE, "wavefront");
}
DEVI void set_flag(char* ws, unsigned foff, unsigned e) { __hip_atomic_store((GAS unsigned*)(ws + foff), e, __ATOMIC_RELAXED, __HIP_MEMORY_SCOPE_AGENT); }
DEVI unsigned flag_off(int ph, int idx) { return WS_FLAG + (ph * 256 + idx) * 128; }

constexpr float GC0 = -2.f * 0.7978845608028654f * 1.4426950408889634f, GC1 = GC0 * 0.044715f;
DEVI float gelu_t(float x) {
    float z = x * __builtin_fmaf(x * x, GC1, GC0);
    return x * __builtin_amdgcn_rcpf(1.f + __builtin_amdgcn_exp2f(z));
}
struct GeluK { float nmean, k1, k0, irs; };
DEVI void gelu_step(int o, float& y, float& z, float c, const GeluK& g) {
    if (o == 0) y = fma1(g.nmean, c, y);
    else if (o == 1) z = mul1(y, y);
    else if (o == 2) z = fma1(z, g.k1, g.k0);
    else if (o == 3) z = mul1(y, z);
    else if (o == 4) z = __builtin_amdgcn_exp2f(z);
    else if (o == 5) z = fma1(z, g.irs, g.irs);
    else if (o == 6) z = __builtin_amdgcn_rcpf(z);
    else y = mul1(y, z);
}

#define STAMP(k) do {} while (0)
#define STAMP_DECL
#define STAMP_FLUSH() do {} while (0)

#define LANE_GEOM() int lane_ = threadIdx.x & 63; asm volatile("" : "+v"(lane_)); const int lane = lane_, r32 = lane & 31, hh = lane >> 5; \
    const int trq = (lane & 15) >> 2, trQ_ = ((lane >> 4) & 1) * 4 + (lane & 3), gq = (trQ_ & 4) | ((trQ_ & 1) << 1) | ((trQ_ >> 1) & 1); \
    const unsigned vx = (hh * XR + r32) * 16; (void)trq; (void)gq; (void)vx; (void)r32; (void)hh
constexpr unsigned L_FLG = L_MISC + 1920, F_RDY = 0, F_DONE = 16;
DEVI unsigned ldsw_ld(unsigned i) { return *(volatile LAS unsigned*)(uintptr_t)(L_FLG + 4 * i); }
DEVI u32x2 ldsw_ld2(unsigned i) { return *(volatile LAS u32x2*)(uintptr_t)(L_FLG + 4 * i); }
DEVI void ldsw_st(unsigned i, unsigned v) { *(volatile LAS unsigned*)(uintptr_t)(L_FLG + 4 * i) = v; }
DEVI void ring_wait(int c) {
#pragma nounroll
    for (unsigned sp = 0; sp < SPIN_CAP * 16u; ++sp) { const u32x2 v = ldsw_ld2(F_RDY + 2 * c); if (__builtin_amdgcn_readfirstlane(v.x & v.y)) break; __builtin_amdgcn_s_sleep(1); }
}
DEVI void slot_wait(unsigned n, int lane) {
#pragma nounroll
    for (unsigned sp = 0; sp < SPIN_CAP * 16u; ++sp) { const unsigned v = ldsw_ld(F_DONE + (lane < 6 ? lane : 0)); if (wall(v >= n)) break; __builtin_amdgcn_s_sleep(1); }
}
#define LB(N) asm volatile("s_waitcnt vmcnt(" #N ")\n\ts_barrier" ::: "memory")
#define BAR() asm volatile("s_barrier" ::: "memory")

__global__ __launch_bounds__(512, 2) void fused_fwd(Params P) {
    __shared__ __attribute__((aligned(16))) char smem[163840];
    const unsigned L0 = (unsigned)(uintptr_t)(LAS char*)smem;
    const int tid = threadIdx.x, wv = __builtin_amdgcn_readfirstlane(tid >> 6), w = wv & 3;
    const int b = blockIdx.x, xcd = b & 7, slot = b >> 3;
    const int m = 2 * xcd + (slot >> 4), h = slot & 15;
    const int g = slot & 3, nt = g < 2 ? 4 : 3, tb = g < 2 ? 4 * g : 3 * g + 2;
    const int ti = 8 * xcd + (slot >> 2);
    const int s16 = slot & 15, pr = s16 >> 1;
    const int nt6 = ((s16 ^ pr) & 1) ? 3 : 4, tb6 = 7 * pr + ((s16 & 1) ? (pr & 1 ? 3 : 4) : 0);
    const bool act7 = slot < 28;
    const int j7 = 4 * xcd + (act7 ? slot / 7 : 0), g7 = act7 ? slot % 7 : 0;
    const unsigned wst = L0 + w * L_WST;
    const unsigned shift = (56 * h) & 15u;
    STAMP_DECL

    if (wv >= 4) {
        LANE_GEOM();
        const unsigned wrd = 16384 + hh * 6 * 256 + trq * 64 + ((shift + gq * 8) & 63);
        LAS float* csp = (LAS float*)(uintptr_t)(L0 + L_MISC);
        if (w < 2) {
            const unsigned xoffE = (lane >> 3) * 896 + (((lane & 7) ^ (lane >> 4)) << 4);
            const unsigned xoffO = (lane >> 3) * 896 + (((lane & 7) ^ (4 + (lane >> 4))) << 4);
            const unsigned colb = (56 * h) & ~15u;
            unsigned woff[3];
#pragma unroll
            for (int j = 0; j < 3; ++j) {
                const int bl = 4 * j + (lane >> 4), kql = bl / 3, tile = bl % 3;
                woff[j] = (4 * kql + ((lane >> 2) & 3)) * 2688 + tile * 896 + colb + (lane & 3) * 16;
            }
            const char* xb = (const char*)P.x + (size_t)(128 * m + 64 * w) * 896;
            const char* wb = (const char*)P.wqkv + (size_t)(32 * w) * 2688;
            LBAR();
#pragma unroll 1
            for (int kc = 0; kc < 7; ++kc) {
                if (kc >= 5) slot_wait(kc - 4, lane);
                const unsigned base = L0 + (kc >= 5 ? kc - 5 : kc) * L_SLOT;
#pragma unroll
                for (int i = 0; i < 6; ++i) dma1(wb + (size_t)kc * 64 * 2688 + (i / 3) * 16 * 2688, woff[i % 3], base + 16384 + (6 * w + i) * 1024);
#pragma unroll
                for (int i = 0; i < 8; ++i) dma1(xb + kc * 128 + i * 8 * 896, (i & 1) ? xoffO : xoffE, base + (8 * w + i) * 1024);
                if (kc > 0) { WAIT_VM(14); ldsw_st(F_RDY + 2 * (kc - 1) + w, 1); }
            }
            WAIT_VM(0); ldsw_st(F_RDY + 12 + w, 1);
        } else {
            const int srow = 128 * m + 8 * h + 4 * (w - 2) + (lane >> 4);
            const char* sp = (const char*)P.x + (size_t)srow * 896 + (lane & 15) * 16;
            u32x4 sr[4];
            asm volatile("global_load_dwordx4 %0, %4, off\n\tglobal_load_dwordx4 %1, %4, off offset:256\n\tglobal_load_dwordx4 %2, %4, off offset:512\n\tglobal_load_dwordx4 %3, %5, off"
                         : "=&v"(sr[0]), "=&v"(sr[1]), "=&v"(sr[2]), "=&v"(sr[3]) : "v"(sp), "v"(sp + ((lane & 15) < 8 ? 768 : 0)) : "memory");
            if (w == 2 && lane < 32) ldsw_st(lane, 0);
            LBAR();
            {
                asm volatile("s_waitcnt vmcnt(0)" : "+v"(sr[0]), "+v"(sr[1]), "+v"(sr[2]), "+v"(sr[3]) :: "memory");
                if ((lane & 15) >= 8) sr[3] = (u32x4){0u, 0u, 0u, 0u};
                float t1 = 0.f, t2 = 0.f;
#pragma unroll
                for (int q = 0; q < 4; ++q)
#pragma unroll
                    for (int i = 0; i < 4; ++i) { t1 = dot2(sr[q][i], ONES, t1); t2 = dot2(sr[q][i], sr[q][i], t2); }
                t1 += xor32<1>(t1); t2 += xor32<1>(t2); t1 += xor32<2>(t1); t2 += xor32<2>(t2); t1 += xor32<4>(t1); t2 += xor32<4>(t2); t1 += xor32<8>(t1); t2 += xor32<8>(t2);
                const float mean = t1 * (1.f / D), var = __builtin_fmaxf(t2 * (1.f / D) - mean * mean, 0.f);
                const unsigned e = *(const unsigned*)(P.ws + WS_EP) + 1u;
                const __amdgpu_buffer_rsrc_t rs = __builtin_amdgcn_make_buffer_rsrc((void*)P.ws, 0, WS_END, 0x00020000);
                if ((lane & 15) == 0) gst(rs, (u32x4){f2u(mean), e, f2u(__builtin_amdgcn_rsqf(var + EPS)), e}, srow * 16, WS_ST1);
            }
            float cs[3][4] = {};
            const unsigned wrd2 = wrd + (w - 2) * 2 * 3072;
#pragma unroll 1
            for (int c = 0; c < 7; ++c) {
                ring_wait(c);
                if (c == LATE_TOUCH) {
                    unsigned long long pc; unsigned back;
                    asm volatile("s_getpc_b64 %0\nmk_code_here:\n\ts_mov_b32 %1, mk_code_here-_ZN2mk9fused_fwdENS_6ParamsE" : "=s"(pc), "=s"(back));
                    constexpr unsigned len = CODE_TOUCH_BYTES;
                    const unsigned oc = (slot * 18 + (lane < 18 ? lane : 0)) * 128, o3 = (b * 64 + lane) * 128, o2 = (b * 16 + (lane & 15)) * 128;
                    const char* t0 = w == 2 ? (const char*)(pc - back) + (oc < len ? oc : 0) : (const char*)P.w3 + (o3 < 1605632 ? o3 : 0);
                    const char* t1 = w == 2 ? (lane < 16 ? (const char*)P.wo : lane < 32 ? (const char*)P.w1 : (const char*)P.w2) + (o2 < 401408 ? o2 : 0) : (const char*)P.w4 + (o3 < 1605632 ? o3 : 0);
                    const unsigned oo = (slot * 14 + (lane < 14 ? lane : 0)) * 4096, li = slot * 16 + ((lane - 16) & 15);
                    const unsigned wo_ = li < 64 ? WS_O + li * XROW + 32768 : li < 456 ? WS_H + (li - 64) * XROW + 32768 : li < 472 ? WS_KV + (li - 456) * KVH + 262144 : WS_SPARE;
                    const char* t2 = (TOUCH_OUT && w == 2 && lane < 16) ? (const char*)P.out + oo : P.ws + ((TOUCH_WS && w == 2 && lane >= 16 && lane < 32) ? wo_ : WS_SPARE);
                    asm volatile("s_mov_b32 m0, %3\n\ts_nop 0\n\tglobal_load_lds_dword %0, off\n\tglobal_load_lds_dword %1, off\n\tglobal_load_lds_dword %2, off" :: "v"(t0), "v"(t1), "v"(t2), "s"(L0 + L_MISC + 2048) : "memory");
                }
                const unsigned a = L0 + (c >= 5 ? c - 5 : c) * L_SLOT + wrd2;
#pragma unroll
                for (int ks = 0; ks < 2; ++ks)
#pragma unroll
                    for (int t = 0; t < 3; ++t) { const u32x4 f = wfrag(a + ks * 3072 + t * 256, a + ks * 3072 + 768 + t * 256);
#pragma unroll
                        for (int i = 0; i < 4; ++i) cs[t][i] = dot2(f[i], ONES, cs[t][i]); }
                WAIT_LGKM();
                ldsw_st(F_DONE + 2 + w, c + 1);
            }
#pragma unroll
            for (int t = 0; t < 3; ++t) { const float v = hsum((cs[t][0] + cs[t][1]) + (cs[t][2] + cs[t][3])); if (hh == 0) csp[(w - 2) * 96 + t * 32 + r32] = v; }
            WAIT_VM(0);
            WAIT_LGKM();
            ldsw_st(F_DONE + 8 + w, 1);
            if (w == 2) {
#pragma nounroll
                for (unsigned sp2 = 0; sp2 < SPIN_CAP * 16u && __builtin_amdgcn_readfirstlane(ldsw_ld(F_DONE + 11)) == 0; ++sp2) __builtin_amdgcn_s_sleep(1);
                if (hh == 0) {
#pragma unroll
                    for (int t = 0; t < 3; ++t) csp[384 + t * 32 + r32] = csp[t * 32 + r32] + csp[96 + t * 32 + r32];
                }
            }
        }
        LBAR();
        auto stage = [&](const WSrc& s, unsigned lds, int n0, int n1) AI {
#pragma unroll 1
            for (int n = n0; n < n1; ++n) wpiece(s, lds, n);
        };
        BAR();
        BAR();
        stage(wsrc_a((const char*)P.wo + (size_t)(112 * w) * 896 + tb * 64, 896, nt, lane), wst, 0, 28);
        LB(0);
        BAR(); stage(wsrc_a((const char*)P.w1 + (size_t)(112 * w) * 896 + tb * 64, 896, nt, lane), wst, 0, 28); BAR(); LB(0);
        BAR(); stage(wsrc_a((const char*)P.w2 + (size_t)(112 * w) * 896 + tb * 64, 896, nt, lane), wst, 0, 28); BAR(); LB(0);
        BAR(); stage(wsrc_a((const char*)P.w3 + (size_t)(112 * w) * 3584 + tb6 * 64, 3584, nt6, lane), wst, 0, 28); BAR(); LB(0);
        {
            float c[4] = {0.f, 0.f, 0.f, 0.f};
            const unsigned a0 = L0 + trq * 64 + gq * 8 + 2 * hh * 1024 + w * 256;
#pragma unroll 4
            for (int k = 0; k < 28; ++k) { const unsigned a = a0 + k * 4096; const u32x4 f = wfrag(a, a + 1024);
#pragma unroll
                for (int i = 0; i < 4; ++i) c[i] = dot2(f[i], ONES, c[i]); }
            const float cc = hsum((c[0] + c[1]) + (c[2] + c[3]));
            if (hh == 0) ((LAS float*)(uintptr_t)(L0 + L_M2 + 1024))[w * 32 + r32] = cc;
        }
        if (b == 0 && w == 3) {
            const unsigned e = *(const volatile GAS unsigned*)(P.ws + WS_EP) + 1u;
            const GAS unsigned* p = (const GAS unsigned*)(P.ws + flag_off(0, lane));
#pragma nounroll
            for (unsigned sp = 0; sp < SPIN_CAP; ++sp) {
                const unsigned v0 = __hip_atomic_load(p, __ATOMIC_RELAXED, __HIP_MEMORY_SCOPE_AGENT), v1 = __hip_atomic_load(p + 64 * 32, __ATOMIC_RELAXED, __HIP_MEMORY_SCOPE_AGENT);
                const unsigned v2 = __hip_atomic_load(p + 128 * 32, __ATOMIC_RELAXED, __HIP_MEMORY_SCOPE_AGENT), v3 = __hip_atomic_load(p + 192 * 32, __ATOMIC_RELAXED, __HIP_MEMORY_SCOPE_AGENT);
                if (wall(v0 == e && v1 == e && v2 == e && v3 == e)) break;
            }
            if (lane == 0) __hip_atomic_store((GAS unsigned*)(P.ws + WS_EP), e, __ATOMIC_RELAXED, __HIP_MEMORY_SCOPE_AGENT);
        }
        LBAR();
        const WSrc w4s = wsrc_b((const char*)P.w4 + (size_t)(448 * w) * 896 + g7 * 128, 896, lane);
        if (act7) stage(w4s, L0 + L_X + w * 8192, 0, 8);
        BAR();
        if (act7) stage(w4s, wst - 8 * 1024, 8, 36);
        LB(0);
        if (act7) {
            BAR();
            stage(w4s, wst - 36 * 1024, 36, 56);
            LB(0);
            BAR();
        }
        STAMP_FLUSH();
        return;
    }

    const unsigned e = *(const unsigned*)(P.ws + WS_EP) + 1u;
    const __amdgpu_buffer_rsrc_t rs = __builtin_amdgcn_make_buffer_rsrc((void*)P.ws, 0, WS_END, 0x00020000);
    u32x4 qf0, qf1;
    {
        LANE_GEOM();
        const unsigned wrd = 16384 + hh * 6 * 256 + trq * 64 + ((shift + gq * 8) & 63);
        STAMP(15);
        f32x16 aq = {}, ak = {}, av = {};
        const unsigned xrd = (32 * w + r32) * 128 + ((hh ^ ((r32 >> 1) & 7)) << 4);
        struct Fr { u32x4 x, q, k, v; };
        Fr F[4];
        unsigned xb[3][4], wb3[3];
#pragma unroll
        for (int gsl = 0; gsl < 3; ++gsl) {
#pragma unroll
            for (int j = 0; j < 4; ++j) { xb[gsl][j] = L0 + (xrd ^ (j << 5)) + gsl * 2 * L_SLOT; PIN(xb[gsl][j]); }
            wb3[gsl] = L0 + wrd + gsl * 2 * L_SLOT; PIN(wb3[gsl]);
        }
        auto ldx = [&](int sl, int j) AI { F[j].x = lds128(xb[sl / 2][j] + (sl & 1) * L_SLOT); const unsigned a = wb3[sl / 2] + (sl & 1) * L_SLOT + j * 3072; F[j].q = wfrag(a, a + 768); };
        auto ldk = [&](int sl, int j) AI { const unsigned a = wb3[sl / 2] + (sl & 1) * L_SLOT + j * 3072; F[j].k = wfrag(a + 256, a + 1024); };
        auto ldv = [&](int sl, int j) AI { const unsigned a = wb3[sl / 2] + (sl & 1) * L_SLOT + j * 3072; F[j].v = wfrag(a + 512, a + 1280); };
        LAS float* stw = (LAS float*)(uintptr_t)(L0 + L_MISC + 3072 + w * 256);
        BAR();
        ring_wait(0);
        STAMP(16);
#pragma unroll
        for (int j = 0; j < 2; ++j) { ldx(0, j); ldk(0, j); ldv(0, j); }
        SB();
        u32x4 gv = {0u, 0u, 0u, 0u};
        const unsigned gvo = (128 * m + 32 * w + r32) * 16;
        sfor<7>([&](auto cc) AI {
            constexpr int c = decltype(cc)::value, sl = c >= 5 ? c - 5 : c, nsl = c >= 4 ? c - 4 : c + 1;
            u32x2 rdy;
            sfor<4>([&](auto jc) AI {
                constexpr int j = decltype(jc)::value, jn = (j + 2) & 3, lb = j < 2 ? sl : nsl;
                constexpr bool ld = j < 2 || c < 6;
                if constexpr (j == 2 && c < 6) { if (__builtin_amdgcn_readfirstlane(rdy.x & rdy.y) == 0) ring_wait(c + 1); SB(); }
                MFMA(aq, F[j].q, F[j].x, aq);
                if constexpr (ld) ldx(lb, jn);
                SB();
                MFMA(ak, F[j].k, F[j].x, ak);
                if constexpr (ld) ldk(lb, jn);
                if constexpr (j == 0 && c >= 1 && c <= 2) ldsw_st(F_DONE + w, c);
                if constexpr (j == 0 && c == 4) gv = gld(rs, gvo, WS_ST1);
                SB();
                MFMA(av, F[j].x, F[j].v, av);
                if constexpr (ld) ldv(lb, jn);
                if constexpr (j == 0 && c < 6) rdy = ldsw_ld2(F_RDY + 2 * (c + 1));
                SB();
            });
        });
#pragma nounroll
        for (unsigned sp2 = 0; sp2 < SPIN_CAP && !wall(gv.y == e && gv.w == e); ++sp2) gv = gld(rs, gvo, WS_ST1);
        if (hh == 0) { stw[r32] = u2f(gv.x) * u2f(gv.z); stw[32 + r32] = u2f(gv.z); }
        STAMP(1);
        const float mean = u2f(gv.x), rstd = u2f(gv.z);
        LBAR();
        STAMP(17);
        const LAS float* csf = (const LAS float*)(uintptr_t)(L0 + L_MISC + 1536);
        const float cv = csf[64 + r32];
        const float qs = rstd * (0.18898223650461362f * 1.4426950408889634f);
#pragma unroll
        for (int q = 0; q < 4; ++q) {
            const f32x4 c1 = *(const LAS f32x4*)(csf + 8 * q + 4 * hh), c2 = *(const LAS f32x4*)(csf + 32 + 8 * q + 4 * hh);
            const f32x4 mr = *(const LAS f32x4*)(stw + 8 * q + 4 * hh), rr = *(const LAS f32x4*)(stw + 32 + 8 * q + 4 * hh);
#pragma unroll
            for (int i = 0; i < 4; ++i) {
                aq[4 * q + i] = (aq[4 * q + i] - mean * c1[i]) * qs;
                ak[4 * q + i] = (ak[4 * q + i] - mean * c2[i]) * rstd;
                av[4 * q + i] = av[4 * q + i] * rr[i] - mr[i] * cv;
            }
        }
        if (hh == 1) { aq[12] = 1.f; aq[13] = 0.f; aq[14] = 0.f; aq[15] = 0.f; ak[12] = 0.f; ak[13] = 0.f; ak[14] = 0.f; ak[15] = 0.f; }
        if (r32 >= 28) {
            const float pv = r32 == 28 ? 1.f : 0.f;
#pragma unroll
            for (int i = 0; i < 16; ++i) av[i] = pv;
        }
        const u32x4 k0 = pack8(ak, 0), k1 = pack8(ak, 1), v0 = pack8(av, 0), v1 = pack8(av, 1);
        const unsigned go = WS_KV + h * KVH + (4 * m + w) * 4096;
        gst(rs, k0, lane * 16, go); gst(rs, k1, lane * 16, go + 1024); gst(rs, v0, lane * 16, go + 2048); gst(rs, v1, lane * 16, go + 3072);
        const unsigned lo = L0 + L_OWNKV + w * 4096 + lane * 16;
        sts128(lo, k0); sts128(lo + 1024, k1); sts128(lo + 2048, v0); sts128(lo + 3072, v1);
        qf0 = pack8(aq, 0); qf1 = pack8(aq, 1);
        STAMP(18);
        WAIT_BAR(0);
        if (tid == 0) set_flag(P.ws, flag_off(0, m * 16 + h), e);
        STAMP(2);
    }
    {
        LANE_GEOM();
        const int mt = m - 2 + (w >> 1);
        u32x4 kv[8];
        if (mt >= 0) {
            wait_flags(P.ws, flag_off(0, mt * 16 + h), 1, e, lane);
            const unsigned go = WS_KV + h * KVH + (4 * m - 8 + 2 * w) * 4096;
#pragma unroll
            for (int i = 0; i < 8; ++i) kv[i] = gld(rs, lane * 16, go + i * 1024);
        } else {
#pragma unroll
            for (int i = 0; i < 8; ++i) kv[i] = (u32x4){0u, 0u, (i & 3) == 1 && hh == 1 ? 0xF149u : 0u, 0u};
        }
        f32x16 cdiag, cold;
#pragma unroll
        for (int i = 0; i < 16; ++i) { const int kr = (i & 3) + 8 * (i >> 2) + 4 * hh; cdiag[i] = kr <= r32 ? 0.f : -1e30f; cold[i] = kr > r32 ? 0.f : -1e30f; }
#pragma unroll
        for (int i = 0; i < 8; ++i) sts128(L0 + L_KV + 2 * w * 4096 + i * 1024 + lane * 16, kv[i]);
        LBAR();
        STAMP(3);
        unsigned kvb = L0 + L_KV + w * 4096 + lane * 16;
        PIN(kvb);
        u32x4 kf[9][2], vf[9][2];
        const f32x16 zero16 = {};
        auto ldK = [&](int i) AI { kf[i][0] = lds128(kvb + i * 4096); kf[i][1] = lds128(kvb + i * 4096 + 1024); };
        auto ldV = [&](int i) AI { vf[i][0] = lds128(kvb + i * 4096 + 2048); vf[i][1] = lds128(kvb + i * 4096 + 3072); };
        auto smax = [&](const f32x16& s, float a) AI {
#pragma unroll
            for (int i = 0; i < 16; i += 2) a = max3(a, s[i], s[i + 1]);
            return a; };
        ldK(0); ldK(1);
        float mx = -3e38f;
        f32x16 s[2];
        auto smax8 = [&](const f32x16& v, int o, float a) AI {
#pragma unroll
            for (int i = 0; i < 8; i += 2) a = max3(a, v[o + i], v[o + i + 1]);
            return a; };
        sfor<9>([&](auto ic) AI {
            constexpr int i = decltype(ic)::value;
            if constexpr (i + 2 < 9) ldK(i + 2);
            MFMA(s[i & 1], kf[i][0], qf0, i == 0 ? cold : i == 8 ? cdiag : zero16);
            SB();
            MFMA(s[i & 1], kf[i][1], qf1, s[i & 1]);
            SB();
            if constexpr (i > 0) { mx = smax8(s[(i - 1) & 1], 0, mx); mx = smax8(s[(i - 1) & 1], 8, mx); PIN(mx); }
            SB();
        });
        ldK(0); ldK(1); ldV(0);
        mx = hmax(smax(s[0], mx));
        STAMP(25);
        f32x16 o = {}, cm;
#pragma unroll
        for (int i = 0; i < 16; ++i) { cm[i] = -mx; cdiag[i] -= mx; cold[i] -= mx; }
        MFMA(s[0], kf[0][0], qf0, cold); MFMA(s[0], kf[0][1], qf1, s[0]);
        SB();
        sfor<9>([&](auto ic) AI {
            constexpr int i = decltype(ic)::value;
            f32x16& c = s[i & 1];
            f32x16& n = s[(i + 1) & 1];
            if constexpr (i + 2 < 9) ldK(i + 2);
            if constexpr (i + 1 < 9) ldV(i + 1);
            if constexpr (i + 1 < 9) MFMA(n, kf[i + 1][0], qf0, i + 1 == 8 ? cdiag : cm);
#pragma unroll
            for (int j = 0; j < 8; ++j) c[j] = __builtin_amdgcn_exp2f(c[j]);
            u32x4 p0 = pack8(c, 0);
            PIN(p0);
            SB();
            if constexpr (i + 1 < 9) MFMA(n, kf[i + 1][1], qf1, n);
            MFMA(o, vf[i][0], p0, o);
#pragma unroll
            for (int j = 8; j < 16; ++j) c[j] = __builtin_amdgcn_exp2f(c[j]);
            const u32x4 p1 = pack8(c, 1);
            MFMA(o, vf[i][1], p1, o);
            SB();
        });
        STAMP(26);
        auto lr = __builtin_amdgcn_permlane32_swap(f2u(o[12]), f2u(o[12]), false, false);
        const float inv = __builtin_amdgcn_rcpf(u2f(lr[1]));
#pragma unroll
        for (int i = 0; i < 16; ++i) o[i] *= inv;
        if (hh == 1) { o[12] = 0.f; o[13] = 0.f; o[14] = 0.f; o[15] = 0.f; }
        const unsigned vo = (hh * XR + r32) * 16, so = WS_O + ((4 * h) * XR + 128 * m + 32 * w) * 16;
        gst(rs, pack8(o, 0), vo, so); gst(rs, pack8(o, 1), vo, so + 2 * XROW);
        WAIT_BAR(0);
        if (tid == 0) set_flag(P.ws, flag_off(1, m * 16 + h), e);
        STAMP(4);
    }
    const bool act = w < nt;
    const int tw = tb + w;
#define GEMM_GEOM() LANE_GEOM(); \
    unsigned trW[2]; trW[0] = L0 + (w < nt ? w : nt - 1) * 256 + trq * 64 + gq * 8 + 2 * hh * 1024; trW[1] = trW[0] + 57344; PIN(trW[0]); PIN(trW[1]); \
    auto wimg = [&](int kq0, int kq1) AI { return wfrag(kq0 < 56 ? trW[0] + kq0 * 1024 : trW[1] + (kq0 - 56) * 1024, kq1 < 56 ? trW[0] + kq1 * 1024 : trW[1] + (kq1 - 56) * 1024); }; \
    unsigned xl = L0 + L_X + lane * 16; PIN(xl); \
    LAS float* m2 = (LAS float*)(uintptr_t)(L0 + L_M2 + w * 256); (void)m2
    auto gemm = [&](auto nk, const u32x4* wr, unsigned xl) AI -> f32x16 {
        constexpr int NK = decltype(nk)::value, PD = 4;
        f32x16 acc = {};
        u32x4 xf[NK];
#pragma unroll
        for (int k = 0; k < PD; ++k) xf[k] = lds128(xl + k * 1024);
        SB();
        sfor<NK>([&](auto kc) AI {
            constexpr int k = decltype(kc)::value;
            if constexpr (k + PD < NK) xf[k + PD] = lds128(xl + (k + PD) * 1024);
            MFMA(acc, wr[k], xf[k], acc);
            SB();
        });
        return acc;
    };
    u32x4 hp[2];
    float meanH, rstdH;
    {
        GEMM_GEOM();
        u32x4 xr[2];
        const char* xp = (const char*)P.x + (size_t)(32 * ti + r32) * 896 + 64 * (act ? tw : tb) + 16 * hh;
        xr[0] = *(const GAS u32x4*)xp; xr[1] = *(const GAS u32x4*)(xp + 32);
        u32x4 wr[32];
        sfor<32>([&](auto kc) AI { constexpr int k = decltype(kc)::value, kq = 7 * (k >> 1) + 4 * (k & 1);
            if constexpr (k < 31) wr[k] = wimg(kq, kq + 1);
            else { const u32x2 p = ldstr(trW[1] + (kq - 56) * 1024), q = ldstr(trW[1] + (kq - 56) * 1024 + (hh ? 0 : 1024)); wr[k] = (u32x4){p.x, p.y, q.x, q.y}; } });
        STAMP(19);
        wait_flags(P.ws, flag_off(1, (ti >> 2) * 16 + 4 * w), 4, e, lane);
        STAMP(20);
        u32x4 xf[8];
#pragma unroll
        for (int k = 0; k < 8; ++k) xf[k] = gld(rs, vx, WS_O + ((16 * w + 2 * k) * XR + 32 * ti) * 16);
#pragma unroll
        for (int k = 0; k < 8; ++k) sts128(xl + (8 * w + k) * 1024, xf[k]);
        LBAR();
        STAMP(5);
        f32x16 r = gemm(IC(32), wr, xl);
        STAMP(21);
        float s1 = 0.f, s2 = 0.f;
#pragma unroll
        for (int s = 0; s < 2; ++s) {
#pragma unroll
            for (int i = 0; i < 4; ++i) {
                hp[s][i] = pk(r[8 * s + 2 * i] + blo(xr[s][i]), r[8 * s + 2 * i + 1] + bhi(xr[s][i]));
                s1 = dot2(hp[s][i], ONES, s1); s2 = dot2(hp[s][i], hp[s][i], s2);
            }
            if (act) gst(rs, hp[s], vx, WS_H + ((4 * tw + 2 * s) * XR + 32 * ti) * 16);
        }
        s1 = hsum(s1); s2 = hsum(s2);
        if (act && hh == 0) gst8(rs, (u32x2){f2u(s1), f2u(s2)}, r32 * 8, WS_STH + (tw * 2048 + 32 * ti) * 8);
        STAMP(22);
        WAIT_BAR(0);
        if (tid == 0) set_flag(P.ws, flag_off(2, ti * 4 + g), e);
        STAMP(6);
    }
#define LOAD_WR() u32x4 wr[28]; sfor<28>([&](auto kc) AI { constexpr int k = decltype(kc)::value; wr[k] = wimg(4 * k, 4 * k + 1); })
    const int fsn = w == 0 ? 1 : 2, fsl = w <= 1 ? 0 : w - 1;
    {
        GEMM_GEOM();
        BAR();
        LOAD_WR();
        {
            float cs[4] = {0.f, 0.f, 0.f, 0.f};
#pragma unroll
            for (int k = 0; k < 28; ++k)
#pragma unroll
                for (int i = 0; i < 4; ++i) cs[i] = dot2(wr[k][i], ONES, cs[i]);
            const float c = hsum((cs[0] + cs[1]) + (cs[2] + cs[3]));
            if (hh == 0) m2[r32] = c;
        }
        wait_flags(P.ws, flag_off(2, ti * 4), 4, e, lane);
        u32x4 xf[7];
#pragma unroll
        for (int k = 0; k < 7; ++k) xf[k] = gld(rs, vx, WS_H + ((14 * w + 2 * k) * XR + 32 * ti) * 16);
        u32x2 sv[7];
#pragma unroll
        for (int q = 0; q < 7; ++q) sv[q] = gld8(rs, (7 * hh * 2048 + r32) * 8, WS_STH + (q * 2048 + 32 * ti) * 8);
#pragma unroll
        for (int k = 0; k < 7; ++k) sts128(xl + (7 * w + k) * 1024, xf[k]);
        LBAR();
        STAMP(7);
        f32x16 r = gemm(IC(28), wr, xl);
        float t1 = 0.f, t2 = 0.f;
#pragma unroll
        for (int q = 0; q < 7; ++q) { t1 += u2f(sv[q].x); t2 += u2f(sv[q].y); }
        t1 = hsum(t1); t2 = hsum(t2);
        meanH = t1 * (1.f / D);
        const float var = __builtin_fmaxf(t2 * (1.f / D) - meanH * meanH, 0.f);
        rstdH = __builtin_amdgcn_rsqf(var + EPS);
        const float sc = rstdH * __builtin_amdgcn_rsqf(var * rstdH * rstdH + EPS);
#pragma unroll
        for (int q = 0; q < 4; ++q) {
            const f32x4 c = *(const LAS f32x4*)(m2 + 8 * q + 4 * hh);
#pragma unroll
            for (int i = 0; i < 4; ++i) r[4 * q + i] = gelu_t((r[4 * q + i] - meanH * c[i]) * sc);
        }
#pragma unroll
        for (int s = 0; s < 2; ++s) if (act) gst(rs, pack8(r, s), vx, WS_HH + ((4 * tw + 2 * s) * XR + 32 * ti) * 16);
        WAIT_BAR(0);
        if (tid == 0) set_flag(P.ws, flag_off(3, ti * 4 + g), e);
        STAMP(8);
    }
    {
        GEMM_GEOM();
        BAR();
        LOAD_WR();
        const int sh = w == 0 ? -1 : w == 1 ? 1 : w == 2 ? -2 : 2;
        const int tn = ti + (sh < 0 ? -1 : 1);
        {
            const bool nb = tn >= 0 && tn < 64;
            const int li = lane & 3;
            const GAS unsigned* p = (const GAS unsigned*)(P.ws + flag_off(3, (((li & 2) && nb) ? tn : ti) * 4 + fsl + ((li & 1) && fsn == 2 ? 1 : 0)));
#pragma nounroll
            for (unsigned sp = 0; sp < SPIN_CAP; ++sp) { unsigned v = __hip_atomic_load(p, __ATOMIC_RELAXED, __HIP_MEMORY_SCOPE_AGENT); if (wall(v == e)) break; }
            __builtin_amdgcn_fence(__ATOMIC_ACQUIRE, "wavefront");
        }
        const int tok = 32 * ti + r32 + sh;
        const bool ok = tok >= 0 && tok < S;
        const unsigned vxs = ok ? (hh * XR + tok) * 16 : 0x7ffffff0u;
        u32x4 xf[7];
#pragma unroll
        for (int k = 0; k < 7; ++k) { xf[k] = gld(rs, vxs, WS_HH + (14 * w + 2 * k) * XROW); }
#pragma unroll
        for (int k = 0; k < 7; ++k) sts128(xl + (7 * w + k) * 1024, xf[k]);
        LBAR();
        STAMP(9);
        f32x16 r = gemm(IC(28), wr, xl);
        float s1 = 0.f, s2 = 0.f;
#pragma unroll
        for (int s = 0; s < 2; ++s) {
            u32x4 xp;
#pragma unroll
            for (int i = 0; i < 4; ++i) {
                xp[i] = pk(r[8 * s + 2 * i] + (blo(hp[s][i]) - meanH) * rstdH, r[8 * s + 2 * i + 1] + (bhi(hp[s][i]) - meanH) * rstdH);
                s1 = dot2(xp[i], ONES, s1); s2 = dot2(xp[i], xp[i], s2);
            }
            if (act) gst(rs, xp, vx, WS_X1 + ((4 * tw + 2 * s) * XR + 32 * ti) * 16);
        }
        s1 = hsum(s1); s2 = hsum(s2);
        if (act && hh == 0) gst8(rs, (u32x2){f2u(s1), f2u(s2)}, r32 * 8, WS_STX + (tw * 2048 + 32 * ti) * 8);
        WAIT_BAR(0);
        if (tid == 0) set_flag(P.ws, flag_off(4, ti * 4 + g), e);
        STAMP(10);
    }
    {
        LANE_GEOM();
        unsigned tr6[2];
        tr6[0] = L0 + trq * 64 + gq * 8 + 2 * hh * 1024; tr6[1] = tr6[0] + 57344; PIN(tr6[0]); PIN(tr6[1]);
        auto wf6 = [&](int t, int k) AI { const unsigned a = (k < 14 ? tr6[0] + k * 4096 : tr6[1] + (k - 14) * 4096) + t * 256; return wfrag(a, a + 1024); };
        const LAS float* cs6 = (const LAS float*)(uintptr_t)(L0 + L_M2 + 1024);
        wait_flags(P.ws, flag_off(4, (4 * m + w) * 4), 4, e, lane);
        const int tk = 128 * m + 32 * w;
        auto ldx1 = [&](int k) AI { return gld(rs, vx, WS_X1 + (2 * k * XR + tk) * 16); };
        unsigned vx2 = vx; PIN(vx2);
        auto ldx2 = [&](int k) AI { return gld(rs, vx2, WS_X1 + (2 * k * XR + tk) * 16); };
        constexpr int PX = 10, PW = 3;
        f32x16 a0 = {}, a1 = {};
        u32x2 sv[7];
        u32x4 xg[28];
        constexpr int PG = 6;
        {
            u32x4 xf[28], wA[28], wB[28];
#pragma unroll
            for (int k = 0; k < PX; ++k) xf[k] = ldx1(k);
#pragma unroll
            for (int q = 0; q < 7; ++q) sv[q] = gld8(rs, (7 * hh * 2048 + r32) * 8, WS_STX + (q * 2048 + tk) * 8);
            BAR();
            STAMP(11);
#pragma unroll
            for (int k = 0; k < PW; ++k) { wA[k] = wf6(0, k); wB[k] = wf6(1, k); }
            SB();
            sfor<28>([&](auto kc) AI {
                constexpr int k = decltype(kc)::value;
                if constexpr (k + PW < 28) { wA[k + PW] = wf6(0, k + PW); wB[k + PW] = wf6(1, k + PW); }
                MFMA(a0, wA[k], xf[k], a0);
                if constexpr (k + PX < 28) xf[k + PX] = ldx1(k + PX);
                else if constexpr (k + PX - 28 < PG) xg[k + PX - 28] = ldx2(k + PX - 28);
                MFMA(a1, wB[k], xf[k], a1);
                SB();
            });
        }
        float t1 = 0.f, t2 = 0.f;
#pragma unroll
        for (int q = 0; q < 7; ++q) { t1 += u2f(sv[q].x); t2 += u2f(sv[q].y); }
        t1 = hsum(t1); t2 = hsum(t2);
        const float mean = t1 * (1.f / D), var = __builtin_fmaxf(t2 * (1.f / D) - mean * mean, 0.f) + EPS, rstd = __builtin_amdgcn_rsqf(var);
        const GeluK gk = {-mean, GC1 * rstd * rstd * rstd, GC0 * rstd, var * rstd};
        const unsigned uo = WS_U + ((4 * tb6) * XR + tk) * 16;
        auto epi2 = [&](auto nsc, auto&& pre, f32x16& ya, int ta, f32x16& yb, int tb_, bool storeb) AI {
            constexpr int NS = decltype(nsc)::value, OPS = NS >= 28 ? 1 : 2, SK = NS >= 28 ? 5 : 2;
            f32x4 c[8];
            float z[32];
            sfor<NS>([&](auto kc) AI {
                constexpr int k = decltype(kc)::value;
                pre(kc);
                sfor<8>([&](auto qc) AI { constexpr int q = decltype(qc)::value, st = (4 * q * SK) / 8;
                    if constexpr (k == (st >= 2 ? st - 2 : 0)) c[q] = *(const LAS f32x4*)(cs6 + (q < 4 ? ta : tb_) * 32 + 8 * (q & 3) + 4 * hh); });
                sfor<32>([&](auto ec) AI {
                    constexpr int el = decltype(ec)::value, st = (el * SK) / 8;
                    sfor<8>([&](auto oc) AI { constexpr int o = decltype(oc)::value;
                        if constexpr (o / OPS == k - st) { f32x16& y = el < 16 ? ya : yb; float yy = y[el & 15]; gelu_step(o, yy, z[el], c[el >> 2][el & 3], gk); y[el & 15] = yy; } });
                });
                sfor<32>([&](auto ec) AI {
                    constexpr int el = decltype(ec)::value, st = (el * SK) / 8, o = k - st;
                    if constexpr (o >= 0 && o < 8) { if constexpr (o == 0 || o == 7) { f32x16& y = el < 16 ? ya : yb; float yy = y[el & 15]; PIN(yy); y[el & 15] = yy; } else PIN(z[el]); }
                    if constexpr (el == 15 && k == st + 8) { gst(rs, pack8(ya, 0), vx, uo + 4 * ta * XROW); gst(rs, pack8(ya, 1), vx, uo + (4 * ta + 2) * XROW); }
                });
                SB();
            });
            if (storeb) { gst(rs, pack8(yb, 0), vx, uo + 4 * tb_ * XROW); gst(rs, pack8(yb, 1), vx, uo + (4 * tb_ + 2) * XROW); }
        };
        BAR();
        STAMP(23);
        f32x16 a2 = {}, a3 = {};
        {
            u32x4 wA[28], wB[28];
#pragma unroll
            for (int k = 0; k < PW; ++k) { wA[k] = wf6(2, k); wB[k] = wf6(3, k); }
            SB();
            epi2(IC(28), [&](auto kc) AI {
                constexpr int k = decltype(kc)::value;
                if constexpr (k + PW < 28) { wA[k + PW] = wf6(2, k + PW); wB[k + PW] = wf6(3, k + PW); }
                MFMA(a2, wA[k], xg[k], a2);
                if constexpr (k + PG < 28) xg[k + PG] = ldx2(k + PG);
                MFMA(a3, wB[k], xg[k], a3);
            }, a0, 0, a1, 1, true);
        }
        STAMP(24);
        LBAR();
        auto epi1 = [&](f32x16& y, int te) AI {
#pragma unroll
            for (int q = 0; q < 4; ++q) {
                const f32x4 c = *(const LAS f32x4*)(cs6 + te * 32 + 8 * q + 4 * hh);
#pragma unroll
                for (int i = 0; i < 4; ++i) {
                    const float v = __builtin_fmaf(gk.nmean, c[i], y[4 * q + i]);
                    const float z = v * __builtin_fmaf(v * v, gk.k1, gk.k0);
                    y[4 * q + i] = v * __builtin_amdgcn_rcpf(__builtin_fmaf(__builtin_amdgcn_exp2f(z), gk.irs, gk.irs));
                }
            }
            gst(rs, pack8(y, 0), vx, uo + 4 * te * XROW); gst(rs, pack8(y, 1), vx, uo + (4 * te + 2) * XROW);
        };
        epi1(a2, 2);
        if (nt6 == 4) epi1(a3, 3);
        WAIT_BAR(0);
        if (tid == 0) set_flag(P.ws, flag_off(5, m * 16 + s16), e);
        STAMP(12);
    }
    if (act7) {
        LANE_GEOM();
        const unsigned lanep = trq * 64 + gq * 8 + 2 * hh * 512;
        unsigned tA = L0 + L_X + w * 8192 + lanep, tB = wst + lanep; PIN(tA); PIN(tB);
        auto w4f = [&](int k, int t) AI { const unsigned a = (k < 4 ? tA + k * 2048 : k < 18 ? tB + (k - 4) * 2048 : tB + (k - 18) * 2048) + t * 256; return wfrag(a, a + 512); };
        u32x4 wr[28][2];
#pragma unroll
        for (int k = 0; k < 14; ++k) { wr[k][0] = w4f(k, 0); wr[k][1] = w4f(k, 1); }
        LBAR();
        const int tk = 64 * j7;
        const int tt = w >> 1, nt7 = w & 1;
        wait_flags(P.ws, flag_off(5, (j7 >> 1) * 16 + 4 * w), 4, e, lane);
        constexpr int PX = 5;
        u32x4 xa[28][2];
#pragma unroll
        for (int k = 0; k < PX; ++k) { xa[k][0] = gld(rs, vx, WS_U + ((56 * w + 2 * k) * XR + tk) * 16); xa[k][1] = gld(rs, vx, WS_U + ((56 * w + 2 * k) * XR + tk + 32) * 16); }
        STAMP(13);
        SB();
        f32x16 acc[4] = {};
        u32x4 rh[2], rx[2];
        sfor<28>([&](auto kc) AI {
            constexpr int k = decltype(kc)::value;
            if constexpr (k == 13) { BAR(); SB(); }
            if constexpr (k + 3 >= 14 && k + 3 < 28) { wr[k + 3][0] = w4f(k + 3, 0); wr[k + 3][1] = w4f(k + 3, 1); }
            MFMA(acc[0], wr[k][0], xa[k][0], acc[0]); MFMA(acc[1], wr[k][1], xa[k][0], acc[1]);
            if constexpr (k + PX < 28) xa[k + PX][0] = gld(rs, vx, WS_U + ((56 * w + 2 * (k + PX)) * XR + tk) * 16);
            MFMA(acc[2], wr[k][0], xa[k][1], acc[2]); MFMA(acc[3], wr[k][1], xa[k][1], acc[3]);
            if constexpr (k + PX < 28) xa[k + PX][1] = gld(rs, vx, WS_U + ((56 * w + 2 * (k + PX)) * XR + tk + 32) * 16);
            if constexpr (k == 24 || k == 25) {
                constexpr int s = k - 24;
                const unsigned so = ((8 * g7 + 4 * nt7 + 2 * s) * XR + tk + 32 * tt) * 16;
                rh[s] = gld(rs, vx, WS_H + so); rx[s] = gld(rs, vx, WS_X1 + so);
            }
            SB();
        });
        STAMP(27);
#pragma unroll
        for (int t = 0; t < 4; ++t) {
            if (t != w) {
                const unsigned a = L0 + L_X + t * 12288 + (w < t ? w : w - 1) * 4096 + lane * 16;
#pragma unroll
                for (int q = 0; q < 4; ++q) stsf4(a + q * 1024, (f32x4){acc[t][4 * q], acc[t][4 * q + 1], acc[t][4 * q + 2], acc[t][4 * q + 3]});
            }
        }
        LBAR();
        f32x16 r = w == 0 ? acc[0] : w == 1 ? acc[1] : w == 2 ? acc[2] : acc[3];
        const unsigned a = L0 + L_X + w * 12288 + lane * 16;
#pragma unroll
        for (int j = 0; j < 3; ++j)
#pragma unroll
            for (int q = 0; q < 4; ++q) { const f32x4 v = ldsf4(a + j * 4096 + q * 1024); r[4 * q] += v[0]; r[4 * q + 1] += v[1]; r[4 * q + 2] += v[2]; r[4 * q + 3] += v[3]; }
        char* op = (char*)P.out + (size_t)(tk + 32 * tt + r32) * 896 + 128 * g7 + 64 * nt7 + 16 * hh;
#pragma unroll
        for (int s = 0; s < 2; ++s) {
            u32x4 ov;
#pragma unroll
            for (int i = 0; i < 4; ++i)
                ov[i] = pk(r[8 * s + 2 * i] + blo(rx[s][i]) + blo(rh[s][i]), r[8 * s + 2 * i + 1] + bhi(rx[s][i]) + bhi(rh[s][i]));
            *(GAS u32x4*)(op + 32 * s) = ov;
        }
        STAMP(14);
    }
    STAMP_FLUSH();
}
}

extern "C" void kernel_launch(void* const* d_in, const int* in_sizes, int n_in, void* d_out, int out_size, void* d_ws, size_t ws_size,
                              hipStream_t stream) {
    mk::Params P;
    P.x = (const u16*)d_in[0]; P.wqkv = (const u16*)d_in[1]; P.wo = (const u16*)d_in[2];
    P.w1 = (const u16*)d_in[4]; P.w2 = (const u16*)d_in[5]; P.w3 = (const u16*)d_in[6]; P.w4 = (const u16*)d_in[7];
    P.out = (u16*)d_out; P.ws = (char*)d_ws;
    mk::fused_fwd<<<256, 512, 0, stream>>>(P);
}
```

```cpp
#define OUT_MODE 1
#define P6_PX 10
#define P6_PW 3
#define P6_PG 6
#define P7_PX 5
#define HALF 12
#define POLL2 0
#define POLL_GAP 3
#define TOUCH_DELAY 12
#define EARLY_TOUCH 1
#define TOUCH_OUT 1
#define TOUCH_WS 1
#define LATE_TOUCH 3
#include <hip/hip_runtime.h>
#include <hip/hip_bf16.h>
#include <stdint.h>
#include <type_traits>
#include <utility>

typedef unsigned short u16;
typedef __attribute__((ext_vector_type(8))) short bf16x8;
typedef __attribute__((ext_vector_type(4))) short s16x4;
typedef __attribute__((ext_vector_type(16))) float f32x16;
typedef __attribute__((ext_vector_type(4))) float f32x4;
typedef __attribute__((ext_vector_type(2))) float f32x2;
typedef __attribute__((ext_vector_type(4))) unsigned u32x4;
typedef __attribute__((ext_vector_type(2))) unsigned u32x2;
typedef __attribute__((ext_vector_type(2))) __bf16 bf2;
#define LAS __attribute__((address_space(3)))
#define GAS __attribute__((address_space(1)))
#define DEVI __device__ __forceinline__

#define MK_STAMP 0

namespace mk {
constexpr int S = 2048, D = 448;
constexpr unsigned XR = 2056, XROW = XR * 16;
constexpr unsigned KVH = 64 * 4096 + 128;
constexpr unsigned WS_EP = 0;
constexpr unsigned WS_FLAG = 4096;
constexpr unsigned WS_KV = 262144;
constexpr unsigned WS_O = WS_KV + 16 * KVH;
constexpr unsigned WS_H = WS_O + 64 * XROW;
constexpr unsigned WS_HH = WS_H + 56 * XROW;
constexpr unsigned WS_X1 = WS_HH + 56 * XROW;
constexpr unsigned WS_U = WS_X1 + 56 * XROW;
constexpr unsigned WS_STH = WS_U + 224 * XROW;
constexpr unsigned WS_STX = WS_STH + 262144;
constexpr unsigned WS_ST1 = WS_STX + 262144;
constexpr unsigned WS_SPARE = WS_ST1 + 32768;
constexpr unsigned WS_STAMP = WS_SPARE + 128;
constexpr unsigned WS_END = WS_STAMP + 16 * 512 * 8;
constexpr unsigned L_SLOT = 28672;
constexpr unsigned L_MISC = 143360;
constexpr unsigned L_KV = 114688;
constexpr unsigned L_OWNKV = L_KV + 8 * 4096;
constexpr unsigned L_WST = 28672;
constexpr unsigned L_X = 114688;
constexpr unsigned L_M2 = 147456;
constexpr unsigned CODE_TOUCH_BYTES = 36000;
constexpr float EPS = 1e-5f;
constexpr unsigned SPIN_CAP = 1u << 18;

struct Params {
    const u16* x; const u16* wqkv; const u16* wo; const u16* w1; const u16* w2; const u16* w3; const u16* w4;
    u16* out; char* ws;
};

DEVI unsigned f2u(float f) { return __builtin_bit_cast(unsigned, f); }
DEVI float u2f(unsigned u) { return __builtin_bit_cast(float, u); }
DEVI bool wall(bool p) { return __builtin_amdgcn_ballot_w64(!p) == 0; }
template <int X> DEVI float xor32(float v) { return u2f((unsigned)__builtin_amdgcn_ds_swizzle((int)f2u(v), (X << 10) | 0x1f)); }
DEVI unsigned pk(float lo, float hi) { bf2 v = {(__bf16)lo, (__bf16)hi}; return __builtin_bit_cast(unsigned, v); }
DEVI float blo(unsigned u) { return u2f(u << 16); }
DEVI float bhi(unsigned u) { return u2f(u & 0xffff0000u); }
DEVI float dot2(unsigned a, unsigned b, float c) { return __builtin_amdgcn_fdot2_f32_bf16(__builtin_bit_cast(bf2, a), __builtin_bit_cast(bf2, b), c, false); }
constexpr unsigned ONES = 0x3f803f80u;
DEVI float hsum(float v) { auto r = __builtin_amdgcn_permlane32_swap(f2u(v), f2u(v), false, false); return u2f(r[0]) + u2f(r[1]); }
DEVI float hmax(float v) { auto r = __builtin_amdgcn_permlane32_swap(f2u(v), f2u(v), false, false); return __builtin_fmaxf(u2f(r[0]), u2f(r[1])); }
DEVI f32x16 mfma(u32x4 a, u32x4 b, f32x16 c) { return __builtin_amdgcn_mfma_f32_32x32x16_bf16(__builtin_bit_cast(bf16x8, a), __builtin_bit_cast(bf16x8, b), c, 0, 0, 0); }
#define MFMA(acc, a, b, c) do { acc = mfma(a, b, c); asm volatile("" : "+v"(acc)); } while (0)
DEVI u32x4 lds128(unsigned a) { return *(const LAS u32x4*)(uintptr_t)a; }
DEVI f32x4 ldsf4(unsigned a) { return *(const LAS f32x4*)(uintptr_t)a; }
DEVI void sts128(unsigned a, u32x4 v) { *(LAS u32x4*)(uintptr_t)a = v; }
DEVI void stsf4(unsigned a, f32x4 v) { *(LAS f32x4*)(uintptr_t)a = v; }
DEVI u32x2 ldstr(unsigned a) { return __builtin_bit_cast(u32x2, __builtin_amdgcn_ds_read_tr16_b64_v4i16((LAS s16x4*)(uintptr_t)a)); }
DEVI u32x4 wfrag(unsigned a0, unsigned a1) { u32x2 p = ldstr(a0), q = ldstr(a1); return (u32x4){p.x, p.y, q.x, q.y}; }
DEVI u32x4 pack8(const f32x16& a, int s) { return (u32x4){pk(a[8 * s], a[8 * s + 1]), pk(a[8 * s + 2], a[8 * s + 3]), pk(a[8 * s + 4], a[8 * s + 5]), pk(a[8 * s + 6], a[8 * s + 7])}; }
constexpr int SC1 = 16;
DEVI u32x4 gld(__amdgpu_buffer_rsrc_t rs, unsigned voff, unsigned soff) { return __builtin_amdgcn_raw_buffer_load_b128(rs, voff, soff, SC1); }
DEVI u32x2 gld8(__amdgpu_buffer_rsrc_t rs, unsigned voff, unsigned soff) { return __builtin_amdgcn_raw_buffer_load_b64(rs, voff, soff, SC1); }
DEVI void gst(__amdgpu_buffer_rsrc_t rs, u32x4 v, unsigned voff, unsigned soff) { __builtin_amdgcn_raw_buffer_store_b128(v, rs, voff + soff, 0, SC1); }
DEVI void gst8(__amdgpu_buffer_rsrc_t rs, u32x2 v, unsigned voff, unsigned soff) { __builtin_amdgcn_raw_buffer_store_b64(v, rs, voff, soff, SC1); }
#define WAIT_VM(N) asm volatile("s_waitcnt vmcnt(" #N ")" ::: "memory")
#define WAIT_LGKM() asm volatile("s_waitcnt lgkmcnt(0)" ::: "memory")
#define WAIT_BAR(N) asm volatile("s_waitcnt vmcnt(" #N ") lgkmcnt(0)\n\ts_barrier" ::: "memory")
#define LBAR() asm volatile("s_waitcnt lgkmcnt(0)\n\ts_barrier" ::: "memory")
#define SB() __builtin_amdgcn_sched_barrier(0)
#define PIN(x) asm volatile("" : "+v"(x))
DEVI float fma1(float a, float b, float c) { return __builtin_fmaf(a, b, c); }
DEVI float mul1(float a, float b) { return a * b; }
DEVI float max3(float a, float b, float c) { return __builtin_fmaxf(__builtin_fmaxf(a, b), c); }
template <int... Is, class F> DEVI void sfor_impl(std::integer_sequence<int, Is...>, F&& f) { (f(std::integral_constant<int, Is>{}), ...); }
template <int N, class F> DEVI void sfor(F&& f) { sfor_impl(std::make_integer_sequence<int, N>{}, f); }
#define AI __attribute__((always_inline))
#define IC(n) std::integral_constant<int, n>{}

DEVI void dma1(const char* sbase, unsigned voff, unsigned lds) {
    asm volatile("s_mov_b32 m0, %2\n\ts_nop 0\n\tglobal_load_lds_dwordx4 %0, %1" :: "v"(voff), "s"(sbase), "s"(lds) : "memory");
}
DEVI void touch(const char* sbase, unsigned voff, unsigned lds) {
    asm volatile("s_mov_b32 m0, %2\n\ts_nop 0\n\tglobal_load_lds_dword %0, %1" :: "v"(voff), "s"(sbase), "s"(lds) : "memory");
}
struct WSrc { const char* base; unsigned step, voff; };
DEVI void wpiece(const WSrc& s, unsigned lds, int n) { dma1(s.base + (size_t)n * s.step, s.voff, lds + n * 1024); }
DEVI WSrc wsrc_a(const char* base, unsigned rs, int nt, int lane) { const int t = (lane >> 4) < nt ? (lane >> 4) : nt - 1; return WSrc{base, 4 * rs, ((lane >> 2) & 3) * rs + t * 64 + (lane & 3) * 16}; }
DEVI WSrc wsrc_b(const char* base, unsigned rs, int lane) { return WSrc{base, 8 * rs, (4 * (lane >> 5) + ((lane >> 2) & 3)) * rs + ((lane >> 4) & 1) * 64 + (lane & 3) * 16}; }

DEVI void poll_word(const GAS unsigned* p, unsigned e) {
    unsigned a = __hip_atomic_load(p, __ATOMIC_RELAXED, __HIP_MEMORY_SCOPE_AGENT);
#pragma nounroll
    for (unsigned sp = 0; sp < SPIN_CAP; ++sp) {
        __builtin_amdgcn_s_sleep(POLL_GAP);
        const unsigned b2 = __hip_atomic_load(p, __ATOMIC_RELAXED, __HIP_MEMORY_SCOPE_AGENT);
        if (wall(a == e)) break;
        __builtin_amdgcn_s_sleep(POLL_GAP);
        a = __hip_atomic_load(p, __ATOMIC_RELAXED, __HIP_MEMORY_SCOPE_AGENT);
        if (wall(b2 == e)) break;
    }
    __builtin_amdgcn_fence(__ATOMIC_ACQUIRE, "wavefront");
}
DEVI void wait_flags(const char* ws, unsigned foff, int n, unsigned e, int lane) {
    const GAS unsigned* p = (const GAS unsigned*)(ws + foff) + (lane < n ? lane : 0) * 32;
    if (POLL2) poll_word(p, e);
    else {
#pragma nounroll
        for (unsigned sp = 0; sp < SPIN_CAP; ++sp) {
            unsigned v = __hip_atomic_load(p, __ATOMIC_RELAXED, __HIP_MEMORY_SCOPE_AGENT);
            if (wall(v == e)) break;
        }
        __builtin_amdgcn_fence(__ATOMIC_ACQUIRE, "wavefront");
    }
}
DEVI void set_flag(char* ws, unsigned foff, unsigned e) { __hip_atomic_store((GAS unsigned*)(ws + foff), e, __ATOMIC_RELAXED, __HIP_MEMORY_SCOPE_AGENT); }
DEVI unsigned flag_off(int ph, int idx) { return WS_FLAG + (ph * 256 + idx) * 128; }

constexpr float GC0 = -2.f * 0.7978845608028654f * 1.4426950408889634f, GC1 = GC0 * 0.044715f;
DEVI float gelu_t(float x) {
    float z = x * __builtin_fmaf(x * x, GC1, GC0);
    return x * __builtin_amdgcn_rcpf(1.f + __builtin_amdgcn_exp2f(z));
}
struct GeluK { float nmean, k1, k0, irs; };
DEVI void gelu_step(int o, float& y, float& z, float c, const GeluK& g) {
    if (o == 0) y = fma1(g.nmean, c, y);
    else if (o == 1) z = mul1(y, y);
    else if (o == 2) z = fma1(z, g.k1, g.k0);
    else if (o == 3) z = mul1(y, z);
    else if (o == 4) z = __builtin_amdgcn_exp2f(z);
    else if (o == 5) z = fma1(z, g.irs, g.irs);
    else if (o == 6) z = __builtin_amdgcn_rcpf(z);
    else y = mul1(y, z);
}

#define STAMP(k) do {} while (0)
#define STAMP_DECL
#define STAMP_FLUSH() do {} while (0)

#define LANE_GEOM() int lane_ = threadIdx.x & 63; asm volatile("" : "+v"(lane_)); const int lane = lane_, r32 = lane & 31, hh = lane >> 5; \
    const int trq = (lane & 15) >> 2, trQ_ = ((lane >> 4) & 1) * 4 + (lane & 3), gq = (trQ_ & 4) | ((trQ_ & 1) << 1) | ((trQ_ >> 1) & 1); \
    const unsigned vx = (hh * XR + r32) * 16; (void)trq; (void)gq; (void)vx; (void)r32; (void)hh
constexpr unsigned L_FLG = L_MISC + 1920, F_RDY = 0, F_DONE = 16;
DEVI unsigned ldsw_ld(unsigned i) { return *(volatile LAS unsigned*)(uintptr_t)(L_FLG + 4 * i); }
DEVI u32x2 ldsw_ld2(unsigned i) { return *(volatile LAS u32x2*)(uintptr_t)(L_FLG + 4 * i); }
DEVI void ldsw_st(unsigned i, unsigned v) { *(volatile LAS unsigned*)(uintptr_t)(L_FLG + 4 * i) = v; }
DEVI void ring_wait(int c) {
#pragma nounroll
    for (unsigned sp = 0; sp < SPIN_CAP * 16u; ++sp) { const u32x2 v = ldsw_ld2(F_RDY + 2 * c); if (__builtin_amdgcn_readfirstlane(v.x & v.y)) break; __builtin_amdgcn_s_sleep(1); }
}
DEVI void slot_wait(unsigned n, int lane) {
#pragma nounroll
    for (unsigned sp = 0; sp < SPIN_CAP * 16u; ++sp) { const unsigned v = ldsw_ld(F_DONE + (lane < 6 ? lane : 0)); if (wall(v >= n)) break; __builtin_amdgcn_s_sleep(1); }
}
#define LB(N) asm volatile("s_waitcnt vmcnt(" #N ")\n\ts_barrier" ::: "memory")
#define BAR() asm volatile("s_barrier" ::: "memory")

__global__ __launch_bounds__(512, 2) void fused_fwd(Params P) {
    __shared__ __attribute__((aligned(16))) char smem[163840];
    const unsigned L0 = (unsigned)(uintptr_t)(LAS char*)smem;
    const int tid = threadIdx.x, wv = __builtin_amdgcn_readfirstlane(tid >> 6), w = wv & 3;
    const int b = blockIdx.x, xcd = b & 7, slot = b >> 3;
    const int m = 2 * xcd + (slot >> 4), h = slot & 15;
    const int g = slot & 3, nt = g < 2 ? 4 : 3, tb = g < 2 ? 4 * g : 3 * g + 2;
    const int ti = 8 * xcd + (slot >> 2);
    const int s16 = slot & 15, pr = s16 >> 1;
    const int nt6 = ((s16 ^ pr) & 1) ? 3 : 4, tb6 = 7 * pr + ((s16 & 1) ? (pr & 1 ? 3 : 4) : 0);
    const bool act7 = slot < 28;
    const int j7 = 4 * xcd + (act7 ? slot / 7 : 0), g7 = act7 ? slot % 7 : 0;
    const unsigned wst = L0 + w * L_WST;
    const unsigned shift = (56 * h) & 15u;
    STAMP_DECL

    if (wv >= 4) {
        LANE_GEOM();
        const unsigned wrd = 16384 + hh * 6 * 256 + trq * 64 + ((shift + gq * 8) & 63);
        LAS float* csp = (LAS float*)(uintptr_t)(L0 + L_MISC);
        if (w < 2) {
            const unsigned xoffE = (lane >> 3) * 896 + (((lane & 7) ^ (lane >> 4)) << 4);
            const unsigned xoffO = (lane >> 3) * 896 + (((lane & 7) ^ (4 + (lane >> 4))) << 4);
            const unsigned colb = (56 * h) & ~15u;
            unsigned woff[3];
#pragma unroll
            for (int j = 0; j < 3; ++j) {
                const int bl = 4 * j + (lane >> 4), kql = bl / 3, tile = bl % 3;
                woff[j] = (4 * kql + ((lane >> 2) & 3)) * 2688 + tile * 896 + colb + (lane & 3) * 16;
            }
            const char* xb = (const char*)P.x + (size_t)(128 * m + 64 * w) * 896;
            const char* wb = (const char*)P.wqkv + (size_t)(32 * w) * 2688;
            LBAR();
#pragma unroll 1
            for (int kc = 0; kc < 7; ++kc) {
                if (kc >= 5) slot_wait(kc - 4, lane);
                const unsigned base = L0 + (kc >= 5 ? kc - 5 : kc) * L_SLOT;
#pragma unroll
                for (int i = 0; i < 6; ++i) dma1(wb + (size_t)kc * 64 * 2688 + (i / 3) * 16 * 2688, woff[i % 3], base + 16384 + (6 * w + i) * 1024);
#pragma unroll
                for (int i = 0; i < 8; ++i) dma1(xb + kc * 128 + i * 8 * 896, (i & 1) ? xoffO : xoffE, base + (8 * w + i) * 1024);
                if (kc > 0) { WAIT_VM(14); ldsw_st(F_RDY + 2 * (kc - 1) + w, 1); }
            }
            WAIT_VM(0); ldsw_st(F_RDY + 12 + w, 1);
        } else {
            const int srow = 128 * m + 8 * h + 4 * (w - 2) + (lane >> 4);
            const char* sp = (const char*)P.x + (size_t)srow * 896 + (lane & 15) * 16;
            u32x4 sr[4];
            asm volatile("global_load_dwordx4 %0, %4, off\n\tglobal_load_dwordx4 %1, %4, off offset:256\n\tglobal_load_dwordx4 %2, %4, off offset:512\n\tglobal_load_dwordx4 %3, %5, off"
                         : "=&v"(sr[0]), "=&v"(sr[1]), "=&v"(sr[2]), "=&v"(sr[3]) : "v"(sp), "v"(sp + ((lane & 15) < 8 ? 768 : 0)) : "memory");
            if (w == 2 && lane < 32) ldsw_st(lane, 0);
            LBAR();
            {
                const unsigned lx = lane < 56 ? lane : 0, lw = b * 37 + (lane < 37 ? lane : 0);
                const char* tp = !EARLY_TOUCH ? P.ws + WS_SPARE : w == 2 ? (const char*)P.x + (size_t)(8 * b + lx / 7) * 896 + (lx % 7) * 128 : (const char*)P.wqkv + (size_t)(lw < 9408 ? lw : 0) * 128;
                if (TOUCH_DELAY) __builtin_amdgcn_s_sleep(TOUCH_DELAY);
                asm volatile("s_mov_b32 m0, %1\n\ts_nop 0\n\tglobal_load_lds_dword %0, off" :: "v"(tp), "s"(L0 + L_MISC + 2048) : "memory");
            }
            {
                asm volatile("s_waitcnt vmcnt(1)" : "+v"(sr[0]), "+v"(sr[1]), "+v"(sr[2]), "+v"(sr[3]) :: "memory");
                if ((lane & 15) >= 8) sr[3] = (u32x4){0u, 0u, 0u, 0u};
                float t1 = 0.f, t2 = 0.f;
#pragma unroll
                for (int q = 0; q < 4; ++q)
#pragma unroll
                    for (int i = 0; i < 4; ++i) { t1 = dot2(sr[q][i], ONES, t1); t2 = dot2(sr[q][i], sr[q][i], t2); }
                t1 += xor32<1>(t1); t2 += xor32<1>(t2); t1 += xor32<2>(t1); t2 += xor32<2>(t2); t1 += xor32<4>(t1); t2 += xor32<4>(t2); t1 += xor32<8>(t1); t2 += xor32<8>(t2);
                const float mean = t1 * (1.f / D), var = __builtin_fmaxf(t2 * (1.f / D) - mean * mean, 0.f);
                const unsigned e = *(const unsigned*)(P.ws + WS_EP) + 1u;
                const __amdgpu_buffer_rsrc_t rs = __builtin_amdgcn_make_buffer_rsrc((void*)P.ws, 0, WS_END, 0x00020000);
                if ((lane & 15) == 0) gst(rs, (u32x4){f2u(mean), e, f2u(__builtin_amdgcn_rsqf(var + EPS)), e}, srow * 16, WS_ST1);
            }
            float cs[3][4] = {};
            const unsigned wrd2 = wrd + (w - 2) * 2 * 3072;
#pragma unroll 1
            for (int c = 0; c < 7; ++c) {
                ring_wait(c);
                if (c == LATE_TOUCH) {
                    unsigned long long pc; unsigned back;
                    asm volatile("s_getpc_b64 %0\nmk_code_here:\n\ts_mov_b32 %1, mk_code_here-_ZN2mk9fused_fwdENS_6ParamsE" : "=s"(pc), "=s"(back));
                    constexpr unsigned len = CODE_TOUCH_BYTES;
                    const unsigned oc = (slot * 18 + (lane < 18 ? lane : 0)) * 128, o3 = (b * 64 + lane) * 128, o2 = (b * 16 + (lane & 15)) * 128;
                    const char* t0 = w == 2 ? (const char*)(pc - back) + (oc < len ? oc : 0) : (const char*)P.w3 + (o3 < 1605632 ? o3 : 0);
                    const char* t1 = w == 2 ? (lane < 16 ? (const char*)P.wo : lane < 32 ? (const char*)P.w1 : (const char*)P.w2) + (o2 < 401408 ? o2 : 0) : (const char*)P.w4 + (o3 < 1605632 ? o3 : 0);
                    const unsigned oo = (slot * 14 + (lane < 14 ? lane : 0)) * 4096, li = slot * 16 + ((lane - 16) & 15);
                    const unsigned wo_ = li < 64 ? WS_O + li * XROW + 32768 : li < 456 ? WS_H + (li - 64) * XROW + 32768 : li < 472 ? WS_KV + (li - 456) * KVH + 262144 : WS_SPARE;
                    const char* t2 = (TOUCH_OUT && w == 2 && lane < 16) ? (const char*)P.out + oo : P.ws + ((TOUCH_WS && w == 2 && lane >= 16 && lane < 32) ? wo_ : WS_SPARE);
                    asm volatile("s_mov_b32 m0, %3\n\ts_nop 0\n\tglobal_load_lds_dword %0, off\n\tglobal_load_lds_dword %1, off\n\tglobal_load_lds_dword %2, off" :: "v"(t0), "v"(t1), "v"(t2), "s"(L0 + L_MISC + 2048) : "memory");
                }
                const unsigned a = L0 + (c >= 5 ? c - 5 : c) * L_SLOT + wrd2;
#pragma unroll
                for (int ks = 0; ks < 2; ++ks)
#pragma unroll
                    for (int t = 0; t < 3; ++t) { const u32x4 f = wfrag(a + ks * 3072 + t * 256, a + ks * 3072 + 768 + t * 256);
#pragma unroll
                        for (int i = 0; i < 4; ++i) cs[t][i] = dot2(f[i], ONES, cs[t][i]); }
                WAIT_LGKM();
                ldsw_st(F_DONE + 2 + w, c + 1);
            }
#pragma unroll
            for (int t = 0; t < 3; ++t) { const float v = hsum((cs[t][0] + cs[t][1]) + (cs[t][2] + cs[t][3])); if (hh == 0) csp[(w - 2) * 96 + t * 32 + r32] = v; }
            WAIT_VM(0);
            WAIT_LGKM();
            ldsw_st(F_DONE + 8 + w, 1);
            if (w == 2) {
#pragma nounroll
                for (unsigned sp2 = 0; sp2 < SPIN_CAP * 16u && __builtin_amdgcn_readfirstlane(ldsw_ld(F_DONE + 11)) == 0; ++sp2) __builtin_amdgcn_s_sleep(1);
                if (hh == 0) {
#pragma unroll
                    for (int t = 0; t < 3; ++t) csp[384 + t * 32 + r32] = csp[t * 32 + r32] + csp[96 + t * 32 + r32];
                }
            }
        }
        LBAR();
        auto stage = [&](const WSrc& s, unsigned lds, int n0, int n1) AI {
#pragma unroll 1
            for (int n = n0; n < n1; ++n) wpiece(s, lds, n);
        };
        BAR();
        BAR();
        stage(wsrc_a((const char*)P.wo + (size_t)(112 * w) * 896 + tb * 64, 896, nt, lane), wst, 0, 28);
        LB(0);
        { const WSrc s_ = wsrc_a((const char*)P.w1 + (size_t)(112 * w) * 896 + tb * 64, 896, nt, lane); BAR(); stage(s_, wst, 0, HALF); BAR(); stage(s_, wst, HALF, 28); BAR(); LB(0); }
        { const WSrc s_ = wsrc_a((const char*)P.w2 + (size_t)(112 * w) * 896 + tb * 64, 896, nt, lane); BAR(); stage(s_, wst, 0, HALF); BAR(); stage(s_, wst, HALF, 28); BAR(); LB(0); }
        { const WSrc s_ = wsrc_a((const char*)P.w3 + (size_t)(112 * w) * 3584 + tb6 * 64, 3584, nt6, lane); BAR(); stage(s_, wst, 0, HALF); BAR(); stage(s_, wst, HALF, 28); BAR(); LB(0); }
        {
            float c[4] = {0.f, 0.f, 0.f, 0.f};
            const unsigned a0 = L0 + trq * 64 + gq * 8 + 2 * hh * 1024 + w * 256;
#pragma unroll 4
            for (int k = 0; k < 28; ++k) { const unsigned a = a0 + k * 4096; const u32x4 f = wfrag(a, a + 1024);
#pragma unroll
                for (int i = 0; i < 4; ++i) c[i] = dot2(f[i], ONES, c[i]); }
            const float cc = hsum((c[0] + c[1]) + (c[2] + c[3]));
            if (hh == 0) ((LAS float*)(uintptr_t)(L0 + L_M2 + 1024))[w * 32 + r32] = cc;
        }
        if (b == 0 && w == 3) {
            const unsigned e = *(const volatile GAS unsigned*)(P.ws + WS_EP) + 1u;
            const GAS unsigned* p = (const GAS unsigned*)(P.ws + flag_off(0, lane));
#pragma nounroll
            for (unsigned sp = 0; sp < SPIN_CAP; ++sp) {
                const unsigned v0 = __hip_atomic_load(p, __ATOMIC_RELAXED, __HIP_MEMORY_SCOPE_AGENT), v1 = __hip_atomic_load(p + 64 * 32, __ATOMIC_RELAXED, __HIP_MEMORY_SCOPE_AGENT);
                const unsigned v2 = __hip_atomic_load(p + 128 * 32, __ATOMIC_RELAXED, __HIP_MEMORY_SCOPE_AGENT), v3 = __hip_atomic_load(p + 192 * 32, __ATOMIC_RELAXED, __HIP_MEMORY_SCOPE_AGENT);
                if (wall(v0 == e && v1 == e && v2 == e && v3 == e)) break;
            }
            if (lane == 0) __hip_atomic_store((GAS unsigned*)(P.ws + WS_EP), e, __ATOMIC_RELAXED, __HIP_MEMORY_SCOPE_AGENT);
        }
        LBAR();
        const WSrc w4s = wsrc_b((const char*)P.w4 + (size_t)(448 * w) * 896 + g7 * 128, 896, lane);
        if (act7) stage(w4s, L0 + L_X + w * 8192, 0, 8);
        BAR();
        if (act7) stage(w4s, wst - 8 * 1024, 8, 36);
        BAR();
        LB(0);
        if (act7) {
            BAR();
            stage(w4s, wst - 36 * 1024, 36, 56);
            LB(0);
            BAR();
        }
        STAMP_FLUSH();
        return;
    }

    const unsigned e = *(const unsigned*)(P.ws + WS_EP) + 1u;
    const __amdgpu_buffer_rsrc_t rs = __builtin_amdgcn_make_buffer_rsrc((void*)P.ws, 0, WS_END, 0x00020000);
    u32x4 qf0, qf1;
    {
        LANE_GEOM();
        const unsigned wrd = 16384 + hh * 6 * 256 + trq * 64 + ((shift + gq * 8) & 63);
        STAMP(15);
        f32x16 aq = {}, ak = {}, av = {};
        const unsigned xrd = (32 * w + r32) * 128 + ((hh ^ ((r32 >> 1) & 7)) << 4);
        struct Fr { u32x4 x, q, k, v; };
        Fr F[4];
        unsigned xb[3][4], wb3[3];
#pragma unroll
        for (int gsl = 0; gsl < 3; ++gsl) {
#pragma unroll
            for (int j = 0; j < 4; ++j) { xb[gsl][j] = L0 + (xrd ^ (j << 5)) + gsl * 2 * L_SLOT; PIN(xb[gsl][j]); }
            wb3[gsl] = L0 + wrd + gsl * 2 * L_SLOT; PIN(wb3[gsl]);
        }
        auto ldx = [&](int sl, int j) AI { F[j].x = lds128(xb[sl / 2][j] + (sl & 1) * L_SLOT); const unsigned a = wb3[sl / 2] + (sl & 1) * L_SLOT + j * 3072; F[j].q = wfrag(a, a + 768); };
        auto ldk = [&](int sl, int j) AI { const unsigned a = wb3[sl / 2] + (sl & 1) * L_SLOT + j * 3072; F[j].k = wfrag(a + 256, a + 1024); };
        auto ldv = [&](int sl, int j) AI { const unsigned a = wb3[sl / 2] + (sl & 1) * L_SLOT + j * 3072; F[j].v = wfrag(a + 512, a + 1280); };
        LAS float* stw = (LAS float*)(uintptr_t)(L0 + L_MISC + 3072 + w * 256);
        BAR();
        ring_wait(0);
        STAMP(16);
#pragma unroll
        for (int j = 0; j < 2; ++j) { ldx(0, j); ldk(0, j); ldv(0, j); }
        SB();
        u32x4 gv = {0u, 0u, 0u, 0u};
        const unsigned gvo = (128 * m + 32 * w + r32) * 16;
        sfor<7>([&](auto cc) AI {
            constexpr int c = decltype(cc)::value, sl = c >= 5 ? c - 5 : c, nsl = c >= 4 ? c - 4 : c + 1;
            u32x2 rdy;
            sfor<4>([&](auto jc) AI {
                constexpr int j = decltype(jc)::value, jn = (j + 2) & 3, lb = j < 2 ? sl : nsl;
                constexpr bool ld = j < 2 || c < 6;
                if constexpr (j == 2 && c < 6) { if (__builtin_amdgcn_readfirstlane(rdy.x & rdy.y) == 0) ring_wait(c + 1); SB(); }
                MFMA(aq, F[j].q, F[j].x, aq);
                if constexpr (ld) ldx(lb, jn);
                SB();
                MFMA(ak, F[j].k, F[j].x, ak);
                if constexpr (ld) ldk(lb, jn);
                if constexpr (j == 0 && c >= 1 && c <= 2) ldsw_st(F_DONE + w, c);
                if constexpr (j == 0 && c == 4) gv = gld(rs, gvo, WS_ST1);
                SB();
                MFMA(av, F[j].x, F[j].v, av);
                if constexpr (ld) ldv(lb, jn);
                if constexpr (j == 0 && c < 6) rdy = ldsw_ld2(F_RDY + 2 * (c + 1));
                SB();
            });
        });
#pragma nounroll
        for (unsigned sp2 = 0; sp2 < SPIN_CAP && !wall(gv.y == e && gv.w == e); ++sp2) gv = gld(rs, gvo, WS_ST1);
        if (hh == 0) { stw[r32] = u2f(gv.x) * u2f(gv.z); stw[32 + r32] = u2f(gv.z); }
        STAMP(1);
        const float mean = u2f(gv.x), rstd = u2f(gv.z);
        LBAR();
        STAMP(17);
        const LAS float* csf = (const LAS float*)(uintptr_t)(L0 + L_MISC + 1536);
        const float cv = csf[64 + r32];
        const float qs = rstd * (0.18898223650461362f * 1.4426950408889634f);
#pragma unroll
        for (int q = 0; q < 4; ++q) {
            const f32x4 c1 = *(const LAS f32x4*)(csf + 8 * q + 4 * hh), c2 = *(const LAS f32x4*)(csf + 32 + 8 * q + 4 * hh);
            const f32x4 mr = *(const LAS f32x4*)(stw + 8 * q + 4 * hh), rr = *(const LAS f32x4*)(stw + 32 + 8 * q + 4 * hh);
#pragma unroll
            for (int i = 0; i < 4; ++i) {
                aq[4 * q + i] = (aq[4 * q + i] - mean * c1[i]) * qs;
                ak[4 * q + i] = (ak[4 * q + i] - mean * c2[i]) * rstd;
                av[4 * q + i] = av[4 * q + i] * rr[i] - mr[i] * cv;
            }
        }
        if (hh == 1) { aq[12] = 1.f; aq[13] = 0.f; aq[14] = 0.f; aq[15] = 0.f; ak[12] = 0.f; ak[13] = 0.f; ak[14] = 0.f; ak[15] = 0.f; }
        if (r32 >= 28) {
            const float pv = r32 == 28 ? 1.f : 0.f;
#pragma unroll
            for (int i = 0; i < 16; ++i) av[i] = pv;
        }
        const u32x4 k0 = pack8(ak, 0), k1 = pack8(ak, 1), v0 = pack8(av, 0), v1 = pack8(av, 1);
        const unsigned go = WS_KV + h * KVH + (4 * m + w) * 4096;
        gst(rs, k0, lane * 16, go); gst(rs, k1, lane * 16, go + 1024); gst(rs, v0, lane * 16, go + 2048); gst(rs, v1, lane * 16, go + 3072);
        const unsigned lo = L0 + L_OWNKV + w * 4096 + lane * 16;
        sts128(lo, k0); sts128(lo + 1024, k1); sts128(lo + 2048, v0); sts128(lo + 3072, v1);
        qf0 = pack8(aq, 0); qf1 = pack8(aq, 1);
        STAMP(18);
        WAIT_BAR(0);
        if (tid == 0) set_flag(P.ws, flag_off(0, m * 16 + h), e);
        STAMP(2);
    }
    {
        LANE_GEOM();
        const int mt = m - 2 + (w >> 1);
        u32x4 kv[8];
        if (mt >= 0) {
            wait_flags(P.ws, flag_off(0, mt * 16 + h), 1, e, lane);
            const unsigned go = WS_KV + h * KVH + (4 * m - 8 + 2 * w) * 4096;
#pragma unroll
            for (int i = 0; i < 8; ++i) kv[i] = gld(rs, lane * 16, go + i * 1024);
        } else {
#pragma unroll
            for (int i = 0; i < 8; ++i) kv[i] = (u32x4){0u, 0u, (i & 3) == 1 && hh == 1 ? 0xF149u : 0u, 0u};
        }
        f32x16 cdiag, cold;
#pragma unroll
        for (int i = 0; i < 16; ++i) { const int kr = (i & 3) + 8 * (i >> 2) + 4 * hh; cdiag[i] = kr <= r32 ? 0.f : -1e30f; cold[i] = kr > r32 ? 0.f : -1e30f; }
#pragma unroll
        for (int i = 0; i < 8; ++i) sts128(L0 + L_KV + 2 * w * 4096 + i * 1024 + lane * 16, kv[i]);
        LBAR();
        STAMP(3);
        unsigned kvb = L0 + L_KV + w * 4096 + lane * 16;
        PIN(kvb);
        u32x4 kf[9][2], vf[9][2];
        const f32x16 zero16 = {};
        auto ldK = [&](int i) AI { kf[i][0] = lds128(kvb + i * 4096); kf[i][1] = lds128(kvb + i * 4096 + 1024); };
        auto ldV = [&](int i) AI { vf[i][0] = lds128(kvb + i * 4096 + 2048); vf[i][1] = lds128(kvb + i * 4096 + 3072); };
        auto smax = [&](const f32x16& s, float a) AI {
#pragma unroll
            for (int i = 0; i < 16; i += 2) a = max3(a, s[i], s[i + 1]);
            return a; };
        ldK(0); ldK(1);
        float mx = -3e38f;
        f32x16 s[2];
        auto smax8 = [&](const f32x16& v, int o, float a) AI {
#pragma unroll
            for (int i = 0; i < 8; i += 2) a = max3(a, v[o + i], v[o + i + 1]);
            return a; };
        sfor<9>([&](auto ic) AI {
            constexpr int i = decltype(ic)::value;
            if constexpr (i + 2 < 9) ldK(i + 2);
            MFMA(s[i & 1], kf[i][0], qf0, i == 0 ? cold : i == 8 ? cdiag : zero16);
            SB();
            MFMA(s[i & 1], kf[i][1], qf1, s[i & 1]);
            SB();
            if constexpr (i > 0) { mx = smax8(s[(i - 1) & 1], 0, mx); mx = smax8(s[(i - 1) & 1], 8, mx); PIN(mx); }
            SB();
        });
        ldK(0); ldK(1); ldV(0);
        mx = hmax(smax(s[0], mx));
        STAMP(25);
        f32x16 o = {}, cm;
#pragma unroll
        for (int i = 0; i < 16; ++i) { cm[i] = -mx; cdiag[i] -= mx; cold[i] -= mx; }
        MFMA(s[0], kf[0][0], qf0, cold); MFMA(s[0], kf[0][1], qf1, s[0]);
        SB();
        sfor<9>([&](auto ic) AI {
            constexpr int i = decltype(ic)::value;
            f32x16& c = s[i & 1];
            f32x16& n = s[(i + 1) & 1];
            if constexpr (i + 2 < 9) ldK(i + 2);
            if constexpr (i + 1 < 9) ldV(i + 1);
            if constexpr (i + 1 < 9) MFMA(n, kf[i + 1][0], qf0, i + 1 == 8 ? cdiag : cm);
#pragma unroll
            for (int j = 0; j < 8; ++j) c[j] = __builtin_amdgcn_exp2f(c[j]);
            u32x4 p0 = pack8(c, 0);
            PIN(p0);
            SB();
            if constexpr (i + 1 < 9) MFMA(n, kf[i + 1][1], qf1, n);
            MFMA(o, vf[i][0], p0, o);
#pragma unroll
            for (int j = 8; j < 16; ++j) c[j] = __builtin_amdgcn_exp2f(c[j]);
            const u32x4 p1 = pack8(c, 1);
            MFMA(o, vf[i][1], p1, o);
            SB();
        });
        STAMP(26);
        auto lr = __builtin_amdgcn_permlane32_swap(f2u(o[12]), f2u(o[12]), false, false);
        const float inv = __builtin_amdgcn_rcpf(u2f(lr[1]));
#pragma unroll
        for (int i = 0; i < 16; ++i) o[i] *= inv;
        if (hh == 1) { o[12] = 0.f; o[13] = 0.f; o[14] = 0.f; o[15] = 0.f; }
        const unsigned vo = (hh * XR + r32) * 16, so = WS_O + ((4 * h) * XR + 128 * m + 32 * w) * 16;
        gst(rs, pack8(o, 0), vo, so); gst(rs, pack8(o, 1), vo, so + 2 * XROW);
        WAIT_BAR(0);
        if (tid == 0) set_flag(P.ws, flag_off(1, m * 16 + h), e);
        STAMP(4);
    }
    const bool act = w < nt;
    const int tw = tb + w;
#define GEMM_GEOM() LANE_GEOM(); \
    unsigned trW[2]; trW[0] = L0 + (w < nt ? w : nt - 1) * 256 + trq * 64 + gq * 8 + 2 * hh * 1024; trW[1] = trW[0] + 57344; PIN(trW[0]); PIN(trW[1]); \
    auto wimg = [&](int kq0, int kq1) AI { return wfrag(kq0 < 56 ? trW[0] + kq0 * 1024 : trW[1] + (kq0 - 56) * 1024, kq1 < 56 ? trW[0] + kq1 * 1024 : trW[1] + (kq1 - 56) * 1024); }; \
    unsigned xl = L0 + L_X + lane * 16; PIN(xl); \
    LAS float* m2 = (LAS float*)(uintptr_t)(L0 + L_M2 + w * 256); (void)m2
    auto gemm = [&](auto nk, const u32x4* wr, unsigned xl) AI -> f32x16 {
        constexpr int NK = decltype(nk)::value, PD = 4;
        f32x16 acc = {};
        u32x4 xf[NK];
#pragma unroll
        for (int k = 0; k < PD; ++k) xf[k] = lds128(xl + k * 1024);
        SB();
        sfor<NK>([&](auto kc) AI {
            constexpr int k = decltype(kc)::value;
            if constexpr (k + PD < NK) xf[k + PD] = lds128(xl + (k + PD) * 1024);
            MFMA(acc, wr[k], xf[k], acc);
            SB();
        });
        return acc;
    };
    u32x4 hp[2];
    float meanH, rstdH;
    {
        GEMM_GEOM();
        u32x4 xr[2];
        const char* xp = (const char*)P.x + (size_t)(32 * ti + r32) * 896 + 64 * (act ? tw : tb) + 16 * hh;
        xr[0] = *(const GAS u32x4*)xp; xr[1] = *(const GAS u32x4*)(xp + 32);
        u32x4 wr[32];
        sfor<32>([&](auto kc) AI { constexpr int k = decltype(kc)::value, kq = 7 * (k >> 1) + 4 * (k & 1);
            if constexpr (k < 31) wr[k] = wimg(kq, kq + 1);
            else { const u32x2 p = ldstr(trW[1] + (kq - 56) * 1024), q = ldstr(trW[1] + (kq - 56) * 1024 + (hh ? 0 : 1024)); wr[k] = (u32x4){p.x, p.y, q.x, q.y}; } });
        LBAR();
        STAMP(19);
        wait_flags(P.ws, flag_off(1, (ti >> 2) * 16 + 4 * w), 4, e, lane);
        STAMP(20);
        u32x4 xf[8];
#pragma unroll
        for (int k = 0; k < 8; ++k) xf[k] = gld(rs, vx, WS_O + ((16 * w + 2 * k) * XR + 32 * ti) * 16);
#pragma unroll
        for (int k = 0; k < 8; ++k) sts128(xl + (8 * w + k) * 1024, xf[k]);
        LBAR();
        STAMP(5);
        f32x16 r = gemm(IC(32), wr, xl);
        STAMP(21);
        float s1 = 0.f, s2 = 0.f;
#pragma unroll
        for (int s = 0; s < 2; ++s) {
#pragma unroll
            for (int i = 0; i < 4; ++i) {
                hp[s][i] = pk(r[8 * s + 2 * i] + blo(xr[s][i]), r[8 * s + 2 * i + 1] + bhi(xr[s][i]));
                s1 = dot2(hp[s][i], ONES, s1); s2 = dot2(hp[s][i], hp[s][i], s2);
            }
            if (act) gst(rs, hp[s], vx, WS_H + ((4 * tw + 2 * s) * XR + 32 * ti) * 16);
        }
        s1 = hsum(s1); s2 = hsum(s2);
        if (act && hh == 0) gst8(rs, (u32x2){f2u(s1), f2u(s2)}, r32 * 8, WS_STH + (tw * 2048 + 32 * ti) * 8);
        STAMP(22);
        WAIT_BAR(0);
        if (tid == 0) set_flag(P.ws, flag_off(2, ti * 4 + g), e);
        STAMP(6);
    }
#define LOAD_WR() u32x4 wr[28]; sfor<28>([&](auto kc) AI { constexpr int k = decltype(kc)::value; wr[k] = wimg(4 * k, 4 * k + 1); })
    const int fsn = w == 0 ? 1 : 2, fsl = w <= 1 ? 0 : w - 1;
    {
        GEMM_GEOM();
        BAR();
        LOAD_WR();
        LBAR();
        {
            float cs[4] = {0.f, 0.f, 0.f, 0.f};
#pragma unroll
            for (int k = 0; k < 28; ++k)
#pragma unroll
                for (int i = 0; i < 4; ++i) cs[i] = dot2(wr[k][i], ONES, cs[i]);
            const float c = hsum((cs[0] + cs[1]) + (cs[2] + cs[3]));
            if (hh == 0) m2[r32] = c;
        }
        wait_flags(P.ws, flag_off(2, ti * 4), 4, e, lane);
        u32x4 xf[7];
#pragma unroll
        for (int k = 0; k < 7; ++k) xf[k] = gld(rs, vx, WS_H + ((14 * w + 2 * k) * XR + 32 * ti) * 16);
        u32x2 sv[7];
#pragma unroll
        for (int q = 0; q < 7; ++q) sv[q] = gld8(rs, (7 * hh * 2048 + r32) * 8, WS_STH + (q * 2048 + 32 * ti) * 8);
#pragma unroll
        for (int k = 0; k < 7; ++k) sts128(xl + (7 * w + k) * 1024, xf[k]);
        LBAR();
        STAMP(7);
        f32x16 r = gemm(IC(28), wr, xl);
        float t1 = 0.f, t2 = 0.f;
#pragma unroll
        for (int q = 0; q < 7; ++q) { t1 += u2f(sv[q].x); t2 += u2f(sv[q].y); }
        t1 = hsum(t1); t2 = hsum(t2);
        meanH = t1 * (1.f / D);
        const float var = __builtin_fmaxf(t2 * (1.f / D) - meanH * meanH, 0.f);
        rstdH = __builtin_amdgcn_rsqf(var + EPS);
        const float sc = rstdH * __builtin_amdgcn_rsqf(var * rstdH * rstdH + EPS);
#pragma unroll
        for (int q = 0; q < 4; ++q) {
            const f32x4 c = *(const LAS f32x4*)(m2 + 8 * q + 4 * hh);
#pragma unroll
            for (int i = 0; i < 4; ++i) r[4 * q + i] = gelu_t((r[4 * q + i] - meanH * c[i]) * sc);
        }
#pragma unroll
        for (int s = 0; s < 2; ++s) if (act) gst(rs, pack8(r, s), vx, WS_HH + ((4 * tw + 2 * s) * XR + 32 * ti) * 16);
        WAIT_BAR(0);
        if (tid == 0) set_flag(P.ws, flag_off(3, ti * 4 + g), e);
        STAMP(8);
    }
    {
        GEMM_GEOM();
        BAR();
        LOAD_WR();
        LBAR();
        const int sh = w == 0 ? -1 : w == 1 ? 1 : w == 2 ? -2 : 2;
        const int tn = ti + (sh < 0 ? -1 : 1);
        {
            const bool nb = tn >= 0 && tn < 64;
            const int li = lane & 3;
            const GAS unsigned* p = (const GAS unsigned*)(P.ws + flag_off(3, (((li & 2) && nb) ? tn : ti) * 4 + fsl + ((li & 1) && fsn == 2 ? 1 : 0)));
            poll_word(p, e);
        }
        const int tok = 32 * ti + r32 + sh;
        const bool ok = tok >= 0 && tok < S;
        const unsigned vxs = ok ? (hh * XR + tok) * 16 : 0x7ffffff0u;
        u32x4 xf[7];
#pragma unroll
        for (int k = 0; k < 7; ++k) { xf[k] = gld(rs, vxs, WS_HH + (14 * w + 2 * k) * XROW); }
#pragma unroll
        for (int k = 0; k < 7; ++k) sts128(xl + (7 * w + k) * 1024, xf[k]);
        LBAR();
        STAMP(9);
        f32x16 r = gemm(IC(28), wr, xl);
        float s1 = 0.f, s2 = 0.f;
#pragma unroll
        for (int s = 0; s < 2; ++s) {
            u32x4 xp;
#pragma unroll
            for (int i = 0; i < 4; ++i) {
                xp[i] = pk(r[8 * s + 2 * i] + (blo(hp[s][i]) - meanH) * rstdH, r[8 * s + 2 * i + 1] + (bhi(hp[s][i]) - meanH) * rstdH);
                s1 = dot2(xp[i], ONES, s1); s2 = dot2(xp[i], xp[i], s2);
            }
            if (act) gst(rs, xp, vx, WS_X1 + ((4 * tw + 2 * s) * XR + 32 * ti) * 16);
        }
        s1 = hsum(s1); s2 = hsum(s2);
        if (act && hh == 0) gst8(rs, (u32x2){f2u(s1), f2u(s2)}, r32 * 8, WS_STX + (tw * 2048 + 32 * ti) * 8);
        WAIT_BAR(0);
        if (tid == 0) set_flag(P.ws, flag_off(4, ti * 4 + g), e);
        STAMP(10);
    }
    {
        LANE_GEOM();
        unsigned tr6[2];
        tr6[0] = L0 + trq * 64 + gq * 8 + 2 * hh * 1024; tr6[1] = tr6[0] + 57344; PIN(tr6[0]); PIN(tr6[1]);
        auto wf6 = [&](int t, int k) AI { const unsigned a = (k < 14 ? tr6[0] + k * 4096 : tr6[1] + (k - 14) * 4096) + t * 256; return wfrag(a, a + 1024); };
        const LAS float* cs6 = (const LAS float*)(uintptr_t)(L0 + L_M2 + 1024);
        wait_flags(P.ws, flag_off(4, (4 * m + w) * 4), 4, e, lane);
        STAMP(28);
        const int tk = 128 * m + 32 * w;
        auto ldx1 = [&](int k) AI { return gld(rs, vx, WS_X1 + (2 * k * XR + tk) * 16); };
        unsigned vx2 = vx; PIN(vx2);
        auto ldx2 = [&](int k) AI { return gld(rs, vx2, WS_X1 + (2 * k * XR + tk) * 16); };
        constexpr int PX = P6_PX, PW = P6_PW;
        f32x16 a0 = {}, a1 = {};
        u32x2 sv[7];
        u32x4 xg[28];
        constexpr int PG = P6_PG;
        {
            u32x4 xf[28], wA[28], wB[28];
#pragma unroll
            for (int k = 0; k < PX; ++k) xf[k] = ldx1(k);
#pragma unroll
            for (int q = 0; q < 7; ++q) sv[q] = gld8(rs, (7 * hh * 2048 + r32) * 8, WS_STX + (q * 2048 + tk) * 8);
            BAR();
            STAMP(11);
#pragma unroll
            for (int k = 0; k < PW; ++k) { wA[k] = wf6(0, k); wB[k] = wf6(1, k); }
            SB();
            sfor<28>([&](auto kc) AI {
                constexpr int k = decltype(kc)::value;
                if constexpr (k + PW < 28) { wA[k + PW] = wf6(0, k + PW); wB[k + PW] = wf6(1, k + PW); }
                MFMA(a0, wA[k], xf[k], a0);
                if constexpr (k + PX < 28) xf[k + PX] = ldx1(k + PX);
                else if constexpr (k + PX - 28 < PG) xg[k + PX - 28] = ldx2(k + PX - 28);
                MFMA(a1, wB[k], xf[k], a1);
                SB();
            });
        }
        float t1 = 0.f, t2 = 0.f;
#pragma unroll
        for (int q = 0; q < 7; ++q) { t1 += u2f(sv[q].x); t2 += u2f(sv[q].y); }
        t1 = hsum(t1); t2 = hsum(t2);
        const float mean = t1 * (1.f / D), var = __builtin_fmaxf(t2 * (1.f / D) - mean * mean, 0.f) + EPS, rstd = __builtin_amdgcn_rsqf(var);
        const GeluK gk = {-mean, GC1 * rstd * rstd * rstd, GC0 * rstd, var * rstd};
        const unsigned uo = WS_U + ((4 * tb6) * XR + tk) * 16;
        auto epi2 = [&](auto nsc, auto&& pre, f32x16& ya, int ta, f32x16& yb, int tb_, bool storeb) AI {
            constexpr int NS = decltype(nsc)::value, OPS = NS >= 28 ? 1 : 2, SK = NS >= 28 ? 5 : 2;
            f32x4 c[8];
            float z[32];
            sfor<NS>([&](auto kc) AI {
                constexpr int k = decltype(kc)::value;
                pre(kc);
                sfor<8>([&](auto qc) AI { constexpr int q = decltype(qc)::value, st = (4 * q * SK) / 8;
                    if constexpr (k == (st >= 2 ? st - 2 : 0)) c[q] = *(const LAS f32x4*)(cs6 + (q < 4 ? ta : tb_) * 32 + 8 * (q & 3) + 4 * hh); });
                sfor<32>([&](auto ec) AI {
                    constexpr int el = decltype(ec)::value, st = (el * SK) / 8;
                    sfor<8>([&](auto oc) AI { constexpr int o = decltype(oc)::value;
                        if constexpr (o / OPS == k - st) { f32x16& y = el < 16 ? ya : yb; float yy = y[el & 15]; gelu_step(o, yy, z[el], c[el >> 2][el & 3], gk); y[el & 15] = yy; } });
                });
                sfor<32>([&](auto ec) AI {
                    constexpr int el = decltype(ec)::value, st = (el * SK) / 8, o = k - st;
                    if constexpr (o >= 0 && o < 8) { if constexpr (o == 0 || o == 7) { f32x16& y = el < 16 ? ya : yb; float yy = y[el & 15]; PIN(yy); y[el & 15] = yy; } else PIN(z[el]); }
                    if constexpr (el == 15 && k == st + 8) { gst(rs, pack8(ya, 0), vx, uo + 4 * ta * XROW); gst(rs, pack8(ya, 1), vx, uo + (4 * ta + 2) * XROW); }
                });
                SB();
            });
            if (storeb) { gst(rs, pack8(yb, 0), vx, uo + 4 * tb_ * XROW); gst(rs, pack8(yb, 1), vx, uo + (4 * tb_ + 2) * XROW); }
        };
        BAR();
        STAMP(23);
        f32x16 a2 = {}, a3 = {};
        {
            u32x4 wA[28], wB[28];
#pragma unroll
            for (int k = 0; k < PW; ++k) { wA[k] = wf6(2, k); wB[k] = wf6(3, k); }
            SB();
            epi2(IC(28), [&](auto kc) AI {
                constexpr int k = decltype(kc)::value;
                if constexpr (k + PW < 28) { wA[k + PW] = wf6(2, k + PW); wB[k + PW] = wf6(3, k + PW); }
                MFMA(a2, wA[k], xg[k], a2);
                if constexpr (k + PG < 28) xg[k + PG] = ldx2(k + PG);
                MFMA(a3, wB[k], xg[k], a3);
            }, a0, 0, a1, 1, true);
        }
        STAMP(24);
        LBAR();
        auto epi1 = [&](f32x16& y, int te) AI {
#pragma unroll
            for (int q = 0; q < 4; ++q) {
                const f32x4 c = *(const LAS f32x4*)(cs6 + te * 32 + 8 * q + 4 * hh);
#pragma unroll
                for (int i = 0; i < 4; ++i) {
                    const float v = __builtin_fmaf(gk.nmean, c[i], y[4 * q + i]);
                    const float z = v * __builtin_fmaf(v * v, gk.k1, gk.k0);
                    y[4 * q + i] = v * __builtin_amdgcn_rcpf(__builtin_fmaf(__builtin_amdgcn_exp2f(z), gk.irs, gk.irs));
                }
            }
            gst(rs, pack8(y, 0), vx, uo + 4 * te * XROW); gst(rs, pack8(y, 1), vx, uo + (4 * te + 2) * XROW);
        };
        epi1(a2, 2);
        if (nt6 == 4) epi1(a3, 3);
        STAMP(29);
        WAIT_BAR(0);
        if (tid == 0) set_flag(P.ws, flag_off(5, m * 16 + s16), e);
        BAR();
        STAMP(12);
    }
    if (act7) {
        LANE_GEOM();
        const unsigned lanep = trq * 64 + gq * 8 + 2 * hh * 512;
        unsigned tA = L0 + L_X + w * 8192 + lanep, tB = wst + lanep; PIN(tA); PIN(tB);
        auto w4f = [&](int k, int t) AI { const unsigned a = (k < 4 ? tA + k * 2048 : k < 18 ? tB + (k - 4) * 2048 : tB + (k - 18) * 2048) + t * 256; return wfrag(a, a + 512); };
        u32x4 wr[28][2];
#pragma unroll
        for (int k = 0; k < 14; ++k) { wr[k][0] = w4f(k, 0); wr[k][1] = w4f(k, 1); }
        LBAR();
        const int tk = 64 * j7;
        const int tt = w >> 1, nt7 = w & 1;
        wait_flags(P.ws, flag_off(5, (j7 >> 1) * 16 + 4 * w), 4, e, lane);
        constexpr int PX = P7_PX;
        u32x4 xa[28][2];
#pragma unroll
        for (int k = 0; k < PX; ++k) { xa[k][0] = gld(rs, vx, WS_U + ((56 * w + 2 * k) * XR + tk) * 16); xa[k][1] = gld(rs, vx, WS_U + ((56 * w + 2 * k) * XR + tk + 32) * 16); }
        STAMP(13);
        SB();
        f32x16 acc[4] = {};
        u32x4 rh[2], rx[2];
        sfor<28>([&](auto kc) AI {
            constexpr int k = decltype(kc)::value;
            if constexpr (k == 13) { BAR(); SB(); }
            if constexpr (k + 3 >= 14 && k + 3 < 28) { wr[k + 3][0] = w4f(k + 3, 0); wr[k + 3][1] = w4f(k + 3, 1); }
            MFMA(acc[0], wr[k][0], xa[k][0], acc[0]); MFMA(acc[1], wr[k][1], xa[k][0], acc[1]);
            if constexpr (k + PX < 28) xa[k + PX][0] = gld(rs, vx, WS_U + ((56 * w + 2 * (k + PX)) * XR + tk) * 16);
            MFMA(acc[2], wr[k][0], xa[k][1], acc[2]); MFMA(acc[3], wr[k][1], xa[k][1], acc[3]);
            if constexpr (k + PX < 28) xa[k + PX][1] = gld(rs, vx, WS_U + ((56 * w + 2 * (k + PX)) * XR + tk + 32) * 16);
            if constexpr (k == 24 || k == 25) {
                constexpr int s = k - 24;
                const unsigned so = ((8 * g7 + 4 * nt7 + 2 * s) * XR + tk + 32 * tt) * 16;
                rh[s] = gld(rs, vx, WS_H + so); rx[s] = gld(rs, vx, WS_X1 + so);
            }
            SB();
        });
        STAMP(27);
#pragma unroll
        for (int t = 0; t < 4; ++t) {
            if (t != w) {
                const unsigned a = L0 + L_X + t * 12288 + (w < t ? w : w - 1) * 4096 + lane * 16;
#pragma unroll
                for (int q = 0; q < 4; ++q) stsf4(a + q * 1024, (f32x4){acc[t][4 * q], acc[t][4 * q + 1], acc[t][4 * q + 2], acc[t][4 * q + 3]});
            }
        }
        LBAR();
        f32x16 r = w == 0 ? acc[0] : w == 1 ? acc[1] : w == 2 ? acc[2] : acc[3];
        const unsigned a = L0 + L_X + w * 12288 + lane * 16;
#pragma unroll
        for (int j = 0; j < 3; ++j)
#pragma unroll
            for (int q = 0; q < 4; ++q) { const f32x4 v = ldsf4(a + j * 4096 + q * 1024); r[4 * q] += v[0]; r[4 * q + 1] += v[1]; r[4 * q + 2] += v[2]; r[4 * q + 3] += v[3]; }
        const unsigned oo = (tk + 32 * tt + r32) * 896 + 128 * g7 + 64 * nt7 + 16 * hh;
        char* op = (char*)P.out + oo;
        const __amdgpu_buffer_rsrc_t ro = __builtin_amdgcn_make_buffer_rsrc((void*)P.out, 0, S * D * 2, 0x00020000); (void)ro; (void)op;
#pragma unroll
        for (int s = 0; s < 2; ++s) {
            u32x4 ov;
#pragma unroll
            for (int i = 0; i < 4; ++i)
                ov[i] = pk(r[8 * s + 2 * i] + blo(rx[s][i]) + blo(rh[s][i]), r[8 * s + 2 * i + 1] + bhi(rx[s][i]) + bhi(rh[s][i]));
            __builtin_amdgcn_raw_buffer_store_b128(ov, ro, oo + 32 * s, 0, 16);
        }
        STAMP(14);
    }
    STAMP_FLUSH();
}
}

extern "C" void kernel_launch(void* const* d_in, const int* in_sizes, int n_in, void* d_out, int out_size, void* d_ws, size_t ws_size,
                              hipStream_t stream) {
    mk::Params P;
    P.x = (const u16*)d_in[0]; P.wqkv = (const u16*)d_in[1]; P.wo = (const u16*)d_in[2];
    P.w1 = (const u16*)d_in[4]; P.w2 = (const u16*)d_in[5]; P.w3 = (const u16*)d_in[6]; P.w4 = (const u16*)d_in[7];
    P.out = (u16*)d_out; P.ws = (char*)d_ws;
    mk::fused_fwd<<<256, 512, 0, stream>>>(P);
}
```

```cpp
#define NT_W 0
#define NT_QKV 0
#define NT_X 0
#define OUT_MODE 0
#define P6_PX 10
#define P6_PW 3
#define P6_PG 6
#define P7_PX 5
#define HALF 12
#define POLL2 0
#define POLL_GAP 3
#define TOUCH_DELAY 12
#define EARLY_TOUCH 1
#define TOUCH_OUT 1
#define TOUCH_WS 1
#define LATE_TOUCH 3
#include <hip/hip_runtime.h>
#include <hip/hip_bf16.h>
#include <stdint.h>
#include <type_traits>
#include <utility>

typedef unsigned short u16;
typedef __attribute__((ext_vector_type(8))) short bf16x8;
typedef __attribute__((ext_vector_type(4))) short s16x4;
typedef __attribute__((ext_vector_type(16))) float f32x16;
typedef __attribute__((ext_vector_type(4))) float f32x4;
typedef __attribute__((ext_vector_type(2))) float f32x2;
typedef __attribute__((ext_vector_type(4))) unsigned u32x4;
typedef __attribute__((ext_vector_type(2))) unsigned u32x2;
typedef __attribute__((ext_vector_type(2))) __bf16 bf2;
#define LAS __attribute__((address_space(3)))
#define GAS __attribute__((address_space(1)))
#define DEVI __device__ __forceinline__

#define MK_STAMP 0

namespace mk {
constexpr int S = 2048, D = 448;
constexpr unsigned XR = 2056, XROW = XR * 16;
constexpr unsigned KVH = 64 * 4096 + 128;
constexpr unsigned WS_EP = 0;
constexpr unsigned WS_FLAG = 4096;
constexpr unsigned WS_KV = 262144;
constexpr unsigned WS_O = WS_KV + 16 * KVH;
constexpr unsigned WS_H = WS_O + 64 * XROW;
constexpr unsigned WS_HH = WS_H + 56 * XROW;
constexpr unsigned WS_X1 = WS_HH + 56 * XROW;
constexpr unsigned WS_U = WS_X1 + 56 * XROW;
constexpr unsigned WS_STH = WS_U + 224 * XROW;
constexpr unsigned WS_STX = WS_STH + 262144;
constexpr unsigned WS_ST1 = WS_STX + 262144;
constexpr unsigned WS_SPARE = WS_ST1 + 32768;
constexpr unsigned WS_STAMP = WS_SPARE + 128;
constexpr unsigned WS_END = WS_STAMP + 16 * 512 * 8;
constexpr unsigned L_SLOT = 28672;
constexpr unsigned L_MISC = 143360;
constexpr unsigned L_KV = 114688;
constexpr unsigned L_OWNKV = L_KV + 8 * 4096;
constexpr unsigned L_WST = 28672;
constexpr unsigned L_X = 114688;
constexpr unsigned L_M2 = 147456;
constexpr unsigned CODE_TOUCH_BYTES = 36000;
constexpr float EPS = 1e-5f;
constexpr unsigned SPIN_CAP = 1u << 18;

struct Params {
    const u16* x; const u16* wqkv; const u16* wo; const u16* w1; const u16* w2; const u16* w3; const u16* w4;
    u16* out; char* ws;
};

DEVI unsigned f2u(float f) { return __builtin_bit_cast(unsigned, f); }
DEVI float u2f(unsigned u) { return __builtin_bit_cast(float, u); }
DEVI bool wall(bool p) { return __builtin_amdgcn_ballot_w64(!p) == 0; }
template <int X> DEVI float xor32(float v) { return u2f((unsigned)__builtin_amdgcn_ds_swizzle((int)f2u(v), (X << 10) | 0x1f)); }
DEVI unsigned pk(float lo, float hi) { bf2 v = {(__bf16)lo, (__bf16)hi}; return __builtin_bit_cast(unsigned, v); }
DEVI float blo(unsigned u) { return u2f(u << 16); }
DEVI float bhi(unsigned u) { return u2f(u & 0xffff0000u); }
DEVI float dot2(unsigned a, unsigned b, float c) { return __builtin_amdgcn_fdot2_f32_bf16(__builtin_bit_cast(bf2, a), __builtin_bit_cast(bf2, b), c, false); }
constexpr unsigned ONES = 0x3f803f80u;
DEVI float hsum(float v) { auto r = __builtin_amdgcn_permlane32_swap(f2u(v), f2u(v), false, false); return u2f(r[0]) + u2f(r[1]); }
DEVI float hmax(float v) { auto r = __builtin_amdgcn_permlane32_swap(f2u(v), f2u(v), false, false); return __builtin_fmaxf(u2f(r[0]), u2f(r[1])); }
DEVI f32x16 mfma(u32x4 a, u32x4 b, f32x16 c) { return __builtin_amdgcn_mfma_f32_32x32x16_bf16(__builtin_bit_cast(bf16x8, a), __builtin_bit_cast(bf16x8, b), c, 0, 0, 0); }
#define MFMA(acc, a, b, c) do { acc = mfma(a, b, c); asm volatile("" : "+v"(acc)); } while (0)
DEVI u32x4 lds128(unsigned a) { return *(const LAS u32x4*)(uintptr_t)a; }
DEVI f32x4 ldsf4(unsigned a) { return *(const LAS f32x4*)(uintptr_t)a; }
DEVI void sts128(unsigned a, u32x4 v) { *(LAS u32x4*)(uintptr_t)a = v; }
DEVI void stsf4(unsigned a, f32x4 v) { *(LAS f32x4*)(uintptr_t)a = v; }
DEVI u32x2 ldstr(unsigned a) { return __builtin_bit_cast(u32x2, __builtin_amdgcn_ds_read_tr16_b64_v4i16((LAS s16x4*)(uintptr_t)a)); }
DEVI u32x4 wfrag(unsigned a0, unsigned a1) { u32x2 p = ldstr(a0), q = ldstr(a1); return (u32x4){p.x, p.y, q.x, q.y}; }
DEVI u32x4 pack8(const f32x16& a, int s) { return (u32x4){pk(a[8 * s], a[8 * s + 1]), pk(a[8 * s + 2], a[8 * s + 3]), pk(a[8 * s + 4], a[8 * s + 5]), pk(a[8 * s + 6], a[8 * s + 7])}; }
constexpr int SC1 = 16;
DEVI u32x4 gld(__amdgpu_buffer_rsrc_t rs, unsigned voff, unsigned soff) { return __builtin_amdgcn_raw_buffer_load_b128(rs, voff, soff, SC1); }
DEVI u32x2 gld8(__amdgpu_buffer_rsrc_t rs, unsigned voff, unsigned soff) { return __builtin_amdgcn_raw_buffer_load_b64(rs, voff, soff, SC1); }
DEVI void gst(__amdgpu_buffer_rsrc_t rs, u32x4 v, unsigned voff, unsigned soff) { __builtin_amdgcn_raw_buffer_store_b128(v, rs, voff + soff, 0, SC1); }
DEVI void gst8(__amdgpu_buffer_rsrc_t rs, u32x2 v, unsigned voff, unsigned soff) { __builtin_amdgcn_raw_buffer_store_b64(v, rs, voff, soff, SC1); }
#define WAIT_VM(N) asm volatile("s_waitcnt vmcnt(" #N ")" ::: "memory")
#define WAIT_LGKM() asm volatile("s_waitcnt lgkmcnt(0)" ::: "memory")
#define WAIT_BAR(N) asm volatile("s_waitcnt vmcnt(" #N ") lgkmcnt(0)\n\ts_barrier" ::: "memory")
#define LBAR() asm volatile("s_waitcnt lgkmcnt(0)\n\ts_barrier" ::: "memory")
#define SB() __builtin_amdgcn_sched_barrier(0)
#define PIN(x) asm volatile("" : "+v"(x))
DEVI float fma1(float a, float b, float c) { return __builtin_fmaf(a, b, c); }
DEVI float mul1(float a, float b) { return a * b; }
DEVI float max3(float a, float b, float c) { return __builtin_fmaxf(__builtin_fmaxf(a, b), c); }
template <int... Is, class F> DEVI void sfor_impl(std::integer_sequence<int, Is...>, F&& f) { (f(std::integral_constant<int, Is>{}), ...); }
template <int N, class F> DEVI void sfor(F&& f) { sfor_impl(std::make_integer_sequence<int, N>{}, f); }
#define AI __attribute__((always_inline))
#define IC(n) std::integral_constant<int, n>{}

template <int NT = 0> DEVI void dma1(const char* sbase, unsigned voff, unsigned lds) {
    if (NT) asm volatile("s_mov_b32 m0, %2\n\ts_nop 0\n\tglobal_load_lds_dwordx4 %0, %1 nt" :: "v"(voff), "s"(sbase), "s"(lds) : "memory");
    else asm volatile("s_mov_b32 m0, %2\n\ts_nop 0\n\tglobal_load_lds_dwordx4 %0, %1" :: "v"(voff), "s"(sbase), "s"(lds) : "memory");
}
DEVI void touch(const char* sbase, unsigned voff, unsigned lds) {
    asm volatile("s_mov_b32 m0, %2\n\ts_nop 0\n\tglobal_load_lds_dword %0, %1" :: "v"(voff), "s"(sbase), "s"(lds) : "memory");
}
struct WSrc { const char* base; unsigned step, voff; };
DEVI void wpiece(const WSrc& s, unsigned lds, int n) { dma1<NT_W>(s.base + (size_t)n * s.step, s.voff, lds + n * 1024); }
DEVI WSrc wsrc_a(const char* base, unsigned rs, int nt, int lane) { const int t = (lane >> 4) < nt ? (lane >> 4) : nt - 1; return WSrc{base, 4 * rs, ((lane >> 2) & 3) * rs + t * 64 + (lane & 3) * 16}; }
DEVI WSrc wsrc_b(const char* base, unsigned rs, int lane) { return WSrc{base, 8 * rs, (4 * (lane >> 5) + ((lane >> 2) & 3)) * rs + ((lane >> 4) & 1) * 64 + (lane & 3) * 16}; }

DEVI void poll_word(const GAS unsigned* p, unsigned e) {
    unsigned a = __hip_atomic_load(p, __ATOMIC_RELAXED, __HIP_MEMORY_SCOPE_AGENT);
#pragma nounroll
    for (unsigned sp = 0; sp < SPIN_CAP; ++sp) {
        __builtin_amdgcn_s_sleep(POLL_GAP);
        const unsigned b2 = __hip_atomic_load(p, __ATOMIC_RELAXED, __HIP_MEMORY_SCOPE_AGENT);
        if (wall(a == e)) break;
        __builtin_amdgcn_s_sleep(POLL_GAP);
        a = __hip_atomic_load(p, __ATOMIC_RELAXED, __HIP_MEMORY_SCOPE_AGENT);
        if (wall(b2 == e)) break;
    }
    __builtin_amdgcn_fence(__ATOMIC_ACQUIRE, "wavefront");
}
DEVI void wait_flags(const char* ws, unsigned foff, int n, unsigned e, int lane) {
    const GAS unsigned* p = (const GAS unsigned*)(ws + foff) + (lane < n ? lane : 0) * 32;
    if (POLL2) poll_word(p, e);
    else {
#pragma nounroll
        for (unsigned sp = 0; sp < SPIN_CAP; ++sp) {
            unsigned v = __hip_atomic_load(p, __ATOMIC_RELAXED, __HIP_MEMORY_SCOPE_AGENT);
            if (wall(v == e)) break;
        }
        __builtin_amdgcn_fence(__ATOMIC_ACQUIRE, "wavefront");
    }
}
DEVI void set_flag(char* ws, unsigned foff, unsigned e) { __hip_atomic_store((GAS unsigned*)(ws + foff), e, __ATOMIC_RELAXED, __HIP_MEMORY_SCOPE_AGENT); }
DEVI unsigned flag_off(int ph, int idx) { return WS_FLAG + (ph * 256 + idx) * 128; }

constexpr float GC0 = -2.f * 0.7978845608028654f * 1.4426950408889634f, GC1 = GC0 * 0.044715f;
DEVI float gelu_t(float x) {
    float z = x * __builtin_fmaf(x * x, GC1, GC0);
    return x * __builtin_amdgcn_rcpf(1.f + __builtin_amdgcn_exp2f(z));
}
struct GeluK { float nmean, k1, k0, irs; };
DEVI void gelu_step(int o, float& y, float& z, float c, const GeluK& g) {
    if (o == 0) y = fma1(g.nmean, c, y);
    else if (o == 1) z = mul1(y, y);
    else if (o == 2) z = fma1(z, g.k1, g.k0);
    else if (o == 3) z = mul1(y, z);
    else if (o == 4) z = __builtin_amdgcn_exp2f(z);
    else if (o == 5) z = fma1(z, g.irs, g.irs);
    else if (o == 6) z = __builtin_amdgcn_rcpf(z);
    else y = mul1(y, z);
}

#define STAMP(k) do {} while (0)
#define STAMP_DECL
#define STAMP_FLUSH() do {} while (0)

#define LANE_GEOM() int lane_ = threadIdx.x & 63; asm volatile("" : "+v"(lane_)); const int lane = lane_, r32 = lane & 31, hh = lane >> 5; \
    const int trq = (lane & 15) >> 2, trQ_ = ((lane >> 4) & 1) * 4 + (lane & 3), gq = (trQ_ & 4) | ((trQ_ & 1) << 1) | ((trQ_ >> 1) & 1); \
    const unsigned vx = (hh * XR + r32) * 16; (void)trq; (void)gq; (void)vx; (void)r32; (void)hh
constexpr unsigned L_FLG = L_MISC + 1920, F_RDY = 0, F_DONE = 16;
DEVI unsigned ldsw_ld(unsigned i) { return *(volatile LAS unsigned*)(uintptr_t)(L_FLG + 4 * i); }
DEVI u32x2 ldsw_ld2(unsigned i) { return *(volatile LAS u32x2*)(uintptr_t)(L_FLG + 4 * i); }
DEVI void ldsw_st(unsigned i, unsigned v) { *(volatile LAS unsigned*)(uintptr_t)(L_FLG + 4 * i) = v; }
DEVI void ring_wait(int c) {
#pragma nounroll
    for (unsigned sp = 0; sp < SPIN_CAP * 16u; ++sp) { const u32x2 v = ldsw_ld2(F_RDY + 2 * c); if (__builtin_amdgcn_readfirstlane(v.x & v.y)) break; __builtin_amdgcn_s_sleep(1); }
}
DEVI void slot_wait(unsigned n, int lane) {
#pragma nounroll
    for (unsigned sp = 0; sp < SPIN_CAP * 16u; ++sp) { const unsigned v = ldsw_ld(F_DONE + (lane < 6 ? lane : 0)); if (wall(v >= n)) break; __builtin_amdgcn_s_sleep(1); }
}
#define LB(N) asm volatile("s_waitcnt vmcnt(" #N ")\n\ts_barrier" ::: "memory")
#define BAR() asm volatile("s_barrier" ::: "memory")

__global__ __launch_bounds__(512, 2) void fused_fwd(Params P) {
    __shared__ __attribute__((aligned(16))) char smem[163840];
    const unsigned L0 = (unsigned)(uintptr_t)(LAS char*)smem;
    const int tid = threadIdx.x, wv = __builtin_amdgcn_readfirstlane(tid >> 6), w = wv & 3;
    const int b = blockIdx.x, xcd = b & 7, slot = b >> 3;
    const int m = 2 * xcd + (slot >> 4), h = slot & 15;
    const int g = slot & 3, nt = g < 2 ? 4 : 3, tb = g < 2 ? 4 * g : 3 * g + 2;
    const int ti = 8 * xcd + (slot >> 2);
    const int s16 = slot & 15, pr = s16 >> 1;
    const int nt6 = ((s16 ^ pr) & 1) ? 3 : 4, tb6 = 7 * pr + ((s16 & 1) ? (pr & 1 ? 3 : 4) : 0);
    const bool act7 = slot < 28;
    const int j7 = 4 * xcd + (act7 ? slot / 7 : 0), g7 = act7 ? slot % 7 : 0;
    const unsigned wst = L0 + w * L_WST;
    const unsigned shift = (56 * h) & 15u;
    STAMP_DECL

    if (wv >= 4) {
        LANE_GEOM();
        const unsigned wrd = 16384 + hh * 6 * 256 + trq * 64 + ((shift + gq * 8) & 63);
        LAS float* csp = (LAS float*)(uintptr_t)(L0 + L_MISC);
        if (w < 2) {
            const unsigned xoffE = (lane >> 3) * 896 + (((lane & 7) ^ (lane >> 4)) << 4);
            const unsigned xoffO = (lane >> 3) * 896 + (((lane & 7) ^ (4 + (lane >> 4))) << 4);
            const unsigned colb = (56 * h) & ~15u;
            unsigned woff[3];
#pragma unroll
            for (int j = 0; j < 3; ++j) {
                const int bl = 4 * j + (lane >> 4), kql = bl / 3, tile = bl % 3;
                woff[j] = (4 * kql + ((lane >> 2) & 3)) * 2688 + tile * 896 + colb + (lane & 3) * 16;
            }
            const char* xb = (const char*)P.x + (size_t)(128 * m + 64 * w) * 896;
            const char* wb = (const char*)P.wqkv + (size_t)(32 * w) * 2688;
            LBAR();
#pragma unroll 1
            for (int kc = 0; kc < 7; ++kc) {
                if (kc >= 5) slot_wait(kc - 4, lane);
                const unsigned base = L0 + (kc >= 5 ? kc - 5 : kc) * L_SLOT;
#pragma unroll
                for (int i = 0; i < 6; ++i) dma1<NT_QKV>(wb + (size_t)kc * 64 * 2688 + (i / 3) * 16 * 2688, woff[i % 3], base + 16384 + (6 * w + i) * 1024);
#pragma unroll
                for (int i = 0; i < 8; ++i) dma1<NT_X>(xb + kc * 128 + i * 8 * 896, (i & 1) ? xoffO : xoffE, base + (8 * w + i) * 1024);
                if (kc > 0) { WAIT_VM(14); ldsw_st(F_RDY + 2 * (kc - 1) + w, 1); }
            }
            WAIT_VM(0); ldsw_st(F_RDY + 12 + w, 1);
        } else {
            const int srow = 128 * m + 8 * h + 4 * (w - 2) + (lane >> 4);
            const char* sp = (const char*)P.x + (size_t)srow * 896 + (lane & 15) * 16;
            u32x4 sr[4];
            asm volatile("global_load_dwordx4 %0, %4, off\n\tglobal_load_dwordx4 %1, %4, off offset:256\n\tglobal_load_dwordx4 %2, %4, off offset:512\n\tglobal_load_dwordx4 %3, %5, off"
                         : "=&v"(sr[0]), "=&v"(sr[1]), "=&v"(sr[2]), "=&v"(sr[3]) : "v"(sp), "v"(sp + ((lane & 15) < 8 ? 768 : 0)) : "memory");
            if (w == 2 && lane < 32) ldsw_st(lane, 0);
            LBAR();
            {
                const unsigned lx = lane < 56 ? lane : 0, lw = b * 37 + (lane < 37 ? lane : 0);
                const char* tp = !EARLY_TOUCH ? P.ws + WS_SPARE : w == 2 ? (const char*)P.x + (size_t)(8 * b + lx / 7) * 896 + (lx % 7) * 128 : (const char*)P.wqkv + (size_t)(lw < 9408 ? lw : 0) * 128;
                if (TOUCH_DELAY) __builtin_amdgcn_s_sleep(TOUCH_DELAY);
                asm volatile("s_mov_b32 m0, %1\n\ts_nop 0\n\tglobal_load_lds_dword %0, off" :: "v"(tp), "s"(L0 + L_MISC + 2048) : "memory");
            }
            {
                asm volatile("s_waitcnt vmcnt(1)" : "+v"(sr[0]), "+v"(sr[1]), "+v"(sr[2]), "+v"(sr[3]) :: "memory");
                if ((lane & 15) >= 8) sr[3] = (u32x4){0u, 0u, 0u, 0u};
                float t1 = 0.f, t2 = 0.f;
#pragma unroll
                for (int q = 0; q < 4; ++q)
#pragma unroll
                    for (int i = 0; i < 4; ++i) { t1 = dot2(sr[q][i], ONES, t1); t2 = dot2(sr[q][i], sr[q][i], t2); }
                t1 += xor32<1>(t1); t2 += xor32<1>(t2); t1 += xor32<2>(t1); t2 += xor32<2>(t2); t1 += xor32<4>(t1); t2 += xor32<4>(t2); t1 += xor32<8>(t1); t2 += xor32<8>(t2);
                const float mean = t1 * (1.f / D), var = __builtin_fmaxf(t2 * (1.f / D) - mean * mean, 0.f);
                const unsigned e = *(const unsigned*)(P.ws + WS_EP) + 1u;
                const __amdgpu_buffer_rsrc_t rs = __builtin_amdgcn_make_buffer_rsrc((void*)P.ws, 0, WS_END, 0x00020000);
                if ((lane & 15) == 0) gst(rs, (u32x4){f2u(mean), e, f2u(__builtin_amdgcn_rsqf(var + EPS)), e}, srow * 16, WS_ST1);
            }
            float cs[3][4] = {};
            const unsigned wrd2 = wrd + (w - 2) * 2 * 3072;
#pragma unroll 1
            for (int c = 0; c < 7; ++c) {
                ring_wait(c);
                if (c == LATE_TOUCH) {
                    unsigned long long pc; unsigned back;
                    asm volatile("s_getpc_b64 %0\nmk_code_here:\n\ts_mov_b32 %1, mk_code_here-_ZN2mk9fused_fwdENS_6ParamsE" : "=s"(pc), "=s"(back));
                    constexpr unsigned len = CODE_TOUCH_BYTES;
                    const unsigned oc = (slot * 18 + (lane < 18 ? lane : 0)) * 128, o3 = (b * 64 + lane) * 128, o2 = (b * 16 + (lane & 15)) * 128;
                    const char* t0 = w == 2 ? (const char*)(pc - back) + (oc < len ? oc : 0) : (const char*)P.w3 + (o3 < 1605632 ? o3 : 0);
                    const char* t1 = w == 2 ? (lane < 16 ? (const char*)P.wo : lane < 32 ? (const char*)P.w1 : (const char*)P.w2) + (o2 < 401408 ? o2 : 0) : (const char*)P.w4 + (o3 < 1605632 ? o3 : 0);
                    const unsigned oo = (slot * 14 + (lane < 14 ? lane : 0)) * 4096, li = slot * 16 + ((lane - 16) & 15);
                    const unsigned wo_ = li < 64 ? WS_O + li * XROW + 32768 : li < 456 ? WS_H + (li - 64) * XROW + 32768 : li < 472 ? WS_KV + (li - 456) * KVH + 262144 : WS_SPARE;
                    const char* t2 = (TOUCH_OUT && w == 2 && lane < 16) ? (const char*)P.out + oo : P.ws + ((TOUCH_WS && w == 2 && lane >= 16 && lane < 32) ? wo_ : WS_SPARE);
                    asm volatile("s_mov_b32 m0, %3\n\ts_nop 0\n\tglobal_load_lds_dword %0, off\n\tglobal_load_lds_dword %1, off\n\tglobal_load_lds_dword %2, off" :: "v"(t0), "v"(t1), "v"(t2), "s"(L0 + L_MISC + 2048) : "memory");
                }
                const unsigned a = L0 + (c >= 5 ? c - 5 : c) * L_SLOT + wrd2;
#pragma unroll
                for (int ks = 0; ks < 2; ++ks)
#pragma unroll
                    for (int t = 0; t < 3; ++t) { const u32x4 f = wfrag(a + ks * 3072 + t * 256, a + ks * 3072 + 768 + t * 256);
#pragma unroll
                        for (int i = 0; i < 4; ++i) cs[t][i] = dot2(f[i], ONES, cs[t][i]); }
                WAIT_LGKM();
                ldsw_st(F_DONE + 2 + w, c + 1);
            }
#pragma unroll
            for (int t = 0; t < 3; ++t) { const float v = hsum((cs[t][0] + cs[t][1]) + (cs[t][2] + cs[t][3])); if (hh == 0) csp[(w - 2) * 96 + t * 32 + r32] = v; }
            WAIT_VM(0);
            WAIT_LGKM();
            ldsw_st(F_DONE + 8 + w, 1);
            if (w == 2) {
#pragma nounroll
                for (unsigned sp2 = 0; sp2 < SPIN_CAP * 16u && __builtin_amdgcn_readfirstlane(ldsw_ld(F_DONE + 11)) == 0; ++sp2) __builtin_amdgcn_s_sleep(1);
                if (hh == 0) {
#pragma unroll
                    for (int t = 0; t < 3; ++t) csp[384 + t * 32 + r32] = csp[t * 32 + r32] + csp[96 + t * 32 + r32];
                }
            }
        }
        LBAR();
        auto stage = [&](const WSrc& s, unsigned lds, int n0, int n1) AI {
#pragma unroll 1
            for (int n = n0; n < n1; ++n) wpiece(s, lds, n);
        };
        BAR();
        BAR();
        stage(wsrc_a((const char*)P.wo + (size_t)(112 * w) * 896 + tb * 64, 896, nt, lane), wst, 0, 28);
        LB(0);
        { const WSrc s_ = wsrc_a((const char*)P.w1 + (size_t)(112 * w) * 896 + tb * 64, 896, nt, lane); BAR(); stage(s_, wst, 0, HALF); BAR(); stage(s_, wst, HALF, 28); BAR(); LB(0); }
        { const WSrc s_ = wsrc_a((const char*)P.w2 + (size_t)(112 * w) * 896 + tb * 64, 896, nt, lane); BAR(); stage(s_, wst, 0, HALF); BAR(); stage(s_, wst, HALF, 28); BAR(); LB(0); }
        { const WSrc s_ = wsrc_a((const char*)P.w3 + (size_t)(112 * w) * 3584 + tb6 * 64, 3584, nt6, lane); BAR(); stage(s_, wst, 0, HALF); BAR(); stage(s_, wst, HALF, 28); BAR(); LB(0); }
        {
            float c[4] = {0.f, 0.f, 0.f, 0.f};
            const unsigned a0 = L0 + trq * 64 + gq * 8 + 2 * hh * 1024 + w * 256;
#pragma unroll 4
            for (int k = 0; k < 28; ++k) { const unsigned a = a0 + k * 4096; const u32x4 f = wfrag(a, a + 1024);
#pragma unroll
                for (int i = 0; i < 4; ++i) c[i] = dot2(f[i], ONES, c[i]); }
            const float cc = hsum((c[0] + c[1]) + (c[2] + c[3]));
            if (hh == 0) ((LAS float*)(uintptr_t)(L0 + L_M2 + 1024))[w * 32 + r32] = cc;
        }
        if (b == 0 && w == 3) {
            const unsigned e = *(const volatile GAS unsigned*)(P.ws + WS_EP) + 1u;
            const GAS unsigned* p = (const GAS unsigned*)(P.ws + flag_off(0, lane));
#pragma nounroll
            for (unsigned sp = 0; sp < SPIN_CAP; ++sp) {
                const unsigned v0 = __hip_atomic_load(p, __ATOMIC_RELAXED, __HIP_MEMORY_SCOPE_AGENT), v1 = __hip_atomic_load(p + 64 * 32, __ATOMIC_RELAXED, __HIP_MEMORY_SCOPE_AGENT);
                const unsigned v2 = __hip_atomic_load(p + 128 * 32, __ATOMIC_RELAXED, __HIP_MEMORY_SCOPE_AGENT), v3 = __hip_atomic_load(p + 192 * 32, __ATOMIC_RELAXED, __HIP_MEMORY_SCOPE_AGENT);
                if (wall(v0 == e && v1 == e && v2 == e && v3 == e)) break;
            }
            if (lane == 0) __hip_atomic_store((GAS unsigned*)(P.ws + WS_EP), e, __ATOMIC_RELAXED, __HIP_MEMORY_SCOPE_AGENT);
        }
        LBAR();
        const WSrc w4s = wsrc_b((const char*)P.w4 + (size_t)(448 * w) * 896 + g7 * 128, 896, lane);
        if (act7) stage(w4s, L0 + L_X + w * 8192, 0, 8);
        BAR();
        if (act7) stage(w4s, wst - 8 * 1024, 8, 36);
        BAR();
        LB(0);
        if (act7) {
            BAR();
            stage(w4s, wst - 36 * 1024, 36, 56);
            LB(0);
            BAR();
        }
        STAMP_FLUSH();
        return;
    }

    const unsigned e = *(const unsigned*)(P.ws + WS_EP) + 1u;
    const __amdgpu_buffer_rsrc_t rs = __builtin_amdgcn_make_buffer_rsrc((void*)P.ws, 0, WS_END, 0x00020000);
    u32x4 qf0, qf1;
    {
        LANE_GEOM();
        const unsigned wrd = 16384 + hh * 6 * 256 + trq * 64 + ((shift + gq * 8) & 63);
        STAMP(15);
        f32x16 aq = {}, ak = {}, av = {};
        const unsigned xrd = (32 * w + r32) * 128 + ((hh ^ ((r32 >> 1) & 7)) << 4);
        struct Fr { u32x4 x, q, k, v; };
        Fr F[4];
        unsigned xb[3][4], wb3[3];
#pragma unroll
        for (int gsl = 0; gsl < 3; ++gsl) {
#pragma unroll
            for (int j = 0; j < 4; ++j) { xb[gsl][j] = L0 + (xrd ^ (j << 5)) + gsl * 2 * L_SLOT; PIN(xb[gsl][j]); }
            wb3[gsl] = L0 + wrd + gsl * 2 * L_SLOT; PIN(wb3[gsl]);
        }
        auto ldx = [&](int sl, int j) AI { F[j].x = lds128(xb[sl / 2][j] + (sl & 1) * L_SLOT); const unsigned a = wb3[sl / 2] + (sl & 1) * L_SLOT + j * 3072; F[j].q = wfrag(a, a + 768); };
        auto ldk = [&](int sl, int j) AI { const unsigned a = wb3[sl / 2] + (sl & 1) * L_SLOT + j * 3072; F[j].k = wfrag(a + 256, a + 1024); };
        auto ldv = [&](int sl, int j) AI { const unsigned a = wb3[sl / 2] + (sl & 1) * L_SLOT + j * 3072; F[j].v = wfrag(a + 512, a + 1280); };
        LAS float* stw = (LAS float*)(uintptr_t)(L0 + L_MISC + 3072 + w * 256);
        BAR();
        ring_wait(0);
        STAMP(16);
#pragma unroll
        for (int j = 0; j < 2; ++j) { ldx(0, j); ldk(0, j); ldv(0, j); }
        SB();
        u32x4 gv = {0u, 0u, 0u, 0u};
        const unsigned gvo = (128 * m + 32 * w + r32) * 16;
        sfor<7>([&](auto cc) AI {
            constexpr int c = decltype(cc)::value, sl = c >= 5 ? c - 5 : c, nsl = c >= 4 ? c - 4 : c + 1;
            u32x2 rdy;
            sfor<4>([&](auto jc) AI {
                constexpr int j = decltype(jc)::value, jn = (j + 2) & 3, lb = j < 2 ? sl : nsl;
                constexpr bool ld = j < 2 || c < 6;
                if constexpr (j == 2 && c < 6) { if (__builtin_amdgcn_readfirstlane(rdy.x & rdy.y) == 0) ring_wait(c + 1); SB(); }
                MFMA(aq, F[j].q, F[j].x, aq);
                if constexpr (ld) ldx(lb, jn);
                SB();
                MFMA(ak, F[j].k, F[j].x, ak);
                if constexpr (ld) ldk(lb, jn);
                if constexpr (j == 0 && c >= 1 && c <= 2) ldsw_st(F_DONE + w, c);
                if constexpr (j == 0 && c == 4) gv = gld(rs, gvo, WS_ST1);
                SB();
                MFMA(av, F[j].x, F[j].v, av);
                if constexpr (ld) ldv(lb, jn);
                if constexpr (j == 0 && c < 6) rdy = ldsw_ld2(F_RDY + 2 * (c + 1));
                SB();
            });
        });
#pragma nounroll
        for (unsigned sp2 = 0; sp2 < SPIN_CAP && !wall(gv.y == e && gv.w == e); ++sp2) gv = gld(rs, gvo, WS_ST1);
        if (hh == 0) { stw[r32] = u2f(gv.x) * u2f(gv.z); stw[32 + r32] = u2f(gv.z); }
        STAMP(1);
        const float mean = u2f(gv.x), rstd = u2f(gv.z);
        LBAR();
        STAMP(17);
        const LAS float* csf = (const LAS float*)(uintptr_t)(L0 + L_MISC + 1536);
        const float cv = csf[64 + r32];
        const float qs = rstd * (0.18898223650461362f * 1.4426950408889634f);
#pragma unroll
        for (int q = 0; q < 4; ++q) {
            const f32x4 c1 = *(const LAS f32x4*)(csf + 8 * q + 4 * hh), c2 = *(const LAS f32x4*)(csf + 32 + 8 * q + 4 * hh);
            const f32x4 mr = *(const LAS f32x4*)(stw + 8 * q + 4 * hh), rr = *(const LAS f32x4*)(stw + 32 + 8 * q + 4 * hh);
#pragma unroll
            for (int i = 0; i < 4; ++i) {
                aq[4 * q + i] = (aq[4 * q + i] - mean * c1[i]) * qs;
                ak[4 * q + i] = (ak[4 * q + i] - mean * c2[i]) * rstd;
                av[4 * q + i] = av[4 * q + i] * rr[i] - mr[i] * cv;
            }
        }
        if (hh == 1) { aq[12] = 1.f; aq[13] = 0.f; aq[14] = 0.f; aq[15] = 0.f; ak[12] = 0.f; ak[13] = 0.f; ak[14] = 0.f; ak[15] = 0.f; }
        if (r32 >= 28) {
            const float pv = r32 == 28 ? 1.f : 0.f;
#pragma unroll
            for (int i = 0; i < 16; ++i) av[i] = pv;
        }
        const u32x4 k0 = pack8(ak, 0), k1 = pack8(ak, 1), v0 = pack8(av, 0), v1 = pack8(av, 1);
        const unsigned go = WS_KV + h * KVH + (4 * m + w) * 4096;
        gst(rs, k0, lane * 16, go); gst(rs, k1, lane * 16, go + 1024); gst(rs, v0, lane * 16, go + 2048); gst(rs, v1, lane * 16, go + 3072);
        const unsigned lo = L0 + L_OWNKV + w * 4096 + lane * 16;
        sts128(lo, k0); sts128(lo + 1024, k1); sts128(lo + 2048, v0); sts128(lo + 3072, v1);
        qf0 = pack8(aq, 0); qf1 = pack8(aq, 1);
        STAMP(18);
        WAIT_BAR(0);
        if (tid == 0) set_flag(P.ws, flag_off(0, m * 16 + h), e);
        STAMP(2);
    }
    {
        LANE_GEOM();
        const int mt = m - 2 + (w >> 1);
        u32x4 kv[8];
        if (mt >= 0) {
            wait_flags(P.ws, flag_off(0, mt * 16 + h), 1, e, lane);
            const unsigned go = WS_KV + h * KVH + (4 * m - 8 + 2 * w) * 4096;
#pragma unroll
            for (int i = 0; i < 8; ++i) kv[i] = gld(rs, lane * 16, go + i * 1024);
        } else {
#pragma unroll
            for (int i = 0; i < 8; ++i) kv[i] = (u32x4){0u, 0u, (i & 3) == 1 && hh == 1 ? 0xF149u : 0u, 0u};
        }
        f32x16 cdiag, cold;
#pragma unroll
        for (int i = 0; i < 16; ++i) { const int kr = (i & 3) + 8 * (i >> 2) + 4 * hh; cdiag[i] = kr <= r32 ? 0.f : -1e30f; cold[i] = kr > r32 ? 0.f : -1e30f; }
#pragma unroll
        for (int i = 0; i < 8; ++i) sts128(L0 + L_KV + 2 * w * 4096 + i * 1024 + lane * 16, kv[i]);
        LBAR();
        STAMP(3);
        unsigned kvb = L0 + L_KV + w * 4096 + lane * 16;
        PIN(kvb);
        u32x4 kf[9][2], vf[9][2];
        const f32x16 zero16 = {};
        auto ldK = [&](int i) AI { kf[i][0] = lds128(kvb + i * 4096); kf[i][1] = lds128(kvb + i * 4096 + 1024); };
        auto ldV = [&](int i) AI { vf[i][0] = lds128(kvb + i * 4096 + 2048); vf[i][1] = lds128(kvb + i * 4096 + 3072); };
        auto smax = [&](const f32x16& s, float a) AI {
#pragma unroll
            for (int i = 0; i < 16; i += 2) a = max3(a, s[i], s[i + 1]);
            return a; };
        ldK(0); ldK(1);
        float mx = -3e38f;
        f32x16 s[2];
        auto smax8 = [&](const f32x16& v, int o, float a) AI {
#pragma unroll
            for (int i = 0; i < 8; i += 2) a = max3(a, v[o + i], v[o + i + 1]);
            return a; };
        sfor<9>([&](auto ic) AI {
            constexpr int i = decltype(ic)::value;
            if constexpr (i + 2 < 9) ldK(i + 2);
            MFMA(s[i & 1], kf[i][0], qf0, i == 0 ? cold : i == 8 ? cdiag : zero16);
            SB();
            MFMA(s[i & 1], kf[i][1], qf1, s[i & 1]);
            SB();
            if constexpr (i > 0) { mx = smax8(s[(i - 1) & 1], 0, mx); mx = smax8(s[(i - 1) & 1], 8, mx); PIN(mx); }
            SB();
        });
        ldK(0); ldK(1); ldV(0);
        mx = hmax(smax(s[0], mx));
        STAMP(25);
        f32x16 o = {}, cm;
#pragma unroll
        for (int i = 0; i < 16; ++i) { cm[i] = -mx; cdiag[i] -= mx; cold[i] -= mx; }
        MFMA(s[0], kf[0][0], qf0, cold); MFMA(s[0], kf[0][1], qf1, s[0]);
        SB();
        sfor<9>([&](auto ic) AI {
            constexpr int i = decltype(ic)::value;
            f32x16& c = s[i & 1];
            f32x16& n = s[(i + 1) & 1];
            if constexpr (i + 2 < 9) ldK(i + 2);
            if constexpr (i + 1 < 9) ldV(i + 1);
            if constexpr (i + 1 < 9) MFMA(n, kf[i + 1][0], qf0, i + 1 == 8 ? cdiag : cm);
#pragma unroll
            for (int j = 0; j < 8; ++j) c[j] = __builtin_amdgcn_exp2f(c[j]);
            u32x4 p0 = pack8(c, 0);
            PIN(p0);
            SB();
            if constexpr (i + 1 < 9) MFMA(n, kf[i + 1][1], qf1, n);
            MFMA(o, vf[i][0], p0, o);
#pragma unroll
            for (int j = 8; j < 16; ++j) c[j] = __builtin_amdgcn_exp2f(c[j]);
            const u32x4 p1 = pack8(c, 1);
            MFMA(o, vf[i][1], p1, o);
            SB();
        });
        STAMP(26);
        auto lr = __builtin_amdgcn_permlane32_swap(f2u(o[12]), f2u(o[12]), false, false);
        const float inv = __builtin_amdgcn_rcpf(u2f(lr[1]));
#pragma unroll
        for (int i = 0; i < 16; ++i) o[i] *= inv;
        if (hh == 1) { o[12] = 0.f; o[13] = 0.f; o[14] = 0.f; o[15] = 0.f; }
        const unsigned vo = (hh * XR + r32) * 16, so = WS_O + ((4 * h) * XR + 128 * m + 32 * w) * 16;
        gst(rs, pack8(o, 0), vo, so); gst(rs, pack8(o, 1), vo, so + 2 * XROW);
        WAIT_BAR(0);
        if (tid == 0) set_flag(P.ws, flag_off(1, m * 16 + h), e);
        STAMP(4);
    }
    const bool act = w < nt;
    const int tw = tb + w;
#define GEMM_GEOM() LANE_GEOM(); \
    unsigned trW[2]; trW[0] = L0 + (w < nt ? w : nt - 1) * 256 + trq * 64 + gq * 8 + 2 * hh * 1024; trW[1] = trW[0] + 57344; PIN(trW[0]); PIN(trW[1]); \
    auto wimg = [&](int kq0, int kq1) AI { return wfrag(kq0 < 56 ? trW[0] + kq0 * 1024 : trW[1] + (kq0 - 56) * 1024, kq1 < 56 ? trW[0] + kq1 * 1024 : trW[1] + (kq1 - 56) * 1024); }; \
    unsigned xl = L0 + L_X + lane * 16; PIN(xl); \
    LAS float* m2 = (LAS float*)(uintptr_t)(L0 + L_M2 + w * 256); (void)m2
    auto gemm = [&](auto nk, const u32x4* wr, unsigned xl) AI -> f32x16 {
        constexpr int NK = decltype(nk)::value, PD = 4;
        f32x16 acc = {};
        u32x4 xf[NK];
#pragma unroll
        for (int k = 0; k < PD; ++k) xf[k] = lds128(xl + k * 1024);
        SB();
        sfor<NK>([&](auto kc) AI {
            constexpr int k = decltype(kc)::value;
            if constexpr (k + PD < NK) xf[k + PD] = lds128(xl + (k + PD) * 1024);
            MFMA(acc, wr[k], xf[k], acc);
            SB();
        });
        return acc;
    };
    u32x4 hp[2];
    float meanH, rstdH;
    {
        GEMM_GEOM();
        u32x4 xr[2];
        const char* xp = (const char*)P.x + (size_t)(32 * ti + r32) * 896 + 64 * (act ? tw : tb) + 16 * hh;
        xr[0] = *(const GAS u32x4*)xp; xr[1] = *(const GAS u32x4*)(xp + 32);
        u32x4 wr[32];
        sfor<32>([&](auto kc) AI { constexpr int k = decltype(kc)::value, kq = 7 * (k >> 1) + 4 * (k & 1);
            if constexpr (k < 31) wr[k] = wimg(kq, kq + 1);
            else { const u32x2 p = ldstr(trW[1] + (kq - 56) * 1024), q = ldstr(trW[1] + (kq - 56) * 1024 + (hh ? 0 : 1024)); wr[k] = (u32x4){p.x, p.y, q.x, q.y}; } });
        LBAR();
        STAMP(19);
        wait_flags(P.ws, flag_off(1, (ti >> 2) * 16 + 4 * w), 4, e, lane);
        STAMP(20);
        u32x4 xf[8];
#pragma unroll
        for (int k = 0; k < 8; ++k) xf[k] = gld(rs, vx, WS_O + ((16 * w + 2 * k) * XR + 32 * ti) * 16);
#pragma unroll
        for (int k = 0; k < 8; ++k) sts128(xl + (8 * w + k) * 1024, xf[k]);
        LBAR();
        STAMP(5);
        f32x16 r = gemm(IC(32), wr, xl);
        STAMP(21);
        float s1 = 0.f, s2 = 0.f;
#pragma unroll
        for (int s = 0; s < 2; ++s) {
#pragma unroll
            for (int i = 0; i < 4; ++i) {
                hp[s][i] = pk(r[8 * s + 2 * i] + blo(xr[s][i]), r[8 * s + 2 * i + 1] + bhi(xr[s][i]));
                s1 = dot2(hp[s][i], ONES, s1); s2 = dot2(hp[s][i], hp[s][i], s2);
            }
            if (act) gst(rs, hp[s], vx, WS_H + ((4 * tw + 2 * s) * XR + 32 * ti) * 16);
        }
        s1 = hsum(s1); s2 = hsum(s2);
        if (act && hh == 0) gst8(rs, (u32x2){f2u(s1), f2u(s2)}, r32 * 8, WS_STH + (tw * 2048 + 32 * ti) * 8);
        STAMP(22);
        WAIT_BAR(0);
        if (tid == 0) set_flag(P.ws, flag_off(2, ti * 4 + g), e);
        STAMP(6);
    }
#define LOAD_WR() u32x4 wr[28]; sfor<28>([&](auto kc) AI { constexpr int k = decltype(kc)::value; wr[k] = wimg(4 * k, 4 * k + 1); })
    const int fsn = w == 0 ? 1 : 2, fsl = w <= 1 ? 0 : w - 1;
    {
        GEMM_GEOM();
        BAR();
        LOAD_WR();
        LBAR();
        {
            float cs[4] = {0.f, 0.f, 0.f, 0.f};
#pragma unroll
            for (int k = 0; k < 28; ++k)
#pragma unroll
                for (int i = 0; i < 4; ++i) cs[i] = dot2(wr[k][i], ONES, cs[i]);
            const float c = hsum((cs[0] + cs[1]) + (cs[2] + cs[3]));
            if (hh == 0) m2[r32] = c;
        }
        wait_flags(P.ws, flag_off(2, ti * 4), 4, e, lane);
        u32x4 xf[7];
#pragma unroll
        for (int k = 0; k < 7; ++k) xf[k] = gld(rs, vx, WS_H + ((14 * w + 2 * k) * XR + 32 * ti) * 16);
        u32x2 sv[7];
#pragma unroll
        for (int q = 0; q < 7; ++q) sv[q] = gld8(rs, (7 * hh * 2048 + r32) * 8, WS_STH + (q * 2048 + 32 * ti) * 8);
#pragma unroll
        for (int k = 0; k < 7; ++k) sts128(xl + (7 * w + k) * 1024, xf[k]);
        LBAR();
        STAMP(7);
        f32x16 r = gemm(IC(28), wr, xl);
        float t1 = 0.f, t2 = 0.f;
#pragma unroll
        for (int q = 0; q < 7; ++q) { t1 += u2f(sv[q].x); t2 += u2f(sv[q].y); }
        t1 = hsum(t1); t2 = hsum(t2);
        meanH = t1 * (1.f / D);
        const float var = __builtin_fmaxf(t2 * (1.f / D) - meanH * meanH, 0.f);
        rstdH = __builtin_amdgcn_rsqf(var + EPS);
        const float sc = rstdH * __builtin_amdgcn_rsqf(var * rstdH * rstdH + EPS);
#pragma unroll
        for (int q = 0; q < 4; ++q) {
            const f32x4 c = *(const LAS f32x4*)(m2 + 8 * q + 4 * hh);
#pragma unroll
            for (int i = 0; i < 4; ++i) r[4 * q + i] = gelu_t((r[4 * q + i] - meanH * c[i]) * sc);
        }
#pragma unroll
        for (int s = 0; s < 2; ++s) if (act) gst(rs, pack8(r, s), vx, WS_HH + ((4 * tw + 2 * s) * XR + 32 * ti) * 16);
        WAIT_BAR(0);
        if (tid == 0) set_flag(P.ws, flag_off(3, ti * 4 + g), e);
        STAMP(8);
    }
    {
        GEMM_GEOM();
        BAR();
        LOAD_WR();
        LBAR();
        const int sh = w == 0 ? -1 : w == 1 ? 1 : w == 2 ? -2 : 2;
        const int tn = ti + (sh < 0 ? -1 : 1);
        {
            const bool nb = tn >= 0 && tn < 64;
            const int li = lane & 3;
            const GAS unsigned* p = (const GAS unsigned*)(P.ws + flag_off(3, (((li & 2) && nb) ? tn : ti) * 4 + fsl + ((li & 1) && fsn == 2 ? 1 : 0)));
            poll_word(p, e);
        }
        const int tok = 32 * ti + r32 + sh;
        const bool ok = tok >= 0 && tok < S;
        const unsigned vxs = ok ? (hh * XR + tok) * 16 : 0x7ffffff0u;
        u32x4 xf[7];
#pragma unroll
        for (int k = 0; k < 7; ++k) { xf[k] = gld(rs, vxs, WS_HH + (14 * w + 2 * k) * XROW); }
#pragma unroll
        for (int k = 0; k < 7; ++k) sts128(xl + (7 * w + k) * 1024, xf[k]);
        LBAR();
        STAMP(9);
        f32x16 r = gemm(IC(28), wr, xl);
        float s1 = 0.f, s2 = 0.f;
#pragma unroll
        for (int s = 0; s < 2; ++s) {
            u32x4 xp;
#pragma unroll
            for (int i = 0; i < 4; ++i) {
                xp[i] = pk(r[8 * s + 2 * i] + (blo(hp[s][i]) - meanH) * rstdH, r[8 * s + 2 * i + 1] + (bhi(hp[s][i]) - meanH) * rstdH);
                s1 = dot2(xp[i], ONES, s1); s2 = dot2(xp[i], xp[i], s2);
            }
            if (act) gst(rs, xp, vx, WS_X1 + ((4 * tw + 2 * s) * XR + 32 * ti) * 16);
        }
        s1 = hsum(s1); s2 = hsum(s2);
        if (act && hh == 0) gst8(rs, (u32x2){f2u(s1), f2u(s2)}, r32 * 8, WS_STX + (tw * 2048 + 32 * ti) * 8);
        WAIT_BAR(0);
        if (tid == 0) set_flag(P.ws, flag_off(4, ti * 4 + g), e);
        STAMP(10);
    }
    {
        LANE_GEOM();
        unsigned tr6[2];
        tr6[0] = L0 + trq * 64 + gq * 8 + 2 * hh * 1024; tr6[1] = tr6[0] + 57344; PIN(tr6[0]); PIN(tr6[1]);
        auto wf6 = [&](int t, int k) AI { const unsigned a = (k < 14 ? tr6[0] + k * 4096 : tr6[1] + (k - 14) * 4096) + t * 256; return wfrag(a, a + 1024); };
        const LAS float* cs6 = (const LAS float*)(uintptr_t)(L0 + L_M2 + 1024);
        wait_flags(P.ws, flag_off(4, (4 * m + w) * 4), 4, e, lane);
        STAMP(28);
        const int tk = 128 * m + 32 * w;
        auto ldx1 = [&](int k) AI { return gld(rs, vx, WS_X1 + (2 * k * XR + tk) * 16); };
        unsigned vx2 = vx; PIN(vx2);
        auto ldx2 = [&](int k) AI { return gld(rs, vx2, WS_X1 + (2 * k * XR + tk) * 16); };
        constexpr int PX = P6_PX, PW = P6_PW;
        f32x16 a0 = {}, a1 = {};
        u32x2 sv[7];
        u32x4 xg[28];
        constexpr int PG = P6_PG;
        {
            u32x4 xf[28], wA[28], wB[28];
#pragma unroll
            for (int k = 0; k < PX; ++k) xf[k] = ldx1(k);
#pragma unroll
            for (int q = 0; q < 7; ++q) sv[q] = gld8(rs, (7 * hh * 2048 + r32) * 8, WS_STX + (q * 2048 + tk) * 8);
            BAR();
            STAMP(11);
#pragma unroll
            for (int k = 0; k < PW; ++k) { wA[k] = wf6(0, k); wB[k] = wf6(1, k); }
            SB();
            sfor<28>([&](auto kc) AI {
                constexpr int k = decltype(kc)::value;
                if constexpr (k + PW < 28) { wA[k + PW] = wf6(0, k + PW); wB[k + PW] = wf6(1, k + PW); }
                MFMA(a0, wA[k], xf[k], a0);
                if constexpr (k + PX < 28) xf[k + PX] = ldx1(k + PX);
                else if constexpr (k + PX - 28 < PG) xg[k + PX - 28] = ldx2(k + PX - 28);
                MFMA(a1, wB[k], xf[k], a1);
                SB();
            });
        }
        float t1 = 0.f, t2 = 0.f;
#pragma unroll
        for (int q = 0; q < 7; ++q) { t1 += u2f(sv[q].x); t2 += u2f(sv[q].y); }
        t1 = hsum(t1); t2 = hsum(t2);
        const float mean = t1 * (1.f / D), var = __builtin_fmaxf(t2 * (1.f / D) - mean * mean, 0.f) + EPS, rstd = __builtin_amdgcn_rsqf(var);
        const GeluK gk = {-mean, GC1 * rstd * rstd * rstd, GC0 * rstd, var * rstd};
        const unsigned uo = WS_U + ((4 * tb6) * XR + tk) * 16;
        auto epi2 = [&](auto nsc, auto&& pre, f32x16& ya, int ta, f32x16& yb, int tb_, bool storeb) AI {
            constexpr int NS = decltype(nsc)::value, OPS = NS >= 28 ? 1 : 2, SK = NS >= 28 ? 5 : 2;
            f32x4 c[8];
            float z[32];
            sfor<NS>([&](auto kc) AI {
                constexpr int k = decltype(kc)::value;
                pre(kc);
                sfor<8>([&](auto qc) AI { constexpr int q = decltype(qc)::value, st = (4 * q * SK) / 8;
                    if constexpr (k == (st >= 2 ? st - 2 : 0)) c[q] = *(const LAS f32x4*)(cs6 + (q < 4 ? ta : tb_) * 32 + 8 * (q & 3) + 4 * hh); });
                sfor<32>([&](auto ec) AI {
                    constexpr int el = decltype(ec)::value, st = (el * SK) / 8;
                    sfor<8>([&](auto oc) AI { constexpr int o = decltype(oc)::value;
                        if constexpr (o / OPS == k - st) { f32x16& y = el < 16 ? ya : yb; float yy = y[el & 15]; gelu_step(o, yy, z[el], c[el >> 2][el & 3], gk); y[el & 15] = yy; } });
                });
                sfor<32>([&](auto ec) AI {
                    constexpr int el = decltype(ec)::value, st = (el * SK) / 8, o = k - st;
                    if constexpr (o >= 0 && o < 8) { if constexpr (o == 0 || o == 7) { f32x16& y = el < 16 ? ya : yb; float yy = y[el & 15]; PIN(yy); y[el & 15] = yy; } else PIN(z[el]); }
                    if constexpr (el == 15 && k == st + 8) { gst(rs, pack8(ya, 0), vx, uo + 4 * ta * XROW); gst(rs, pack8(ya, 1), vx, uo + (4 * ta + 2) * XROW); }
                });
                SB();
            });
            if (storeb) { gst(rs, pack8(yb, 0), vx, uo + 4 * tb_ * XROW); gst(rs, pack8(yb, 1), vx, uo + (4 * tb_ + 2) * XROW); }
        };
        BAR();
        STAMP(23);
        f32x16 a2 = {}, a3 = {};
        {
            u32x4 wA[28], wB[28];
#pragma unroll
            for (int k = 0; k < PW; ++k) { wA[k] = wf6(2, k); wB[k] = wf6(3, k); }
            SB();
            epi2(IC(28), [&](auto kc) AI {
                constexpr int k = decltype(kc)::value;
                if constexpr (k + PW < 28) { wA[k + PW] = wf6(2, k + PW); wB[k + PW] = wf6(3, k + PW); }
                MFMA(a2, wA[k], xg[k], a2);
                if constexpr (k + PG < 28) xg[k + PG] = ldx2(k + PG);
                MFMA(a3, wB[k], xg[k], a3);
            }, a0, 0, a1, 1, true);
        }
        STAMP(24);
        LBAR();
        auto epi1 = [&](f32x16& y, int te) AI {
#pragma unroll
            for (int q = 0; q < 4; ++q) {
                const f32x4 c = *(const LAS f32x4*)(cs6 + te * 32 + 8 * q + 4 * hh);
#pragma unroll
                for (int i = 0; i < 4; ++i) {
                    const float v = __builtin_fmaf(gk.nmean, c[i], y[4 * q + i]);
                    const float z = v * __builtin_fmaf(v * v, gk.k1, gk.k0);
                    y[4 * q + i] = v * __builtin_amdgcn_rcpf(__builtin_fmaf(__builtin_amdgcn_exp2f(z), gk.irs, gk.irs));
                }
            }
            gst(rs, pack8(y, 0), vx, uo + 4 * te * XROW); gst(rs, pack8(y, 1), vx, uo + (4 * te + 2) * XROW);
        };
        epi1(a2, 2);
        if (nt6 == 4) epi1(a3, 3);
        STAMP(29);
        WAIT_BAR(0);
        if (tid == 0) set_flag(P.ws, flag_off(5, m * 16 + s16), e);
        BAR();
        STAMP(12);
    }
    if (act7) {
        LANE_GEOM();
        const unsigned lanep = trq * 64 + gq * 8 + 2 * hh * 512;
        unsigned tA = L0 + L_X + w * 8192 + lanep, tB = wst + lanep; PIN(tA); PIN(tB);
        auto w4f = [&](int k, int t) AI { const unsigned a = (k < 4 ? tA + k * 2048 : k < 18 ? tB + (k - 4) * 2048 : tB + (k - 18) * 2048) + t * 256; return wfrag(a, a + 512); };
        u32x4 wr[28][2];
#pragma unroll
        for (int k = 0; k < 14; ++k) { wr[k][0] = w4f(k, 0); wr[k][1] = w4f(k, 1); }
        LBAR();
        const int tk = 64 * j7;
        const int tt = w >> 1, nt7 = w & 1;
        wait_flags(P.ws, flag_off(5, (j7 >> 1) * 16 + 4 * w), 4, e, lane);
        constexpr int PX = P7_PX;
        u32x4 xa[28][2];
#pragma unroll
        for (int k = 0; k < PX; ++k) { xa[k][0] = gld(rs, vx, WS_U + ((56 * w + 2 * k) * XR + tk) * 16); xa[k][1] = gld(rs, vx, WS_U + ((56 * w + 2 * k) * XR + tk + 32) * 16); }
        STAMP(13);
        SB();
        f32x16 acc[4] = {};
        u32x4 rh[2], rx[2];
        sfor<28>([&](auto kc) AI {
            constexpr int k = decltype(kc)::value;
            if constexpr (k == 13) { BAR(); SB(); }
            if constexpr (k + 3 >= 14 && k + 3 < 28) { wr[k + 3][0] = w4f(k + 3, 0); wr[k + 3][1] = w4f(k + 3, 1); }
            MFMA(acc[0], wr[k][0], xa[k][0], acc[0]); MFMA(acc[1], wr[k][1], xa[k][0], acc[1]);
            if constexpr (k + PX < 28) xa[k + PX][0] = gld(rs, vx, WS_U + ((56 * w + 2 * (k + PX)) * XR + tk) * 16);
            MFMA(acc[2], wr[k][0], xa[k][1], acc[2]); MFMA(acc[3], wr[k][1], xa[k][1], acc[3]);
            if constexpr (k + PX < 28) xa[k + PX][1] = gld(rs, vx, WS_U + ((56 * w + 2 * (k + PX)) * XR + tk + 32) * 16);
            if constexpr (k == 24 || k == 25) {
                constexpr int s = k - 24;
                const unsigned so = ((8 * g7 + 4 * nt7 + 2 * s) * XR + tk + 32 * tt) * 16;
                rh[s] = gld(rs, vx, WS_H + so); rx[s] = gld(rs, vx, WS_X1 + so);
            }
            SB();
        });
        STAMP(27);
#pragma unroll
        for (int t = 0; t < 4; ++t) {
            if (t != w) {
                const unsigned a = L0 + L_X + t * 12288 + (w < t ? w : w - 1) * 4096 + lane * 16;
#pragma unroll
                for (int q = 0; q < 4; ++q) stsf4(a + q * 1024, (f32x4){acc[t][4 * q], acc[t][4 * q + 1], acc[t][4 * q + 2], acc[t][4 * q + 3]});
            }
        }
        LBAR();
        f32x16 r = w == 0 ? acc[0] : w == 1 ? acc[1] : w == 2 ? acc[2] : acc[3];
        const unsigned a = L0 + L_X + w * 12288 + lane * 16;
#pragma unroll
        for (int j = 0; j < 3; ++j)
#pragma unroll
            for (int q = 0; q < 4; ++q) { const f32x4 v = ldsf4(a + j * 4096 + q * 1024); r[4 * q] += v[0]; r[4 * q + 1] += v[1]; r[4 * q + 2] += v[2]; r[4 * q + 3] += v[3]; }
        const unsigned oo = (tk + 32 * tt + r32) * 896 + 128 * g7 + 64 * nt7 + 16 * hh;
        char* op = (char*)P.out + oo;
        const __amdgpu_buffer_rsrc_t ro = __builtin_amdgcn_make_buffer_rsrc((void*)P.out, 0, S * D * 2, 0x00020000); (void)ro; (void)op;
#pragma unroll
        for (int s = 0; s < 2; ++s) {
            u32x4 ov;
#pragma unroll
            for (int i = 0; i < 4; ++i)
                ov[i] = pk(r[8 * s + 2 * i] + blo(rx[s][i]) + blo(rh[s][i]), r[8 * s + 2 * i + 1] + bhi(rx[s][i]) + bhi(rh[s][i]));
            *(GAS u32x4*)(op + 32 * s) = ov;
        }
        STAMP(14);
    }
    STAMP_FLUSH();
}
}

extern "C" void kernel_launch(void* const* d_in, const int* in_sizes, int n_in, void* d_out, int out_size, void* d_ws, size_t ws_size,
                              hipStream_t stream) {
    mk::Params P;
    P.x = (const u16*)d_in[0]; P.wqkv = (const u16*)d_in[1]; P.wo = (const u16*)d_in[2];
    P.w1 = (const u16*)d_in[4]; P.w2 = (const u16*)d_in[5]; P.w3 = (const u16*)d_in[6]; P.w4 = (const u16*)d_in[7];
    P.out = (u16*)d_out; P.ws = (char*)d_ws;
    mk::fused_fwd<<<256, 512, 0, stream>>>(P);
}
```

```cpp
#include <hip/hip_runtime.h>
#include <hip/hip_bf16.h>
#include <stdint.h>
#include <type_traits>
#include <utility>

typedef unsigned short u16;
typedef __attribute__((ext_vector_type(8))) short bf16x8;
typedef __attribute__((ext_vector_type(4))) short s16x4;
typedef __attribute__((ext_vector_type(16))) float f32x16;
typedef __attribute__((ext_vector_type(4))) float f32x4;
typedef __attribute__((ext_vector_type(2))) float f32x2;
typedef __attribute__((ext_vector_type(4))) unsigned u32x4;
typedef __attribute__((ext_vector_type(2))) unsigned u32x2;
typedef __attribute__((ext_vector_type(2))) __bf16 bf2;
#define LAS __attribute__((address_space(3)))
#define GAS __attribute__((address_space(1)))
#define DEVI __device__ __forceinline__

namespace mk {
constexpr int S = 2048, D = 448;
constexpr unsigned XR = 2048, XROW = XR * 16;
constexpr unsigned KVH = 64 * 4096;
constexpr unsigned WS_EP = 0;
constexpr unsigned WS_FLAG = 4096;
constexpr unsigned WS_KV = 262144;
constexpr unsigned WS_O = WS_KV + 16 * KVH;
constexpr unsigned WS_H = WS_O + 64 * XROW;
constexpr unsigned WS_HH = WS_H + 56 * XROW;
constexpr unsigned WS_X1 = WS_HH + 56 * XROW;
constexpr unsigned WS_U = WS_X1 + 56 * XROW;
constexpr unsigned WS_STH = WS_U + 224 * XROW;
constexpr unsigned WS_STX = WS_STH + 262144;
constexpr unsigned WS_ST1 = WS_STX + 262144;
constexpr unsigned WS_END = WS_ST1 + 32768;
constexpr unsigned L_SLOT = 28672;
constexpr unsigned L_MISC = 143360;
constexpr unsigned L_KV = 114688;
constexpr unsigned L_OWNKV = L_KV + 8 * 4096;
constexpr unsigned L_WST = 28672;
constexpr unsigned L_X = 114688;
constexpr unsigned L_M2 = 147456;
constexpr unsigned CODE_TOUCH_BYTES = 36000;
constexpr float EPS = 1e-5f;
constexpr unsigned SPIN_CAP = 1u << 18;

struct Params {
    const u16* x; const u16* wqkv; const u16* wo; const u16* w1; const u16* w2; const u16* w3; const u16* w4;
    u16* out; char* ws;
};

DEVI unsigned f2u(float f) { return __builtin_bit_cast(unsigned, f); }
DEVI float u2f(unsigned u) { return __builtin_bit_cast(float, u); }
DEVI bool wall(bool p) { return __builtin_amdgcn_ballot_w64(!p) == 0; }
template <int X> DEVI float xor32(float v) { return u2f((unsigned)__builtin_amdgcn_ds_swizzle((int)f2u(v), (X << 10) | 0x1f)); }
DEVI unsigned pk(float lo, float hi) { bf2 v = {(__bf16)lo, (__bf16)hi}; return __builtin_bit_cast(unsigned, v); }
DEVI float blo(unsigned u) { return u2f(u << 16); }
DEVI float bhi(unsigned u) { return u2f(u & 0xffff0000u); }
DEVI float dot2(unsigned a, unsigned b, float c) { return __builtin_amdgcn_fdot2_f32_bf16(__builtin_bit_cast(bf2, a), __builtin_bit_cast(bf2, b), c, false); }
constexpr unsigned ONES = 0x3f803f80u;
DEVI float hsum(float v) { auto r = __builtin_amdgcn_permlane32_swap(f2u(v), f2u(v), false, false); return u2f(r[0]) + u2f(r[1]); }
DEVI float hmax(float v) { auto r = __builtin_amdgcn_permlane32_swap(f2u(v), f2u(v), false, false); return __builtin_fmaxf(u2f(r[0]), u2f(r[1])); }
DEVI f32x16 mfma(u32x4 a, u32x4 b, f32x16 c) { return __builtin_amdgcn_mfma_f32_32x32x16_bf16(__builtin_bit_cast(bf16x8, a), __builtin_bit_cast(bf16x8, b), c, 0, 0, 0); }
#define MFMA(acc, a, b, c) do { acc = mfma(a, b, c); asm volatile("" : "+v"(acc)); } while (0)
DEVI u32x4 lds128(unsigned a) { return *(const LAS u32x4*)(uintptr_t)a; }
DEVI f32x4 ldsf4(unsigned a) { return *(const LAS f32x4*)(uintptr_t)a; }
DEVI void sts128(unsigned a, u32x4 v) { *(LAS u32x4*)(uintptr_t)a = v; }
DEVI void stsf4(unsigned a, f32x4 v) { *(LAS f32x4*)(uintptr_t)a = v; }
DEVI u32x2 ldstr(unsigned a) { return __builtin_bit_cast(u32x2, __builtin_amdgcn_ds_read_tr16_b64_v4i16((LAS s16x4*)(uintptr_t)a)); }
DEVI u32x4 wfrag(unsigned a0, unsigned a1) { u32x2 p = ldstr(a0), q = ldstr(a1); return (u32x4){p.x, p.y, q.x, q.y}; }
DEVI u32x4 pack8(const f32x16& a, int s) { return (u32x4){pk(a[8 * s], a[8 * s + 1]), pk(a[8 * s + 2], a[8 * s + 3]), pk(a[8 * s + 4], a[8 * s + 5]), pk(a[8 * s + 6], a[8 * s + 7])}; }
constexpr int SC1 = 16;
DEVI u32x4 gld(__amdgpu_buffer_rsrc_t rs, unsigned voff, unsigned soff) { return __builtin_amdgcn_raw_buffer_load_b128(rs, voff, soff, SC1); }
DEVI u32x2 gld8(__amdgpu_buffer_rsrc_t rs, unsigned voff, unsigned soff) { return __builtin_amdgcn_raw_buffer_load_b64(rs, voff, soff, SC1); }
DEVI void gst(__amdgpu_buffer_rsrc_t rs, u32x4 v, unsigned voff, unsigned soff) { __builtin_amdgcn_raw_buffer_store_b128(v, rs, voff + soff, 0, SC1); }
DEVI void gst8(__amdgpu_buffer_rsrc_t rs, u32x2 v, unsigned voff, unsigned soff) { __builtin_amdgcn_raw_buffer_store_b64(v, rs, voff, soff, SC1); }
#define WAIT_VM(N) asm volatile("s_waitcnt vmcnt(" #N ")" ::: "memory")
#define WAIT_LGKM() asm volatile("s_waitcnt lgkmcnt(0)" ::: "memory")
#define WAIT_BAR(N) asm volatile("s_waitcnt vmcnt(" #N ") lgkmcnt(0)\n\ts_barrier" ::: "memory")
#define LBAR() asm volatile("s_waitcnt lgkmcnt(0)\n\ts_barrier" ::: "memory")
#define SB() __builtin_amdgcn_sched_barrier(0)
#define PIN(x) asm volatile("" : "+v"(x))
DEVI float max3(float a, float b, float c) { return __builtin_fmaxf(__builtin_fmaxf(a, b), c); }
template <int... Is, class F> DEVI void sfor_impl(std::integer_sequence<int, Is...>, F&& f) { (f(std::integral_constant<int, Is>{}), ...); }
template <int N, class F> DEVI void sfor(F&& f) { sfor_impl(std::make_integer_sequence<int, N>{}, f); }
#define AI __attribute__((always_inline))
#define IC(n) std::integral_constant<int, n>{}

DEVI void dma1(const char* sbase, unsigned voff, unsigned lds) {
    asm volatile("s_mov_b32 m0, %2\n\ts_nop 0\n\tglobal_load_lds_dwordx4 %0, %1" :: "v"(voff), "s"(sbase), "s"(lds) : "memory");
}
struct WSrc { const char* base; unsigned step, voff; };
DEVI void wpiece(const WSrc& s, unsigned lds, int n) { dma1(s.base + (size_t)n * s.step, s.voff, lds + n * 1024); }
DEVI WSrc wsrc_a(const char* base, unsigned rs, int nt, int lane) { const int t = (lane >> 4) < nt ? (lane >> 4) : nt - 1; return WSrc{base, 4 * rs, ((lane >> 2) & 3) * rs + t * 64 + (lane & 3) * 16}; }
DEVI WSrc wsrc_b(const char* base, unsigned rs, int lane) { return WSrc{base, 8 * rs, (4 * (lane >> 5) + ((lane >> 2) & 3)) * rs + ((lane >> 4) & 1) * 64 + (lane & 3) * 16}; }

DEVI void poll_word(const GAS unsigned* p, unsigned e) {
#pragma nounroll
    for (unsigned sp = 0; sp < SPIN_CAP; ++sp) {
        const unsigned v = __hip_atomic_load(p, __ATOMIC_RELAXED, __HIP_MEMORY_SCOPE_AGENT);
        if (wall(v == e)) break;
    }
    __builtin_amdgcn_fence(__ATOMIC_ACQUIRE, "wavefront");
}
DEVI void wait_flags(const char* ws, unsigned foff, int n, unsigned e, int lane) {
    poll_word((const GAS unsigned*)(ws + foff) + (lane < n ? lane : 0) * 32, e);
}
DEVI void set_flag(char* ws, unsigned foff, unsigned e) { __hip_atomic_store((GAS unsigned*)(ws + foff), e, __ATOMIC_RELAXED, __HIP_MEMORY_SCOPE_AGENT); }
DEVI unsigned flag_off(int ph, int idx) { return WS_FLAG + (ph * 256 + idx) * 128; }

constexpr float GC0 = -2.f * 0.7978845608028654f * 1.4426950408889634f, GC1 = GC0 * 0.044715f;
DEVI float gelu_t(float x) {
    float z = x * __builtin_fmaf(x * x, GC1, GC0);
    return x * __builtin_amdgcn_rcpf(1.f + __builtin_amdgcn_exp2f(z));
}
struct GeluK { float nmean, k1, k0, irs; };

#define LANE_GEOM() int lane_ = threadIdx.x & 63; asm volatile("" : "+v"(lane_)); const int lane = lane_, r32 = lane & 31, hh = lane >> 5; \
    const int trq = (lane & 15) >> 2, trQ_ = ((lane >> 4) & 1) * 4 + (lane & 3), gq = (trQ_ & 4) | ((trQ_ & 1) << 1) | ((trQ_ >> 1) & 1); \
    const unsigned vx = (hh * XR + r32) * 16; (void)trq; (void)gq; (void)vx; (void)r32; (void)hh
constexpr unsigned L_FLG = L_MISC + 1920, F_RDY = 0, F_DONE = 16;
DEVI unsigned ldsw_ld(unsigned i) { return *(volatile LAS unsigned*)(uintptr_t)(L_FLG + 4 * i); }
DEVI u32x2 ldsw_ld2(unsigned i) { return *(volatile LAS u32x2*)(uintptr_t)(L_FLG + 4 * i); }
DEVI void ldsw_st(unsigned i, unsigned v) { *(volatile LAS unsigned*)(uintptr_t)(L_FLG + 4 * i) = v; }
DEVI void ring_wait(int c) {
#pragma nounroll
    for (unsigned sp = 0; sp < SPIN_CAP * 16u; ++sp) { const u32x2 v = ldsw_ld2(F_RDY + 2 * c); if (__builtin_amdgcn_readfirstlane(v.x & v.y)) break; __builtin_amdgcn_s_sleep(1); }
}
DEVI void slot_wait(unsigned n, int lane) {
#pragma nounroll
    for (unsigned sp = 0; sp < SPIN_CAP * 16u; ++sp) { const unsigned v = ldsw_ld(F_DONE + (lane < 6 ? lane : 0)); if (wall(v >= n)) break; __builtin_amdgcn_s_sleep(1); }
}
#define LB(N) asm volatile("s_waitcnt vmcnt(" #N ")\n\ts_barrier" ::: "memory")
#define BAR() asm volatile("s_barrier" ::: "memory")

__global__ __launch_bounds__(512, 2) void fused_fwd(Params P) {
    __shared__ __attribute__((aligned(16))) char smem[163840];
    const unsigned L0 = (unsigned)(uintptr_t)(LAS char*)smem;
    const int tid = threadIdx.x, wv = __builtin_amdgcn_readfirstlane(tid >> 6), w = wv & 3;
    const int b = blockIdx.x, xcd = b & 7, slot = b >> 3;
    const int m = 2 * xcd + (slot >> 4), h = slot & 15;
    const int g = slot & 3, nt = g < 2 ? 4 : 3, tb = g < 2 ? 4 * g : 3 * g + 2;
    const int ti = 8 * xcd + (slot >> 2);
    const int s16 = slot & 15, pr = s16 >> 1;
    const int nt6 = ((s16 ^ pr) & 1) ? 3 : 4, tb6 = 7 * pr + ((s16 & 1) ? (pr & 1 ? 3 : 4) : 0);
    const bool act7 = slot < 28;
    const int j7 = 4 * xcd + (act7 ? slot / 7 : 0), g7 = act7 ? slot % 7 : 0;
    const unsigned wst = L0 + w * L_WST;
    const unsigned shift = (56 * h) & 15u;

    if (wv >= 4) {
        LANE_GEOM();
        const unsigned wrd = 16384 + hh * 6 * 256 + trq * 64 + ((shift + gq * 8) & 63);
        LAS float* csp = (LAS float*)(uintptr_t)(L0 + L_MISC);
        if (w < 2) {
            const unsigned xoffE = (lane >> 3) * 896 + (((lane & 7) ^ (lane >> 4)) << 4);
            const unsigned xoffO = (lane >> 3) * 896 + (((lane & 7) ^ (4 + (lane >> 4))) << 4);
            const unsigned colb = (56 * h) & ~15u;
            unsigned woff[3];
#pragma unroll
            for (int j = 0; j < 3; ++j) {
                const int bl = 4 * j + (lane >> 4), kql = bl / 3, tile = bl % 3;
                woff[j] = (4 * kql + ((lane >> 2) & 3)) * 2688 + tile * 896 + colb + (lane & 3) * 16;
            }
            const char* xb = (const char*)P.x + (size_t)(128 * m + 64 * w) * 896;
            const char* wb = (const char*)P.wqkv + (size_t)(32 * w) * 2688;
            LBAR();
#pragma unroll 1
            for (int kc = 0; kc < 7; ++kc) {
                if (kc >= 5) slot_wait(kc - 4, lane);
                const unsigned base = L0 + (kc >= 5 ? kc - 5 : kc) * L_SLOT;
#pragma unroll
                for (int i = 0; i < 6; ++i) dma1(wb + (size_t)kc * 64 * 2688 + (i / 3) * 16 * 2688, woff[i % 3], base + 16384 + (6 * w + i) * 1024);
#pragma unroll
                for (int i = 0; i < 8; ++i) dma1(xb + kc * 128 + i * 8 * 896, (i & 1) ? xoffO : xoffE, base + (8 * w + i) * 1024);
                if (kc > 0) { WAIT_VM(14); ldsw_st(F_RDY + 2 * (kc - 1) + w, 1); }
            }
            WAIT_VM(0); ldsw_st(F_RDY + 12 + w, 1);
            {
                ring_wait(6);
                const unsigned a = L0 + L_SLOT + wrd + w * 3072;
#pragma unroll
                for (int t = 0; t < 3; ++t) { const u32x4 f = wfrag(a + t * 256, a + 768 + t * 256);
                    const float v = hsum((dot2(f[0], ONES, 0.f) + dot2(f[1], ONES, 0.f)) + (dot2(f[2], ONES, 0.f) + dot2(f[3], ONES, 0.f)));
                    if (hh == 0) csp[(2 + w) * 96 + t * 32 + r32] = v; }
            }
        } else {
            const int srow = 128 * m + 8 * h + 4 * (w - 2) + (lane >> 4);
            const char* sp = (const char*)P.x + (size_t)srow * 896 + (lane & 15) * 16;
            u32x4 sr[4];
            asm volatile("global_load_dwordx4 %0, %4, off\n\tglobal_load_dwordx4 %1, %4, off offset:256\n\tglobal_load_dwordx4 %2, %4, off offset:512\n\tglobal_load_dwordx4 %3, %5, off"
                         : "=&v"(sr[0]), "=&v"(sr[1]), "=&v"(sr[2]), "=&v"(sr[3]) : "v"(sp), "v"(sp + ((lane & 15) < 8 ? 768 : 0)) : "memory");
            if (w == 2 && lane < 32) ldsw_st(lane, 0);
            LBAR();
            {
                const unsigned lx = lane < 56 ? lane : 0, lw = b * 37 + (lane < 37 ? lane : 0);
                const char* tp = w == 2 ? (const char*)P.x + (size_t)(8 * b + lx / 7) * 896 + (lx % 7) * 128 : (const char*)P.wqkv + (size_t)(lw < 9408 ? lw : 0) * 128;
                __builtin_amdgcn_s_sleep(12);
                asm volatile("s_mov_b32 m0, %1\n\ts_nop 0\n\tglobal_load_lds_dword %0, off" :: "v"(tp), "s"(L0 + L_MISC + 2048) : "memory");
            }
            {
                asm volatile("s_waitcnt vmcnt(1)" : "+v"(sr[0]), "+v"(sr[1]), "+v"(sr[2]), "+v"(sr[3]) :: "memory");
                if ((lane & 15) >= 8) sr[3] = (u32x4){0u, 0u, 0u, 0u};
                float t1 = 0.f, t2 = 0.f;
#pragma unroll
                for (int q = 0; q < 4; ++q)
#pragma unroll
                    for (int i = 0; i < 4; ++i) { t1 = dot2(sr[q][i], ONES, t1); t2 = dot2(sr[q][i], sr[q][i], t2); }
                t1 += xor32<1>(t1); t2 += xor32<1>(t2); t1 += xor32<2>(t1); t2 += xor32<2>(t2); t1 += xor32<4>(t1); t2 += xor32<4>(t2); t1 += xor32<8>(t1); t2 += xor32<8>(t2);
                const float mean = t1 * (1.f / D), var = __builtin_fmaxf(t2 * (1.f / D) - mean * mean, 0.f);
                const unsigned e = *(const unsigned*)(P.ws + WS_EP) + 1u;
                const __amdgpu_buffer_rsrc_t rs = __builtin_amdgcn_make_buffer_rsrc((void*)P.ws, 0, WS_END, 0x00020000);
                if ((lane & 15) == 0) gst(rs, (u32x4){f2u(mean), e, f2u(__builtin_amdgcn_rsqf(var + EPS)), e}, srow * 16, WS_ST1);
            }
            float cs[3][4] = {};
            const unsigned wrd2 = wrd + (w - 2) * 2 * 3072;
#pragma unroll 1
            for (int c = 0; c < 7; ++c) {
                ring_wait(c);
                if (c == 3) {
                    unsigned long long pc; unsigned back;
                    asm volatile("s_getpc_b64 %0\nmk_code_here:\n\ts_mov_b32 %1, mk_code_here-_ZN2mk9fused_fwdENS_6ParamsE" : "=s"(pc), "=s"(back));
                    constexpr unsigned len = CODE_TOUCH_BYTES;
                    const unsigned oc = (slot * 18 + (lane < 18 ? lane : 0)) * 128, o3 = (b * 64 + lane) * 128, o2 = (b * 16 + (lane & 15)) * 128;
                    const char* t0 = w == 2 ? (const char*)(pc - back) + (oc < len ? oc : 0) : (const char*)P.w3 + (o3 < 1605632 ? o3 : 0);
                    const char* t1 = w == 2 ? (lane < 16 ? (const char*)P.wo : lane < 32 ? (const char*)P.w1 : (const char*)P.w2) + (o2 < 401408 ? o2 : 0) : (const char*)P.w4 + (o3 < 1605632 ? o3 : 0);
                    asm volatile("s_mov_b32 m0, %2\n\ts_nop 0\n\tglobal_load_lds_dword %0, off\n\tglobal_load_lds_dword %1, off" :: "v"(t0), "v"(t1), "s"(L0 + L_MISC + 2048) : "memory");
                }
                const unsigned a = L0 + (c >= 5 ? c - 5 : c) * L_SLOT + (c < 6 ? wrd2 : wrd + w * 3072);
#pragma unroll
                for (int t = 0; t < 3; ++t) { const u32x4 f = wfrag(a + t * 256, a + 768 + t * 256);
#pragma unroll
                    for (int i = 0; i < 4; ++i) cs[t][i] = dot2(f[i], ONES, cs[t][i]); }
                if (c < 6) {
#pragma unroll
                    for (int t = 0; t < 3; ++t) { const u32x4 f = wfrag(a + 3072 + t * 256, a + 3072 + 768 + t * 256);
#pragma unroll
                        for (int i = 0; i < 4; ++i) cs[t][i] = dot2(f[i], ONES, cs[t][i]); }
                }
                WAIT_LGKM();
                ldsw_st(F_DONE + 2 + w, c + 1);
            }
#pragma unroll
            for (int t = 0; t < 3; ++t) { const float v = hsum((cs[t][0] + cs[t][1]) + (cs[t][2] + cs[t][3])); if (hh == 0) csp[(w - 2) * 96 + t * 32 + r32] = v; }
        }
        LBAR();
        auto stage = [&](const WSrc& s, unsigned lds, int n0, int n1) AI {
#pragma unroll 1
            for (int n = n0; n < n1; ++n) wpiece(s, lds, n);
        };
        LB(0);
        BAR();
        stage(wsrc_a((const char*)P.wo + (size_t)(112 * w) * 896 + tb * 64, 896, nt, lane), wst, 0, 28);
        LB(0);
        { const WSrc s_ = wsrc_a((const char*)P.w1 + (size_t)(112 * w) * 896 + tb * 64, 896, nt, lane); BAR(); stage(s_, wst, 0, 12); BAR(); stage(s_, wst, 12, 28); BAR(); LB(0); }
        { const WSrc s_ = wsrc_a((const char*)P.w2 + (size_t)(112 * w) * 896 + tb * 64, 896, nt, lane); BAR(); stage(s_, wst, 0, 12); BAR(); stage(s_, wst, 12, 28); BAR(); LB(0); }
        { const WSrc s_ = wsrc_a((const char*)P.w3 + (size_t)(112 * w) * 3584 + tb6 * 64, 3584, nt6, lane); BAR(); stage(s_, wst, 0, 12); BAR(); stage(s_, wst, 12, 28); BAR(); LB(0); }
        {
            float c[4] = {0.f, 0.f, 0.f, 0.f};
            const unsigned a0 = L0 + trq * 64 + gq * 8 + 2 * hh * 1024 + w * 256;
#pragma unroll 4
            for (int k = 0; k < 28; ++k) { const unsigned a = a0 + k * 4096; const u32x4 f = wfrag(a, a + 1024);
#pragma unroll
                for (int i = 0; i < 4; ++i) c[i] = dot2(f[i], ONES, c[i]); }
            const float cc = hsum((c[0] + c[1]) + (c[2] + c[3]));
            if (hh == 0) ((LAS float*)(uintptr_t)(L0 + L_M2 + 1024))[w * 32 + r32] = cc;
        }
        if (b == 0 && w == 3) {
            const unsigned e = *(const volatile GAS unsigned*)(P.ws + WS_EP) + 1u;
            const GAS unsigned* p = (const GAS unsigned*)(P.ws + flag_off(0, lane));
#pragma nounroll
            for (unsigned sp = 0; sp < SPIN_CAP; ++sp) {
                const unsigned v0 = __hip_atomic_load(p, __ATOMIC_RELAXED, __HIP_MEMORY_SCOPE_AGENT), v1 = __hip_atomic_load(p + 64 * 32, __ATOMIC_RELAXED, __HIP_MEMORY_SCOPE_AGENT);
                const unsigned v2 = __hip_atomic_load(p + 128 * 32, __ATOMIC_RELAXED, __HIP_MEMORY_SCOPE_AGENT), v3 = __hip_atomic_load(p + 192 * 32, __ATOMIC_RELAXED, __HIP_MEMORY_SCOPE_AGENT);
                if (wall(v0 == e && v1 == e && v2 == e && v3 == e)) break;
            }
            if (lane == 0) __hip_atomic_store((GAS unsigned*)(P.ws + WS_EP), e, __ATOMIC_RELAXED, __HIP_MEMORY_SCOPE_AGENT);
        }
        LBAR();
        const WSrc w4s = wsrc_b((const char*)P.w4 + (size_t)(448 * w) * 896 + g7 * 128, 896, lane);
        if (act7) { stage(w4s, L0 + L_X + w * 8192, 0, 8); stage(w4s, wst - 8 * 1024, 8, 36); }
        BAR();
        LB(0);
        if (act7) {
            BAR();
            stage(w4s, wst - 36 * 1024, 36, 56);
            LB(0);
            BAR();
        }
        return;
    }

    __builtin_amdgcn_s_setprio(2);
    const unsigned e = *(const unsigned*)(P.ws + WS_EP) + 1u;
    const __amdgpu_buffer_rsrc_t rs = __builtin_amdgcn_make_buffer_rsrc((void*)P.ws, 0, WS_END, 0x00020000);
    u32x4 qf0, qf1;
    {
        LANE_GEOM();
        const unsigned wrd = 16384 + hh * 6 * 256 + trq * 64 + ((shift + gq * 8) & 63);
        f32x16 aq = {}, ak = {}, av = {};
        const unsigned xrd = (32 * w + r32) * 128 + ((hh ^ ((r32 >> 1) & 7)) << 4);
        struct Fr { u32x4 x, q, k, v; };
        Fr F[4];
        unsigned xb[3][4], wb3[3];
#pragma unroll
        for (int gsl = 0; gsl < 3; ++gsl) {
#pragma unroll
            for (int j = 0; j < 4; ++j) { xb[gsl][j] = L0 + (xrd ^ (j << 5)) + gsl * 2 * L_SLOT; PIN(xb[gsl][j]); }
            wb3[gsl] = L0 + wrd + gsl * 2 * L_SLOT; PIN(wb3[gsl]);
        }
        auto ldx = [&](int sl, int j) AI { F[j].x = lds128(xb[sl / 2][j] + (sl & 1) * L_SLOT); const unsigned a = wb3[sl / 2] + (sl & 1) * L_SLOT + j * 3072; F[j].q = wfrag(a, a + 768); };
        auto ldk = [&](int sl, int j) AI { const unsigned a = wb3[sl / 2] + (sl & 1) * L_SLOT + j * 3072; F[j].k = wfrag(a + 256, a + 1024); };
        auto ldv = [&](int sl, int j) AI { const unsigned a = wb3[sl / 2] + (sl & 1) * L_SLOT + j * 3072; F[j].v = wfrag(a + 512, a + 1280); };
        LAS float* stw = (LAS float*)(uintptr_t)(L0 + L_MISC + 3072 + w * 256);
        BAR();
        ring_wait(0);
#pragma unroll
        for (int j = 0; j < 2; ++j) { ldx(0, j); ldk(0, j); ldv(0, j); }
        SB();
        u32x4 gv = {0u, 0u, 0u, 0u};
        const unsigned gvo = (128 * m + 32 * w + r32) * 16;
        sfor<7>([&](auto cc) AI {
            constexpr int c = decltype(cc)::value, sl = c >= 5 ? c - 5 : c, nsl = c >= 4 ? c - 4 : c + 1;
            u32x2 rdy;
            sfor<4>([&](auto jc) AI {
                constexpr int j = decltype(jc)::value, jn = (j + 2) & 3, lb = j < 2 ? sl : nsl;
                constexpr bool ld = j < 2 || c < 6;
                if constexpr (j == 2 && c < 6) { if (__builtin_amdgcn_readfirstlane(rdy.x & rdy.y) == 0) ring_wait(c + 1); SB(); }
                MFMA(aq, F[j].q, F[j].x, aq);
                if constexpr (ld) ldx(lb, jn);
                SB();
                MFMA(ak, F[j].k, F[j].x, ak);
                if constexpr (ld) ldk(lb, jn);
                if constexpr (j == 0 && c >= 1 && c <= 2) ldsw_st(F_DONE + w, c);
                if constexpr (j == 0 && c == 4) gv = gld(rs, gvo, WS_ST1);
                SB();
                MFMA(av, F[j].x, F[j].v, av);
                if constexpr (ld) ldv(lb, jn);
                if constexpr (j == 0 && c < 6) rdy = ldsw_ld2(F_RDY + 2 * (c + 1));
                SB();
            });
        });
#pragma nounroll
        for (unsigned sp2 = 0; sp2 < SPIN_CAP && !wall(gv.y == e && gv.w == e); ++sp2) { asm volatile("" ::: "memory"); gv = gld(rs, gvo, WS_ST1); }
        if (hh == 0) { stw[r32] = u2f(gv.x) * u2f(gv.z); stw[32 + r32] = u2f(gv.z); }
        const float mean = u2f(gv.x), rstd = u2f(gv.z);
        LBAR();
        const LAS float* csf = (const LAS float*)(uintptr_t)(L0 + L_MISC);
        const float cv = (csf[64 + r32] + csf[96 + 64 + r32]) + (csf[192 + 64 + r32] + csf[288 + 64 + r32]);
        const float qs = rstd * (0.18898223650461362f * 1.4426950408889634f);
#pragma unroll
        for (int q = 0; q < 4; ++q) {
            auto c4 = [&](int o) AI { return (*(const LAS f32x4*)(csf + o) + *(const LAS f32x4*)(csf + 96 + o)) + (*(const LAS f32x4*)(csf + 192 + o) + *(const LAS f32x4*)(csf + 288 + o)); };
            const f32x4 c1 = c4(8 * q + 4 * hh), c2 = c4(32 + 8 * q + 4 * hh);
            const f32x4 mr = *(const LAS f32x4*)(stw + 8 * q + 4 * hh), rr = *(const LAS f32x4*)(stw + 32 + 8 * q + 4 * hh);
#pragma unroll
            for (int i = 0; i < 4; ++i) {
                aq[4 * q + i] = (aq[4 * q + i] - mean * c1[i]) * qs;
                ak[4 * q + i] = (ak[4 * q + i] - mean * c2[i]) * rstd;
                av[4 * q + i] = av[4 * q + i] * rr[i] - mr[i] * cv;
            }
        }
        if (hh == 1) { aq[12] = 1.f; aq[13] = 0.f; aq[14] = 0.f; aq[15] = 0.f; ak[12] = 0.f; ak[13] = 0.f; ak[14] = 0.f; ak[15] = 0.f; }
        if (r32 >= 28) {
            const float pv = r32 == 28 ? 1.f : 0.f;
#pragma unroll
            for (int i = 0; i < 16; ++i) av[i] = pv;
        }
        const u32x4 k0 = pack8(ak, 0), k1 = pack8(ak, 1), v0 = pack8(av, 0), v1 = pack8(av, 1);
        const unsigned go = WS_KV + h * KVH + (4 * m + w) * 4096;
        gst(rs, k0, lane * 16, go); gst(rs, k1, lane * 16, go + 1024); gst(rs, v0, lane * 16, go + 2048); gst(rs, v1, lane * 16, go + 3072);
        const unsigned lo = L0 + L_OWNKV + w * 4096 + lane * 16;
        sts128(lo, k0); sts128(lo + 1024, k1); sts128(lo + 2048, v0); sts128(lo + 3072, v1);
        qf0 = pack8(aq, 0); qf1 = pack8(aq, 1);
        WAIT_BAR(0);
        if (tid == 0) set_flag(P.ws, flag_off(0, m * 16 + h), e);
    }
    {
        LANE_GEOM();
        const int mt = m - 2 + (w >> 1);
        u32x4 kv[8];
        if (mt >= 0) {
            wait_flags(P.ws, flag_off(0, mt * 16 + h), 1, e, lane);
            const unsigned go = WS_KV + h * KVH + (4 * m - 8 + 2 * w) * 4096;
#pragma unroll
            for (int i = 0; i < 8; ++i) kv[i] = gld(rs, lane * 16, go + i * 1024);
        } else {
#pragma unroll
            for (int i = 0; i < 8; ++i) kv[i] = (u32x4){0u, 0u, (i & 3) == 1 && hh == 1 ? 0xF149u : 0u, 0u};
        }
        f32x16 cdiag, cold;
#pragma unroll
        for (int i = 0; i < 16; ++i) { const int kr = (i & 3) + 8 * (i >> 2) + 4 * hh; cdiag[i] = kr <= r32 ? 0.f : -1e30f; cold[i] = kr > r32 ? 0.f : -1e30f; }
#pragma unroll
        for (int i = 0; i < 8; ++i) sts128(L0 + L_KV + 2 * w * 4096 + i * 1024 + lane * 16, kv[i]);
        LBAR();
        unsigned kvb = L0 + L_KV + w * 4096 + lane * 16;
        PIN(kvb);
        u32x4 kf[9][2], vf[9][2];
        const f32x16 zero16 = {};
        auto ldK = [&](int i) AI { kf[i][0] = lds128(kvb + i * 4096); kf[i][1] = lds128(kvb + i * 4096 + 1024); };
        auto ldV = [&](int i) AI { vf[i][0] = lds128(kvb + i * 4096 + 2048); vf[i][1] = lds128(kvb + i * 4096 + 3072); };
        auto smax = [&](const f32x16& s, float a) AI {
#pragma unroll
            for (int i = 0; i < 16; i += 2) a = max3(a, s[i], s[i + 1]);
            return a; };
        ldK(0); ldK(1);
        float mx = -3e38f;
        f32x16 s[2];
        auto smax8 = [&](const f32x16& v, int o, float a) AI {
#pragma unroll
            for (int i = 0; i < 8; i += 2) a = max3(a, v[o + i], v[o + i + 1]);
            return a; };
        sfor<9>([&](auto ic) AI {
            constexpr int i = decltype(ic)::value;
            if constexpr (i + 2 < 9) ldK(i + 2);
            MFMA(s[i & 1], kf[i][0], qf0, i == 0 ? cold : i == 8 ? cdiag : zero16);
            SB();
            MFMA(s[i & 1], kf[i][1], qf1, s[i & 1]);
            SB();
            if constexpr (i > 0) { mx = smax8(s[(i - 1) & 1], 0, mx); mx = smax8(s[(i - 1) & 1], 8, mx); PIN(mx); }
            SB();
        });
        ldK(0); ldK(1); ldV(0);
        mx = hmax(smax(s[0], mx));
        f32x16 o = {}, cm;
#pragma unroll
        for (int i = 0; i < 16; ++i) { cm[i] = -mx; cdiag[i] -= mx; cold[i] -= mx; }
        MFMA(s[0], kf[0][0], qf0, cold); MFMA(s[0], kf[0][1], qf1, s[0]);
        SB();
        sfor<9>([&](auto ic) AI {
            constexpr int i = decltype(ic)::value;
            f32x16& c = s[i & 1];
            f32x16& n = s[(i + 1) & 1];
            if constexpr (i + 2 < 9) ldK(i + 2);
            if constexpr (i + 1 < 9) ldV(i + 1);
            if constexpr (i + 1 < 9) MFMA(n, kf[i + 1][0], qf0, i + 1 == 8 ? cdiag : cm);
#pragma unroll
            for (int j = 0; j < 8; ++j) c[j] = __builtin_amdgcn_exp2f(c[j]);
            u32x4 p0 = pack8(c, 0);
            PIN(p0);
            SB();
            if constexpr (i + 1 < 9) MFMA(n, kf[i + 1][1], qf1, n);
            MFMA(o, vf[i][0], p0, o);
#pragma unroll
            for (int j = 8; j < 16; ++j) c[j] = __builtin_amdgcn_exp2f(c[j]);
            const u32x4 p1 = pack8(c, 1);
            MFMA(o, vf[i][1], p1, o);
            SB();
        });
        auto lr = __builtin_amdgcn_permlane32_swap(f2u(o[12]), f2u(o[12]), false, false);
        const float inv = __builtin_amdgcn_rcpf(u2f(lr[1]));
#pragma unroll
        for (int i = 0; i < 16; ++i) o[i] *= inv;
        if (hh == 1) { o[12] = 0.f; o[13] = 0.f; o[14] = 0.f; o[15] = 0.f; }
        const unsigned vo = (hh * XR + r32) * 16, so = WS_O + ((4 * h) * XR + 128 * m + 32 * w) * 16;
        gst(rs, pack8(o, 0), vo, so); gst(rs, pack8(o, 1), vo, so + 2 * XROW);
        WAIT_BAR(0);
        if (tid == 0) set_flag(P.ws, flag_off(1, m * 16 + h), e);
    }
    const bool act = w < nt;
    const int tw = tb + w;
#define GEMM_GEOM() LANE_GEOM(); \
    unsigned trW[2]; trW[0] = L0 + (w < nt ? w : nt - 1) * 256 + trq * 64 + gq * 8 + 2 * hh * 1024; trW[1] = trW[0] + 57344; PIN(trW[0]); PIN(trW[1]); \
    auto wimg = [&](int kq0, int kq1) AI { return wfrag(kq0 < 56 ? trW[0] + kq0 * 1024 : trW[1] + (kq0 - 56) * 1024, kq1 < 56 ? trW[0] + kq1 * 1024 : trW[1] + (kq1 - 56) * 1024); }; \
    unsigned xl = L0 + L_X + lane * 16; PIN(xl); \
    LAS float* m2 = (LAS float*)(uintptr_t)(L0 + L_M2 + w * 256); (void)m2
    auto gemm = [&](auto nk, const u32x4* wr, unsigned xl) AI -> f32x16 {
        constexpr int NK = decltype(nk)::value, PD = 4;
        f32x16 acc = {};
        u32x4 xf[NK];
#pragma unroll
        for (int k = 0; k < PD; ++k) xf[k] = lds128(xl + k * 1024);
        SB();
        sfor<NK>([&](auto kc) AI {
            constexpr int k = decltype(kc)::value;
            if constexpr (k + PD < NK) xf[k + PD] = lds128(xl + (k + PD) * 1024);
            MFMA(acc, wr[k], xf[k], acc);
            SB();
        });
        return acc;
    };
    u32x4 hp[2];
    float meanH, rstdH;
    {
        GEMM_GEOM();
        u32x4 xr[2];
        const char* xp = (const char*)P.x + (size_t)(32 * ti + r32) * 896 + 64 * (act ? tw : tb) + 16 * hh;
        xr[0] = *(const GAS u32x4*)xp; xr[1] = *(const GAS u32x4*)(xp + 32);
        u32x4 wr[32];
        sfor<32>([&](auto kc) AI { constexpr int k = decltype(kc)::value, kq = 7 * (k >> 1) + 4 * (k & 1);
            if constexpr (k < 31) wr[k] = wimg(kq, kq + 1);
            else { const u32x2 p = ldstr(trW[1] + (kq - 56) * 1024), q = ldstr(trW[1] + (kq - 56) * 1024 + (hh ? 0 : 1024)); wr[k] = (u32x4){p.x, p.y, q.x, q.y}; } });
        LBAR();
        wait_flags(P.ws, flag_off(1, (ti >> 2) * 16 + 4 * w), 4, e, lane);
        u32x4 xf[8];
#pragma unroll
        for (int k = 0; k < 8; ++k) xf[k] = gld(rs, vx, WS_O + ((16 * w + 2 * k) * XR + 32 * ti) * 16);
#pragma unroll
        for (int k = 0; k < 8; ++k) sts128(xl + (8 * w + k) * 1024, xf[k]);
        LBAR();
        f32x16 r = gemm(IC(32), wr, xl);
        float s1 = 0.f, s2 = 0.f;
#pragma unroll
        for (int s = 0; s < 2; ++s) {
#pragma unroll
            for (int i = 0; i < 4; ++i) {
                hp[s][i] = pk(r[8 * s + 2 * i] + blo(xr[s][i]), r[8 * s + 2 * i + 1] + bhi(xr[s][i]));
                s1 = dot2(hp[s][i], ONES, s1); s2 = dot2(hp[s][i], hp[s][i], s2);
            }
            if (act) gst(rs, hp[s], vx, WS_H + ((4 * tw + 2 * s) * XR + 32 * ti) * 16);
        }
        s1 = hsum(s1); s2 = hsum(s2);
        if (act && hh == 0) gst8(rs, (u32x2){f2u(s1), f2u(s2)}, r32 * 8, WS_STH + (tw * 2048 + 32 * ti) * 8);
        WAIT_BAR(0);
        if (tid == 0) set_flag(P.ws, flag_off(2, ti * 4 + g), e);
    }
#define LOAD_WR() u32x4 wr[28]; sfor<28>([&](auto kc) AI { constexpr int k = decltype(kc)::value; wr[k] = wimg(4 * k, 4 * k + 1); })
    const int fsn = w == 0 ? 1 : 2, fsl = w <= 1 ? 0 : w - 1;
    {
        GEMM_GEOM();
        BAR();
        LOAD_WR();
        LBAR();
        {
            float cs[4] = {0.f, 0.f, 0.f, 0.f};
#pragma unroll
            for (int k = 0; k < 28; ++k)
#pragma unroll
                for (int i = 0; i < 4; ++i) cs[i] = dot2(wr[k][i], ONES, cs[i]);
            const float c = hsum((cs[0] + cs[1]) + (cs[2] + cs[3]));
            if (hh == 0) m2[r32] = c;
        }
        wait_flags(P.ws, flag_off(2, ti * 4), 4, e, lane);
        u32x4 xf[7];
#pragma unroll
        for (int k = 0; k < 7; ++k) xf[k] = gld(rs, vx, WS_H + ((14 * w + 2 * k) * XR + 32 * ti) * 16);
        u32x2 sv[7];
#pragma unroll
        for (int q = 0; q < 7; ++q) sv[q] = gld8(rs, (7 * hh * 2048 + r32) * 8, WS_STH + (q * 2048 + 32 * ti) * 8);
#pragma unroll
        for (int k = 0; k < 7; ++k) sts128(xl + (7 * w + k) * 1024, xf[k]);
        LBAR();
        f32x16 r = gemm(IC(28), wr, xl);
        float t1 = 0.f, t2 = 0.f;
#pragma unroll
        for (int q = 0; q < 7; ++q) { t1 += u2f(sv[q].x); t2 += u2f(sv[q].y); }
        t1 = hsum(t1); t2 = hsum(t2);
        meanH = t1 * (1.f / D);
        const float var = __builtin_fmaxf(t2 * (1.f / D) - meanH * meanH, 0.f);
        rstdH = __builtin_amdgcn_rsqf(var + EPS);
        const float sc = rstdH * __builtin_amdgcn_rsqf(var * rstdH * rstdH + EPS);
#pragma unroll
        for (int q = 0; q < 4; ++q) {
            const f32x4 c = *(const LAS f32x4*)(m2 + 8 * q + 4 * hh);
#pragma unroll
            for (int i = 0; i < 4; ++i) r[4 * q + i] = gelu_t((r[4 * q + i] - meanH * c[i]) * sc);
        }
#pragma unroll
        for (int s = 0; s < 2; ++s) if (act) gst(rs, pack8(r, s), vx, WS_HH + ((4 * tw + 2 * s) * XR + 32 * ti) * 16);
        WAIT_BAR(0);
        if (tid == 0) set_flag(P.ws, flag_off(3, ti * 4 + g), e);
    }
    {
        GEMM_GEOM();
        BAR();
        LOAD_WR();
        LBAR();
        const int sh = w == 0 ? -1 : w == 1 ? 1 : w == 2 ? -2 : 2;
        const int tn = ti + (sh < 0 ? -1 : 1);
        {
            const bool nb = tn >= 0 && tn < 64;
            const int li = lane & 3;
            const GAS unsigned* p = (const GAS unsigned*)(P.ws + flag_off(3, (((li & 2) && nb) ? tn : ti) * 4 + fsl + ((li & 1) && fsn == 2 ? 1 : 0)));
            poll_word(p, e);
        }
        const int tok = 32 * ti + r32 + sh;
        const bool ok = tok >= 0 && tok < S;
        const unsigned vxs = ok ? (hh * XR + tok) * 16 : 0x7ffffff0u;
        u32x4 xf[7];
#pragma unroll
        for (int k = 0; k < 7; ++k) { xf[k] = gld(rs, vxs, WS_HH + (14 * w + 2 * k) * XROW); }
#pragma unroll
        for (int k = 0; k < 7; ++k) sts128(xl + (7 * w + k) * 1024, xf[k]);
        LBAR();
        f32x16 r = gemm(IC(28), wr, xl);
        float s1 = 0.f, s2 = 0.f;
#pragma unroll
        for (int s = 0; s < 2; ++s) {
            u32x4 xp;
#pragma unroll
            for (int i = 0; i < 4; ++i) {
                xp[i] = pk(r[8 * s + 2 * i] + (blo(hp[s][i]) - meanH) * rstdH, r[8 * s + 2 * i + 1] + (bhi(hp[s][i]) - meanH) * rstdH);
                s1 = dot2(xp[i], ONES, s1); s2 = dot2(xp[i], xp[i], s2);
            }
            if (act) gst(rs, xp, vx, WS_X1 + ((4 * tw + 2 * s) * XR + 32 * ti) * 16);
        }
        s1 = hsum(s1); s2 = hsum(s2);
        if (act && hh == 0) gst8(rs, (u32x2){f2u(s1), f2u(s2)}, r32 * 8, WS_STX + (tw * 2048 + 32 * ti) * 8);
        WAIT_BAR(0);
        if (tid == 0) set_flag(P.ws, flag_off(4, ti * 4 + g), e);
    }
    {
        LANE_GEOM();
        unsigned tr6[2];
        tr6[0] = L0 + trq * 64 + gq * 8 + 2 * hh * 1024; tr6[1] = tr6[0] + 57344; PIN(tr6[0]); PIN(tr6[1]);
        auto wf6 = [&](int t, int k) AI { const unsigned a = (k < 14 ? tr6[0] + k * 4096 : tr6[1] + (k - 14) * 4096) + t * 256; return wfrag(a, a + 1024); };
        const LAS float* cs6 = (const LAS float*)(uintptr_t)(L0 + L_M2 + 1024);
        wait_flags(P.ws, flag_off(4, (4 * m + w) * 4), 4, e, lane);
        const int tk = 128 * m + 32 * w;
        auto ldx1 = [&](int k) AI { return gld(rs, vx, WS_X1 + (2 * k * XR + tk) * 16); };
        constexpr int PX = 10, PW = 2;
        f32x16 acc[4] = {};
        u32x2 sv[7];
        {
            u32x4 xf[28], wf[28][4];
#pragma unroll
            for (int k = 0; k < PX; ++k) xf[k] = ldx1(k);
#pragma unroll
            for (int q = 0; q < 7; ++q) sv[q] = gld8(rs, (7 * hh * 2048 + r32) * 8, WS_STX + (q * 2048 + tk) * 8);
            BAR();
#pragma unroll
            for (int k = 0; k < PW; ++k)
#pragma unroll
                for (int t = 0; t < 4; ++t) wf[k][t] = wf6(t, k);
            SB();
            sfor<28>([&](auto kc) AI {
                constexpr int k = decltype(kc)::value;
                MFMA(acc[0], wf[k][0], xf[k], acc[0]);
                if constexpr (k + PW < 28) { wf[k + PW][0] = wf6(0, k + PW); }
                SB();
                MFMA(acc[1], wf[k][1], xf[k], acc[1]);
                if constexpr (k + PW < 28) { wf[k + PW][1] = wf6(1, k + PW); }
                if constexpr (k + PX < 28) xf[k + PX] = ldx1(k + PX);
                SB();
                MFMA(acc[2], wf[k][2], xf[k], acc[2]);
                if constexpr (k + PW < 28) { wf[k + PW][2] = wf6(2, k + PW); }
                SB();
                MFMA(acc[3], wf[k][3], xf[k], acc[3]);
                if constexpr (k + PW < 28) { wf[k + PW][3] = wf6(3, k + PW); }
                SB();
            });
        }
        LBAR();
        float t1 = 0.f, t2 = 0.f;
#pragma unroll
        for (int q = 0; q < 7; ++q) { t1 += u2f(sv[q].x); t2 += u2f(sv[q].y); }
        t1 = hsum(t1); t2 = hsum(t2);
        const float mean = t1 * (1.f / D), var = __builtin_fmaxf(t2 * (1.f / D) - mean * mean, 0.f) + EPS, rstd = __builtin_amdgcn_rsqf(var);
        const GeluK gk = {-mean, GC1 * rstd * rstd * rstd, GC0 * rstd, var * rstd};
        const unsigned uo = WS_U + ((4 * tb6) * XR + tk) * 16;
        auto epi1 = [&](f32x16& y, int te) AI {
#pragma unroll
            for (int q = 0; q < 4; ++q) {
                const f32x4 c = *(const LAS f32x4*)(cs6 + te * 32 + 8 * q + 4 * hh);
#pragma unroll
                for (int i = 0; i < 4; ++i) {
                    const float v = __builtin_fmaf(gk.nmean, c[i], y[4 * q + i]);
                    const float z = v * __builtin_fmaf(v * v, gk.k1, gk.k0);
                    y[4 * q + i] = v * __builtin_amdgcn_rcpf(__builtin_fmaf(__builtin_amdgcn_exp2f(z), gk.irs, gk.irs));
                }
            }
            gst(rs, pack8(y, 0), vx, uo + 4 * te * XROW); gst(rs, pack8(y, 1), vx, uo + (4 * te + 2) * XROW);
            SB();
        };
        epi1(acc[0], 0); epi1(acc[1], 1); epi1(acc[2], 2);
        if (nt6 == 4) epi1(acc[3], 3);
        WAIT_BAR(0);
        if (tid == 0) set_flag(P.ws, flag_off(5, m * 16 + s16), e);
        BAR();
    }
    if (act7) {
        LANE_GEOM();
        const unsigned lanep = trq * 64 + gq * 8 + 2 * hh * 512;
        unsigned tA = L0 + L_X + w * 8192 + lanep, tB = wst + lanep; PIN(tA); PIN(tB);
        auto w4f = [&](int k, int t) AI { const unsigned a = (k < 4 ? tA + k * 2048 : k < 18 ? tB + (k - 4) * 2048 : tB + (k - 18) * 2048) + t * 256; return wfrag(a, a + 512); };
        u32x4 wr[28][2];
#pragma unroll
        for (int k = 0; k < 14; ++k) { wr[k][0] = w4f(k, 0); wr[k][1] = w4f(k, 1); }
        LBAR();
        const int tk = 64 * j7;
        const int tt = w >> 1, nt7 = w & 1;
        wait_flags(P.ws, flag_off(5, (j7 >> 1) * 16 + 4 * w), 4, e, lane);
        constexpr int PX = 5;
        u32x4 xa[28][2];
#pragma unroll
        for (int k = 0; k < PX; ++k) { xa[k][0] = gld(rs, vx, WS_U + ((56 * w + 2 * k) * XR + tk) * 16); xa[k][1] = gld(rs, vx, WS_U + ((56 * w + 2 * k) * XR + tk + 32) * 16); }
        SB();
        f32x16 acc[4] = {};
        u32x4 rh[2], rx[2];
        sfor<28>([&](auto kc) AI {
            constexpr int k = decltype(kc)::value;
            if constexpr (k == 13) { BAR(); SB(); }
            if constexpr (k + 3 >= 14 && k + 3 < 28) { wr[k + 3][0] = w4f(k + 3, 0); wr[k + 3][1] = w4f(k + 3, 1); }
            MFMA(acc[0], wr[k][0], xa[k][0], acc[0]); MFMA(acc[1], wr[k][1], xa[k][0], acc[1]);
            if constexpr (k + PX < 28) xa[k + PX][0] = gld(rs, vx, WS_U + ((56 * w + 2 * (k + PX)) * XR + tk) * 16);
            MFMA(acc[2], wr[k][0], xa[k][1], acc[2]); MFMA(acc[3], wr[k][1], xa[k][1], acc[3]);
            if constexpr (k + PX < 28) xa[k + PX][1] = gld(rs, vx, WS_U + ((56 * w + 2 * (k + PX)) * XR + tk + 32) * 16);
            if constexpr (k == 24 || k == 25) {
                constexpr int s = k - 24;
                const unsigned so = ((8 * g7 + 4 * nt7 + 2 * s) * XR + tk + 32 * tt) * 16;
                rh[s] = gld(rs, vx, WS_H + so); rx[s] = gld(rs, vx, WS_X1 + so);
            }
            SB();
        });
#pragma unroll
        for (int t = 0; t < 4; ++t) {
            if (t != w) {
                const unsigned a = L0 + L_X + t * 12288 + (w < t ? w : w - 1) * 4096 + lane * 16;
#pragma unroll
                for (int q = 0; q < 4; ++q) stsf4(a + q * 1024, (f32x4){acc[t][4 * q], acc[t][4 * q + 1], acc[t][4 * q + 2], acc[t][4 * q + 3]});
            }
        }
        LBAR();
        f32x16 r = w == 0 ? acc[0] : w == 1 ? acc[1] : w == 2 ? acc[2] : acc[3];
        const unsigned a = L0 + L_X + w * 12288 + lane * 16;
#pragma unroll
        for (int j = 0; j < 3; ++j)
#pragma unroll
            for (int q = 0; q < 4; ++q) { const f32x4 v = ldsf4(a + j * 4096 + q * 1024); r[4 * q] += v[0]; r[4 * q + 1] += v[1]; r[4 * q + 2] += v[2]; r[4 * q + 3] += v[3]; }
        char* op = (char*)P.out + (size_t)(tk + 32 * tt + r32) * 896 + 128 * g7 + 64 * nt7 + 16 * hh;
#pragma unroll
        for (int s = 0; s < 2; ++s) {
            u32x4 ov;
#pragma unroll
            for (int i = 0; i < 4; ++i)
                ov[i] = pk(r[8 * s + 2 * i] + blo(rx[s][i]) + blo(rh[s][i]), r[8 * s + 2 * i + 1] + bhi(rx[s][i]) + bhi(rh[s][i]));
            *(GAS u32x4*)(op + 32 * s) = ov;
        }
    }
}
}


extern "C" void kernel_launch(void* const* d_in, const int* in_sizes, int n_in, void* d_out, int out_size, void* d_ws, size_t ws_size,
                              hipStream_t stream) {
    mk::Params P;
    P.x = (const u16*)d_in[0]; P.wqkv = (const u16*)d_in[1]; P.wo = (const u16*)d_in[2];
    P.w1 = (const u16*)d_in[4]; P.w2 = (const u16*)d_in[5]; P.w3 = (const u16*)d_in[6]; P.w4 = (const u16*)d_in[7];
    P.out = (u16*)d_out; P.ws = (char*)d_ws;
    mk::fused_fwd<<<256, 512, 0, stream>>>(P);
}
```

```cpp
#define TOUCH_FLAGS 1
#define W4SPLIT 36
#define PRIO_C 2
#define PRIO_L 0
#define NT_W 0
#define NT_QKV 0
#define NT_X 0
#define OUT_MODE 0
#define P6_PX 10
#define P6_PW 3
#define P6_PG 6
#define P7_PX 5
#define HALF 12
#define POLL2 0
#define POLL_GAP 3
#define TOUCH_DELAY 12
#define EARLY_TOUCH 1
#define LATE_TOUCH 3
#include <hip/hip_runtime.h>
#include <hip/hip_bf16.h>
#include <stdint.h>
#include <type_traits>
#include <utility>

typedef unsigned short u16;
typedef __attribute__((ext_vector_type(8))) short bf16x8;
typedef __attribute__((ext_vector_type(4))) short s16x4;
typedef __attribute__((ext_vector_type(16))) float f32x16;
typedef __attribute__((ext_vector_type(4))) float f32x4;
typedef __attribute__((ext_vector_type(2))) float f32x2;
typedef __attribute__((ext_vector_type(4))) unsigned u32x4;
typedef __attribute__((ext_vector_type(2))) unsigned u32x2;
typedef __attribute__((ext_vector_type(2))) __bf16 bf2;
#define LAS __attribute__((address_space(3)))
#define GAS __attribute__((address_space(1)))
#define DEVI __device__ __forceinline__

#define MK_STAMP 0

namespace mk {
constexpr int S = 2048, D = 448;
constexpr unsigned XR = 2056, XROW = XR * 16;
constexpr unsigned KVH = 64 * 4096 + 128;
constexpr unsigned WS_EP = 0;
constexpr unsigned WS_FLAG = 4096;
constexpr unsigned WS_KV = 262144;
constexpr unsigned WS_O = WS_KV + 16 * KVH;
constexpr unsigned WS_H = WS_O + 64 * XROW;
constexpr unsigned WS_HH = WS_H + 56 * XROW;
constexpr unsigned WS_X1 = WS_HH + 56 * XROW;
constexpr unsigned WS_U = WS_X1 + 56 * XROW;
constexpr unsigned WS_STH = WS_U + 224 * XROW;
constexpr unsigned WS_STX = WS_STH + 262144;
constexpr unsigned WS_ST1 = WS_STX + 262144;
constexpr unsigned WS_SPARE = WS_ST1 + 32768;
constexpr unsigned WS_STAMP = WS_SPARE + 128;
constexpr unsigned WS_END = WS_STAMP + 16 * 512 * 8;
constexpr unsigned L_SLOT = 28672;
constexpr unsigned L_MISC = 143360;
constexpr unsigned L_KV = 114688;
constexpr unsigned L_OWNKV = L_KV + 8 * 4096;
constexpr unsigned L_WST = 28672;
constexpr unsigned L_X = 114688;
constexpr unsigned L_M2 = 147456;
constexpr unsigned CODE_TOUCH_BYTES = 36000;
constexpr float EPS = 1e-5f;
constexpr unsigned SPIN_CAP = 1u << 18;

struct Params {
    const u16* x; const u16* wqkv; const u16* wo; const u16* w1; const u16* w2; const u16* w3; const u16* w4;
    u16* out; char* ws;
};

DEVI unsigned f2u(float f) { return __builtin_bit_cast(unsigned, f); }
DEVI float u2f(unsigned u) { return __builtin_bit_cast(float, u); }
DEVI bool wall(bool p) { return __builtin_amdgcn_ballot_w64(!p) == 0; }
template <int X> DEVI float xor32(float v) { return u2f((unsigned)__builtin_amdgcn_ds_swizzle((int)f2u(v), (X << 10) | 0x1f)); }
DEVI unsigned pk(float lo, float hi) { bf2 v = {(__bf16)lo, (__bf16)hi}; return __builtin_bit_cast(unsigned, v); }
DEVI float blo(unsigned u) { return u2f(u << 16); }
DEVI float bhi(unsigned u) { return u2f(u & 0xffff0000u); }
DEVI float dot2(unsigned a, unsigned b, float c) { return __builtin_amdgcn_fdot2_f32_bf16(__builtin_bit_cast(bf2, a), __builtin_bit_cast(bf2, b), c, false); }
constexpr unsigned ONES = 0x3f803f80u;
DEVI float hsum(float v) { auto r = __builtin_amdgcn_permlane32_swap(f2u(v), f2u(v), false, false); return u2f(r[0]) + u2f(r[1]); }
DEVI float hmax(float v) { auto r = __builtin_amdgcn_permlane32_swap(f2u(v), f2u(v), false, false); return __builtin_fmaxf(u2f(r[0]), u2f(r[1])); }
DEVI f32x16 mfma(u32x4 a, u32x4 b, f32x16 c) { return __builtin_amdgcn_mfma_f32_32x32x16_bf16(__builtin_bit_cast(bf16x8, a), __builtin_bit_cast(bf16x8, b), c, 0, 0, 0); }
#define MFMA(acc, a, b, c) do { acc = mfma(a, b, c); asm volatile("" : "+v"(acc)); } while (0)
DEVI u32x4 lds128(unsigned a) { return *(const LAS u32x4*)(uintptr_t)a; }
DEVI f32x4 ldsf4(unsigned a) { return *(const LAS f32x4*)(uintptr_t)a; }
DEVI void sts128(unsigned a, u32x4 v) { *(LAS u32x4*)(uintptr_t)a = v; }
DEVI void stsf4(unsigned a, f32x4 v) { *(LAS f32x4*)(uintptr_t)a = v; }
DEVI u32x2 ldstr(unsigned a) { return __builtin_bit_cast(u32x2, __builtin_amdgcn_ds_read_tr16_b64_v4i16((LAS s16x4*)(uintptr_t)a)); }
DEVI u32x4 wfrag(unsigned a0, unsigned a1) { u32x2 p = ldstr(a0), q = ldstr(a1); return (u32x4){p.x, p.y, q.x, q.y}; }
DEVI u32x4 pack8(const f32x16& a, int s) { return (u32x4){pk(a[8 * s], a[8 * s + 1]), pk(a[8 * s + 2], a[8 * s + 3]), pk(a[8 * s + 4], a[8 * s + 5]), pk(a[8 * s + 6], a[8 * s + 7])}; }
constexpr int SC1 = 16;
DEVI u32x4 gld(__amdgpu_buffer_rsrc_t rs, unsigned voff, unsigned soff) { return __builtin_amdgcn_raw_buffer_load_b128(rs, voff, soff, SC1); }
DEVI u32x2 gld8(__amdgpu_buffer_rsrc_t rs, unsigned voff, unsigned soff) { return __builtin_amdgcn_raw_buffer_load_b64(rs, voff, soff, SC1); }
DEVI void gst(__amdgpu_buffer_rsrc_t rs, u32x4 v, unsigned voff, unsigned soff) { __builtin_amdgcn_raw_buffer_store_b128(v, rs, voff + soff, 0, SC1); }
DEVI void gst8(__amdgpu_buffer_rsrc_t rs, u32x2 v, unsigned voff, unsigned soff) { __builtin_amdgcn_raw_buffer_store_b64(v, rs, voff, soff, SC1); }
#define WAIT_VM(N) asm volatile("s_waitcnt vmcnt(" #N ")" ::: "memory")
#define WAIT_LGKM() asm volatile("s_waitcnt lgkmcnt(0)" ::: "memory")
#define WAIT_BAR(N) asm volatile("s_waitcnt vmcnt(" #N ") lgkmcnt(0)\n\ts_barrier" ::: "memory")
#define LBAR() asm volatile("s_waitcnt lgkmcnt(0)\n\ts_barrier" ::: "memory")
#define SB() __builtin_amdgcn_sched_barrier(0)
#define PIN(x) asm volatile("" : "+v"(x))
DEVI float fma1(float a, float b, float c) { return __builtin_fmaf(a, b, c); }
DEVI float mul1(float a, float b) { return a * b; }
DEVI float max3(float a, float b, float c) { return __builtin_fmaxf(__builtin_fmaxf(a, b), c); }
template <int... Is, class F> DEVI void sfor_impl(std::integer_sequence<int, Is...>, F&& f) { (f(std::integral_constant<int, Is>{}), ...); }
template <int N, class F> DEVI void sfor(F&& f) { sfor_impl(std::make_integer_sequence<int, N>{}, f); }
#define AI __attribute__((always_inline))
#define IC(n) std::integral_constant<int, n>{}

template <int NT = 0> DEVI void dma1(const char* sbase, unsigned voff, unsigned lds) {
    if (NT) asm volatile("s_mov_b32 m0, %2\n\ts_nop 0\n\tglobal_load_lds_dwordx4 %0, %1 nt" :: "v"(voff), "s"(sbase), "s"(lds) : "memory");
    else asm volatile("s_mov_b32 m0, %2\n\ts_nop 0\n\tglobal_load_lds_dwordx4 %0, %1" :: "v"(voff), "s"(sbase), "s"(lds) : "memory");
}
DEVI void touch(const char* sbase, unsigned voff, unsigned lds) {
    asm volatile("s_mov_b32 m0, %2\n\ts_nop 0\n\tglobal_load_lds_dword %0, %1" :: "v"(voff), "s"(sbase), "s"(lds) : "memory");
}
struct WSrc { const char* base; unsigned step, voff; };
DEVI void wpiece(const WSrc& s, unsigned lds, int n) { dma1<NT_W>(s.base + (size_t)n * s.step, s.voff, lds + n * 1024); }
DEVI WSrc wsrc_a(const char* base, unsigned rs, int nt, int lane) { const int t = (lane >> 4) < nt ? (lane >> 4) : nt - 1; return WSrc{base, 4 * rs, ((lane >> 2) & 3) * rs + t * 64 + (lane & 3) * 16}; }
DEVI WSrc wsrc_b(const char* base, unsigned rs, int lane) { return WSrc{base, 8 * rs, (4 * (lane >> 5) + ((lane >> 2) & 3)) * rs + ((lane >> 4) & 1) * 64 + (lane & 3) * 16}; }

DEVI void poll_word(const GAS unsigned* p, unsigned e) {
    unsigned a = __hip_atomic_load(p, __ATOMIC_RELAXED, __HIP_MEMORY_SCOPE_AGENT);
#pragma nounroll
    for (unsigned sp = 0; sp < SPIN_CAP; ++sp) {
        __builtin_amdgcn_s_sleep(POLL_GAP);
        const unsigned b2 = __hip_atomic_load(p, __ATOMIC_RELAXED, __HIP_MEMORY_SCOPE_AGENT);
        if (wall(a == e)) break;
        __builtin_amdgcn_s_sleep(POLL_GAP);
        a = __hip_atomic_load(p, __ATOMIC_RELAXED, __HIP_MEMORY_SCOPE_AGENT);
        if (wall(b2 == e)) break;
    }
    __builtin_amdgcn_fence(__ATOMIC_ACQUIRE, "wavefront");
}
DEVI void wait_flags(const char* ws, unsigned foff, int n, unsigned e, int lane) {
    const GAS unsigned* p = (const GAS unsigned*)(ws + foff) + (lane < n ? lane : 0) * 32;
    if (POLL2) poll_word(p, e);
    else {
#pragma nounroll
        for (unsigned sp = 0; sp < SPIN_CAP; ++sp) {
            unsigned v = __hip_atomic_load(p, __ATOMIC_RELAXED, __HIP_MEMORY_SCOPE_AGENT);
            if (wall(v == e)) break;
        }
        __builtin_amdgcn_fence(__ATOMIC_ACQUIRE, "wavefront");
    }
}
DEVI void set_flag(char* ws, unsigned foff, unsigned e) { __hip_atomic_store((GAS unsigned*)(ws + foff), e, __ATOMIC_RELAXED, __HIP_MEMORY_SCOPE_AGENT); }
DEVI unsigned flag_off(int ph, int idx) { return WS_FLAG + (ph * 256 + idx) * 128; }

constexpr float GC0 = -2.f * 0.7978845608028654f * 1.4426950408889634f, GC1 = GC0 * 0.044715f;
DEVI float gelu_t(float x) {
    float z = x * __builtin_fmaf(x * x, GC1, GC0);
    return x * __builtin_amdgcn_rcpf(1.f + __builtin_amdgcn_exp2f(z));
}
struct GeluK { float nmean, k1, k0, irs; };
DEVI void gelu_step(int o, float& y, float& z, float c, const GeluK& g) {
    if (o == 0) y = fma1(g.nmean, c, y);
    else if (o == 1) z = mul1(y, y);
    else if (o == 2) z = fma1(z, g.k1, g.k0);
    else if (o == 3) z = mul1(y, z);
    else if (o == 4) z = __builtin_amdgcn_exp2f(z);
    else if (o == 5) z = fma1(z, g.irs, g.irs);
    else if (o == 6) z = __builtin_amdgcn_rcpf(z);
    else y = mul1(y, z);
}

#define STAMP(k) do {} while (0)
#define STAMP_DECL
#define STAMP_FLUSH() do {} while (0)

#define LANE_GEOM() int lane_ = threadIdx.x & 63; asm volatile("" : "+v"(lane_)); const int lane = lane_, r32 = lane & 31, hh = lane >> 5; \
    const int trq = (lane & 15) >> 2, trQ_ = ((lane >> 4) & 1) * 4 + (lane & 3), gq = (trQ_ & 4) | ((trQ_ & 1) << 1) | ((trQ_ >> 1) & 1); \
    const unsigned vx = (hh * XR + r32) * 16; (void)trq; (void)gq; (void)vx; (void)r32; (void)hh
constexpr unsigned L_FLG = L_MISC + 1920, F_RDY = 0, F_DONE = 16;
DEVI unsigned ldsw_ld(unsigned i) { return *(volatile LAS unsigned*)(uintptr_t)(L_FLG + 4 * i); }
DEVI u32x2 ldsw_ld2(unsigned i) { return *(volatile LAS u32x2*)(uintptr_t)(L_FLG + 4 * i); }
DEVI void ldsw_st(unsigned i, unsigned v) { *(volatile LAS unsigned*)(uintptr_t)(L_FLG + 4 * i) = v; }
DEVI void ring_wait(int c) {
#pragma nounroll
    for (unsigned sp = 0; sp < SPIN_CAP * 16u; ++sp) { const u32x2 v = ldsw_ld2(F_RDY + 2 * c); if (__builtin_amdgcn_readfirstlane(v.x & v.y)) break; __builtin_amdgcn_s_sleep(1); }
}
DEVI void slot_wait(unsigned n, int lane) {
#pragma nounroll
    for (unsigned sp = 0; sp < SPIN_CAP * 16u; ++sp) { const unsigned v = ldsw_ld(F_DONE + (lane < 6 ? lane : 0)); if (wall(v >= n)) break; __builtin_amdgcn_s_sleep(1); }
}
#define LB(N) asm volatile("s_waitcnt vmcnt(" #N ")\n\ts_barrier" ::: "memory")
#define BAR() asm volatile("s_barrier" ::: "memory")

__global__ __launch_bounds__(512, 2) void fused_fwd(Params P) {
    __shared__ __attribute__((aligned(16))) char smem[163840];
    const unsigned L0 = (unsigned)(uintptr_t)(LAS char*)smem;
    const int tid = threadIdx.x, wv = __builtin_amdgcn_readfirstlane(tid >> 6), w = wv & 3;
    const int b = blockIdx.x, xcd = b & 7, slot = b >> 3;
    const int m = 2 * xcd + (slot >> 4), h = slot & 15;
    const int g = slot & 3, nt = g < 2 ? 4 : 3, tb = g < 2 ? 4 * g : 3 * g + 2;
    const int ti = 8 * xcd + (slot >> 2);
    const int s16 = slot & 15, pr = s16 >> 1;
    const int nt6 = ((s16 ^ pr) & 1) ? 3 : 4, tb6 = 7 * pr + ((s16 & 1) ? (pr & 1 ? 3 : 4) : 0);
    const bool act7 = slot < 28;
    const int j7 = 4 * xcd + (act7 ? slot / 7 : 0), g7 = act7 ? slot % 7 : 0;
    const unsigned wst = L0 + w * L_WST;
    const unsigned shift = (56 * h) & 15u;
    STAMP_DECL

    if (wv >= 4) {
        if (PRIO_L) __builtin_amdgcn_s_setprio(PRIO_L);
        LANE_GEOM();
        const unsigned wrd = 16384 + hh * 6 * 256 + trq * 64 + ((shift + gq * 8) & 63);
        LAS float* csp = (LAS float*)(uintptr_t)(L0 + L_MISC);
        if (w < 2) {
            const unsigned xoffE = (lane >> 3) * 896 + (((lane & 7) ^ (lane >> 4)) << 4);
            const unsigned xoffO = (lane >> 3) * 896 + (((lane & 7) ^ (4 + (lane >> 4))) << 4);
            const unsigned colb = (56 * h) & ~15u;
            unsigned woff[3];
#pragma unroll
            for (int j = 0; j < 3; ++j) {
                const int bl = 4 * j + (lane >> 4), kql = bl / 3, tile = bl % 3;
                woff[j] = (4 * kql + ((lane >> 2) & 3)) * 2688 + tile * 896 + colb + (lane & 3) * 16;
            }
            const char* xb = (const char*)P.x + (size_t)(128 * m + 64 * w) * 896;
            const char* wb = (const char*)P.wqkv + (size_t)(32 * w) * 2688;
            LBAR();
#pragma unroll 1
            for (int kc = 0; kc < 7; ++kc) {
                if (kc >= 5) slot_wait(kc - 4, lane);
                const unsigned base = L0 + (kc >= 5 ? kc - 5 : kc) * L_SLOT;
#pragma unroll
                for (int i = 0; i < 6; ++i) dma1<NT_QKV>(wb + (size_t)kc * 64 * 2688 + (i / 3) * 16 * 2688, woff[i % 3], base + 16384 + (6 * w + i) * 1024);
#pragma unroll
                for (int i = 0; i < 8; ++i) dma1<NT_X>(xb + kc * 128 + i * 8 * 896, (i & 1) ? xoffO : xoffE, base + (8 * w + i) * 1024);
                if (kc > 0) { WAIT_VM(14); ldsw_st(F_RDY + 2 * (kc - 1) + w, 1); }
            }
            WAIT_VM(0); ldsw_st(F_RDY + 12 + w, 1);
            {
                ring_wait(6);
                const unsigned a = L0 + L_SLOT + wrd + w * 3072;
#pragma unroll
                for (int t = 0; t < 3; ++t) { const u32x4 f = wfrag(a + t * 256, a + 768 + t * 256);
                    const float v = hsum((dot2(f[0], ONES, 0.f) + dot2(f[1], ONES, 0.f)) + (dot2(f[2], ONES, 0.f) + dot2(f[3], ONES, 0.f)));
                    if (hh == 0) csp[(2 + w) * 96 + t * 32 + r32] = v; }
            }
        } else {
            const int srow = 128 * m + 8 * h + 4 * (w - 2) + (lane >> 4);
            const char* sp = (const char*)P.x + (size_t)srow * 896 + (lane & 15) * 16;
            u32x4 sr[4];
            asm volatile("global_load_dwordx4 %0, %4, off\n\tglobal_load_dwordx4 %1, %4, off offset:256\n\tglobal_load_dwordx4 %2, %4, off offset:512\n\tglobal_load_dwordx4 %3, %5, off"
                         : "=&v"(sr[0]), "=&v"(sr[1]), "=&v"(sr[2]), "=&v"(sr[3]) : "v"(sp), "v"(sp + ((lane & 15) < 8 ? 768 : 0)) : "memory");
            if (w == 2 && lane < 32) ldsw_st(lane, 0);
            LBAR();
            {
                const unsigned lx = lane < 56 ? lane : 0, lw = b * 37 + (lane < 37 ? lane : 0);
                const unsigned fl = lane - 56;
                const int fidx = fl < 2 || fl == 5 ? m * 16 + h : ti * 4 + g;
                const char* tf = fl < 6 ? P.ws + flag_off(fl, fidx) : P.ws + WS_ST1 + (128 * m + 8 * h) * 16;
                const char* tp = !EARLY_TOUCH ? P.ws + WS_SPARE : w == 2 ? ((TOUCH_FLAGS && lane >= 56 && lane < 63) ? tf : (const char*)P.x + (size_t)(8 * b + lx / 7) * 896 + (lx % 7) * 128)
                                                                       : (const char*)P.wqkv + (size_t)(lw < 9408 ? lw : 0) * 128;
                if (TOUCH_DELAY) __builtin_amdgcn_s_sleep(TOUCH_DELAY);
                asm volatile("s_mov_b32 m0, %1\n\ts_nop 0\n\tglobal_load_lds_dword %0, off" :: "v"(tp), "s"(L0 + L_MISC + 2048) : "memory");
            }
            {
                asm volatile("s_waitcnt vmcnt(1)" : "+v"(sr[0]), "+v"(sr[1]), "+v"(sr[2]), "+v"(sr[3]) :: "memory");
                if ((lane & 15) >= 8) sr[3] = (u32x4){0u, 0u, 0u, 0u};
                float t1 = 0.f, t2 = 0.f;
#pragma unroll
                for (int q = 0; q < 4; ++q)
#pragma unroll
                    for (int i = 0; i < 4; ++i) { t1 = dot2(sr[q][i], ONES, t1); t2 = dot2(sr[q][i], sr[q][i], t2); }
                t1 += xor32<1>(t1); t2 += xor32<1>(t2); t1 += xor32<2>(t1); t2 += xor32<2>(t2); t1 += xor32<4>(t1); t2 += xor32<4>(t2); t1 += xor32<8>(t1); t2 += xor32<8>(t2);
                const float mean = t1 * (1.f / D), var = __builtin_fmaxf(t2 * (1.f / D) - mean * mean, 0.f);
                const unsigned e = *(const unsigned*)(P.ws + WS_EP) + 1u;
                const __amdgpu_buffer_rsrc_t rs = __builtin_amdgcn_make_buffer_rsrc((void*)P.ws, 0, WS_END, 0x00020000);
                if ((lane & 15) == 0) gst(rs, (u32x4){f2u(mean), e, f2u(__builtin_amdgcn_rsqf(var + EPS)), e}, srow * 16, WS_ST1);
            }
            float cs[3][4] = {};
            const unsigned wrd2 = wrd + (w - 2) * 2 * 3072;
#pragma unroll 1
            for (int c = 0; c < 7; ++c) {
                ring_wait(c);
                if (c == LATE_TOUCH) {
                    unsigned long long pc; unsigned back;
                    asm volatile("s_getpc_b64 %0\nmk_code_here:\n\ts_mov_b32 %1, mk_code_here-_ZN2mk9fused_fwdENS_6ParamsE" : "=s"(pc), "=s"(back));
                    constexpr unsigned len = CODE_TOUCH_BYTES;
                    const unsigned oc = (slot * 18 + (lane < 18 ? lane : 0)) * 128, o3 = (b * 64 + lane) * 128, o2 = (b * 16 + (lane & 15)) * 128;
                    const char* t0 = w == 2 ? (const char*)(pc - back) + (oc < len ? oc : 0) : (const char*)P.w3 + (o3 < 1605632 ? o3 : 0);
                    const char* t1 = w == 2 ? (lane < 16 ? (const char*)P.wo : lane < 32 ? (const char*)P.w1 : (const char*)P.w2) + (o2 < 401408 ? o2 : 0) : (const char*)P.w4 + (o3 < 1605632 ? o3 : 0);
                    asm volatile("s_mov_b32 m0, %2\n\ts_nop 0\n\tglobal_load_lds_dword %0, off\n\tglobal_load_lds_dword %1, off" :: "v"(t0), "v"(t1), "s"(L0 + L_MISC + 2048) : "memory");
                }
                const unsigned a = L0 + (c >= 5 ? c - 5 : c) * L_SLOT + (c < 6 ? wrd2 : wrd + w * 3072);
#pragma unroll
                for (int t = 0; t < 3; ++t) { const u32x4 f = wfrag(a + t * 256, a + 768 + t * 256);
#pragma unroll
                    for (int i = 0; i < 4; ++i) cs[t][i] = dot2(f[i], ONES, cs[t][i]); }
                if (c < 6) {
#pragma unroll
                    for (int t = 0; t < 3; ++t) { const u32x4 f = wfrag(a + 3072 + t * 256, a + 3072 + 768 + t * 256);
#pragma unroll
                        for (int i = 0; i < 4; ++i) cs[t][i] = dot2(f[i], ONES, cs[t][i]); }
                }
                WAIT_LGKM();
                ldsw_st(F_DONE + 2 + w, c + 1);
            }
#pragma unroll
            for (int t = 0; t < 3; ++t) { const float v = hsum((cs[t][0] + cs[t][1]) + (cs[t][2] + cs[t][3])); if (hh == 0) csp[(w - 2) * 96 + t * 32 + r32] = v; }
        }
        LBAR();
        auto stage = [&](const WSrc& s, unsigned lds, int n0, int n1) AI {
#pragma unroll 1
            for (int n = n0; n < n1; ++n) wpiece(s, lds, n);
        };
        LB(0);
        BAR();
        stage(wsrc_a((const char*)P.wo + (size_t)(112 * w) * 896 + tb * 64, 896, nt, lane), wst, 0, 28);
        LB(0);
        { const WSrc s_ = wsrc_a((const char*)P.w1 + (size_t)(112 * w) * 896 + tb * 64, 896, nt, lane); BAR(); stage(s_, wst, 0, HALF); BAR(); stage(s_, wst, HALF, 28); BAR(); LB(0); }
        { const WSrc s_ = wsrc_a((const char*)P.w2 + (size_t)(112 * w) * 896 + tb * 64, 896, nt, lane); BAR(); stage(s_, wst, 0, HALF); BAR(); stage(s_, wst, HALF, 28); BAR(); LB(0); }
        { const WSrc s_ = wsrc_a((const char*)P.w3 + (size_t)(112 * w) * 3584 + tb6 * 64, 3584, nt6, lane); BAR(); stage(s_, wst, 0, HALF); BAR(); stage(s_, wst, HALF, 28); BAR(); LB(0); }
        {
            float c[4] = {0.f, 0.f, 0.f, 0.f};
            const unsigned a0 = L0 + trq * 64 + gq * 8 + 2 * hh * 1024 + w * 256;
#pragma unroll 4
            for (int k = 0; k < 28; ++k) { const unsigned a = a0 + k * 4096; const u32x4 f = wfrag(a, a + 1024);
#pragma unroll
                for (int i = 0; i < 4; ++i) c[i] = dot2(f[i], ONES, c[i]); }
            const float cc = hsum((c[0] + c[1]) + (c[2] + c[3]));
            if (hh == 0) ((LAS float*)(uintptr_t)(L0 + L_M2 + 1024))[w * 32 + r32] = cc;
        }
        if (b == 0 && w == 3) {
            const unsigned e = *(const volatile GAS unsigned*)(P.ws + WS_EP) + 1u;
            const GAS unsigned* p = (const GAS unsigned*)(P.ws + flag_off(0, lane));
#pragma nounroll
            for (unsigned sp = 0; sp < SPIN_CAP; ++sp) {
                const unsigned v0 = __hip_atomic_load(p, __ATOMIC_RELAXED, __HIP_MEMORY_SCOPE_AGENT), v1 = __hip_atomic_load(p + 64 * 32, __ATOMIC_RELAXED, __HIP_MEMORY_SCOPE_AGENT);
                const unsigned v2 = __hip_atomic_load(p + 128 * 32, __ATOMIC_RELAXED, __HIP_MEMORY_SCOPE_AGENT), v3 = __hip_atomic_load(p + 192 * 32, __ATOMIC_RELAXED, __HIP_MEMORY_SCOPE_AGENT);
                if (wall(v0 == e && v1 == e && v2 == e && v3 == e)) break;
            }
            if (lane == 0) __hip_atomic_store((GAS unsigned*)(P.ws + WS_EP), e, __ATOMIC_RELAXED, __HIP_MEMORY_SCOPE_AGENT);
        }
        LBAR();
        const WSrc w4s = wsrc_b((const char*)P.w4 + (size_t)(448 * w) * 896 + g7 * 128, 896, lane);
        if (act7) { stage(w4s, L0 + L_X + w * 8192, 0, 8); stage(w4s, wst - 8 * 1024, 8, W4SPLIT); }
        BAR();
        if (act7) stage(w4s, wst - 8 * 1024, W4SPLIT, 36);
        LB(0);
        if (act7) {
            BAR();
            stage(w4s, wst - 36 * 1024, 36, 56);
            LB(0);
            BAR();
        }
        STAMP_FLUSH();
        return;
    }

    if (PRIO_C) __builtin_amdgcn_s_setprio(PRIO_C);
    const unsigned e = *(const unsigned*)(P.ws + WS_EP) + 1u;
    const __amdgpu_buffer_rsrc_t rs = __builtin_amdgcn_make_buffer_rsrc((void*)P.ws, 0, WS_END, 0x00020000);
    u32x4 qf0, qf1;
    {
        LANE_GEOM();
        const unsigned wrd = 16384 + hh * 6 * 256 + trq * 64 + ((shift + gq * 8) & 63);
        STAMP(15);
        f32x16 aq = {}, ak = {}, av = {};
        const unsigned xrd = (32 * w + r32) * 128 + ((hh ^ ((r32 >> 1) & 7)) << 4);
        struct Fr { u32x4 x, q, k, v; };
        Fr F[4];
        unsigned xb[3][4], wb3[3];
#pragma unroll
        for (int gsl = 0; gsl < 3; ++gsl) {
#pragma unroll
            for (int j = 0; j < 4; ++j) { xb[gsl][j] = L0 + (xrd ^ (j << 5)) + gsl * 2 * L_SLOT; PIN(xb[gsl][j]); }
            wb3[gsl] = L0 + wrd + gsl * 2 * L_SLOT; PIN(wb3[gsl]);
        }
        auto ldx = [&](int sl, int j) AI { F[j].x = lds128(xb[sl / 2][j] + (sl & 1) * L_SLOT); const unsigned a = wb3[sl / 2] + (sl & 1) * L_SLOT + j * 3072; F[j].q = wfrag(a, a + 768); };
        auto ldk = [&](int sl, int j) AI { const unsigned a = wb3[sl / 2] + (sl & 1) * L_SLOT + j * 3072; F[j].k = wfrag(a + 256, a + 1024); };
        auto ldv = [&](int sl, int j) AI { const unsigned a = wb3[sl / 2] + (sl & 1) * L_SLOT + j * 3072; F[j].v = wfrag(a + 512, a + 1280); };
        LAS float* stw = (LAS float*)(uintptr_t)(L0 + L_MISC + 3072 + w * 256);
        BAR();
        ring_wait(0);
        STAMP(16);
#pragma unroll
        for (int j = 0; j < 2; ++j) { ldx(0, j); ldk(0, j); ldv(0, j); }
        SB();
        u32x4 gv = {0u, 0u, 0u, 0u};
        const unsigned gvo = (128 * m + 32 * w + r32) * 16;
        sfor<7>([&](auto cc) AI {
            constexpr int c = decltype(cc)::value, sl = c >= 5 ? c - 5 : c, nsl = c >= 4 ? c - 4 : c + 1;
            u32x2 rdy;
            sfor<4>([&](auto jc) AI {
                constexpr int j = decltype(jc)::value, jn = (j + 2) & 3, lb = j < 2 ? sl : nsl;
                constexpr bool ld = j < 2 || c < 6;
                if constexpr (j == 2 && c < 6) { if (__builtin_amdgcn_readfirstlane(rdy.x & rdy.y) == 0) ring_wait(c + 1); SB(); }
                MFMA(aq, F[j].q, F[j].x, aq);
                if constexpr (ld) ldx(lb, jn);
                SB();
                MFMA(ak, F[j].k, F[j].x, ak);
                if constexpr (ld) ldk(lb, jn);
                if constexpr (j == 0 && c >= 1 && c <= 2) ldsw_st(F_DONE + w, c);
                if constexpr (j == 0 && c == 4) gv = gld(rs, gvo, WS_ST1);
                SB();
                MFMA(av, F[j].x, F[j].v, av);
                if constexpr (ld) ldv(lb, jn);
                if constexpr (j == 0 && c < 6) rdy = ldsw_ld2(F_RDY + 2 * (c + 1));
                SB();
            });
        });
#pragma nounroll
        for (unsigned sp2 = 0; sp2 < SPIN_CAP && !wall(gv.y == e && gv.w == e); ++sp2) { asm volatile("" ::: "memory"); gv = gld(rs, gvo, WS_ST1); }
        if (hh == 0) { stw[r32] = u2f(gv.x) * u2f(gv.z); stw[32 + r32] = u2f(gv.z); }
        STAMP(1);
        const float mean = u2f(gv.x), rstd = u2f(gv.z);
        LBAR();
        STAMP(17);
        const LAS float* csf = (const LAS float*)(uintptr_t)(L0 + L_MISC);
        const float cv = (csf[64 + r32] + csf[96 + 64 + r32]) + (csf[192 + 64 + r32] + csf[288 + 64 + r32]);
        const float qs = rstd * (0.18898223650461362f * 1.4426950408889634f);
#pragma unroll
        for (int q = 0; q < 4; ++q) {
            auto c4 = [&](int o) AI { return (*(const LAS f32x4*)(csf + o) + *(const LAS f32x4*)(csf + 96 + o)) + (*(const LAS f32x4*)(csf + 192 + o) + *(const LAS f32x4*)(csf + 288 + o)); };
            const f32x4 c1 = c4(8 * q + 4 * hh), c2 = c4(32 + 8 * q + 4 * hh);
            const f32x4 mr = *(const LAS f32x4*)(stw + 8 * q + 4 * hh), rr = *(const LAS f32x4*)(stw + 32 + 8 * q + 4 * hh);
#pragma unroll
            for (int i = 0; i < 4; ++i) {
                aq[4 * q + i] = (aq[4 * q + i] - mean * c1[i]) * qs;
                ak[4 * q + i] = (ak[4 * q + i] - mean * c2[i]) * rstd;
                av[4 * q + i] = av[4 * q + i] * rr[i] - mr[i] * cv;
            }
        }
        if (hh == 1) { aq[12] = 1.f; aq[13] = 0.f; aq[14] = 0.f; aq[15] = 0.f; ak[12] = 0.f; ak[13] = 0.f; ak[14] = 0.f; ak[15] = 0.f; }
        if (r32 >= 28) {
            const float pv = r32 == 28 ? 1.f : 0.f;
#pragma unroll
            for (int i = 0; i < 16; ++i) av[i] = pv;
        }
        const u32x4 k0 = pack8(ak, 0), k1 = pack8(ak, 1), v0 = pack8(av, 0), v1 = pack8(av, 1);
        const unsigned go = WS_KV + h * KVH + (4 * m + w) * 4096;
        gst(rs, k0, lane * 16, go); gst(rs, k1, lane * 16, go + 1024); gst(rs, v0, lane * 16, go + 2048); gst(rs, v1, lane * 16, go + 3072);
        const unsigned lo = L0 + L_OWNKV + w * 4096 + lane * 16;
        sts128(lo, k0); sts128(lo + 1024, k1); sts128(lo + 2048, v0); sts128(lo + 3072, v1);
        qf0 = pack8(aq, 0); qf1 = pack8(aq, 1);
        STAMP(18);
        WAIT_BAR(0);
        if (tid == 0) set_flag(P.ws, flag_off(0, m * 16 + h), e);
        STAMP(2);
    }
    {
        LANE_GEOM();
        const int mt = m - 2 + (w >> 1);
        u32x4 kv[8];
        if (mt >= 0) {
            wait_flags(P.ws, flag_off(0, mt * 16 + h), 1, e, lane);
            const unsigned go = WS_KV + h * KVH + (4 * m - 8 + 2 * w) * 4096;
#pragma unroll
            for (int i = 0; i < 8; ++i) kv[i] = gld(rs, lane * 16, go + i * 1024);
        } else {
#pragma unroll
            for (int i = 0; i < 8; ++i) kv[i] = (u32x4){0u, 0u, (i & 3) == 1 && hh == 1 ? 0xF149u : 0u, 0u};
        }
        f32x16 cdiag, cold;
#pragma unroll
        for (int i = 0; i < 16; ++i) { const int kr = (i & 3) + 8 * (i >> 2) + 4 * hh; cdiag[i] = kr <= r32 ? 0.f : -1e30f; cold[i] = kr > r32 ? 0.f : -1e30f; }
#pragma unroll
        for (int i = 0; i < 8; ++i) sts128(L0 + L_KV + 2 * w * 4096 + i * 1024 + lane * 16, kv[i]);
        LBAR();
        STAMP(3);
        unsigned kvb = L0 + L_KV + w * 4096 + lane * 16;
        PIN(kvb);
        u32x4 kf[9][2], vf[9][2];
        const f32x16 zero16 = {};
        auto ldK = [&](int i) AI { kf[i][0] = lds128(kvb + i * 4096); kf[i][1] = lds128(kvb + i * 4096 + 1024); };
        auto ldV = [&](int i) AI { vf[i][0] = lds128(kvb + i * 4096 + 2048); vf[i][1] = lds128(kvb + i * 4096 + 3072); };
        auto smax = [&](const f32x16& s, float a) AI {
#pragma unroll
            for (int i = 0; i < 16; i += 2) a = max3(a, s[i], s[i + 1]);
            return a; };
        ldK(0); ldK(1);
        float mx = -3e38f;
        f32x16 s[2];
        auto smax8 = [&](const f32x16& v, int o, float a) AI {
#pragma unroll
            for (int i = 0; i < 8; i += 2) a = max3(a, v[o + i], v[o + i + 1]);
            return a; };
        sfor<9>([&](auto ic) AI {
            constexpr int i = decltype(ic)::value;
            if constexpr (i + 2 < 9) ldK(i + 2);
            MFMA(s[i & 1], kf[i][0], qf0, i == 0 ? cold : i == 8 ? cdiag : zero16);
            SB();
            MFMA(s[i & 1], kf[i][1], qf1, s[i & 1]);
            SB();
            if constexpr (i > 0) { mx = smax8(s[(i - 1) & 1], 0, mx); mx = smax8(s[(i - 1) & 1], 8, mx); PIN(mx); }
            SB();
        });
        ldK(0); ldK(1); ldV(0);
        mx = hmax(smax(s[0], mx));
        STAMP(25);
        f32x16 o = {}, cm;
#pragma unroll
        for (int i = 0; i < 16; ++i) { cm[i] = -mx; cdiag[i] -= mx; cold[i] -= mx; }
        MFMA(s[0], kf[0][0], qf0, cold); MFMA(s[0], kf[0][1], qf1, s[0]);
        SB();
        sfor<9>([&](auto ic) AI {
            constexpr int i = decltype(ic)::value;
            f32x16& c = s[i & 1];
            f32x16& n = s[(i + 1) & 1];
            if constexpr (i + 2 < 9) ldK(i + 2);
            if constexpr (i + 1 < 9) ldV(i + 1);
            if constexpr (i + 1 < 9) MFMA(n, kf[i + 1][0], qf0, i + 1 == 8 ? cdiag : cm);
#pragma unroll
            for (int j = 0; j < 8; ++j) c[j] = __builtin_amdgcn_exp2f(c[j]);
            u32x4 p0 = pack8(c, 0);
            PIN(p0);
            SB();
            if constexpr (i + 1 < 9) MFMA(n, kf[i + 1][1], qf1, n);
            MFMA(o, vf[i][0], p0, o);
#pragma unroll
            for (int j = 8; j < 16; ++j) c[j] = __builtin_amdgcn_exp2f(c[j]);
            const u32x4 p1 = pack8(c, 1);
            MFMA(o, vf[i][1], p1, o);
            SB();
        });
        STAMP(26);
        auto lr = __builtin_amdgcn_permlane32_swap(f2u(o[12]), f2u(o[12]), false, false);
        const float inv = __builtin_amdgcn_rcpf(u2f(lr[1]));
#pragma unroll
        for (int i = 0; i < 16; ++i) o[i] *= inv;
        if (hh == 1) { o[12] = 0.f; o[13] = 0.f; o[14] = 0.f; o[15] = 0.f; }
        const unsigned vo = (hh * XR + r32) * 16, so = WS_O + ((4 * h) * XR + 128 * m + 32 * w) * 16;
        gst(rs, pack8(o, 0), vo, so); gst(rs, pack8(o, 1), vo, so + 2 * XROW);
        WAIT_BAR(0);
        if (tid == 0) set_flag(P.ws, flag_off(1, m * 16 + h), e);
        STAMP(4);
    }
    const bool act = w < nt;
    const int tw = tb + w;
#define GEMM_GEOM() LANE_GEOM(); \
    unsigned trW[2]; trW[0] = L0 + (w < nt ? w : nt - 1) * 256 + trq * 64 + gq * 8 + 2 * hh * 1024; trW[1] = trW[0] + 57344; PIN(trW[0]); PIN(trW[1]); \
    auto wimg = [&](int kq0, int kq1) AI { return wfrag(kq0 < 56 ? trW[0] + kq0 * 1024 : trW[1] + (kq0 - 56) * 1024, kq1 < 56 ? trW[0] + kq1 * 1024 : trW[1] + (kq1 - 56) * 1024); }; \
    unsigned xl = L0 + L_X + lane * 16; PIN(xl); \
    LAS float* m2 = (LAS float*)(uintptr_t)(L0 + L_M2 + w * 256); (void)m2
    auto gemm = [&](auto nk, const u32x4* wr, unsigned xl) AI -> f32x16 {
        constexpr int NK = decltype(nk)::value, PD = 4;
        f32x16 acc = {};
        u32x4 xf[NK];
#pragma unroll
        for (int k = 0; k < PD; ++k) xf[k] = lds128(xl + k * 1024);
        SB();
        sfor<NK>([&](auto kc) AI {
            constexpr int k = decltype(kc)::value;
            if constexpr (k + PD < NK) xf[k + PD] = lds128(xl + (k + PD) * 1024);
            MFMA(acc, wr[k], xf[k], acc);
            SB();
        });
        return acc;
    };
    u32x4 hp[2];
    float meanH, rstdH;
    {
        GEMM_GEOM();
        u32x4 xr[2];
        const char* xp = (const char*)P.x + (size_t)(32 * ti + r32) * 896 + 64 * (act ? tw : tb) + 16 * hh;
        xr[0] = *(const GAS u32x4*)xp; xr[1] = *(const GAS u32x4*)(xp + 32);
        u32x4 wr[32];
        sfor<32>([&](auto kc) AI { constexpr int k = decltype(kc)::value, kq = 7 * (k >> 1) + 4 * (k & 1);
            if constexpr (k < 31) wr[k] = wimg(kq, kq + 1);
            else { const u32x2 p = ldstr(trW[1] + (kq - 56) * 1024), q = ldstr(trW[1] + (kq - 56) * 1024 + (hh ? 0 : 1024)); wr[k] = (u32x4){p.x, p.y, q.x, q.y}; } });
        LBAR();
        STAMP(19);
        wait_flags(P.ws, flag_off(1, (ti >> 2) * 16 + 4 * w), 4, e, lane);
        STAMP(20);
        u32x4 xf[8];
#pragma unroll
        for (int k = 0; k < 8; ++k) xf[k] = gld(rs, vx, WS_O + ((16 * w + 2 * k) * XR + 32 * ti) * 16);
#pragma unroll
        for (int k = 0; k < 8; ++k) sts128(xl + (8 * w + k) * 1024, xf[k]);
        LBAR();
        STAMP(5);
        f32x16 r = gemm(IC(32), wr, xl);
        STAMP(21);
        float s1 = 0.f, s2 = 0.f;
#pragma unroll
        for (int s = 0; s < 2; ++s) {
#pragma unroll
            for (int i = 0; i < 4; ++i) {
                hp[s][i] = pk(r[8 * s + 2 * i] + blo(xr[s][i]), r[8 * s + 2 * i + 1] + bhi(xr[s][i]));
                s1 = dot2(hp[s][i], ONES, s1); s2 = dot2(hp[s][i], hp[s][i], s2);
            }
            if (act) gst(rs, hp[s], vx, WS_H + ((4 * tw + 2 * s) * XR + 32 * ti) * 16);
        }
        s1 = hsum(s1); s2 = hsum(s2);
        if (act && hh == 0) gst8(rs, (u32x2){f2u(s1), f2u(s2)}, r32 * 8, WS_STH + (tw * 2048 + 32 * ti) * 8);
        STAMP(22);
        WAIT_BAR(0);
        if (tid == 0) set_flag(P.ws, flag_off(2, ti * 4 + g), e);
        STAMP(6);
    }
#define LOAD_WR() u32x4 wr[28]; sfor<28>([&](auto kc) AI { constexpr int k = decltype(kc)::value; wr[k] = wimg(4 * k, 4 * k + 1); })
    const int fsn = w == 0 ? 1 : 2, fsl = w <= 1 ? 0 : w - 1;
    {
        GEMM_GEOM();
        BAR();
        LOAD_WR();
        LBAR();
        wait_flags(P.ws, flag_off(2, ti * 4), 4, e, lane);
        u32x4 xf[7];
#pragma unroll
        for (int k = 0; k < 7; ++k) xf[k] = gld(rs, vx, WS_H + ((14 * w + 2 * k) * XR + 32 * ti) * 16);
        u32x2 sv[7];
#pragma unroll
        for (int q = 0; q < 7; ++q) sv[q] = gld8(rs, (7 * hh * 2048 + r32) * 8, WS_STH + (q * 2048 + 32 * ti) * 8);
        SB();
        {
            float cs[4] = {0.f, 0.f, 0.f, 0.f};
#pragma unroll
            for (int k = 0; k < 28; ++k)
#pragma unroll
                for (int i = 0; i < 4; ++i) cs[i] = dot2(wr[k][i], ONES, cs[i]);
            const float c = hsum((cs[0] + cs[1]) + (cs[2] + cs[3]));
            if (hh == 0) m2[r32] = c;
        }
        SB();
#pragma unroll
        for (int k = 0; k < 7; ++k) sts128(xl + (7 * w + k) * 1024, xf[k]);
        LBAR();
        STAMP(7);
        f32x16 r = gemm(IC(28), wr, xl);
        float t1 = 0.f, t2 = 0.f;
#pragma unroll
        for (int q = 0; q < 7; ++q) { t1 += u2f(sv[q].x); t2 += u2f(sv[q].y); }
        t1 = hsum(t1); t2 = hsum(t2);
        meanH = t1 * (1.f / D);
        const float var = __builtin_fmaxf(t2 * (1.f / D) - meanH * meanH, 0.f);
        rstdH = __builtin_amdgcn_rsqf(var + EPS);
        const float sc = rstdH * __builtin_amdgcn_rsqf(var * rstdH * rstdH + EPS);
#pragma unroll
        for (int q = 0; q < 4; ++q) {
            const f32x4 c = *(const LAS f32x4*)(m2 + 8 * q + 4 * hh);
#pragma unroll
            for (int i = 0; i < 4; ++i) r[4 * q + i] = gelu_t((r[4 * q + i] - meanH * c[i]) * sc);
        }
#pragma unroll
        for (int s = 0; s < 2; ++s) if (act) gst(rs, pack8(r, s), vx, WS_HH + ((4 * tw + 2 * s) * XR + 32 * ti) * 16);
        WAIT_BAR(0);
        if (tid == 0) set_flag(P.ws, flag_off(3, ti * 4 + g), e);
        STAMP(8);
    }
    {
        GEMM_GEOM();
        BAR();
        LOAD_WR();
        LBAR();
        const int sh = w == 0 ? -1 : w == 1 ? 1 : w == 2 ? -2 : 2;
        const int tn = ti + (sh < 0 ? -1 : 1);
        {
            const bool nb = tn >= 0 && tn < 64;
            const int li = lane & 3;
            const GAS unsigned* p = (const GAS unsigned*)(P.ws + flag_off(3, (((li & 2) && nb) ? tn : ti) * 4 + fsl + ((li & 1) && fsn == 2 ? 1 : 0)));
            poll_word(p, e);
        }
        const int tok = 32 * ti + r32 + sh;
        const bool ok = tok >= 0 && tok < S;
        const unsigned vxs = ok ? (hh * XR + tok) * 16 : 0x7ffffff0u;
        u32x4 xf[7];
#pragma unroll
        for (int k = 0; k < 7; ++k) { xf[k] = gld(rs, vxs, WS_HH + (14 * w + 2 * k) * XROW); }
#pragma unroll
        for (int k = 0; k < 7; ++k) sts128(xl + (7 * w + k) * 1024, xf[k]);
        LBAR();
        STAMP(9);
        f32x16 r = gemm(IC(28), wr, xl);
        float s1 = 0.f, s2 = 0.f;
#pragma unroll
        for (int s = 0; s < 2; ++s) {
            u32x4 xp;
#pragma unroll
            for (int i = 0; i < 4; ++i) {
                xp[i] = pk(r[8 * s + 2 * i] + (blo(hp[s][i]) - meanH) * rstdH, r[8 * s + 2 * i + 1] + (bhi(hp[s][i]) - meanH) * rstdH);
                s1 = dot2(xp[i], ONES, s1); s2 = dot2(xp[i], xp[i], s2);
            }
            if (act) gst(rs, xp, vx, WS_X1 + ((4 * tw + 2 * s) * XR + 32 * ti) * 16);
        }
        s1 = hsum(s1); s2 = hsum(s2);
        if (act && hh == 0) gst8(rs, (u32x2){f2u(s1), f2u(s2)}, r32 * 8, WS_STX + (tw * 2048 + 32 * ti) * 8);
        WAIT_BAR(0);
        if (tid == 0) set_flag(P.ws, flag_off(4, ti * 4 + g), e);
        STAMP(10);
    }
    {
        LANE_GEOM();
        unsigned tr6[2];
        tr6[0] = L0 + trq * 64 + gq * 8 + 2 * hh * 1024; tr6[1] = tr6[0] + 57344; PIN(tr6[0]); PIN(tr6[1]);
        auto wf6 = [&](int t, int k) AI { const unsigned a = (k < 14 ? tr6[0] + k * 4096 : tr6[1] + (k - 14) * 4096) + t * 256; return wfrag(a, a + 1024); };
        const LAS float* cs6 = (const LAS float*)(uintptr_t)(L0 + L_M2 + 1024);
        wait_flags(P.ws, flag_off(4, (4 * m + w) * 4), 4, e, lane);
        STAMP(28);
        const int tk = 128 * m + 32 * w;
        auto ldx1 = [&](int k) AI { return gld(rs, vx, WS_X1 + (2 * k * XR + tk) * 16); };
        constexpr int PX = P6_PX, PW = 2;
        f32x16 acc[4] = {};
        u32x2 sv[7];
        {
            u32x4 xf[28], wf[28][4];
#pragma unroll
            for (int k = 0; k < PX; ++k) xf[k] = ldx1(k);
#pragma unroll
            for (int q = 0; q < 7; ++q) sv[q] = gld8(rs, (7 * hh * 2048 + r32) * 8, WS_STX + (q * 2048 + tk) * 8);
            BAR();
            STAMP(11);
#pragma unroll
            for (int k = 0; k < PW; ++k)
#pragma unroll
                for (int t = 0; t < 4; ++t) wf[k][t] = wf6(t, k);
            SB();
            sfor<28>([&](auto kc) AI {
                constexpr int k = decltype(kc)::value;
                MFMA(acc[0], wf[k][0], xf[k], acc[0]);
                if constexpr (k + PW < 28) { wf[k + PW][0] = wf6(0, k + PW); }
                SB();
                MFMA(acc[1], wf[k][1], xf[k], acc[1]);
                if constexpr (k + PW < 28) { wf[k + PW][1] = wf6(1, k + PW); }
                if constexpr (k + PX < 28) xf[k + PX] = ldx1(k + PX);
                SB();
                MFMA(acc[2], wf[k][2], xf[k], acc[2]);
                if constexpr (k + PW < 28) { wf[k + PW][2] = wf6(2, k + PW); }
                SB();
                MFMA(acc[3], wf[k][3], xf[k], acc[3]);
                if constexpr (k + PW < 28) { wf[k + PW][3] = wf6(3, k + PW); }
                SB();
            });
        }
        STAMP(23);
        LBAR();
        STAMP(24);
        float t1 = 0.f, t2 = 0.f;
#pragma unroll
        for (int q = 0; q < 7; ++q) { t1 += u2f(sv[q].x); t2 += u2f(sv[q].y); }
        t1 = hsum(t1); t2 = hsum(t2);
        const float mean = t1 * (1.f / D), var = __builtin_fmaxf(t2 * (1.f / D) - mean * mean, 0.f) + EPS, rstd = __builtin_amdgcn_rsqf(var);
        const GeluK gk = {-mean, GC1 * rstd * rstd * rstd, GC0 * rstd, var * rstd};
        const unsigned uo = WS_U + ((4 * tb6) * XR + tk) * 16;
        auto epi1 = [&](f32x16& y, int te) AI {
#pragma unroll
            for (int q = 0; q < 4; ++q) {
                const f32x4 c = *(const LAS f32x4*)(cs6 + te * 32 + 8 * q + 4 * hh);
#pragma unroll
                for (int i = 0; i < 4; ++i) {
                    const float v = __builtin_fmaf(gk.nmean, c[i], y[4 * q + i]);
                    const float z = v * __builtin_fmaf(v * v, gk.k1, gk.k0);
                    y[4 * q + i] = v * __builtin_amdgcn_rcpf(__builtin_fmaf(__builtin_amdgcn_exp2f(z), gk.irs, gk.irs));
                }
            }
            gst(rs, pack8(y, 0), vx, uo + 4 * te * XROW); gst(rs, pack8(y, 1), vx, uo + (4 * te + 2) * XROW);
            SB();
        };
        epi1(acc[0], 0); epi1(acc[1], 1); epi1(acc[2], 2);
        if (nt6 == 4) epi1(acc[3], 3);
        STAMP(29);
        WAIT_BAR(0);
        if (tid == 0) set_flag(P.ws, flag_off(5, m * 16 + s16), e);
        BAR();
        STAMP(12);
    }
    if (act7) {
        LANE_GEOM();
        const unsigned lanep = trq * 64 + gq * 8 + 2 * hh * 512;
        unsigned tA = L0 + L_X + w * 8192 + lanep, tB = wst + lanep; PIN(tA); PIN(tB);
        auto w4f = [&](int k, int t) AI { const unsigned a = (k < 4 ? tA + k * 2048 : k < 18 ? tB + (k - 4) * 2048 : tB + (k - 18) * 2048) + t * 256; return wfrag(a, a + 512); };
        u32x4 wr[28][2];
#pragma unroll
        for (int k = 0; k < 14; ++k) { wr[k][0] = w4f(k, 0); wr[k][1] = w4f(k, 1); }
        LBAR();
        const int tk = 64 * j7;
        const int tt = w >> 1, nt7 = w & 1;
        wait_flags(P.ws, flag_off(5, (j7 >> 1) * 16 + 4 * w), 4, e, lane);
        constexpr int PX = P7_PX;
        u32x4 xa[28][2];
#pragma unroll
        for (int k = 0; k < PX; ++k) { xa[k][0] = gld(rs, vx, WS_U + ((56 * w + 2 * k) * XR + tk) * 16); xa[k][1] = gld(rs, vx, WS_U + ((56 * w + 2 * k) * XR + tk + 32) * 16); }
        STAMP(13);
        SB();
        f32x16 acc[4] = {};
        u32x4 rh[2], rx[2];
        sfor<28>([&](auto kc) AI {
            constexpr int k = decltype(kc)::value;
            if constexpr (k == 13) { BAR(); SB(); }
            if constexpr (k + 3 >= 14 && k + 3 < 28) { wr[k + 3][0] = w4f(k + 3, 0); wr[k + 3][1] = w4f(k + 3, 1); }
            MFMA(acc[0], wr[k][0], xa[k][0], acc[0]); MFMA(acc[1], wr[k][1], xa[k][0], acc[1]);
            if constexpr (k + PX < 28) xa[k + PX][0] = gld(rs, vx, WS_U + ((56 * w + 2 * (k + PX)) * XR + tk) * 16);
            MFMA(acc[2], wr[k][0], xa[k][1], acc[2]); MFMA(acc[3], wr[k][1], xa[k][1], acc[3]);
            if constexpr (k + PX < 28) xa[k + PX][1] = gld(rs, vx, WS_U + ((56 * w + 2 * (k + PX)) * XR + tk + 32) * 16);
            if constexpr (k == 24 || k == 25) {
                constexpr int s = k - 24;
                const unsigned so = ((8 * g7 + 4 * nt7 + 2 * s) * XR + tk + 32 * tt) * 16;
                rh[s] = gld(rs, vx, WS_H + so); rx[s] = gld(rs, vx, WS_X1 + so);
            }
            SB();
        });
        STAMP(27);
#pragma unroll
        for (int t = 0; t < 4; ++t) {
            if (t != w) {
                const unsigned a = L0 + L_X + t * 12288 + (w < t ? w : w - 1) * 4096 + lane * 16;
#pragma unroll
                for (int q = 0; q < 4; ++q) stsf4(a + q * 1024, (f32x4){acc[t][4 * q], acc[t][4 * q + 1], acc[t][4 * q + 2], acc[t][4 * q + 3]});
            }
        }
        LBAR();
        f32x16 r = w == 0 ? acc[0] : w == 1 ? acc[1] : w == 2 ? acc[2] : acc[3];
        const unsigned a = L0 + L_X + w * 12288 + lane * 16;
#pragma unroll
        for (int j = 0; j < 3; ++j)
#pragma unroll
            for (int q = 0; q < 4; ++q) { const f32x4 v = ldsf4(a + j * 4096 + q * 1024); r[4 * q] += v[0]; r[4 * q + 1] += v[1]; r[4 * q + 2] += v[2]; r[4 * q + 3] += v[3]; }
        const unsigned oo = (tk + 32 * tt + r32) * 896 + 128 * g7 + 64 * nt7 + 16 * hh;
        char* op = (char*)P.out + oo;
        const __amdgpu_buffer_rsrc_t ro = __builtin_amdgcn_make_buffer_rsrc((void*)P.out, 0, S * D * 2, 0x00020000); (void)ro; (void)op;
#pragma unroll
        for (int s = 0; s < 2; ++s) {
            u32x4 ov;
#pragma unroll
            for (int i = 0; i < 4; ++i)
                ov[i] = pk(r[8 * s + 2 * i] + blo(rx[s][i]) + blo(rh[s][i]), r[8 * s + 2 * i + 1] + bhi(rx[s][i]) + bhi(rh[s][i]));
            *(GAS u32x4*)(op + 32 * s) = ov;
        }
        STAMP(14);
    }
    STAMP_FLUSH();
}
}

extern "C" void kernel_launch(void* const* d_in, const int* in_sizes, int n_in, void* d_out, int out_size, void* d_ws, size_t ws_size,
                              hipStream_t stream) {
    mk::Params P;
    P.x = (const u16*)d_in[0]; P.wqkv = (const u16*)d_in[1]; P.wo = (const u16*)d_in[2];
    P.w1 = (const u16*)d_in[4]; P.w2 = (const u16*)d_in[5]; P.w3 = (const u16*)d_in[6]; P.w4 = (const u16*)d_in[7];
    P.out = (u16*)d_out; P.ws = (char*)d_ws;
    mk::fused_fwd<<<256, 512, 0, stream>>>(P);
}
```

```cpp
#define TOUCH_OWN 0
#define TOUCH_FLAGS 1
#define W4SPLIT 36
#define PRIO_C 2
#define PRIO_L 0
#define NT_W 0
#define NT_QKV 0
#define NT_X 0
#define OUT_MODE 0
#define P6_PX 10
#define P6_PW 3
#define P6_PG 6
#define P7_PX 5
#define HALF 12
#define POLL2 0
#define POLL_GAP 3
#define TOUCH_DELAY 12
#define EARLY_TOUCH 1
#define LATE_TOUCH 3
#include <hip/hip_runtime.h>
#include <hip/hip_bf16.h>
#include <stdint.h>
#include <type_traits>
#include <utility>

typedef unsigned short u16;
typedef __attribute__((ext_vector_type(8))) short bf16x8;
typedef __attribute__((ext_vector_type(4))) short s16x4;
typedef __attribute__((ext_vector_type(16))) float f32x16;
typedef __attribute__((ext_vector_type(4))) float f32x4;
typedef __attribute__((ext_vector_type(2))) float f32x2;
typedef __attribute__((ext_vector_type(4))) unsigned u32x4;
typedef __attribute__((ext_vector_type(2))) unsigned u32x2;
typedef __attribute__((ext_vector_type(2))) __bf16 bf2;
#define LAS __attribute__((address_space(3)))
#define GAS __attribute__((address_space(1)))
#define DEVI __device__ __forceinline__

#define MK_STAMP 0

namespace mk {
constexpr int S = 2048, D = 448;
constexpr unsigned XR = 2056, XROW = XR * 16;
constexpr unsigned KVH = 64 * 4096 + 128;
constexpr unsigned WS_EP = 0;
constexpr unsigned WS_FLAG = 4096;
constexpr unsigned WS_KV = 262144;
constexpr unsigned WS_O = WS_KV + 16 * KVH;
constexpr unsigned WS_H = WS_O + 64 * XROW;
constexpr unsigned WS_HH = WS_H + 56 * XROW;
constexpr unsigned WS_X1 = WS_HH + 56 * XROW;
constexpr unsigned WS_U = WS_X1 + 56 * XROW;
constexpr unsigned WS_STH = WS_U + 224 * XROW;
constexpr unsigned WS_STX = WS_STH + 262144;
constexpr unsigned WS_ST1 = WS_STX + 262144;
constexpr unsigned WS_SPARE = WS_ST1 + 32768;
constexpr unsigned WS_STAMP = WS_SPARE + 128;
constexpr unsigned WS_END = WS_STAMP + 16 * 512 * 8;
constexpr unsigned L_SLOT = 28672;
constexpr unsigned L_MISC = 143360;
constexpr unsigned L_KV = 114688;
constexpr unsigned L_OWNKV = L_KV + 8 * 4096;
constexpr unsigned L_WST = 28672;
constexpr unsigned L_X = 114688;
constexpr unsigned L_M2 = 147456;
constexpr unsigned CODE_TOUCH_BYTES = 36000;
constexpr float EPS = 1e-5f;
constexpr unsigned SPIN_CAP = 1u << 18;

struct Params {
    const u16* x; const u16* wqkv; const u16* wo; const u16* w1; const u16* w2; const u16* w3; const u16* w4;
    u16* out; char* ws;
};

DEVI unsigned f2u(float f) { return __builtin_bit_cast(unsigned, f); }
DEVI float u2f(unsigned u) { return __builtin_bit_cast(float, u); }
DEVI bool wall(bool p) { return __builtin_amdgcn_ballot_w64(!p) == 0; }
template <int X> DEVI float xor32(float v) { return u2f((unsigned)__builtin_amdgcn_ds_swizzle((int)f2u(v), (X << 10) | 0x1f)); }
DEVI unsigned pk(float lo, float hi) { bf2 v = {(__bf16)lo, (__bf16)hi}; return __builtin_bit_cast(unsigned, v); }
DEVI float blo(unsigned u) { return u2f(u << 16); }
DEVI float bhi(unsigned u) { return u2f(u & 0xffff0000u); }
DEVI float dot2(unsigned a, unsigned b, float c) { return __builtin_amdgcn_fdot2_f32_bf16(__builtin_bit_cast(bf2, a), __builtin_bit_cast(bf2, b), c, false); }
constexpr unsigned ONES = 0x3f803f80u;
DEVI float hsum(float v) { auto r = __builtin_amdgcn_permlane32_swap(f2u(v), f2u(v), false, false); return u2f(r[0]) + u2f(r[1]); }
DEVI float hmax(float v) { auto r = __builtin_amdgcn_permlane32_swap(f2u(v), f2u(v), false, false); return __builtin_fmaxf(u2f(r[0]), u2f(r[1])); }
DEVI f32x16 mfma(u32x4 a, u32x4 b, f32x16 c) { return __builtin_amdgcn_mfma_f32_32x32x16_bf16(__builtin_bit_cast(bf16x8, a), __builtin_bit_cast(bf16x8, b), c, 0, 0, 0); }
#define MFMA(acc, a, b, c) do { acc = mfma(a, b, c); asm volatile("" : "+v"(acc)); } while (0)
DEVI u32x4 lds128(unsigned a) { return *(const LAS u32x4*)(uintptr_t)a; }
DEVI f32x4 ldsf4(unsigned a) { return *(const LAS f32x4*)(uintptr_t)a; }
DEVI void sts128(unsigned a, u32x4 v) { *(LAS u32x4*)(uintptr_t)a = v; }
DEVI void stsf4(unsigned a, f32x4 v) { *(LAS f32x4*)(uintptr_t)a = v; }
DEVI u32x2 ldstr(unsigned a) { return __builtin_bit_cast(u32x2, __builtin_amdgcn_ds_read_tr16_b64_v4i16((LAS s16x4*)(uintptr_t)a)); }
DEVI u32x4 wfrag(unsigned a0, unsigned a1) { u32x2 p = ldstr(a0), q = ldstr(a1); return (u32x4){p.x, p.y, q.x, q.y}; }
DEVI u32x4 pack8(const f32x16& a, int s) { return (u32x4){pk(a[8 * s], a[8 * s + 1]), pk(a[8 * s + 2], a[8 * s + 3]), pk(a[8 * s + 4], a[8 * s + 5]), pk(a[8 * s + 6], a[8 * s + 7])}; }
constexpr int SC1 = 16;
DEVI u32x4 gld(__amdgpu_buffer_rsrc_t rs, unsigned voff, unsigned soff) { return __builtin_amdgcn_raw_buffer_load_b128(rs, voff, soff, SC1); }
DEVI u32x2 gld8(__amdgpu_buffer_rsrc_t rs, unsigned voff, unsigned soff) { return __builtin_amdgcn_raw_buffer_load_b64(rs, voff, soff, SC1); }
DEVI void gst(__amdgpu_buffer_rsrc_t rs, u32x4 v, unsigned voff, unsigned soff) { __builtin_amdgcn_raw_buffer_store_b128(v, rs, voff + soff, 0, SC1); }
DEVI void gst8(__amdgpu_buffer_rsrc_t rs, u32x2 v, unsigned voff, unsigned soff) { __builtin_amdgcn_raw_buffer_store_b64(v, rs, voff, soff, SC1); }
#define WAIT_VM(N) asm volatile("s_waitcnt vmcnt(" #N ")" ::: "memory")
#define WAIT_LGKM() asm volatile("s_waitcnt lgkmcnt(0)" ::: "memory")
#define WAIT_BAR(N) asm volatile("s_waitcnt vmcnt(" #N ") lgkmcnt(0)\n\ts_barrier" ::: "memory")
#define LBAR() asm volatile("s_waitcnt lgkmcnt(0)\n\ts_barrier" ::: "memory")
#define SB() __builtin_amdgcn_sched_barrier(0)
#define PIN(x) asm volatile("" : "+v"(x))
DEVI float fma1(float a, float b, float c) { return __builtin_fmaf(a, b, c); }
DEVI float mul1(float a, float b) { return a * b; }
DEVI float max3(float a, float b, float c) { return __builtin_fmaxf(__builtin_fmaxf(a, b), c); }
template <int... Is, class F> DEVI void sfor_impl(std::integer_sequence<int, Is...>, F&& f) { (f(std::integral_constant<int, Is>{}), ...); }
template <int N, class F> DEVI void sfor(F&& f) { sfor_impl(std::make_integer_sequence<int, N>{}, f); }
#define AI __attribute__((always_inline))
#define IC(n) std::integral_constant<int, n>{}

template <int NT = 0> DEVI void dma1(const char* sbase, unsigned voff, unsigned lds) {
    if (NT) asm volatile("s_mov_b32 m0, %2\n\ts_nop 0\n\tglobal_load_lds_dwordx4 %0, %1 nt" :: "v"(voff), "s"(sbase), "s"(lds) : "memory");
    else asm volatile("s_mov_b32 m0, %2\n\ts_nop 0\n\tglobal_load_lds_dwordx4 %0, %1" :: "v"(voff), "s"(sbase), "s"(lds) : "memory");
}
DEVI void touch(const char* sbase, unsigned voff, unsigned lds) {
    asm volatile("s_mov_b32 m0, %2\n\ts_nop 0\n\tglobal_load_lds_dword %0, %1" :: "v"(voff), "s"(sbase), "s"(lds) : "memory");
}
struct WSrc { const char* base; unsigned step, voff; };
DEVI void wpiece(const WSrc& s, unsigned lds, int n) { dma1<NT_W>(s.base + (size_t)n * s.step, s.voff, lds + n * 1024); }
DEVI WSrc wsrc_a(const char* base, unsigned rs, int nt, int lane) { const int t = (lane >> 4) < nt ? (lane >> 4) : nt - 1; return WSrc{base, 4 * rs, ((lane >> 2) & 3) * rs + t * 64 + (lane & 3) * 16}; }
DEVI WSrc wsrc_b(const char* base, unsigned rs, int lane) { return WSrc{base, 8 * rs, (4 * (lane >> 5) + ((lane >> 2) & 3)) * rs + ((lane >> 4) & 1) * 64 + (lane & 3) * 16}; }

DEVI void poll_word(const GAS unsigned* p, unsigned e) {
    unsigned a = __hip_atomic_load(p, __ATOMIC_RELAXED, __HIP_MEMORY_SCOPE_AGENT);
#pragma nounroll
    for (unsigned sp = 0; sp < SPIN_CAP; ++sp) {
        __builtin_amdgcn_s_sleep(POLL_GAP);
        const unsigned b2 = __hip_atomic_load(p, __ATOMIC_RELAXED, __HIP_MEMORY_SCOPE_AGENT);
        if (wall(a == e)) break;
        __builtin_amdgcn_s_sleep(POLL_GAP);
        a = __hip_atomic_load(p, __ATOMIC_RELAXED, __HIP_MEMORY_SCOPE_AGENT);
        if (wall(b2 == e)) break;
    }
    __builtin_amdgcn_fence(__ATOMIC_ACQUIRE, "wavefront");
}
DEVI void wait_flags(const char* ws, unsigned foff, int n, unsigned e, int lane) {
    const GAS unsigned* p = (const GAS unsigned*)(ws + foff) + (lane < n ? lane : 0) * 32;
    if (POLL2) poll_word(p, e);
    else {
#pragma nounroll
        for (unsigned sp = 0; sp < SPIN_CAP; ++sp) {
            unsigned v = __hip_atomic_load(p, __ATOMIC_RELAXED, __HIP_MEMORY_SCOPE_AGENT);
            if (wall(v == e)) break;
        }
        __builtin_amdgcn_fence(__ATOMIC_ACQUIRE, "wavefront");
    }
}
DEVI void set_flag(char* ws, unsigned foff, unsigned e) { __hip_atomic_store((GAS unsigned*)(ws + foff), e, __ATOMIC_RELAXED, __HIP_MEMORY_SCOPE_AGENT); }
DEVI unsigned flag_off(int ph, int idx) { return WS_FLAG + (ph * 256 + idx) * 128; }

constexpr float GC0 = -2.f * 0.7978845608028654f * 1.4426950408889634f, GC1 = GC0 * 0.044715f;
DEVI float gelu_t(float x) {
    float z = x * __builtin_fmaf(x * x, GC1, GC0);
    return x * __builtin_amdgcn_rcpf(1.f + __builtin_amdgcn_exp2f(z));
}
struct GeluK { float nmean, k1, k0, irs; };
DEVI void gelu_step(int o, float& y, float& z, float c, const GeluK& g) {
    if (o == 0) y = fma1(g.nmean, c, y);
    else if (o == 1) z = mul1(y, y);
    else if (o == 2) z = fma1(z, g.k1, g.k0);
    else if (o == 3) z = mul1(y, z);
    else if (o == 4) z = __builtin_amdgcn_exp2f(z);
    else if (o == 5) z = fma1(z, g.irs, g.irs);
    else if (o == 6) z = __builtin_amdgcn_rcpf(z);
    else y = mul1(y, z);
}

#define STAMP(k) do {} while (0)
#define STAMP_DECL
#define STAMP_FLUSH() do {} while (0)

#define LANE_GEOM() int lane_ = threadIdx.x & 63; asm volatile("" : "+v"(lane_)); const int lane = lane_, r32 = lane & 31, hh = lane >> 5; \
    const int trq = (lane & 15) >> 2, trQ_ = ((lane >> 4) & 1) * 4 + (lane & 3), gq = (trQ_ & 4) | ((trQ_ & 1) << 1) | ((trQ_ >> 1) & 1); \
    const unsigned vx = (hh * XR + r32) * 16; (void)trq; (void)gq; (void)vx; (void)r32; (void)hh
constexpr unsigned L_FLG = L_MISC + 1920, F_RDY = 0, F_DONE = 16;
DEVI unsigned ldsw_ld(unsigned i) { return *(volatile LAS unsigned*)(uintptr_t)(L_FLG + 4 * i); }
DEVI u32x2 ldsw_ld2(unsigned i) { return *(volatile LAS u32x2*)(uintptr_t)(L_FLG + 4 * i); }
DEVI void ldsw_st(unsigned i, unsigned v) { *(volatile LAS unsigned*)(uintptr_t)(L_FLG + 4 * i) = v; }
DEVI void ring_wait(int c) {
#pragma nounroll
    for (unsigned sp = 0; sp < SPIN_CAP * 16u; ++sp) { const u32x2 v = ldsw_ld2(F_RDY + 2 * c); if (__builtin_amdgcn_readfirstlane(v.x & v.y)) break; __builtin_amdgcn_s_sleep(1); }
}
DEVI void slot_wait(unsigned n, int lane) {
#pragma nounroll
    for (unsigned sp = 0; sp < SPIN_CAP * 16u; ++sp) { const unsigned v = ldsw_ld(F_DONE + (lane < 6 ? lane : 0)); if (wall(v >= n)) break; __builtin_amdgcn_s_sleep(1); }
}
#define LB(N) asm volatile("s_waitcnt vmcnt(" #N ")\n\ts_barrier" ::: "memory")
#define BAR() asm volatile("s_barrier" ::: "memory")

__global__ __launch_bounds__(512, 2) void fused_fwd(Params P) {
    __shared__ __attribute__((aligned(16))) char smem[163840];
    const unsigned L0 = (unsigned)(uintptr_t)(LAS char*)smem;
    const int tid = threadIdx.x, wv = __builtin_amdgcn_readfirstlane(tid >> 6), w = wv & 3;
    const int b = blockIdx.x, xcd = b & 7, slot = b >> 3;
    const int m = 2 * xcd + (slot >> 4), h = slot & 15;
    const int g = slot & 3, nt = g < 2 ? 4 : 3, tb = g < 2 ? 4 * g : 3 * g + 2;
    const int ti = 8 * xcd + (slot >> 2);
    const int s16 = slot & 15, pr = s16 >> 1;
    const int nt6 = ((s16 ^ pr) & 1) ? 3 : 4, tb6 = 7 * pr + ((s16 & 1) ? (pr & 1 ? 3 : 4) : 0);
    const bool act7 = slot < 28;
    const int j7 = 4 * xcd + (act7 ? slot / 7 : 0), g7 = act7 ? slot % 7 : 0;
    const unsigned wst = L0 + w * L_WST;
    const unsigned shift = (56 * h) & 15u;
    STAMP_DECL

    if (wv >= 4) {
        if (PRIO_L) __builtin_amdgcn_s_setprio(PRIO_L);
        LANE_GEOM();
        const unsigned wrd = 16384 + hh * 6 * 256 + trq * 64 + ((shift + gq * 8) & 63);
        LAS float* csp = (LAS float*)(uintptr_t)(L0 + L_MISC);
        if (w < 2) {
            const unsigned xoffE = (lane >> 3) * 896 + (((lane & 7) ^ (lane >> 4)) << 4);
            const unsigned xoffO = (lane >> 3) * 896 + (((lane & 7) ^ (4 + (lane >> 4))) << 4);
            const unsigned colb = (56 * h) & ~15u;
            unsigned woff[3];
#pragma unroll
            for (int j = 0; j < 3; ++j) {
                const int bl = 4 * j + (lane >> 4), kql = bl / 3, tile = bl % 3;
                woff[j] = (4 * kql + ((lane >> 2) & 3)) * 2688 + tile * 896 + colb + (lane & 3) * 16;
            }
            const char* xb = (const char*)P.x + (size_t)(128 * m + 64 * w) * 896;
            const char* wb = (const char*)P.wqkv + (size_t)(32 * w) * 2688;
            LBAR();
#pragma unroll 1
            for (int kc = 0; kc < 7; ++kc) {
                if (kc >= 5) slot_wait(kc - 4, lane);
                const unsigned base = L0 + (kc >= 5 ? kc - 5 : kc) * L_SLOT;
#pragma unroll
                for (int i = 0; i < 6; ++i) dma1<NT_QKV>(wb + (size_t)kc * 64 * 2688 + (i / 3) * 16 * 2688, woff[i % 3], base + 16384 + (6 * w + i) * 1024);
#pragma unroll
                for (int i = 0; i < 8; ++i) dma1<NT_X>(xb + kc * 128 + i * 8 * 896, (i & 1) ? xoffO : xoffE, base + (8 * w + i) * 1024);
                if (kc > 0) { WAIT_VM(14); ldsw_st(F_RDY + 2 * (kc - 1) + w, 1); }
            }
            WAIT_VM(0); ldsw_st(F_RDY + 12 + w, 1);
            {
                ring_wait(6);
                const unsigned a = L0 + L_SLOT + wrd + w * 3072;
#pragma unroll
                for (int t = 0; t < 3; ++t) { const u32x4 f = wfrag(a + t * 256, a + 768 + t * 256);
                    const float v = hsum((dot2(f[0], ONES, 0.f) + dot2(f[1], ONES, 0.f)) + (dot2(f[2], ONES, 0.f) + dot2(f[3], ONES, 0.f)));
                    if (hh == 0) csp[(2 + w) * 96 + t * 32 + r32] = v; }
            }
        } else {
            const int srow = 128 * m + 8 * h + 4 * (w - 2) + (lane >> 4);
            const char* sp = (const char*)P.x + (size_t)srow * 896 + (lane & 15) * 16;
            u32x4 sr[4];
            asm volatile("global_load_dwordx4 %0, %4, off\n\tglobal_load_dwordx4 %1, %4, off offset:256\n\tglobal_load_dwordx4 %2, %4, off offset:512\n\tglobal_load_dwordx4 %3, %5, off"
                         : "=&v"(sr[0]), "=&v"(sr[1]), "=&v"(sr[2]), "=&v"(sr[3]) : "v"(sp), "v"(sp + ((lane & 15) < 8 ? 768 : 0)) : "memory");
            if (w == 2 && lane < 32) ldsw_st(lane, 0);
            LBAR();
            {
                const unsigned lx = lane < 56 ? lane : 0, lw = b * 37 + (lane < 37 ? lane : 0);
                const unsigned fl = lane - 56;
                const int fidx = fl < 2 || fl == 5 ? m * 16 + h : ti * 4 + g;
                const char* tf = fl < 6 ? P.ws + flag_off(fl, fidx) : P.ws + WS_ST1 + (128 * m + 8 * h) * 16;
                const int lo_ = lane - 40, tw_ = tb + ((lo_ & 3) < nt ? (lo_ & 3) : 0);
                const unsigned town = lo_ < 4 ? WS_KV + h * KVH + (4 * m + lo_) * 4096 : lo_ < 8 ? (unsigned)(WS_O + (4 * h * XR + 128 * m) * 16 + (lo_ - 4) * XROW)
                                    : lo_ == 8 ? WS_H + (4 * tb * XR + 32 * ti) * 16 : lo_ == 9 ? WS_HH + (4 * tb * XR + 32 * ti) * 16 : lo_ == 10 ? WS_X1 + (4 * tb * XR + 32 * ti) * 16
                                    : lo_ == 11 ? WS_STH + (tb * 2048 + 32 * ti) * 8 : lo_ == 12 ? WS_STX + (tb * 2048 + 32 * ti) * 8
                                    : WS_U + ((4 * tb6 + 4 * (lo_ - 13)) * XR + 128 * m) * 16;
                (void)tw_;
                const char* tp = !EARLY_TOUCH ? P.ws + WS_SPARE : w == 2 ? ((TOUCH_FLAGS && lane >= 56 && lane < 63) ? tf : (const char*)P.x + (size_t)(8 * b + lx / 7) * 896 + (lx % 7) * 128)
                                                                       : ((TOUCH_OWN && lane >= 40 && lane < 56) ? P.ws + town : (const char*)P.wqkv + (size_t)(lw < 9408 ? lw : 0) * 128);
                if (TOUCH_DELAY) __builtin_amdgcn_s_sleep(TOUCH_DELAY);
                asm volatile("s_mov_b32 m0, %1\n\ts_nop 0\n\tglobal_load_lds_dword %0, off" :: "v"(tp), "s"(L0 + L_MISC + 2048) : "memory");
            }
            {
                asm volatile("s_waitcnt vmcnt(1)" : "+v"(sr[0]), "+v"(sr[1]), "+v"(sr[2]), "+v"(sr[3]) :: "memory");
                if ((lane & 15) >= 8) sr[3] = (u32x4){0u, 0u, 0u, 0u};
                float t1 = 0.f, t2 = 0.f;
#pragma unroll
                for (int q = 0; q < 4; ++q)
#pragma unroll
                    for (int i = 0; i < 4; ++i) { t1 = dot2(sr[q][i], ONES, t1); t2 = dot2(sr[q][i], sr[q][i], t2); }
                t1 += xor32<1>(t1); t2 += xor32<1>(t2); t1 += xor32<2>(t1); t2 += xor32<2>(t2); t1 += xor32<4>(t1); t2 += xor32<4>(t2); t1 += xor32<8>(t1); t2 += xor32<8>(t2);
                const float mean = t1 * (1.f / D), var = __builtin_fmaxf(t2 * (1.f / D) - mean * mean, 0.f);
                const unsigned e = *(const unsigned*)(P.ws + WS_EP) + 1u;
                const __amdgpu_buffer_rsrc_t rs = __builtin_amdgcn_make_buffer_rsrc((void*)P.ws, 0, WS_END, 0x00020000);
                if ((lane & 15) == 0) gst(rs, (u32x4){f2u(mean), e, f2u(__builtin_amdgcn_rsqf(var + EPS)), e}, srow * 16, WS_ST1);
            }
            float cs[3][4] = {};
            const unsigned wrd2 = wrd + (w - 2) * 2 * 3072;
#pragma unroll 1
            for (int c = 0; c < 7; ++c) {
                ring_wait(c);
                if (c == LATE_TOUCH) {
                    unsigned long long pc; unsigned back;
                    asm volatile("s_getpc_b64 %0\nmk_code_here:\n\ts_mov_b32 %1, mk_code_here-_ZN2mk9fused_fwdENS_6ParamsE" : "=s"(pc), "=s"(back));
                    constexpr unsigned len = CODE_TOUCH_BYTES;
                    const unsigned oc = (slot * 18 + (lane < 18 ? lane : 0)) * 128, o3 = (b * 64 + lane) * 128, o2 = (b * 16 + (lane & 15)) * 128;
                    const char* t0 = w == 2 ? (const char*)(pc - back) + (oc < len ? oc : 0) : (const char*)P.w3 + (o3 < 1605632 ? o3 : 0);
                    const char* t1 = w == 2 ? (lane < 16 ? (const char*)P.wo : lane < 32 ? (const char*)P.w1 : (const char*)P.w2) + (o2 < 401408 ? o2 : 0) : (const char*)P.w4 + (o3 < 1605632 ? o3 : 0);
                    asm volatile("s_mov_b32 m0, %2\n\ts_nop 0\n\tglobal_load_lds_dword %0, off\n\tglobal_load_lds_dword %1, off" :: "v"(t0), "v"(t1), "s"(L0 + L_MISC + 2048) : "memory");
                }
                const unsigned a = L0 + (c >= 5 ? c - 5 : c) * L_SLOT + (c < 6 ? wrd2 : wrd + w * 3072);
#pragma unroll
                for (int t = 0; t < 3; ++t) { const u32x4 f = wfrag(a + t * 256, a + 768 + t * 256);
#pragma unroll
                    for (int i = 0; i < 4; ++i) cs[t][i] = dot2(f[i], ONES, cs[t][i]); }
                if (c < 6) {
#pragma unroll
                    for (int t = 0; t < 3; ++t) { const u32x4 f = wfrag(a + 3072 + t * 256, a + 3072 + 768 + t * 256);
#pragma unroll
                        for (int i = 0; i < 4; ++i) cs[t][i] = dot2(f[i], ONES, cs[t][i]); }
                }
                WAIT_LGKM();
                ldsw_st(F_DONE + 2 + w, c + 1);
            }
#pragma unroll
            for (int t = 0; t < 3; ++t) { const float v = hsum((cs[t][0] + cs[t][1]) + (cs[t][2] + cs[t][3])); if (hh == 0) csp[(w - 2) * 96 + t * 32 + r32] = v; }
        }
        LBAR();
        auto stage = [&](const WSrc& s, unsigned lds, int n0, int n1) AI {
#pragma unroll 1
            for (int n = n0; n < n1; ++n) wpiece(s, lds, n);
        };
        LB(0);
        BAR();
        stage(wsrc_a((const char*)P.wo + (size_t)(112 * w) * 896 + tb * 64, 896, nt, lane), wst, 0, 28);
        LB(0);
        { const WSrc s_ = wsrc_a((const char*)P.w1 + (size_t)(112 * w) * 896 + tb * 64, 896, nt, lane); BAR(); stage(s_, wst, 0, HALF); BAR(); stage(s_, wst, HALF, 28); BAR(); LB(0); }
        { const WSrc s_ = wsrc_a((const char*)P.w2 + (size_t)(112 * w) * 896 + tb * 64, 896, nt, lane); BAR(); stage(s_, wst, 0, HALF); BAR(); stage(s_, wst, HALF, 28); BAR(); LB(0); }
        { const WSrc s_ = wsrc_a((const char*)P.w3 + (size_t)(112 * w) * 3584 + tb6 * 64, 3584, nt6, lane); BAR(); stage(s_, wst, 0, HALF); BAR(); stage(s_, wst, HALF, 28); BAR(); LB(0); }
        {
            float c[4] = {0.f, 0.f, 0.f, 0.f};
            const unsigned a0 = L0 + trq * 64 + gq * 8 + 2 * hh * 1024 + w * 256;
#pragma unroll 4
            for (int k = 0; k < 28; ++k) { const unsigned a = a0 + k * 4096; const u32x4 f = wfrag(a, a + 1024);
#pragma unroll
                for (int i = 0; i < 4; ++i) c[i] = dot2(f[i], ONES, c[i]); }
            const float cc = hsum((c[0] + c[1]) + (c[2] + c[3]));
            if (hh == 0) ((LAS float*)(uintptr_t)(L0 + L_M2 + 1024))[w * 32 + r32] = cc;
        }
        if (b == 0 && w == 3) {
            const unsigned e = *(const volatile GAS unsigned*)(P.ws + WS_EP) + 1u;
            const GAS unsigned* p = (const GAS unsigned*)(P.ws + flag_off(0, lane));
#pragma nounroll
            for (unsigned sp = 0; sp < SPIN_CAP; ++sp) {
                const unsigned v0 = __hip_atomic_load(p, __ATOMIC_RELAXED, __HIP_MEMORY_SCOPE_AGENT), v1 = __hip_atomic_load(p + 64 * 32, __ATOMIC_RELAXED, __HIP_MEMORY_SCOPE_AGENT);
                const unsigned v2 = __hip_atomic_load(p + 128 * 32, __ATOMIC_RELAXED, __HIP_MEMORY_SCOPE_AGENT), v3 = __hip_atomic_load(p + 192 * 32, __ATOMIC_RELAXED, __HIP_MEMORY_SCOPE_AGENT);
                if (wall(v0 == e && v1 == e && v2 == e && v3 == e)) break;
            }
            if (lane == 0) __hip_atomic_store((GAS unsigned*)(P.ws + WS_EP), e, __ATOMIC_RELAXED, __HIP_MEMORY_SCOPE_AGENT);
        }
        LBAR();
        const WSrc w4s = wsrc_b((const char*)P.w4 + (size_t)(448 * w) * 896 + g7 * 128, 896, lane);
        if (act7) { stage(w4s, L0 + L_X + w * 8192, 0, 8); stage(w4s, wst - 8 * 1024, 8, W4SPLIT); }
        BAR();
        if (act7) stage(w4s, wst - 8 * 1024, W4SPLIT, 36);
        LB(0);
        if (act7) {
            BAR();
            stage(w4s, wst - 36 * 1024, 36, 56);
            LB(0);
            BAR();
        }
        STAMP_FLUSH();
        return;
    }

    if (PRIO_C) __builtin_amdgcn_s_setprio(PRIO_C);
    const unsigned e = *(const unsigned*)(P.ws + WS_EP) + 1u;
    const __amdgpu_buffer_rsrc_t rs = __builtin_amdgcn_make_buffer_rsrc((void*)P.ws, 0, WS_END, 0x00020000);
    u32x4 qf0, qf1;
    {
        LANE_GEOM();
        const unsigned wrd = 16384 + hh * 6 * 256 + trq * 64 + ((shift + gq * 8) & 63);
        STAMP(15);
        f32x16 aq = {}, ak = {}, av = {};
        const unsigned xrd = (32 * w + r32) * 128 + ((hh ^ ((r32 >> 1) & 7)) << 4);
        struct Fr { u32x4 x, q, k, v; };
        Fr F[4];
        unsigned xb[3][4], wb3[3];
#pragma unroll
        for (int gsl = 0; gsl < 3; ++gsl) {
#pragma unroll
            for (int j = 0; j < 4; ++j) { xb[gsl][j] = L0 + (xrd ^ (j << 5)) + gsl * 2 * L_SLOT; PIN(xb[gsl][j]); }
            wb3[gsl] = L0 + wrd + gsl * 2 * L_SLOT; PIN(wb3[gsl]);
        }
        auto ldx = [&](int sl, int j) AI { F[j].x = lds128(xb[sl / 2][j] + (sl & 1) * L_SLOT); const unsigned a = wb3[sl / 2] + (sl & 1) * L_SLOT + j * 3072; F[j].q = wfrag(a, a + 768); };
        auto ldk = [&](int sl, int j) AI { const unsigned a = wb3[sl / 2] + (sl & 1) * L_SLOT + j * 3072; F[j].k = wfrag(a + 256, a + 1024); };
        auto ldv = [&](int sl, int j) AI { const unsigned a = wb3[sl / 2] + (sl & 1) * L_SLOT + j * 3072; F[j].v = wfrag(a + 512, a + 1280); };
        LAS float* stw = (LAS float*)(uintptr_t)(L0 + L_MISC + 3072 + w * 256);
        BAR();
        ring_wait(0);
        STAMP(16);
#pragma unroll
        for (int j = 0; j < 2; ++j) { ldx(0, j); ldk(0, j); ldv(0, j); }
        SB();
        u32x4 gv = {0u, 0u, 0u, 0u};
        const unsigned gvo = (128 * m + 32 * w + r32) * 16;
        sfor<7>([&](auto cc) AI {
            constexpr int c = decltype(cc)::value, sl = c >= 5 ? c - 5 : c, nsl = c >= 4 ? c - 4 : c + 1;
            u32x2 rdy;
            sfor<4>([&](auto jc) AI {
                constexpr int j = decltype(jc)::value, jn = (j + 2) & 3, lb = j < 2 ? sl : nsl;
                constexpr bool ld = j < 2 || c < 6;
                if constexpr (j == 2 && c < 6) { if (__builtin_amdgcn_readfirstlane(rdy.x & rdy.y) == 0) ring_wait(c + 1); SB(); }
                MFMA(aq, F[j].q, F[j].x, aq);
                if constexpr (ld) ldx(lb, jn);
                SB();
                MFMA(ak, F[j].k, F[j].x, ak);
                if constexpr (ld) ldk(lb, jn);
                if constexpr (j == 0 && c >= 1 && c <= 2) ldsw_st(F_DONE + w, c);
                if constexpr (j == 0 && c == 4) gv = gld(rs, gvo, WS_ST1);
                SB();
                MFMA(av, F[j].x, F[j].v, av);
                if constexpr (ld) ldv(lb, jn);
                if constexpr (j == 0 && c < 6) rdy = ldsw_ld2(F_RDY + 2 * (c + 1));
                SB();
            });
        });
#pragma nounroll
        for (unsigned sp2 = 0; sp2 < SPIN_CAP && !wall(gv.y == e && gv.w == e); ++sp2) { asm volatile("" ::: "memory"); gv = gld(rs, gvo, WS_ST1); }
        if (hh == 0) { stw[r32] = u2f(gv.x) * u2f(gv.z); stw[32 + r32] = u2f(gv.z); }
        STAMP(1);
        const float mean = u2f(gv.x), rstd = u2f(gv.z);
        LBAR();
        STAMP(17);
        const LAS float* csf = (const LAS float*)(uintptr_t)(L0 + L_MISC);
        const float cv = (csf[64 + r32] + csf[96 + 64 + r32]) + (csf[192 + 64 + r32] + csf[288 + 64 + r32]);
        const float qs = rstd * (0.18898223650461362f * 1.4426950408889634f);
#pragma unroll
        for (int q = 0; q < 4; ++q) {
            auto c4 = [&](int o) AI { return (*(const LAS f32x4*)(csf + o) + *(const LAS f32x4*)(csf + 96 + o)) + (*(const LAS f32x4*)(csf + 192 + o) + *(const LAS f32x4*)(csf + 288 + o)); };
            const f32x4 c1 = c4(8 * q + 4 * hh), c2 = c4(32 + 8 * q + 4 * hh);
            const f32x4 mr = *(const LAS f32x4*)(stw + 8 * q + 4 * hh), rr = *(const LAS f32x4*)(stw + 32 + 8 * q + 4 * hh);
#pragma unroll
            for (int i = 0; i < 4; ++i) {
                aq[4 * q + i] = (aq[4 * q + i] - mean * c1[i]) * qs;
                ak[4 * q + i] = (ak[4 * q + i] - mean * c2[i]) * rstd;
                av[4 * q + i] = av[4 * q + i] * rr[i] - mr[i] * cv;
            }
        }
        if (hh == 1) { aq[12] = 1.f; aq[13] = 0.f; aq[14] = 0.f; aq[15] = 0.f; ak[12] = 0.f; ak[13] = 0.f; ak[14] = 0.f; ak[15] = 0.f; }
        if (r32 >= 28) {
            const float pv = r32 == 28 ? 1.f : 0.f;
#pragma unroll
            for (int i = 0; i < 16; ++i) av[i] = pv;
        }
        const u32x4 k0 = pack8(ak, 0), k1 = pack8(ak, 1), v0 = pack8(av, 0), v1 = pack8(av, 1);
        const unsigned go = WS_KV + h * KVH + (4 * m + w) * 4096;
        gst(rs, k0, lane * 16, go); gst(rs, k1, lane * 16, go + 1024); gst(rs, v0, lane * 16, go + 2048); gst(rs, v1, lane * 16, go + 3072);
        const unsigned lo = L0 + L_OWNKV + w * 4096 + lane * 16;
        sts128(lo, k0); sts128(lo + 1024, k1); sts128(lo + 2048, v0); sts128(lo + 3072, v1);
        qf0 = pack8(aq, 0); qf1 = pack8(aq, 1);
        STAMP(18);
        WAIT_BAR(0);
        if (tid == 0) set_flag(P.ws, flag_off(0, m * 16 + h), e);
        STAMP(2);
    }
    {
        LANE_GEOM();
        const int mt = m - 2 + (w >> 1);
        u32x4 kv[8];
        if (mt >= 0) {
            wait_flags(P.ws, flag_off(0, mt * 16 + h), 1, e, lane);
            const unsigned go = WS_KV + h * KVH + (4 * m - 8 + 2 * w) * 4096;
#pragma unroll
            for (int i = 0; i < 8; ++i) kv[i] = gld(rs, lane * 16, go + i * 1024);
        } else {
#pragma unroll
            for (int i = 0; i < 8; ++i) kv[i] = (u32x4){0u, 0u, (i & 3) == 1 && hh == 1 ? 0xF149u : 0u, 0u};
        }
        f32x16 cdiag, cold;
#pragma unroll
        for (int i = 0; i < 16; ++i) { const int kr = (i & 3) + 8 * (i >> 2) + 4 * hh; cdiag[i] = kr <= r32 ? 0.f : -1e30f; cold[i] = kr > r32 ? 0.f : -1e30f; }
#pragma unroll
        for (int i = 0; i < 8; ++i) sts128(L0 + L_KV + 2 * w * 4096 + i * 1024 + lane * 16, kv[i]);
        LBAR();
        STAMP(3);
        unsigned kvb = L0 + L_KV + w * 4096 + lane * 16;
        PIN(kvb);
        u32x4 kf[9][2], vf[9][2];
        const f32x16 zero16 = {};
        auto ldK = [&](int i) AI { kf[i][0] = lds128(kvb + i * 4096); kf[i][1] = lds128(kvb + i * 4096 + 1024); };
        auto ldV = [&](int i) AI { vf[i][0] = lds128(kvb + i * 4096 + 2048); vf[i][1] = lds128(kvb + i * 4096 + 3072); };
        auto smax = [&](const f32x16& s, float a) AI {
#pragma unroll
            for (int i = 0; i < 16; i += 2) a = max3(a, s[i], s[i + 1]);
            return a; };
        ldK(0); ldK(1);
        float mx = -3e38f;
        f32x16 s[2];
        auto smax8 = [&](const f32x16& v, int o, float a) AI {
#pragma unroll
            for (int i = 0; i < 8; i += 2) a = max3(a, v[o + i], v[o + i + 1]);
            return a; };
        sfor<9>([&](auto ic) AI {
            constexpr int i = decltype(ic)::value;
            if constexpr (i + 2 < 9) ldK(i + 2);
            MFMA(s[i & 1], kf[i][0], qf0, i == 0 ? cold : i == 8 ? cdiag : zero16);
            SB();
            MFMA(s[i & 1], kf[i][1], qf1, s[i & 1]);
            SB();
            if constexpr (i > 0) { mx = smax8(s[(i - 1) & 1], 0, mx); mx = smax8(s[(i - 1) & 1], 8, mx); PIN(mx); }
            SB();
        });
        ldK(0); ldK(1); ldV(0);
        mx = hmax(smax(s[0], mx));
        STAMP(25);
        f32x16 o = {}, cm;
#pragma unroll
        for (int i = 0; i < 16; ++i) { cm[i] = -mx; cdiag[i] -= mx; cold[i] -= mx; }
        MFMA(s[0], kf[0][0], qf0, cold); MFMA(s[0], kf[0][1], qf1, s[0]);
        SB();
        sfor<9>([&](auto ic) AI {
            constexpr int i = decltype(ic)::value;
            f32x16& c = s[i & 1];
            f32x16& n = s[(i + 1) & 1];
            if constexpr (i + 2 < 9) ldK(i + 2);
            if constexpr (i + 1 < 9) ldV(i + 1);
            if constexpr (i + 1 < 9) MFMA(n, kf[i + 1][0], qf0, i + 1 == 8 ? cdiag : cm);
#pragma unroll
            for (int j = 0; j < 8; ++j) c[j] = __builtin_amdgcn_exp2f(c[j]);
            u32x4 p0 = pack8(c, 0);
            PIN(p0);
            SB();
            if constexpr (i + 1 < 9) MFMA(n, kf[i + 1][1], qf1, n);
            MFMA(o, vf[i][0], p0, o);
#pragma unroll
            for (int j = 8; j < 16; ++j) c[j] = __builtin_amdgcn_exp2f(c[j]);
            const u32x4 p1 = pack8(c, 1);
            MFMA(o, vf[i][1], p1, o);
            SB();
        });
        STAMP(26);
        auto lr = __builtin_amdgcn_permlane32_swap(f2u(o[12]), f2u(o[12]), false, false);
        const float inv = __builtin_amdgcn_rcpf(u2f(lr[1]));
#pragma unroll
        for (int i = 0; i < 16; ++i) o[i] *= inv;
        if (hh == 1) { o[12] = 0.f; o[13] = 0.f; o[14] = 0.f; o[15] = 0.f; }
        const unsigned vo = (hh * XR + r32) * 16, so = WS_O + ((4 * h) * XR + 128 * m + 32 * w) * 16;
        gst(rs, pack8(o, 0), vo, so); gst(rs, pack8(o, 1), vo, so + 2 * XROW);
        WAIT_BAR(0);
        if (tid == 0) set_flag(P.ws, flag_off(1, m * 16 + h), e);
        STAMP(4);
    }
    const bool act = w < nt;
    const int tw = tb + w;
#define GEMM_GEOM() LANE_GEOM(); \
    unsigned trW[2]; trW[0] = L0 + (w < nt ? w : nt - 1) * 256 + trq * 64 + gq * 8 + 2 * hh * 1024; trW[1] = trW[0] + 57344; PIN(trW[0]); PIN(trW[1]); \
    auto wimg = [&](int kq0, int kq1) AI { return wfrag(kq0 < 56 ? trW[0] + kq0 * 1024 : trW[1] + (kq0 - 56) * 1024, kq1 < 56 ? trW[0] + kq1 * 1024 : trW[1] + (kq1 - 56) * 1024); }; \
    unsigned xl = L0 + L_X + lane * 16; PIN(xl); \
    LAS float* m2 = (LAS float*)(uintptr_t)(L0 + L_M2 + w * 256); (void)m2
    auto gemm = [&](auto nk, const u32x4* wr, unsigned xl) AI -> f32x16 {
        constexpr int NK = decltype(nk)::value, PD = 4;
        f32x16 acc = {};
        u32x4 xf[NK];
#pragma unroll
        for (int k = 0; k < PD; ++k) xf[k] = lds128(xl + k * 1024);
        SB();
        sfor<NK>([&](auto kc) AI {
            constexpr int k = decltype(kc)::value;
            if constexpr (k + PD < NK) xf[k + PD] = lds128(xl + (k + PD) * 1024);
            MFMA(acc, wr[k], xf[k], acc);
            SB();
        });
        return acc;
    };
    u32x4 hp[2];
    float meanH, rstdH;
    {
        GEMM_GEOM();
        u32x4 xr[2];
        const char* xp = (const char*)P.x + (size_t)(32 * ti + r32) * 896 + 64 * (act ? tw : tb) + 16 * hh;
        xr[0] = *(const GAS u32x4*)xp; xr[1] = *(const GAS u32x4*)(xp + 32);
        u32x4 wr[32];
        sfor<32>([&](auto kc) AI { constexpr int k = decltype(kc)::value, kq = 7 * (k >> 1) + 4 * (k & 1);
            if constexpr (k < 31) wr[k] = wimg(kq, kq + 1);
            else { const u32x2 p = ldstr(trW[1] + (kq - 56) * 1024), q = ldstr(trW[1] + (kq - 56) * 1024 + (hh ? 0 : 1024)); wr[k] = (u32x4){p.x, p.y, q.x, q.y}; } });
        LBAR();
        STAMP(19);
        wait_flags(P.ws, flag_off(1, (ti >> 2) * 16 + 4 * w), 4, e, lane);
        STAMP(20);
        u32x4 xf[8];
#pragma unroll
        for (int k = 0; k < 8; ++k) xf[k] = gld(rs, vx, WS_O + ((16 * w + 2 * k) * XR + 32 * ti) * 16);
#pragma unroll
        for (int k = 0; k < 8; ++k) sts128(xl + (8 * w + k) * 1024, xf[k]);
        LBAR();
        STAMP(5);
        f32x16 r = gemm(IC(32), wr, xl);
        STAMP(21);
        float s1 = 0.f, s2 = 0.f;
#pragma unroll
        for (int s = 0; s < 2; ++s) {
#pragma unroll
            for (int i = 0; i < 4; ++i) {
                hp[s][i] = pk(r[8 * s + 2 * i] + blo(xr[s][i]), r[8 * s + 2 * i + 1] + bhi(xr[s][i]));
                s1 = dot2(hp[s][i], ONES, s1); s2 = dot2(hp[s][i], hp[s][i], s2);
            }
            if (act) gst(rs, hp[s], vx, WS_H + ((4 * tw + 2 * s) * XR + 32 * ti) * 16);
        }
        s1 = hsum(s1); s2 = hsum(s2);
        if (act && hh == 0) gst8(rs, (u32x2){f2u(s1), f2u(s2)}, r32 * 8, WS_STH + (tw * 2048 + 32 * ti) * 8);
        STAMP(22);
        WAIT_BAR(0);
        if (tid == 0) set_flag(P.ws, flag_off(2, ti * 4 + g), e);
        STAMP(6);
    }
#define LOAD_WR() u32x4 wr[28]; sfor<28>([&](auto kc) AI { constexpr int k = decltype(kc)::value; wr[k] = wimg(4 * k, 4 * k + 1); })
    const int fsn = w == 0 ? 1 : 2, fsl = w <= 1 ? 0 : w - 1;
    {
        GEMM_GEOM();
        BAR();
        LOAD_WR();
        LBAR();
        wait_flags(P.ws, flag_off(2, ti * 4), 4, e, lane);
        u32x4 xf[7];
#pragma unroll
        for (int k = 0; k < 7; ++k) xf[k] = gld(rs, vx, WS_H + ((14 * w + 2 * k) * XR + 32 * ti) * 16);
        u32x2 sv[7];
#pragma unroll
        for (int q = 0; q < 7; ++q) sv[q] = gld8(rs, (7 * hh * 2048 + r32) * 8, WS_STH + (q * 2048 + 32 * ti) * 8);
        SB();
        {
            float cs[4] = {0.f, 0.f, 0.f, 0.f};
#pragma unroll
            for (int k = 0; k < 28; ++k)
#pragma unroll
                for (int i = 0; i < 4; ++i) cs[i] = dot2(wr[k][i], ONES, cs[i]);
            const float c = hsum((cs[0] + cs[1]) + (cs[2] + cs[3]));
            if (hh == 0) m2[r32] = c;
        }
        SB();
#pragma unroll
        for (int k = 0; k < 7; ++k) sts128(xl + (7 * w + k) * 1024, xf[k]);
        LBAR();
        STAMP(7);
        f32x16 r = gemm(IC(28), wr, xl);
        float t1 = 0.f, t2 = 0.f;
#pragma unroll
        for (int q = 0; q < 7; ++q) { t1 += u2f(sv[q].x); t2 += u2f(sv[q].y); }
        t1 = hsum(t1); t2 = hsum(t2);
        meanH = t1 * (1.f / D);
        const float var = __builtin_fmaxf(t2 * (1.f / D) - meanH * meanH, 0.f);
        rstdH = __builtin_amdgcn_rsqf(var + EPS);
        const float sc = rstdH * __builtin_amdgcn_rsqf(var * rstdH * rstdH + EPS);
#pragma unroll
        for (int q = 0; q < 4; ++q) {
            const f32x4 c = *(const LAS f32x4*)(m2 + 8 * q + 4 * hh);
#pragma unroll
            for (int i = 0; i < 4; ++i) r[4 * q + i] = gelu_t((r[4 * q + i] - meanH * c[i]) * sc);
        }
#pragma unroll
        for (int s = 0; s < 2; ++s) if (act) gst(rs, pack8(r, s), vx, WS_HH + ((4 * tw + 2 * s) * XR + 32 * ti) * 16);
        WAIT_BAR(0);
        if (tid == 0) set_flag(P.ws, flag_off(3, ti * 4 + g), e);
        STAMP(8);
    }
    {
        GEMM_GEOM();
        BAR();
        LOAD_WR();
        LBAR();
        const int sh = w == 0 ? -1 : w == 1 ? 1 : w == 2 ? -2 : 2;
        const int tn = ti + (sh < 0 ? -1 : 1);
        {
            const bool nb = tn >= 0 && tn < 64;
            const int li = lane & 3;
            const GAS unsigned* p = (const GAS unsigned*)(P.ws + flag_off(3, (((li & 2) && nb) ? tn : ti) * 4 + fsl + ((li & 1) && fsn == 2 ? 1 : 0)));
            poll_word(p, e);
        }
        const int tok = 32 * ti + r32 + sh;
        const bool ok = tok >= 0 && tok < S;
        const unsigned vxs = ok ? (hh * XR + tok) * 16 : 0x7ffffff0u;
        u32x4 xf[7];
#pragma unroll
        for (int k = 0; k < 7; ++k) { xf[k] = gld(rs, vxs, WS_HH + (14 * w + 2 * k) * XROW); }
#pragma unroll
        for (int k = 0; k < 7; ++k) sts128(xl + (7 * w + k) * 1024, xf[k]);
        LBAR();
        STAMP(9);
        f32x16 r = gemm(IC(28), wr, xl);
        float s1 = 0.f, s2 = 0.f;
#pragma unroll
        for (int s = 0; s < 2; ++s) {
            u32x4 xp;
#pragma unroll
            for (int i = 0; i < 4; ++i) {
                xp[i] = pk(r[8 * s + 2 * i] + (blo(hp[s][i]) - meanH) * rstdH, r[8 * s + 2 * i + 1] + (bhi(hp[s][i]) - meanH) * rstdH);
                s1 = dot2(xp[i], ONES, s1); s2 = dot2(xp[i], xp[i], s2);
            }
            if (act) gst(rs, xp, vx, WS_X1 + ((4 * tw + 2 * s) * XR + 32 * ti) * 16);
        }
        s1 = hsum(s1); s2 = hsum(s2);
        if (act && hh == 0) gst8(rs, (u32x2){f2u(s1), f2u(s2)}, r32 * 8, WS_STX + (tw * 2048 + 32 * ti) * 8);
        WAIT_BAR(0);
        if (tid == 0) set_flag(P.ws, flag_off(4, ti * 4 + g), e);
        STAMP(10);
    }
    {
        LANE_GEOM();
        unsigned tr6[2];
        tr6[0] = L0 + trq * 64 + gq * 8 + 2 * hh * 1024; tr6[1] = tr6[0] + 57344; PIN(tr6[0]); PIN(tr6[1]);
        auto wf6 = [&](int t, int k) AI { const unsigned a = (k < 14 ? tr6[0] + k * 4096 : tr6[1] + (k - 14) * 4096) + t * 256; return wfrag(a, a + 1024); };
        const LAS float* cs6 = (const LAS float*)(uintptr_t)(L0 + L_M2 + 1024);
        wait_flags(P.ws, flag_off(4, (4 * m + w) * 4), 4, e, lane);
        STAMP(28);
        const int tk = 128 * m + 32 * w;
        auto ldx1 = [&](int k) AI { return gld(rs, vx, WS_X1 + (2 * k * XR + tk) * 16); };
        constexpr int PX = P6_PX, PW = 2;
        f32x16 acc[4] = {};
        u32x2 sv[7];
        {
            u32x4 xf[28], wf[28][4];
#pragma unroll
            for (int k = 0; k < PX; ++k) xf[k] = ldx1(k);
#pragma unroll
            for (int q = 0; q < 7; ++q) sv[q] = gld8(rs, (7 * hh * 2048 + r32) * 8, WS_STX + (q * 2048 + tk) * 8);
            BAR();
            STAMP(11);
#pragma unroll
            for (int k = 0; k < PW; ++k)
#pragma unroll
                for (int t = 0; t < 4; ++t) wf[k][t] = wf6(t, k);
            SB();
            sfor<28>([&](auto kc) AI {
                constexpr int k = decltype(kc)::value;
                MFMA(acc[0], wf[k][0], xf[k], acc[0]);
                if constexpr (k + PW < 28) { wf[k + PW][0] = wf6(0, k + PW); }
                SB();
                MFMA(acc[1], wf[k][1], xf[k], acc[1]);
                if constexpr (k + PW < 28) { wf[k + PW][1] = wf6(1, k + PW); }
                if constexpr (k + PX < 28) xf[k + PX] = ldx1(k + PX);
                SB();
                MFMA(acc[2], wf[k][2], xf[k], acc[2]);
                if constexpr (k + PW < 28) { wf[k + PW][2] = wf6(2, k + PW); }
                SB();
                MFMA(acc[3], wf[k][3], xf[k], acc[3]);
                if constexpr (k + PW < 28) { wf[k + PW][3] = wf6(3, k + PW); }
                SB();
            });
        }
        STAMP(23);
        LBAR();
        STAMP(24);
        float t1 = 0.f, t2 = 0.f;
#pragma unroll
        for (int q = 0; q < 7; ++q) { t1 += u2f(sv[q].x); t2 += u2f(sv[q].y); }
        t1 = hsum(t1); t2 = hsum(t2);
        const float mean = t1 * (1.f / D), var = __builtin_fmaxf(t2 * (1.f / D) - mean * mean, 0.f) + EPS, rstd = __builtin_amdgcn_rsqf(var);
        const GeluK gk = {-mean, GC1 * rstd * rstd * rstd, GC0 * rstd, var * rstd};
        const unsigned uo = WS_U + ((4 * tb6) * XR + tk) * 16;
        auto epi1 = [&](f32x16& y, int te) AI {
#pragma unroll
            for (int q = 0; q < 4; ++q) {
                const f32x4 c = *(const LAS f32x4*)(cs6 + te * 32 + 8 * q + 4 * hh);
#pragma unroll
                for (int i = 0; i < 4; ++i) {
                    const float v = __builtin_fmaf(gk.nmean, c[i], y[4 * q + i]);
                    const float z = v * __builtin_fmaf(v * v, gk.k1, gk.k0);
                    y[4 * q + i] = v * __builtin_amdgcn_rcpf(__builtin_fmaf(__builtin_amdgcn_exp2f(z), gk.irs, gk.irs));
                }
            }
            gst(rs, pack8(y, 0), vx, uo + 4 * te * XROW); gst(rs, pack8(y, 1), vx, uo + (4 * te + 2) * XROW);
            SB();
        };
        epi1(acc[0], 0); epi1(acc[1], 1); epi1(acc[2], 2);
        if (nt6 == 4) epi1(acc[3], 3);
        STAMP(29);
        WAIT_BAR(0);
        if (tid == 0) set_flag(P.ws, flag_off(5, m * 16 + s16), e);
        BAR();
        STAMP(12);
    }
    if (act7) {
        LANE_GEOM();
        const unsigned lanep = trq * 64 + gq * 8 + 2 * hh * 512;
        unsigned tA = L0 + L_X + w * 8192 + lanep, tB = wst + lanep; PIN(tA); PIN(tB);
        auto w4f = [&](int k, int t) AI { const unsigned a = (k < 4 ? tA + k * 2048 : k < 18 ? tB + (k - 4) * 2048 : tB + (k - 18) * 2048) + t * 256; return wfrag(a, a + 512); };
        u32x4 wr[28][2];
#pragma unroll
        for (int k = 0; k < 14; ++k) { wr[k][0] = w4f(k, 0); wr[k][1] = w4f(k, 1); }
        LBAR();
        const int tk = 64 * j7;
        const int tt = w >> 1, nt7 = w & 1;
        wait_flags(P.ws, flag_off(5, (j7 >> 1) * 16 + 4 * w), 4, e, lane);
        constexpr int PX = P7_PX;
        u32x4 xa[28][2];
#pragma unroll
        for (int k = 0; k < PX; ++k) { xa[k][0] = gld(rs, vx, WS_U + ((56 * w + 2 * k) * XR + tk) * 16); xa[k][1] = gld(rs, vx, WS_U + ((56 * w + 2 * k) * XR + tk + 32) * 16); }
        STAMP(13);
        SB();
        f32x16 acc[4] = {};
        u32x4 rh[2], rx[2];
        sfor<28>([&](auto kc) AI {
            constexpr int k = decltype(kc)::value;
            if constexpr (k == 13) { BAR(); SB(); }
            if constexpr (k + 3 >= 14 && k + 3 < 28) { wr[k + 3][0] = w4f(k + 3, 0); wr[k + 3][1] = w4f(k + 3, 1); }
            MFMA(acc[0], wr[k][0], xa[k][0], acc[0]); MFMA(acc[1], wr[k][1], xa[k][0], acc[1]);
            if constexpr (k + PX < 28) xa[k + PX][0] = gld(rs, vx, WS_U + ((56 * w + 2 * (k + PX)) * XR + tk) * 16);
            MFMA(acc[2], wr[k][0], xa[k][1], acc[2]); MFMA(acc[3], wr[k][1], xa[k][1], acc[3]);
            if constexpr (k + PX < 28) xa[k + PX][1] = gld(rs, vx, WS_U + ((56 * w + 2 * (k + PX)) * XR + tk + 32) * 16);
            if constexpr (k == 24 || k == 25) {
                constexpr int s = k - 24;
                const unsigned so = ((8 * g7 + 4 * nt7 + 2 * s) * XR + tk + 32 * tt) * 16;
                rh[s] = gld(rs, vx, WS_H + so); rx[s] = gld(rs, vx, WS_X1 + so);
            }
            SB();
        });
        STAMP(27);
#pragma unroll
        for (int t = 0; t < 4; ++t) {
            if (t != w) {
                const unsigned a = L0 + L_X + t * 12288 + (w < t ? w : w - 1) * 4096 + lane * 16;
#pragma unroll
                for (int q = 0; q < 4; ++q) stsf4(a + q * 1024, (f32x4){acc[t][4 * q], acc[t][4 * q + 1], acc[t][4 * q + 2], acc[t][4 * q + 3]});
            }
        }
        LBAR();
        f32x16 r = w == 0 ? acc[0] : w == 1 ? acc[1] : w == 2 ? acc[2] : acc[3];
        const unsigned a = L0 + L_X + w * 12288 + lane * 16;
#pragma unroll
        for (int j = 0; j < 3; ++j)
#pragma unroll
            for (int q = 0; q < 4; ++q) { const f32x4 v = ldsf4(a + j * 4096 + q * 1024); r[4 * q] += v[0]; r[4 * q + 1] += v[1]; r[4 * q + 2] += v[2]; r[4 * q + 3] += v[3]; }
        const unsigned oo = (tk + 32 * tt + r32) * 896 + 128 * g7 + 64 * nt7 + 16 * hh;
        char* op = (char*)P.out + oo;
        const __amdgpu_buffer_rsrc_t ro = __builtin_amdgcn_make_buffer_rsrc((void*)P.out, 0, S * D * 2, 0x00020000); (void)ro; (void)op;
#pragma unroll
        for (int s = 0; s < 2; ++s) {
            u32x4 ov;
#pragma unroll
            for (int i = 0; i < 4; ++i)
                ov[i] = pk(r[8 * s + 2 * i] + blo(rx[s][i]) + blo(rh[s][i]), r[8 * s + 2 * i + 1] + bhi(rx[s][i]) + bhi(rh[s][i]));
            *(GAS u32x4*)(op + 32 * s) = ov;
        }
        STAMP(14);
    }
    STAMP_FLUSH();
}
}

extern "C" void kernel_launch(void* const* d_in, const int* in_sizes, int n_in, void* d_out, int out_size, void* d_ws, size_t ws_size,
                              hipStream_t stream) {
    mk::Params P;
    P.x = (const u16*)d_in[0]; P.wqkv = (const u16*)d_in[1]; P.wo = (const u16*)d_in[2];
    P.w1 = (const u16*)d_in[4]; P.w2 = (const u16*)d_in[5]; P.w3 = (const u16*)d_in[6]; P.w4 = (const u16*)d_in[7];
    P.out = (u16*)d_out; P.ws = (char*)d_ws;
    mk::fused_fwd<<<256, 512, 0, stream>>>(P);
}
```

```cpp
#define TOUCH_PAGES 1
#define TOUCH_FLAGS 1
#define W4SPLIT 36
#define PRIO_C 2
#define PRIO_L 0
#define NT_W 0
#define NT_QKV 0
#define NT_X 0
#define OUT_MODE 0
#define P6_PX 10
#define P6_PW 3
#define P6_PG 6
#define P7_PX 5
#define HALF 12
#define POLL2 0
#define POLL_GAP 3
#define TOUCH_DELAY 12
#define EARLY_TOUCH 1
#define LATE_TOUCH 1
#include <hip/hip_runtime.h>
#include <hip/hip_bf16.h>
#include <stdint.h>
#include <type_traits>
#include <utility>

typedef unsigned short u16;
typedef __attribute__((ext_vector_type(8))) short bf16x8;
typedef __attribute__((ext_vector_type(4))) short s16x4;
typedef __attribute__((ext_vector_type(16))) float f32x16;
typedef __attribute__((ext_vector_type(4))) float f32x4;
typedef __attribute__((ext_vector_type(2))) float f32x2;
typedef __attribute__((ext_vector_type(4))) unsigned u32x4;
typedef __attribute__((ext_vector_type(2))) unsigned u32x2;
typedef __attribute__((ext_vector_type(2))) __bf16 bf2;
#define LAS __attribute__((address_space(3)))
#define GAS __attribute__((address_space(1)))
#define DEVI __device__ __forceinline__

#define MK_STAMP 0

namespace mk {
constexpr int S = 2048, D = 448;
constexpr unsigned XR = 2056, XROW = XR * 16;
constexpr unsigned KVH = 64 * 4096 + 128;
constexpr unsigned WS_EP = 0;
constexpr unsigned WS_FLAG = 4096;
constexpr unsigned WS_KV = 262144;
constexpr unsigned WS_O = WS_KV + 16 * KVH;
constexpr unsigned WS_H = WS_O + 64 * XROW;
constexpr unsigned WS_HH = WS_H + 56 * XROW;
constexpr unsigned WS_X1 = WS_HH + 56 * XROW;
constexpr unsigned WS_U = WS_X1 + 56 * XROW;
constexpr unsigned WS_STH = WS_U + 224 * XROW;
constexpr unsigned WS_STX = WS_STH + 262144;
constexpr unsigned WS_ST1 = WS_STX + 262144;
constexpr unsigned WS_SPARE = WS_ST1 + 32768;
constexpr unsigned WS_STAMP = WS_SPARE + 128;
constexpr unsigned WS_END = WS_STAMP + 16 * 512 * 8;
constexpr unsigned L_SLOT = 28672;
constexpr unsigned L_MISC = 143360;
constexpr unsigned L_KV = 114688;
constexpr unsigned L_OWNKV = L_KV + 8 * 4096;
constexpr unsigned L_WST = 28672;
constexpr unsigned L_X = 114688;
constexpr unsigned L_M2 = 147456;
constexpr unsigned CODE_TOUCH_BYTES = 36000;
constexpr float EPS = 1e-5f;
constexpr unsigned SPIN_CAP = 1u << 18;

struct Params {
    const u16* x; const u16* wqkv; const u16* wo; const u16* w1; const u16* w2; const u16* w3; const u16* w4;
    u16* out; char* ws;
};

DEVI unsigned f2u(float f) { return __builtin_bit_cast(unsigned, f); }
DEVI float u2f(unsigned u) { return __builtin_bit_cast(float, u); }
DEVI bool wall(bool p) { return __builtin_amdgcn_ballot_w64(!p) == 0; }
template <int X> DEVI float xor32(float v) { return u2f((unsigned)__builtin_amdgcn_ds_swizzle((int)f2u(v), (X << 10) | 0x1f)); }
DEVI unsigned pk(float lo, float hi) { bf2 v = {(__bf16)lo, (__bf16)hi}; return __builtin_bit_cast(unsigned, v); }
DEVI float blo(unsigned u) { return u2f(u << 16); }
DEVI float bhi(unsigned u) { return u2f(u & 0xffff0000u); }
DEVI float dot2(unsigned a, unsigned b, float c) { return __builtin_amdgcn_fdot2_f32_bf16(__builtin_bit_cast(bf2, a), __builtin_bit_cast(bf2, b), c, false); }
constexpr unsigned ONES = 0x3f803f80u;
DEVI float hsum(float v) { auto r = __builtin_amdgcn_permlane32_swap(f2u(v), f2u(v), false, false); return u2f(r[0]) + u2f(r[1]); }
DEVI float hmax(float v) { auto r = __builtin_amdgcn_permlane32_swap(f2u(v), f2u(v), false, false); return __builtin_fmaxf(u2f(r[0]), u2f(r[1])); }
DEVI f32x16 mfma(u32x4 a, u32x4 b, f32x16 c) { return __builtin_amdgcn_mfma_f32_32x32x16_bf16(__builtin_bit_cast(bf16x8, a), __builtin_bit_cast(bf16x8, b), c, 0, 0, 0); }
#define MFMA(acc, a, b, c) do { acc = mfma(a, b, c); asm volatile("" : "+v"(acc)); } while (0)
DEVI u32x4 lds128(unsigned a) { return *(const LAS u32x4*)(uintptr_t)a; }
DEVI f32x4 ldsf4(unsigned a) { return *(const LAS f32x4*)(uintptr_t)a; }
DEVI void sts128(unsigned a, u32x4 v) { *(LAS u32x4*)(uintptr_t)a = v; }
DEVI void stsf4(unsigned a, f32x4 v) { *(LAS f32x4*)(uintptr_t)a = v; }
DEVI u32x2 ldstr(unsigned a) { return __builtin_bit_cast(u32x2, __builtin_amdgcn_ds_read_tr16_b64_v4i16((LAS s16x4*)(uintptr_t)a)); }
DEVI u32x4 wfrag(unsigned a0, unsigned a1) { u32x2 p = ldstr(a0), q = ldstr(a1); return (u32x4){p.x, p.y, q.x, q.y}; }
DEVI u32x4 pack8(const f32x16& a, int s) { return (u32x4){pk(a[8 * s], a[8 * s + 1]), pk(a[8 * s + 2], a[8 * s + 3]), pk(a[8 * s + 4], a[8 * s + 5]), pk(a[8 * s + 6], a[8 * s + 7])}; }
constexpr int SC1 = 16;
DEVI u32x4 gld(__amdgpu_buffer_rsrc_t rs, unsigned voff, unsigned soff) { return __builtin_amdgcn_raw_buffer_load_b128(rs, voff, soff, SC1); }
DEVI u32x2 gld8(__amdgpu_buffer_rsrc_t rs, unsigned voff, unsigned soff) { return __builtin_amdgcn_raw_buffer_load_b64(rs, voff, soff, SC1); }
DEVI void gst(__amdgpu_buffer_rsrc_t rs, u32x4 v, unsigned voff, unsigned soff) { __builtin_amdgcn_raw_buffer_store_b128(v, rs, voff + soff, 0, SC1); }
DEVI void gst8(__amdgpu_buffer_rsrc_t rs, u32x2 v, unsigned voff, unsigned soff) { __builtin_amdgcn_raw_buffer_store_b64(v, rs, voff, soff, SC1); }
#define WAIT_VM(N) asm volatile("s_waitcnt vmcnt(" #N ")" ::: "memory")
#define WAIT_LGKM() asm volatile("s_waitcnt lgkmcnt(0)" ::: "memory")
#define WAIT_BAR(N) asm volatile("s_waitcnt vmcnt(" #N ") lgkmcnt(0)\n\ts_barrier" ::: "memory")
#define LBAR() asm volatile("s_waitcnt lgkmcnt(0)\n\ts_barrier" ::: "memory")
#define SB() __builtin_amdgcn_sched_barrier(0)
#define PIN(x) asm volatile("" : "+v"(x))
DEVI float fma1(float a, float b, float c) { return __builtin_fmaf(a, b, c); }
DEVI float mul1(float a, float b) { return a * b; }
DEVI float max3(float a, float b, float c) { return __builtin_fmaxf(__builtin_fmaxf(a, b), c); }
template <int... Is, class F> DEVI void sfor_impl(std::integer_sequence<int, Is...>, F&& f) { (f(std::integral_constant<int, Is>{}), ...); }
template <int N, class F> DEVI void sfor(F&& f) { sfor_impl(std::make_integer_sequence<int, N>{}, f); }
#define AI __attribute__((always_inline))
#define IC(n) std::integral_constant<int, n>{}

template <int NT = 0> DEVI void dma1(const char* sbase, unsigned voff, unsigned lds) {
    if (NT) asm volatile("s_mov_b32 m0, %2\n\ts_nop 0\n\tglobal_load_lds_dwordx4 %0, %1 nt" :: "v"(voff), "s"(sbase), "s"(lds) : "memory");
    else asm volatile("s_mov_b32 m0, %2\n\ts_nop 0\n\tglobal_load_lds_dwordx4 %0, %1" :: "v"(voff), "s"(sbase), "s"(lds) : "memory");
}
DEVI void touch(const char* sbase, unsigned voff, unsigned lds) {
    asm volatile("s_mov_b32 m0, %2\n\ts_nop 0\n\tglobal_load_lds_dword %0, %1" :: "v"(voff), "s"(sbase), "s"(lds) : "memory");
}
struct WSrc { const char* base; unsigned step, voff; };
DEVI void wpiece(const WSrc& s, unsigned lds, int n) { dma1<NT_W>(s.base + (size_t)n * s.step, s.voff, lds + n * 1024); }
DEVI WSrc wsrc_a(const char* base, unsigned rs, int nt, int lane) { const int t = (lane >> 4) < nt ? (lane >> 4) : nt - 1; return WSrc{base, 4 * rs, ((lane >> 2) & 3) * rs + t * 64 + (lane & 3) * 16}; }
DEVI WSrc wsrc_b(const char* base, unsigned rs, int lane) { return WSrc{base, 8 * rs, (4 * (lane >> 5) + ((lane >> 2) & 3)) * rs + ((lane >> 4) & 1) * 64 + (lane & 3) * 16}; }

DEVI void poll_word(const GAS unsigned* p, unsigned e) {
    unsigned a = __hip_atomic_load(p, __ATOMIC_RELAXED, __HIP_MEMORY_SCOPE_AGENT);
#pragma nounroll
    for (unsigned sp = 0; sp < SPIN_CAP; ++sp) {
        __builtin_amdgcn_s_sleep(POLL_GAP);
        const unsigned b2 = __hip_atomic_load(p, __ATOMIC_RELAXED, __HIP_MEMORY_SCOPE_AGENT);
        if (wall(a == e)) break;
        __builtin_amdgcn_s_sleep(POLL_GAP);
        a = __hip_atomic_load(p, __ATOMIC_RELAXED, __HIP_MEMORY_SCOPE_AGENT);
        if (wall(b2 == e)) break;
    }
    __builtin_amdgcn_fence(__ATOMIC_ACQUIRE, "wavefront");
}
DEVI void wait_flags(const char* ws, unsigned foff, int n, unsigned e, int lane) {
    const GAS unsigned* p = (const GAS unsigned*)(ws + foff) + (lane < n ? lane : 0) * 32;
    if (POLL2) poll_word(p, e);
    else {
#pragma nounroll
        for (unsigned sp = 0; sp < SPIN_CAP; ++sp) {
            unsigned v = __hip_atomic_load(p, __ATOMIC_RELAXED, __HIP_MEMORY_SCOPE_AGENT);
            if (wall(v == e)) break;
        }
        __builtin_amdgcn_fence(__ATOMIC_ACQUIRE, "wavefront");
    }
}
DEVI void set_flag(char* ws, unsigned foff, unsigned e) { __hip_atomic_store((GAS unsigned*)(ws + foff), e, __ATOMIC_RELAXED, __HIP_MEMORY_SCOPE_AGENT); }
DEVI unsigned flag_off(int ph, int idx) { return WS_FLAG + (ph * 256 + idx) * 128; }

constexpr float GC0 = -2.f * 0.7978845608028654f * 1.4426950408889634f, GC1 = GC0 * 0.044715f;
DEVI float gelu_t(float x) {
    float z = x * __builtin_fmaf(x * x, GC1, GC0);
    return x * __builtin_amdgcn_rcpf(1.f + __builtin_amdgcn_exp2f(z));
}
struct GeluK { float nmean, k1, k0, irs; };
DEVI void gelu_step(int o, float& y, float& z, float c, const GeluK& g) {
    if (o == 0) y = fma1(g.nmean, c, y);
    else if (o == 1) z = mul1(y, y);
    else if (o == 2) z = fma1(z, g.k1, g.k0);
    else if (o == 3) z = mul1(y, z);
    else if (o == 4) z = __builtin_amdgcn_exp2f(z);
    else if (o == 5) z = fma1(z, g.irs, g.irs);
    else if (o == 6) z = __builtin_amdgcn_rcpf(z);
    else y = mul1(y, z);
}

#define STAMP(k) do {} while (0)
#define STAMP_DECL
#define STAMP_FLUSH() do {} while (0)

#define LANE_GEOM() int lane_ = threadIdx.x & 63; asm volatile("" : "+v"(lane_)); const int lane = lane_, r32 = lane & 31, hh = lane >> 5; \
    const int trq = (lane & 15) >> 2, trQ_ = ((lane >> 4) & 1) * 4 + (lane & 3), gq = (trQ_ & 4) | ((trQ_ & 1) << 1) | ((trQ_ >> 1) & 1); \
    const unsigned vx = (hh * XR + r32) * 16; (void)trq; (void)gq; (void)vx; (void)r32; (void)hh
constexpr unsigned L_FLG = L_MISC + 1920, F_RDY = 0, F_DONE = 16;
DEVI unsigned ldsw_ld(unsigned i) { return *(volatile LAS unsigned*)(uintptr_t)(L_FLG + 4 * i); }
DEVI u32x2 ldsw_ld2(unsigned i) { return *(volatile LAS u32x2*)(uintptr_t)(L_FLG + 4 * i); }
DEVI void ldsw_st(unsigned i, unsigned v) { *(volatile LAS unsigned*)(uintptr_t)(L_FLG + 4 * i) = v; }
DEVI void ring_wait(int c) {
#pragma nounroll
    for (unsigned sp = 0; sp < SPIN_CAP * 16u; ++sp) { const u32x2 v = ldsw_ld2(F_RDY + 2 * c); if (__builtin_amdgcn_readfirstlane(v.x & v.y)) break; __builtin_amdgcn_s_sleep(1); }
}
DEVI void slot_wait(unsigned n, int lane) {
#pragma nounroll
    for (unsigned sp = 0; sp < SPIN_CAP * 16u; ++sp) { const unsigned v = ldsw_ld(F_DONE + (lane < 6 ? lane : 0)); if (wall(v >= n)) break; __builtin_amdgcn_s_sleep(1); }
}
#define LB(N) asm volatile("s_waitcnt vmcnt(" #N ")\n\ts_barrier" ::: "memory")
#define BAR() asm volatile("s_barrier" ::: "memory")

__global__ __launch_bounds__(512, 2) void fused_fwd(Params P) {
    __shared__ __attribute__((aligned(16))) char smem[163840];
    const unsigned L0 = (unsigned)(uintptr_t)(LAS char*)smem;
    const int tid = threadIdx.x, wv = __builtin_amdgcn_readfirstlane(tid >> 6), w = wv & 3;
    const int b = blockIdx.x, xcd = b & 7, slot = b >> 3;
    const int m = 2 * xcd + (slot >> 4), h = slot & 15;
    const int g = slot & 3, nt = g < 2 ? 4 : 3, tb = g < 2 ? 4 * g : 3 * g + 2;
    const int ti = 8 * xcd + (slot >> 2);
    const int s16 = slot & 15, pr = s16 >> 1;
    const int nt6 = ((s16 ^ pr) & 1) ? 3 : 4, tb6 = 7 * pr + ((s16 & 1) ? (pr & 1 ? 3 : 4) : 0);
    const bool act7 = slot < 28;
    const int j7 = 4 * xcd + (act7 ? slot / 7 : 0), g7 = act7 ? slot % 7 : 0;
    const unsigned wst = L0 + w * L_WST;
    const unsigned shift = (56 * h) & 15u;
    STAMP_DECL

    if (wv >= 4) {
        if (PRIO_L) __builtin_amdgcn_s_setprio(PRIO_L);
        LANE_GEOM();
        const unsigned wrd = 16384 + hh * 6 * 256 + trq * 64 + ((shift + gq * 8) & 63);
        LAS float* csp = (LAS float*)(uintptr_t)(L0 + L_MISC);
        if (w < 2) {
            const unsigned xoffE = (lane >> 3) * 896 + (((lane & 7) ^ (lane >> 4)) << 4);
            const unsigned xoffO = (lane >> 3) * 896 + (((lane & 7) ^ (4 + (lane >> 4))) << 4);
            const unsigned colb = (56 * h) & ~15u;
            unsigned woff[3];
#pragma unroll
            for (int j = 0; j < 3; ++j) {
                const int bl = 4 * j + (lane >> 4), kql = bl / 3, tile = bl % 3;
                woff[j] = (4 * kql + ((lane >> 2) & 3)) * 2688 + tile * 896 + colb + (lane & 3) * 16;
            }
            const char* xb = (const char*)P.x + (size_t)(128 * m + 64 * w) * 896;
            const char* wb = (const char*)P.wqkv + (size_t)(32 * w) * 2688;
            LBAR();
#pragma unroll 1
            for (int kc = 0; kc < 7; ++kc) {
                if (kc >= 5) slot_wait(kc - 4, lane);
                const unsigned base = L0 + (kc >= 5 ? kc - 5 : kc) * L_SLOT;
#pragma unroll
                for (int i = 0; i < 6; ++i) dma1<NT_QKV>(wb + (size_t)kc * 64 * 2688 + (i / 3) * 16 * 2688, woff[i % 3], base + 16384 + (6 * w + i) * 1024);
#pragma unroll
                for (int i = 0; i < 8; ++i) dma1<NT_X>(xb + kc * 128 + i * 8 * 896, (i & 1) ? xoffO : xoffE, base + (8 * w + i) * 1024);
                if (kc > 0) { WAIT_VM(14); ldsw_st(F_RDY + 2 * (kc - 1) + w, 1); }
            }
            WAIT_VM(0); ldsw_st(F_RDY + 12 + w, 1);
            {
                ring_wait(6);
                const unsigned a = L0 + L_SLOT + wrd + w * 3072;
#pragma unroll
                for (int t = 0; t < 3; ++t) { const u32x4 f = wfrag(a + t * 256, a + 768 + t * 256);
                    const float v = hsum((dot2(f[0], ONES, 0.f) + dot2(f[1], ONES, 0.f)) + (dot2(f[2], ONES, 0.f) + dot2(f[3], ONES, 0.f)));
                    if (hh == 0) csp[(2 + w) * 96 + t * 32 + r32] = v; }
            }
        } else {
            const int srow = 128 * m + 8 * h + 4 * (w - 2) + (lane >> 4);
            const char* sp = (const char*)P.x + (size_t)srow * 896 + (lane & 15) * 16;
            u32x4 sr[4];
            asm volatile("global_load_dwordx4 %0, %4, off\n\tglobal_load_dwordx4 %1, %4, off offset:256\n\tglobal_load_dwordx4 %2, %4, off offset:512\n\tglobal_load_dwordx4 %3, %5, off"
                         : "=&v"(sr[0]), "=&v"(sr[1]), "=&v"(sr[2]), "=&v"(sr[3]) : "v"(sp), "v"(sp + ((lane & 15) < 8 ? 768 : 0)) : "memory");
            if (w == 2 && lane < 32) ldsw_st(lane, 0);
            LBAR();
            {
                const unsigned lx = lane < 56 ? lane : 0, lw = b * 37 + (lane < 37 ? lane : 0);
                const unsigned fl = lane - 56;
                const int fidx = fl < 2 || fl == 5 ? m * 16 + h : ti * 4 + g;
                const char* tf = fl < 6 ? P.ws + flag_off(fl, fidx) : P.ws + WS_ST1 + (128 * m + 8 * h) * 16;
                const char* tp = !EARLY_TOUCH ? P.ws + WS_SPARE : w == 2 ? ((TOUCH_FLAGS && lane >= 56 && lane < 63) ? tf : (const char*)P.x + (size_t)(8 * b + lx / 7) * 896 + (lx % 7) * 128)
                                                                       : (const char*)P.wqkv + (size_t)(lw < 9408 ? lw : 0) * 128;
                if (TOUCH_DELAY) __builtin_amdgcn_s_sleep(TOUCH_DELAY);
                asm volatile("s_mov_b32 m0, %1\n\ts_nop 0\n\tglobal_load_lds_dword %0, off" :: "v"(tp), "s"(L0 + L_MISC + 2048) : "memory");
            }
            {
                asm volatile("s_waitcnt vmcnt(1)" : "+v"(sr[0]), "+v"(sr[1]), "+v"(sr[2]), "+v"(sr[3]) :: "memory");
                if ((lane & 15) >= 8) sr[3] = (u32x4){0u, 0u, 0u, 0u};
                float t1 = 0.f, t2 = 0.f;
#pragma unroll
                for (int q = 0; q < 4; ++q)
#pragma unroll
                    for (int i = 0; i < 4; ++i) { t1 = dot2(sr[q][i], ONES, t1); t2 = dot2(sr[q][i], sr[q][i], t2); }
                t1 += xor32<1>(t1); t2 += xor32<1>(t2); t1 += xor32<2>(t1); t2 += xor32<2>(t2); t1 += xor32<4>(t1); t2 += xor32<4>(t2); t1 += xor32<8>(t1); t2 += xor32<8>(t2);
                const float mean = t1 * (1.f / D), var = __builtin_fmaxf(t2 * (1.f / D) - mean * mean, 0.f);
                const unsigned e = *(const unsigned*)(P.ws + WS_EP) + 1u;
                const __amdgpu_buffer_rsrc_t rs = __builtin_amdgcn_make_buffer_rsrc((void*)P.ws, 0, WS_END, 0x00020000);
                if ((lane & 15) == 0) gst(rs, (u32x4){f2u(mean), e, f2u(__builtin_amdgcn_rsqf(var + EPS)), e}, srow * 16, WS_ST1);
            }
            float cs[3][4] = {};
            const unsigned wrd2 = wrd + (w - 2) * 2 * 3072;
#pragma unroll 1
            for (int c = 0; c < 7; ++c) {
                ring_wait(c);
                if (c == LATE_TOUCH) {
                    unsigned long long pc; unsigned back;
                    asm volatile("s_getpc_b64 %0\nmk_code_here:\n\ts_mov_b32 %1, mk_code_here-_ZN2mk9fused_fwdENS_6ParamsE" : "=s"(pc), "=s"(back));
                    constexpr unsigned len = CODE_TOUCH_BYTES;
                    const unsigned oc = (slot * 18 + (lane < 18 ? lane : 0)) * 128, o3 = (b * 64 + lane) * 128, o2 = (b * 16 + (lane & 15)) * 128;
                    const char* t0 = w == 2 ? (const char*)(pc - back) + (oc < len ? oc : 0) : (const char*)P.w3 + (o3 < 1605632 ? o3 : 0);
                    const char* t1 = w == 2 ? (lane < 16 ? (const char*)P.wo : lane < 32 ? (const char*)P.w1 : (const char*)P.w2) + (o2 < 401408 ? o2 : 0) : (const char*)P.w4 + (o3 < 1605632 ? o3 : 0);
                    const unsigned pg = slot * 34 + (lane < 34 ? lane : 0);
                    const char* t2 = !TOUCH_PAGES || w == 2 ? t1 : pg < 98 ? (const char*)P.wo + pg * 4096 : pg < 196 ? (const char*)P.w1 + (pg - 98) * 4096 : pg < 294 ? (const char*)P.w2 + (pg - 196) * 4096
                                   : pg < 686 ? (const char*)P.w3 + (pg - 294) * 4096 : pg < 1078 ? (const char*)P.w4 + (pg - 686) * 4096 : t1;
                    asm volatile("s_mov_b32 m0, %3\n\ts_nop 0\n\tglobal_load_lds_dword %0, off\n\tglobal_load_lds_dword %1, off\n\tglobal_load_lds_dword %2, off" :: "v"(t0), "v"(t1), "v"(t2), "s"(L0 + L_MISC + 2048) : "memory");
                }
                const unsigned a = L0 + (c >= 5 ? c - 5 : c) * L_SLOT + (c < 6 ? wrd2 : wrd + w * 3072);
#pragma unroll
                for (int t = 0; t < 3; ++t) { const u32x4 f = wfrag(a + t * 256, a + 768 + t * 256);
#pragma unroll
                    for (int i = 0; i < 4; ++i) cs[t][i] = dot2(f[i], ONES, cs[t][i]); }
                if (c < 6) {
#pragma unroll
                    for (int t = 0; t < 3; ++t) { const u32x4 f = wfrag(a + 3072 + t * 256, a + 3072 + 768 + t * 256);
#pragma unroll
                        for (int i = 0; i < 4; ++i) cs[t][i] = dot2(f[i], ONES, cs[t][i]); }
                }
                WAIT_LGKM();
                ldsw_st(F_DONE + 2 + w, c + 1);
            }
#pragma unroll
            for (int t = 0; t < 3; ++t) { const float v = hsum((cs[t][0] + cs[t][1]) + (cs[t][2] + cs[t][3])); if (hh == 0) csp[(w - 2) * 96 + t * 32 + r32] = v; }
        }
        LBAR();
        auto stage = [&](const WSrc& s, unsigned lds, int n0, int n1) AI {
#pragma unroll 1
            for (int n = n0; n < n1; ++n) wpiece(s, lds, n);
        };
        LB(0);
        BAR();
        stage(wsrc_a((const char*)P.wo + (size_t)(112 * w) * 896 + tb * 64, 896, nt, lane), wst, 0, 28);
        LB(0);
        { const WSrc s_ = wsrc_a((const char*)P.w1 + (size_t)(112 * w) * 896 + tb * 64, 896, nt, lane); BAR(); stage(s_, wst, 0, HALF); BAR(); stage(s_, wst, HALF, 28); BAR(); LB(0); }
        { const WSrc s_ = wsrc_a((const char*)P.w2 + (size_t)(112 * w) * 896 + tb * 64, 896, nt, lane); BAR(); stage(s_, wst, 0, HALF); BAR(); stage(s_, wst, HALF, 28); BAR(); LB(0); }
        { const WSrc s_ = wsrc_a((const char*)P.w3 + (size_t)(112 * w) * 3584 + tb6 * 64, 3584, nt6, lane); BAR(); stage(s_, wst, 0, HALF); BAR(); stage(s_, wst, HALF, 28); BAR(); LB(0); }
        {
            float c[4] = {0.f, 0.f, 0.f, 0.f};
            const unsigned a0 = L0 + trq * 64 + gq * 8 + 2 * hh * 1024 + w * 256;
#pragma unroll 4
            for (int k = 0; k < 28; ++k) { const unsigned a = a0 + k * 4096; const u32x4 f = wfrag(a, a + 1024);
#pragma unroll
                for (int i = 0; i < 4; ++i) c[i] = dot2(f[i], ONES, c[i]); }
            const float cc = hsum((c[0] + c[1]) + (c[2] + c[3]));
            if (hh == 0) ((LAS float*)(uintptr_t)(L0 + L_M2 + 1024))[w * 32 + r32] = cc;
        }
        if (b == 0 && w == 3) {
            const unsigned e = *(const volatile GAS unsigned*)(P.ws + WS_EP) + 1u;
            const GAS unsigned* p = (const GAS unsigned*)(P.ws + flag_off(0, lane));
#pragma nounroll
            for (unsigned sp = 0; sp < SPIN_CAP; ++sp) {
                const unsigned v0 = __hip_atomic_load(p, __ATOMIC_RELAXED, __HIP_MEMORY_SCOPE_AGENT), v1 = __hip_atomic_load(p + 64 * 32, __ATOMIC_RELAXED, __HIP_MEMORY_SCOPE_AGENT);
                const unsigned v2 = __hip_atomic_load(p + 128 * 32, __ATOMIC_RELAXED, __HIP_MEMORY_SCOPE_AGENT), v3 = __hip_atomic_load(p + 192 * 32, __ATOMIC_RELAXED, __HIP_MEMORY_SCOPE_AGENT);
                if (wall(v0 == e && v1 == e && v2 == e && v3 == e)) break;
            }
            if (lane == 0) __hip_atomic_store((GAS unsigned*)(P.ws + WS_EP), e, __ATOMIC_RELAXED, __HIP_MEMORY_SCOPE_AGENT);
        }
        LBAR();
        const WSrc w4s = wsrc_b((const char*)P.w4 + (size_t)(448 * w) * 896 + g7 * 128, 896, lane);
        if (act7) { stage(w4s, L0 + L_X + w * 8192, 0, 8); stage(w4s, wst - 8 * 1024, 8, W4SPLIT); }
        BAR();
        if (act7) stage(w4s, wst - 8 * 1024, W4SPLIT, 36);
        LB(0);
        if (act7) {
            BAR();
            stage(w4s, wst - 36 * 1024, 36, 56);
            LB(0);
            BAR();
        }
        STAMP_FLUSH();
        return;
    }

    if (PRIO_C) __builtin_amdgcn_s_setprio(PRIO_C);
    const unsigned e = *(const unsigned*)(P.ws + WS_EP) + 1u;
    const __amdgpu_buffer_rsrc_t rs = __builtin_amdgcn_make_buffer_rsrc((void*)P.ws, 0, WS_END, 0x00020000);
    u32x4 qf0, qf1;
    {
        LANE_GEOM();
        const unsigned wrd = 16384 + hh * 6 * 256 + trq * 64 + ((shift + gq * 8) & 63);
        STAMP(15);
        f32x16 aq = {}, ak = {}, av = {};
        const unsigned xrd = (32 * w + r32) * 128 + ((hh ^ ((r32 >> 1) & 7)) << 4);
        struct Fr { u32x4 x, q, k, v; };
        Fr F[4];
        unsigned xb[3][4], wb3[3];
#pragma unroll
        for (int gsl = 0; gsl < 3; ++gsl) {
#pragma unroll
            for (int j = 0; j < 4; ++j) { xb[gsl][j] = L0 + (xrd ^ (j << 5)) + gsl * 2 * L_SLOT; PIN(xb[gsl][j]); }
            wb3[gsl] = L0 + wrd + gsl * 2 * L_SLOT; PIN(wb3[gsl]);
        }
        auto ldx = [&](int sl, int j) AI { F[j].x = lds128(xb[sl / 2][j] + (sl & 1) * L_SLOT); const unsigned a = wb3[sl / 2] + (sl & 1) * L_SLOT + j * 3072; F[j].q = wfrag(a, a + 768); };
        auto ldk = [&](int sl, int j) AI { const unsigned a = wb3[sl / 2] + (sl & 1) * L_SLOT + j * 3072; F[j].k = wfrag(a + 256, a + 1024); };
        auto ldv = [&](int sl, int j) AI { const unsigned a = wb3[sl / 2] + (sl & 1) * L_SLOT + j * 3072; F[j].v = wfrag(a + 512, a + 1280); };
        LAS float* stw = (LAS float*)(uintptr_t)(L0 + L_MISC + 3072 + w * 256);
        BAR();
        ring_wait(0);
        STAMP(16);
#pragma unroll
        for (int j = 0; j < 2; ++j) { ldx(0, j); ldk(0, j); ldv(0, j); }
        SB();
        u32x4 gv = {0u, 0u, 0u, 0u};
        const unsigned gvo = (128 * m + 32 * w + r32) * 16;
        sfor<7>([&](auto cc) AI {
            constexpr int c = decltype(cc)::value, sl = c >= 5 ? c - 5 : c, nsl = c >= 4 ? c - 4 : c + 1;
            u32x2 rdy;
            sfor<4>([&](auto jc) AI {
                constexpr int j = decltype(jc)::value, jn = (j + 2) & 3, lb = j < 2 ? sl : nsl;
                constexpr bool ld = j < 2 || c < 6;
                if constexpr (j == 2 && c < 6) { if (__builtin_amdgcn_readfirstlane(rdy.x & rdy.y) == 0) ring_wait(c + 1); SB(); }
                MFMA(aq, F[j].q, F[j].x, aq);
                if constexpr (ld) ldx(lb, jn);
                SB();
                MFMA(ak, F[j].k, F[j].x, ak);
                if constexpr (ld) ldk(lb, jn);
                if constexpr (j == 0 && c >= 1 && c <= 2) ldsw_st(F_DONE + w, c);
                if constexpr (j == 0 && c == 4) gv = gld(rs, gvo, WS_ST1);
                SB();
                MFMA(av, F[j].x, F[j].v, av);
                if constexpr (ld) ldv(lb, jn);
                if constexpr (j == 0 && c < 6) rdy = ldsw_ld2(F_RDY + 2 * (c + 1));
                SB();
            });
        });
#pragma nounroll
        for (unsigned sp2 = 0; sp2 < SPIN_CAP && !wall(gv.y == e && gv.w == e); ++sp2) { asm volatile("" ::: "memory"); gv = gld(rs, gvo, WS_ST1); }
        if (hh == 0) { stw[r32] = u2f(gv.x) * u2f(gv.z); stw[32 + r32] = u2f(gv.z); }
        STAMP(1);
        const float mean = u2f(gv.x), rstd = u2f(gv.z);
        LBAR();
        STAMP(17);
        const LAS float* csf = (const LAS float*)(uintptr_t)(L0 + L_MISC);
        const float cv = (csf[64 + r32] + csf[96 + 64 + r32]) + (csf[192 + 64 + r32] + csf[288 + 64 + r32]);
        const float qs = rstd * (0.18898223650461362f * 1.4426950408889634f);
#pragma unroll
        for (int q = 0; q < 4; ++q) {
            auto c4 = [&](int o) AI { return (*(const LAS f32x4*)(csf + o) + *(const LAS f32x4*)(csf + 96 + o)) + (*(const LAS f32x4*)(csf + 192 + o) + *(const LAS f32x4*)(csf + 288 + o)); };
            const f32x4 c1 = c4(8 * q + 4 * hh), c2 = c4(32 + 8 * q + 4 * hh);
            const f32x4 mr = *(const LAS f32x4*)(stw + 8 * q + 4 * hh), rr = *(const LAS f32x4*)(stw + 32 + 8 * q + 4 * hh);
#pragma unroll
            for (int i = 0; i < 4; ++i) {
                aq[4 * q + i] = (aq[4 * q + i] - mean * c1[i]) * qs;
                ak[4 * q + i] = (ak[4 * q + i] - mean * c2[i]) * rstd;
                av[4 * q + i] = av[4 * q + i] * rr[i] - mr[i] * cv;
            }
        }
        if (hh == 1) { aq[12] = 1.f; aq[13] = 0.f; aq[14] = 0.f; aq[15] = 0.f; ak[12] = 0.f; ak[13] = 0.f; ak[14] = 0.f; ak[15] = 0.f; }
        if (r32 >= 28) {
            const float pv = r32 == 28 ? 1.f : 0.f;
#pragma unroll
            for (int i = 0; i < 16; ++i) av[i] = pv;
        }
        const u32x4 k0 = pack8(ak, 0), k1 = pack8(ak, 1), v0 = pack8(av, 0), v1 = pack8(av, 1);
        const unsigned go = WS_KV + h * KVH + (4 * m + w) * 4096;
        gst(rs, k0, lane * 16, go); gst(rs, k1, lane * 16, go + 1024); gst(rs, v0, lane * 16, go + 2048); gst(rs, v1, lane * 16, go + 3072);
        const unsigned lo = L0 + L_OWNKV + w * 4096 + lane * 16;
        sts128(lo, k0); sts128(lo + 1024, k1); sts128(lo + 2048, v0); sts128(lo + 3072, v1);
        qf0 = pack8(aq, 0); qf1 = pack8(aq, 1);
        STAMP(18);
        WAIT_BAR(0);
        if (tid == 0) set_flag(P.ws, flag_off(0, m * 16 + h), e);
        STAMP(2);
    }
    {
        LANE_GEOM();
        const int mt = m - 2 + (w >> 1);
        u32x4 kv[8];
        if (mt >= 0) {
            wait_flags(P.ws, flag_off(0, mt * 16 + h), 1, e, lane);
            const unsigned go = WS_KV + h * KVH + (4 * m - 8 + 2 * w) * 4096;
#pragma unroll
            for (int i = 0; i < 8; ++i) kv[i] = gld(rs, lane * 16, go + i * 1024);
        } else {
#pragma unroll
            for (int i = 0; i < 8; ++i) kv[i] = (u32x4){0u, 0u, (i & 3) == 1 && hh == 1 ? 0xF149u : 0u, 0u};
        }
        f32x16 cdiag, cold;
#pragma unroll
        for (int i = 0; i < 16; ++i) { const int kr = (i & 3) + 8 * (i >> 2) + 4 * hh; cdiag[i] = kr <= r32 ? 0.f : -1e30f; cold[i] = kr > r32 ? 0.f : -1e30f; }
#pragma unroll
        for (int i = 0; i < 8; ++i) sts128(L0 + L_KV + 2 * w * 4096 + i * 1024 + lane * 16, kv[i]);
        LBAR();
        STAMP(3);
        unsigned kvb = L0 + L_KV + w * 4096 + lane * 16;
        PIN(kvb);
        u32x4 kf[9][2], vf[9][2];
        const f32x16 zero16 = {};
        auto ldK = [&](int i) AI { kf[i][0] = lds128(kvb + i * 4096); kf[i][1] = lds128(kvb + i * 4096 + 1024); };
        auto ldV = [&](int i) AI { vf[i][0] = lds128(kvb + i * 4096 + 2048); vf[i][1] = lds128(kvb + i * 4096 + 3072); };
        auto smax = [&](const f32x16& s, float a) AI {
#pragma unroll
            for (int i = 0; i < 16; i += 2) a = max3(a, s[i], s[i + 1]);
            return a; };
        ldK(0); ldK(1);
        float mx = -3e38f;
        f32x16 s[2];
        auto smax8 = [&](const f32x16& v, int o, float a) AI {
#pragma unroll
            for (int i = 0; i < 8; i += 2) a = max3(a, v[o + i], v[o + i + 1]);
            return a; };
        sfor<9>([&](auto ic) AI {
            constexpr int i = decltype(ic)::value;
            if constexpr (i + 2 < 9) ldK(i + 2);
            MFMA(s[i & 1], kf[i][0], qf0, i == 0 ? cold : i == 8 ? cdiag : zero16);
            SB();
            MFMA(s[i & 1], kf[i][1], qf1, s[i & 1]);
            SB();
            if constexpr (i > 0) { mx = smax8(s[(i - 1) & 1], 0, mx); mx = smax8(s[(i - 1) & 1], 8, mx); PIN(mx); }
            SB();
        });
        ldK(0); ldK(1); ldV(0);
        mx = hmax(smax(s[0], mx));
        STAMP(25);
        f32x16 o = {}, cm;
#pragma unroll
        for (int i = 0; i < 16; ++i) { cm[i] = -mx; cdiag[i] -= mx; cold[i] -= mx; }
        MFMA(s[0], kf[0][0], qf0, cold); MFMA(s[0], kf[0][1], qf1, s[0]);
        SB();
        sfor<9>([&](auto ic) AI {
            constexpr int i = decltype(ic)::value;
            f32x16& c = s[i & 1];
            f32x16& n = s[(i + 1) & 1];
            if constexpr (i + 2 < 9) ldK(i + 2);
            if constexpr (i + 1 < 9) ldV(i + 1);
            if constexpr (i + 1 < 9) MFMA(n, kf[i + 1][0], qf0, i + 1 == 8 ? cdiag : cm);
#pragma unroll
            for (int j = 0; j < 8; ++j) c[j] = __builtin_amdgcn_exp2f(c[j]);
            u32x4 p0 = pack8(c, 0);
            PIN(p0);
            SB();
            if constexpr (i + 1 < 9) MFMA(n, kf[i + 1][1], qf1, n);
            MFMA(o, vf[i][0], p0, o);
#pragma unroll
            for (int j = 8; j < 16; ++j) c[j] = __builtin_amdgcn_exp2f(c[j]);
            const u32x4 p1 = pack8(c, 1);
            MFMA(o, vf[i][1], p1, o);
            SB();
        });
        STAMP(26);
        auto lr = __builtin_amdgcn_permlane32_swap(f2u(o[12]), f2u(o[12]), false, false);
        const float inv = __builtin_amdgcn_rcpf(u2f(lr[1]));
#pragma unroll
        for (int i = 0; i < 16; ++i) o[i] *= inv;
        if (hh == 1) { o[12] = 0.f; o[13] = 0.f; o[14] = 0.f; o[15] = 0.f; }
        const unsigned vo = (hh * XR + r32) * 16, so = WS_O + ((4 * h) * XR + 128 * m + 32 * w) * 16;
        gst(rs, pack8(o, 0), vo, so); gst(rs, pack8(o, 1), vo, so + 2 * XROW);
        WAIT_BAR(0);
        if (tid == 0) set_flag(P.ws, flag_off(1, m * 16 + h), e);
        STAMP(4);
    }
    const bool act = w < nt;
    const int tw = tb + w;
#define GEMM_GEOM() LANE_GEOM(); \
    unsigned trW[2]; trW[0] = L0 + (w < nt ? w : nt - 1) * 256 + trq * 64 + gq * 8 + 2 * hh * 1024; trW[1] = trW[0] + 57344; PIN(trW[0]); PIN(trW[1]); \
    auto wimg = [&](int kq0, int kq1) AI { return wfrag(kq0 < 56 ? trW[0] + kq0 * 1024 : trW[1] + (kq0 - 56) * 1024, kq1 < 56 ? trW[0] + kq1 * 1024 : trW[1] + (kq1 - 56) * 1024); }; \
    unsigned xl = L0 + L_X + lane * 16; PIN(xl); \
    LAS float* m2 = (LAS float*)(uintptr_t)(L0 + L_M2 + w * 256); (void)m2
    auto gemm = [&](auto nk, const u32x4* wr, unsigned xl) AI -> f32x16 {
        constexpr int NK = decltype(nk)::value, PD = 4;
        f32x16 acc = {};
        u32x4 xf[NK];
#pragma unroll
        for (int k = 0; k < PD; ++k) xf[k] = lds128(xl + k * 1024);
        SB();
        sfor<NK>([&](auto kc) AI {
            constexpr int k = decltype(kc)::value;
            if constexpr (k + PD < NK) xf[k + PD] = lds128(xl + (k + PD) * 1024);
            MFMA(acc, wr[k], xf[k], acc);
            SB();
        });
        return acc;
    };
    u32x4 hp[2];
    float meanH, rstdH;
    {
        GEMM_GEOM();
        u32x4 xr[2];
        const char* xp = (const char*)P.x + (size_t)(32 * ti + r32) * 896 + 64 * (act ? tw : tb) + 16 * hh;
        xr[0] = *(const GAS u32x4*)xp; xr[1] = *(const GAS u32x4*)(xp + 32);
        u32x4 wr[32];
        sfor<32>([&](auto kc) AI { constexpr int k = decltype(kc)::value, kq = 7 * (k >> 1) + 4 * (k & 1);
            if constexpr (k < 31) wr[k] = wimg(kq, kq + 1);
            else { const u32x2 p = ldstr(trW[1] + (kq - 56) * 1024), q = ldstr(trW[1] + (kq - 56) * 1024 + (hh ? 0 : 1024)); wr[k] = (u32x4){p.x, p.y, q.x, q.y}; } });
        LBAR();
        STAMP(19);
        wait_flags(P.ws, flag_off(1, (ti >> 2) * 16 + 4 * w), 4, e, lane);
        STAMP(20);
        u32x4 xf[8];
#pragma unroll
        for (int k = 0; k < 8; ++k) xf[k] = gld(rs, vx, WS_O + ((16 * w + 2 * k) * XR + 32 * ti) * 16);
#pragma unroll
        for (int k = 0; k < 8; ++k) sts128(xl + (8 * w + k) * 1024, xf[k]);
        LBAR();
        STAMP(5);
        f32x16 r = gemm(IC(32), wr, xl);
        STAMP(21);
        float s1 = 0.f, s2 = 0.f;
#pragma unroll
        for (int s = 0; s < 2; ++s) {
#pragma unroll
            for (int i = 0; i < 4; ++i) {
                hp[s][i] = pk(r[8 * s + 2 * i] + blo(xr[s][i]), r[8 * s + 2 * i + 1] + bhi(xr[s][i]));
                s1 = dot2(hp[s][i], ONES, s1); s2 = dot2(hp[s][i], hp[s][i], s2);
            }
            if (act) gst(rs, hp[s], vx, WS_H + ((4 * tw + 2 * s) * XR + 32 * ti) * 16);
        }
        s1 = hsum(s1); s2 = hsum(s2);
        if (act && hh == 0) gst8(rs, (u32x2){f2u(s1), f2u(s2)}, r32 * 8, WS_STH + (tw * 2048 + 32 * ti) * 8);
        STAMP(22);
        WAIT_BAR(0);
        if (tid == 0) set_flag(P.ws, flag_off(2, ti * 4 + g), e);
        STAMP(6);
    }
#define LOAD_WR() u32x4 wr[28]; sfor<28>([&](auto kc) AI { constexpr int k = decltype(kc)::value; wr[k] = wimg(4 * k, 4 * k + 1); })
    const int fsn = w == 0 ? 1 : 2, fsl = w <= 1 ? 0 : w - 1;
    {
        GEMM_GEOM();
        BAR();
        LOAD_WR();
        LBAR();
        wait_flags(P.ws, flag_off(2, ti * 4), 4, e, lane);
        u32x4 xf[7];
#pragma unroll
        for (int k = 0; k < 7; ++k) xf[k] = gld(rs, vx, WS_H + ((14 * w + 2 * k) * XR + 32 * ti) * 16);
        u32x2 sv[7];
#pragma unroll
        for (int q = 0; q < 7; ++q) sv[q] = gld8(rs, (7 * hh * 2048 + r32) * 8, WS_STH + (q * 2048 + 32 * ti) * 8);
        SB();
        {
            float cs[4] = {0.f, 0.f, 0.f, 0.f};
#pragma unroll
            for (int k = 0; k < 28; ++k)
#pragma unroll
                for (int i = 0; i < 4; ++i) cs[i] = dot2(wr[k][i], ONES, cs[i]);
            const float c = hsum((cs[0] + cs[1]) + (cs[2] + cs[3]));
            if (hh == 0) m2[r32] = c;
        }
        SB();
#pragma unroll
        for (int k = 0; k < 7; ++k) sts128(xl + (7 * w + k) * 1024, xf[k]);
        LBAR();
        STAMP(7);
        f32x16 r = gemm(IC(28), wr, xl);
        float t1 = 0.f, t2 = 0.f;
#pragma unroll
        for (int q = 0; q < 7; ++q) { t1 += u2f(sv[q].x); t2 += u2f(sv[q].y); }
        t1 = hsum(t1); t2 = hsum(t2);
        meanH = t1 * (1.f / D);
        const float var = __builtin_fmaxf(t2 * (1.f / D) - meanH * meanH, 0.f);
        rstdH = __builtin_amdgcn_rsqf(var + EPS);
        const float sc = rstdH * __builtin_amdgcn_rsqf(var * rstdH * rstdH + EPS);
#pragma unroll
        for (int q = 0; q < 4; ++q) {
            const f32x4 c = *(const LAS f32x4*)(m2 + 8 * q + 4 * hh);
#pragma unroll
            for (int i = 0; i < 4; ++i) r[4 * q + i] = gelu_t((r[4 * q + i] - meanH * c[i]) * sc);
        }
#pragma unroll
        for (int s = 0; s < 2; ++s) if (act) gst(rs, pack8(r, s), vx, WS_HH + ((4 * tw + 2 * s) * XR + 32 * ti) * 16);
        WAIT_BAR(0);
        if (tid == 0) set_flag(P.ws, flag_off(3, ti * 4 + g), e);
        STAMP(8);
    }
    {
        GEMM_GEOM();
        BAR();
        LOAD_WR();
        LBAR();
        const int sh = w == 0 ? -1 : w == 1 ? 1 : w == 2 ? -2 : 2;
        const int tn = ti + (sh < 0 ? -1 : 1);
        {
            const bool nb = tn >= 0 && tn < 64;
            const int li = lane & 3;
            const GAS unsigned* p = (const GAS unsigned*)(P.ws + flag_off(3, (((li & 2) && nb) ? tn : ti) * 4 + fsl + ((li & 1) && fsn == 2 ? 1 : 0)));
            poll_word(p, e);
        }
        const int tok = 32 * ti + r32 + sh;
        const bool ok = tok >= 0 && tok < S;
        const unsigned vxs = ok ? (hh * XR + tok) * 16 : 0x7ffffff0u;
        u32x4 xf[7];
#pragma unroll
        for (int k = 0; k < 7; ++k) { xf[k] = gld(rs, vxs, WS_HH + (14 * w + 2 * k) * XROW); }
#pragma unroll
        for (int k = 0; k < 7; ++k) sts128(xl + (7 * w + k) * 1024, xf[k]);
        LBAR();
        STAMP(9);
        f32x16 r = gemm(IC(28), wr, xl);
        float s1 = 0.f, s2 = 0.f;
#pragma unroll
        for (int s = 0; s < 2; ++s) {
            u32x4 xp;
#pragma unroll
            for (int i = 0; i < 4; ++i) {
                xp[i] = pk(r[8 * s + 2 * i] + (blo(hp[s][i]) - meanH) * rstdH, r[8 * s + 2 * i + 1] + (bhi(hp[s][i]) - meanH) * rstdH);
                s1 = dot2(xp[i], ONES, s1); s2 = dot2(xp[i], xp[i], s2);
            }
            if (act) gst(rs, xp, vx, WS_X1 + ((4 * tw + 2 * s) * XR + 32 * ti) * 16);
        }
        s1 = hsum(s1); s2 = hsum(s2);
        if (act && hh == 0) gst8(rs, (u32x2){f2u(s1), f2u(s2)}, r32 * 8, WS_STX + (tw * 2048 + 32 * ti) * 8);
        WAIT_BAR(0);
        if (tid == 0) set_flag(P.ws, flag_off(4, ti * 4 + g), e);
        STAMP(10);
    }
    {
        LANE_GEOM();
        unsigned tr6[2];
        tr6[0] = L0 + trq * 64 + gq * 8 + 2 * hh * 1024; tr6[1] = tr6[0] + 57344; PIN(tr6[0]); PIN(tr6[1]);
        auto wf6 = [&](int t, int k) AI { const unsigned a = (k < 14 ? tr6[0] + k * 4096 : tr6[1] + (k - 14) * 4096) + t * 256; return wfrag(a, a + 1024); };
        const LAS float* cs6 = (const LAS float*)(uintptr_t)(L0 + L_M2 + 1024);
        wait_flags(P.ws, flag_off(4, (4 * m + w) * 4), 4, e, lane);
        STAMP(28);
        const int tk = 128 * m + 32 * w;
        auto ldx1 = [&](int k) AI { return gld(rs, vx, WS_X1 + (2 * k * XR + tk) * 16); };
        constexpr int PX = P6_PX, PW = 2;
        f32x16 acc[4] = {};
        u32x2 sv[7];
        {
            u32x4 xf[28], wf[28][4];
#pragma unroll
            for (int k = 0; k < PX; ++k) xf[k] = ldx1(k);
#pragma unroll
            for (int q = 0; q < 7; ++q) sv[q] = gld8(rs, (7 * hh * 2048 + r32) * 8, WS_STX + (q * 2048 + tk) * 8);
            BAR();
            STAMP(11);
#pragma unroll
            for (int k = 0; k < PW; ++k)
#pragma unroll
                for (int t = 0; t < 4; ++t) wf[k][t] = wf6(t, k);
            SB();
            sfor<28>([&](auto kc) AI {
                constexpr int k = decltype(kc)::value;
                MFMA(acc[0], wf[k][0], xf[k], acc[0]);
                if constexpr (k + PW < 28) { wf[k + PW][0] = wf6(0, k + PW); }
                SB();
                MFMA(acc[1], wf[k][1], xf[k], acc[1]);
                if constexpr (k + PW < 28) { wf[k + PW][1] = wf6(1, k + PW); }
                if constexpr (k + PX < 28) xf[k + PX] = ldx1(k + PX);
                SB();
                MFMA(acc[2], wf[k][2], xf[k], acc[2]);
                if constexpr (k + PW < 28) { wf[k + PW][2] = wf6(2, k + PW); }
                SB();
                MFMA(acc[3], wf[k][3], xf[k], acc[3]);
                if constexpr (k + PW < 28) { wf[k + PW][3] = wf6(3, k + PW); }
                SB();
            });
        }
        STAMP(23);
        LBAR();
        STAMP(24);
        float t1 = 0.f, t2 = 0.f;
#pragma unroll
        for (int q = 0; q < 7; ++q) { t1 += u2f(sv[q].x); t2 += u2f(sv[q].y); }
        t1 = hsum(t1); t2 = hsum(t2);
        const float mean = t1 * (1.f / D), var = __builtin_fmaxf(t2 * (1.f / D) - mean * mean, 0.f) + EPS, rstd = __builtin_amdgcn_rsqf(var);
        const GeluK gk = {-mean, GC1 * rstd * rstd * rstd, GC0 * rstd, var * rstd};
        const unsigned uo = WS_U + ((4 * tb6) * XR + tk) * 16;
        auto epi1 = [&](f32x16& y, int te) AI {
#pragma unroll
            for (int q = 0; q < 4; ++q) {
                const f32x4 c = *(const LAS f32x4*)(cs6 + te * 32 + 8 * q + 4 * hh);
#pragma unroll
                for (int i = 0; i < 4; ++i) {
                    const float v = __builtin_fmaf(gk.nmean, c[i], y[4 * q + i]);
                    const float z = v * __builtin_fmaf(v * v, gk.k1, gk.k0);
                    y[4 * q + i] = v * __builtin_amdgcn_rcpf(__builtin_fmaf(__builtin_amdgcn_exp2f(z), gk.irs, gk.irs));
                }
            }
            gst(rs, pack8(y, 0), vx, uo + 4 * te * XROW); gst(rs, pack8(y, 1), vx, uo + (4 * te + 2) * XROW);
            SB();
        };
        epi1(acc[0], 0); epi1(acc[1], 1); epi1(acc[2], 2);
        if (nt6 == 4) epi1(acc[3], 3);
        STAMP(29);
        WAIT_BAR(0);
        if (tid == 0) set_flag(P.ws, flag_off(5, m * 16 + s16), e);
        BAR();
        STAMP(12);
    }
    if (act7) {
        LANE_GEOM();
        const unsigned lanep = trq * 64 + gq * 8 + 2 * hh * 512;
        unsigned tA = L0 + L_X + w * 8192 + lanep, tB = wst + lanep; PIN(tA); PIN(tB);
        auto w4f = [&](int k, int t) AI { const unsigned a = (k < 4 ? tA + k * 2048 : k < 18 ? tB + (k - 4) * 2048 : tB + (k - 18) * 2048) + t * 256; return wfrag(a, a + 512); };
        u32x4 wr[28][2];
#pragma unroll
        for (int k = 0; k < 14; ++k) { wr[k][0] = w4f(k, 0); wr[k][1] = w4f(k, 1); }
        LBAR();
        const int tk = 64 * j7;
        const int tt = w >> 1, nt7 = w & 1;
        wait_flags(P.ws, flag_off(5, (j7 >> 1) * 16 + 4 * w), 4, e, lane);
        constexpr int PX = P7_PX;
        u32x4 xa[28][2];
#pragma unroll
        for (int k = 0; k < PX; ++k) { xa[k][0] = gld(rs, vx, WS_U + ((56 * w + 2 * k) * XR + tk) * 16); xa[k][1] = gld(rs, vx, WS_U + ((56 * w + 2 * k) * XR + tk + 32) * 16); }
        STAMP(13);
        SB();
        f32x16 acc[4] = {};
        u32x4 rh[2], rx[2];
        sfor<28>([&](auto kc) AI {
            constexpr int k = decltype(kc)::value;
            if constexpr (k == 13) { BAR(); SB(); }
            if constexpr (k + 3 >= 14 && k + 3 < 28) { wr[k + 3][0] = w4f(k + 3, 0); wr[k + 3][1] = w4f(k + 3, 1); }
            MFMA(acc[0], wr[k][0], xa[k][0], acc[0]); MFMA(acc[1], wr[k][1], xa[k][0], acc[1]);
            if constexpr (k + PX < 28) xa[k + PX][0] = gld(rs, vx, WS_U + ((56 * w + 2 * (k + PX)) * XR + tk) * 16);
            MFMA(acc[2], wr[k][0], xa[k][1], acc[2]); MFMA(acc[3], wr[k][1], xa[k][1], acc[3]);
            if constexpr (k + PX < 28) xa[k + PX][1] = gld(rs, vx, WS_U + ((56 * w + 2 * (k + PX)) * XR + tk + 32) * 16);
            if constexpr (k == 24 || k == 25) {
                constexpr int s = k - 24;
                const unsigned so = ((8 * g7 + 4 * nt7 + 2 * s) * XR + tk + 32 * tt) * 16;
                rh[s] = gld(rs, vx, WS_H + so); rx[s] = gld(rs, vx, WS_X1 + so);
            }
            SB();
        });
        STAMP(27);
#pragma unroll
        for (int t = 0; t < 4; ++t) {
            if (t != w) {
                const unsigned a = L0 + L_X + t * 12288 + (w < t ? w : w - 1) * 4096 + lane * 16;
#pragma unroll
                for (int q = 0; q < 4; ++q) stsf4(a + q * 1024, (f32x4){acc[t][4 * q], acc[t][4 * q + 1], acc[t][4 * q + 2], acc[t][4 * q + 3]});
            }
        }
        LBAR();
        f32x16 r = w == 0 ? acc[0] : w == 1 ? acc[1] : w == 2 ? acc[2] : acc[3];
        const unsigned a = L0 + L_X + w * 12288 + lane * 16;
#pragma unroll
        for (int j = 0; j < 3; ++j)
#pragma unroll
            for (int q = 0; q < 4; ++q) { const f32x4 v = ldsf4(a + j * 4096 + q * 1024); r[4 * q] += v[0]; r[4 * q + 1] += v[1]; r[4 * q + 2] += v[2]; r[4 * q + 3] += v[3]; }
        const unsigned oo = (tk + 32 * tt + r32) * 896 + 128 * g7 + 64 * nt7 + 16 * hh;
        char* op = (char*)P.out + oo;
        const __amdgpu_buffer_rsrc_t ro = __builtin_amdgcn_make_buffer_rsrc((void*)P.out, 0, S * D * 2, 0x00020000); (void)ro; (void)op;
#pragma unroll
        for (int s = 0; s < 2; ++s) {
            u32x4 ov;
#pragma unroll
            for (int i = 0; i < 4; ++i)
                ov[i] = pk(r[8 * s + 2 * i] + blo(rx[s][i]) + blo(rh[s][i]), r[8 * s + 2 * i + 1] + bhi(rx[s][i]) + bhi(rh[s][i]));
            *(GAS u32x4*)(op + 32 * s) = ov;
        }
        STAMP(14);
    }
    STAMP_FLUSH();
}
}

extern "C" void kernel_launch(void* const* d_in, const int* in_sizes, int n_in, void* d_out, int out_size, void* d_ws, size_t ws_size,
                              hipStream_t stream) {
    mk::Params P;
    P.x = (const u16*)d_in[0]; P.wqkv = (const u16*)d_in[1]; P.wo = (const u16*)d_in[2];
    P.w1 = (const u16*)d_in[4]; P.w2 = (const u16*)d_in[5]; P.w3 = (const u16*)d_in[6]; P.w4 = (const u16*)d_in[7];
    P.out = (u16*)d_out; P.ws = (char*)d_ws;
    mk::fused_fwd<<<256, 512, 0, stream>>>(P);
}
```

```cpp
#define ISLANDS 0
#define TOUCH_FLAGS 1
#define W4SPLIT 36
#define PRIO_C 2
#define PRIO_L 0
#define NT_W 0
#define NT_QKV 0
#define NT_X 0
#define OUT_MODE 0
#define P6_PX 10
#define P6_PW 3
#define P6_PG 6
#define P7_PX 5
#define HALF 12
#define POLL2 0
#define POLL_GAP 3
#define TOUCH_DELAY 12
#define EARLY_TOUCH 1
#define LATE_TOUCH 3
#include <hip/hip_runtime.h>
#include <hip/hip_bf16.h>
#include <stdint.h>
#include <type_traits>
#include <utility>

typedef unsigned short u16;
typedef __attribute__((ext_vector_type(8))) short bf16x8;
typedef __attribute__((ext_vector_type(4))) short s16x4;
typedef __attribute__((ext_vector_type(16))) float f32x16;
typedef __attribute__((ext_vector_type(4))) float f32x4;
typedef __attribute__((ext_vector_type(2))) float f32x2;
typedef __attribute__((ext_vector_type(4))) unsigned u32x4;
typedef __attribute__((ext_vector_type(2))) unsigned u32x2;
typedef __attribute__((ext_vector_type(2))) __bf16 bf2;
#define LAS __attribute__((address_space(3)))
#define GAS __attribute__((address_space(1)))
#define DEVI __device__ __forceinline__

#define MK_STAMP 0

namespace mk {
constexpr int S = 2048, D = 448;
constexpr unsigned XR = 2056, XROW = XR * 16;
constexpr unsigned KVH = 64 * 4096 + 128;
constexpr unsigned WS_EP = 0;
constexpr unsigned WS_FLAG = 4096;
constexpr unsigned WS_KV = 262144;
constexpr unsigned WS_O = WS_KV + 16 * KVH;
constexpr unsigned WS_H = WS_O + 64 * XROW;
constexpr unsigned WS_HH = WS_H + 56 * XROW;
constexpr unsigned WS_X1 = WS_HH + 56 * XROW;
constexpr unsigned WS_U = WS_X1 + 56 * XROW;
constexpr unsigned WS_STH = WS_U + 224 * XROW;
constexpr unsigned WS_STX = WS_STH + 262144;
constexpr unsigned WS_ST1 = WS_STX + 262144;
constexpr unsigned WS_SPARE = WS_ST1 + 32768;
constexpr unsigned WS_STAMP = WS_SPARE + 128;
constexpr unsigned WS_END = WS_STAMP + 16 * 512 * 8;
constexpr unsigned L_SLOT = 28672;
constexpr unsigned L_MISC = 143360;
constexpr unsigned L_KV = 114688;
constexpr unsigned L_OWNKV = L_KV + 8 * 4096;
constexpr unsigned L_WST = 28672;
constexpr unsigned L_X = 114688;
constexpr unsigned L_M2 = 147456;
constexpr unsigned CODE_TOUCH_BYTES = (ISLANDS ? 52000 : 36000);
constexpr float EPS = 1e-5f;
constexpr unsigned SPIN_CAP = 1u << 18;

struct Params {
    const u16* x; const u16* wqkv; const u16* wo; const u16* w1; const u16* w2; const u16* w3; const u16* w4;
    u16* out; char* ws;
};

DEVI unsigned f2u(float f) { return __builtin_bit_cast(unsigned, f); }
DEVI float u2f(unsigned u) { return __builtin_bit_cast(float, u); }
DEVI bool wall(bool p) { return __builtin_amdgcn_ballot_w64(!p) == 0; }
template <int X> DEVI float xor32(float v) { return u2f((unsigned)__builtin_amdgcn_ds_swizzle((int)f2u(v), (X << 10) | 0x1f)); }
DEVI unsigned pk(float lo, float hi) { bf2 v = {(__bf16)lo, (__bf16)hi}; return __builtin_bit_cast(unsigned, v); }
DEVI float blo(unsigned u) { return u2f(u << 16); }
DEVI float bhi(unsigned u) { return u2f(u & 0xffff0000u); }
DEVI float dot2(unsigned a, unsigned b, float c) { return __builtin_amdgcn_fdot2_f32_bf16(__builtin_bit_cast(bf2, a), __builtin_bit_cast(bf2, b), c, false); }
constexpr unsigned ONES = 0x3f803f80u;
DEVI float hsum(float v) { auto r = __builtin_amdgcn_permlane32_swap(f2u(v), f2u(v), false, false); return u2f(r[0]) + u2f(r[1]); }
DEVI float hmax(float v) { auto r = __builtin_amdgcn_permlane32_swap(f2u(v), f2u(v), false, false); return __builtin_fmaxf(u2f(r[0]), u2f(r[1])); }
DEVI f32x16 mfma(u32x4 a, u32x4 b, f32x16 c) { return __builtin_amdgcn_mfma_f32_32x32x16_bf16(__builtin_bit_cast(bf16x8, a), __builtin_bit_cast(bf16x8, b), c, 0, 0, 0); }
#define MFMA(acc, a, b, c) do { acc = mfma(a, b, c); asm volatile("" : "+v"(acc)); } while (0)
DEVI u32x4 lds128(unsigned a) { return *(const LAS u32x4*)(uintptr_t)a; }
DEVI f32x4 ldsf4(unsigned a) { return *(const LAS f32x4*)(uintptr_t)a; }
DEVI void sts128(unsigned a, u32x4 v) { *(LAS u32x4*)(uintptr_t)a = v; }
DEVI void stsf4(unsigned a, f32x4 v) { *(LAS f32x4*)(uintptr_t)a = v; }
DEVI u32x2 ldstr(unsigned a) { return __builtin_bit_cast(u32x2, __builtin_amdgcn_ds_read_tr16_b64_v4i16((LAS s16x4*)(uintptr_t)a)); }
DEVI u32x4 wfrag(unsigned a0, unsigned a1) { u32x2 p = ldstr(a0), q = ldstr(a1); return (u32x4){p.x, p.y, q.x, q.y}; }
DEVI u32x4 pack8(const f32x16& a, int s) { return (u32x4){pk(a[8 * s], a[8 * s + 1]), pk(a[8 * s + 2], a[8 * s + 3]), pk(a[8 * s + 4], a[8 * s + 5]), pk(a[8 * s + 6], a[8 * s + 7])}; }
constexpr int SC1 = 16;
DEVI u32x4 gld(__amdgpu_buffer_rsrc_t rs, unsigned voff, unsigned soff) { return __builtin_amdgcn_raw_buffer_load_b128(rs, voff, soff, SC1); }
DEVI u32x2 gld8(__amdgpu_buffer_rsrc_t rs, unsigned voff, unsigned soff) { return __builtin_amdgcn_raw_buffer_load_b64(rs, voff, soff, SC1); }
DEVI void gst(__amdgpu_buffer_rsrc_t rs, u32x4 v, unsigned voff, unsigned soff) { __builtin_amdgcn_raw_buffer_store_b128(v, rs, voff + soff, 0, SC1); }
DEVI void gst8(__amdgpu_buffer_rsrc_t rs, u32x2 v, unsigned voff, unsigned soff) { __builtin_amdgcn_raw_buffer_store_b64(v, rs, voff, soff, SC1); }
#define WAIT_VM(N) asm volatile("s_waitcnt vmcnt(" #N ")" ::: "memory")
#define WAIT_LGKM() asm volatile("s_waitcnt lgkmcnt(0)" ::: "memory")
#define WAIT_BAR(N) asm volatile("s_waitcnt vmcnt(" #N ") lgkmcnt(0)\n\ts_barrier" ::: "memory")
#define LBAR() asm volatile("s_waitcnt lgkmcnt(0)\n\ts_barrier" ::: "memory")
#define SB() __builtin_amdgcn_sched_barrier(0)
#define PIN(x) asm volatile("" : "+v"(x))
DEVI float fma1(float a, float b, float c) { return __builtin_fmaf(a, b, c); }
DEVI float mul1(float a, float b) { return a * b; }
DEVI float max3(float a, float b, float c) { return __builtin_fmaxf(__builtin_fmaxf(a, b), c); }
template <int... Is, class F> DEVI void sfor_impl(std::integer_sequence<int, Is...>, F&& f) { (f(std::integral_constant<int, Is>{}), ...); }
template <int N, class F> DEVI void sfor(F&& f) { sfor_impl(std::make_integer_sequence<int, N>{}, f); }
#define AI __attribute__((always_inline))
#define IC(n) std::integral_constant<int, n>{}

template <int NT = 0> DEVI void dma1(const char* sbase, unsigned voff, unsigned lds) {
    if (NT) asm volatile("s_mov_b32 m0, %2\n\ts_nop 0\n\tglobal_load_lds_dwordx4 %0, %1 nt" :: "v"(voff), "s"(sbase), "s"(lds) : "memory");
    else asm volatile("s_mov_b32 m0, %2\n\ts_nop 0\n\tglobal_load_lds_dwordx4 %0, %1" :: "v"(voff), "s"(sbase), "s"(lds) : "memory");
}
DEVI void touch(const char* sbase, unsigned voff, unsigned lds) {
    asm volatile("s_mov_b32 m0, %2\n\ts_nop 0\n\tglobal_load_lds_dword %0, %1" :: "v"(voff), "s"(sbase), "s"(lds) : "memory");
}
struct WSrc { const char* base; unsigned step, voff; };
DEVI void wpiece(const WSrc& s, unsigned lds, int n) { dma1<NT_W>(s.base + (size_t)n * s.step, s.voff, lds + n * 1024); }
DEVI WSrc wsrc_a(const char* base, unsigned rs, int nt, int lane) { const int t = (lane >> 4) < nt ? (lane >> 4) : nt - 1; return WSrc{base, 4 * rs, ((lane >> 2) & 3) * rs + t * 64 + (lane & 3) * 16}; }
DEVI WSrc wsrc_b(const char* base, unsigned rs, int lane) { return WSrc{base, 8 * rs, (4 * (lane >> 5) + ((lane >> 2) & 3)) * rs + ((lane >> 4) & 1) * 64 + (lane & 3) * 16}; }

DEVI void poll_word(const GAS unsigned* p, unsigned e) {
    unsigned a = __hip_atomic_load(p, __ATOMIC_RELAXED, __HIP_MEMORY_SCOPE_AGENT);
#pragma nounroll
    for (unsigned sp = 0; sp < SPIN_CAP; ++sp) {
        __builtin_amdgcn_s_sleep(POLL_GAP);
        const unsigned b2 = __hip_atomic_load(p, __ATOMIC_RELAXED, __HIP_MEMORY_SCOPE_AGENT);
        if (wall(a == e)) break;
        __builtin_amdgcn_s_sleep(POLL_GAP);
        a = __hip_atomic_load(p, __ATOMIC_RELAXED, __HIP_MEMORY_SCOPE_AGENT);
        if (wall(b2 == e)) break;
    }
    __builtin_amdgcn_fence(__ATOMIC_ACQUIRE, "wavefront");
}
DEVI void wait_flags(const char* ws, unsigned foff, int n, unsigned e, int lane) {
    const GAS unsigned* p = (const GAS unsigned*)(ws + foff) + (lane < n ? lane : 0) * 32;
    if (POLL2) poll_word(p, e);
    else {
#pragma nounroll
        for (unsigned sp = 0; sp < SPIN_CAP; ++sp) {
            unsigned v = __hip_atomic_load(p, __ATOMIC_RELAXED, __HIP_MEMORY_SCOPE_AGENT);
            if (wall(v == e)) break;
        }
        __builtin_amdgcn_fence(__ATOMIC_ACQUIRE, "wavefront");
    }
}
DEVI void set_flag(char* ws, unsigned foff, unsigned e) { __hip_atomic_store((GAS unsigned*)(ws + foff), e, __ATOMIC_RELAXED, __HIP_MEMORY_SCOPE_AGENT); }
DEVI unsigned flag_off(int ph, int idx) { return WS_FLAG + (ph * 256 + idx) * 128; }

constexpr float GC0 = -2.f * 0.7978845608028654f * 1.4426950408889634f, GC1 = GC0 * 0.044715f;
DEVI float gelu_t(float x) {
    float z = x * __builtin_fmaf(x * x, GC1, GC0);
    return x * __builtin_amdgcn_rcpf(1.f + __builtin_amdgcn_exp2f(z));
}
struct GeluK { float nmean, k1, k0, irs; };
DEVI void gelu_step(int o, float& y, float& z, float c, const GeluK& g) {
    if (o == 0) y = fma1(g.nmean, c, y);
    else if (o == 1) z = mul1(y, y);
    else if (o == 2) z = fma1(z, g.k1, g.k0);
    else if (o == 3) z = mul1(y, z);
    else if (o == 4) z = __builtin_amdgcn_exp2f(z);
    else if (o == 5) z = fma1(z, g.irs, g.irs);
    else if (o == 6) z = __builtin_amdgcn_rcpf(z);
    else y = mul1(y, z);
}

#define STAMP(k) do {} while (0)
#define STAMP_DECL
#define STAMP_FLUSH() do {} while (0)

#define LANE_GEOM() int lane_ = threadIdx.x & 63; asm volatile("" : "+v"(lane_)); const int lane = lane_, r32 = lane & 31, hh = lane >> 5; \
    const int trq = (lane & 15) >> 2, trQ_ = ((lane >> 4) & 1) * 4 + (lane & 3), gq = (trQ_ & 4) | ((trQ_ & 1) << 1) | ((trQ_ >> 1) & 1); \
    const unsigned vx = (hh * XR + r32) * 16; (void)trq; (void)gq; (void)vx; (void)r32; (void)hh
constexpr unsigned L_FLG = L_MISC + 1920, F_RDY = 0, F_DONE = 16;
DEVI unsigned ldsw_ld(unsigned i) { return *(volatile LAS unsigned*)(uintptr_t)(L_FLG + 4 * i); }
DEVI u32x2 ldsw_ld2(unsigned i) { return *(volatile LAS u32x2*)(uintptr_t)(L_FLG + 4 * i); }
DEVI void ldsw_st(unsigned i, unsigned v) { *(volatile LAS unsigned*)(uintptr_t)(L_FLG + 4 * i) = v; }
DEVI void ring_wait(int c) {
#pragma nounroll
    for (unsigned sp = 0; sp < SPIN_CAP * 16u; ++sp) { const u32x2 v = ldsw_ld2(F_RDY + 2 * c); if (__builtin_amdgcn_readfirstlane(v.x & v.y)) break; __builtin_amdgcn_s_sleep(1); }
}
DEVI void slot_wait(unsigned n, int lane) {
#pragma nounroll
    for (unsigned sp = 0; sp < SPIN_CAP * 16u; ++sp) { const unsigned v = ldsw_ld(F_DONE + (lane < 6 ? lane : 0)); if (wall(v >= n)) break; __builtin_amdgcn_s_sleep(1); }
}
#define LB(N) asm volatile("s_waitcnt vmcnt(" #N ")\n\ts_barrier" ::: "memory")
#define BAR() asm volatile("s_barrier" ::: "memory")

__global__ __launch_bounds__(512, 2) void fused_fwd(Params P) {
    __shared__ __attribute__((aligned(16))) char smem[163840];
    const unsigned L0 = (unsigned)(uintptr_t)(LAS char*)smem;
    const int tid = threadIdx.x, wv = __builtin_amdgcn_readfirstlane(tid >> 6), w = wv & 3;
    const int b = blockIdx.x, xcd = b & 7, slot = b >> 3;
    const int m = 2 * xcd + (slot >> 4), h = slot & 15;
    const int g = slot & 3, nt = g < 2 ? 4 : 3, tb = g < 2 ? 4 * g : 3 * g + 2;
    const int ti = 8 * xcd + (slot >> 2);
    const int s16 = slot & 15, pr = s16 >> 1;
    const int nt6 = ((s16 ^ pr) & 1) ? 3 : 4, tb6 = 7 * pr + ((s16 & 1) ? (pr & 1 ? 3 : 4) : 0);
    const bool act7 = slot < 28;
    const int j7 = 4 * xcd + (act7 ? slot / 7 : 0), g7 = act7 ? slot % 7 : 0;
    const unsigned wst = L0 + w * L_WST;
    const unsigned shift = (56 * h) & 15u;
    STAMP_DECL

    if (wv >= 4) {
        if (PRIO_L) __builtin_amdgcn_s_setprio(PRIO_L);
        LANE_GEOM();
        const unsigned wrd = 16384 + hh * 6 * 256 + trq * 64 + ((shift + gq * 8) & 63);
        LAS float* csp = (LAS float*)(uintptr_t)(L0 + L_MISC);
        if (w < 2) {
            const unsigned xoffE = (lane >> 3) * 896 + (((lane & 7) ^ (lane >> 4)) << 4);
            const unsigned xoffO = (lane >> 3) * 896 + (((lane & 7) ^ (4 + (lane >> 4))) << 4);
            const unsigned colb = (56 * h) & ~15u;
            unsigned woff[3];
#pragma unroll
            for (int j = 0; j < 3; ++j) {
                const int bl = 4 * j + (lane >> 4), kql = bl / 3, tile = bl % 3;
                woff[j] = (4 * kql + ((lane >> 2) & 3)) * 2688 + tile * 896 + colb + (lane & 3) * 16;
            }
            const char* xb = (const char*)P.x + (size_t)(128 * m + 64 * w) * 896;
            const char* wb = (const char*)P.wqkv + (size_t)(32 * w) * 2688;
            LBAR();
#pragma unroll 1
            for (int kc = 0; kc < 7; ++kc) {
                if (kc >= 5) slot_wait(kc - 4, lane);
                const unsigned base = L0 + (kc >= 5 ? kc - 5 : kc) * L_SLOT;
#pragma unroll
                for (int i = 0; i < 6; ++i) dma1<NT_QKV>(wb + (size_t)kc * 64 * 2688 + (i / 3) * 16 * 2688, woff[i % 3], base + 16384 + (6 * w + i) * 1024);
#pragma unroll
                for (int i = 0; i < 8; ++i) dma1<NT_X>(xb + kc * 128 + i * 8 * 896, (i & 1) ? xoffO : xoffE, base + (8 * w + i) * 1024);
                if (kc > 0) { WAIT_VM(14); ldsw_st(F_RDY + 2 * (kc - 1) + w, 1); }
            }
            WAIT_VM(0); ldsw_st(F_RDY + 12 + w, 1);
            {
                ring_wait(6);
                const unsigned a = L0 + L_SLOT + wrd + w * 3072;
#pragma unroll
                for (int t = 0; t < 3; ++t) { const u32x4 f = wfrag(a + t * 256, a + 768 + t * 256);
                    const float v = hsum((dot2(f[0], ONES, 0.f) + dot2(f[1], ONES, 0.f)) + (dot2(f[2], ONES, 0.f) + dot2(f[3], ONES, 0.f)));
                    if (hh == 0) csp[(2 + w) * 96 + t * 32 + r32] = v; }
            }
        } else {
            const int srow = 128 * m + 8 * h + 4 * (w - 2) + (lane >> 4);
            const char* sp = (const char*)P.x + (size_t)srow * 896 + (lane & 15) * 16;
            u32x4 sr[4];
            asm volatile("global_load_dwordx4 %0, %4, off\n\tglobal_load_dwordx4 %1, %4, off offset:256\n\tglobal_load_dwordx4 %2, %4, off offset:512\n\tglobal_load_dwordx4 %3, %5, off"
                         : "=&v"(sr[0]), "=&v"(sr[1]), "=&v"(sr[2]), "=&v"(sr[3]) : "v"(sp), "v"(sp + ((lane & 15) < 8 ? 768 : 0)) : "memory");
            if (w == 2 && lane < 32) ldsw_st(lane, 0);
            LBAR();
            {
                const unsigned lx = lane < 56 ? lane : 0, lw = b * 37 + (lane < 37 ? lane : 0);
                const unsigned fl = lane - 56;
                const int fidx = fl < 2 || fl == 5 ? m * 16 + h : ti * 4 + g;
                const char* tf = fl < 6 ? P.ws + flag_off(fl, fidx) : P.ws + WS_ST1 + (128 * m + 8 * h) * 16;
                const char* tp = !EARLY_TOUCH ? P.ws + WS_SPARE : w == 2 ? ((TOUCH_FLAGS && lane >= 56 && lane < 63) ? tf : (const char*)P.x + (size_t)(8 * b + lx / 7) * 896 + (lx % 7) * 128)
                                                                       : (const char*)P.wqkv + (size_t)(lw < 9408 ? lw : 0) * 128;
                if (TOUCH_DELAY) __builtin_amdgcn_s_sleep(TOUCH_DELAY);
                asm volatile("s_mov_b32 m0, %1\n\ts_nop 0\n\tglobal_load_lds_dword %0, off" :: "v"(tp), "s"(L0 + L_MISC + 2048) : "memory");
            }
            {
                asm volatile("s_waitcnt vmcnt(1)" : "+v"(sr[0]), "+v"(sr[1]), "+v"(sr[2]), "+v"(sr[3]) :: "memory");
                if ((lane & 15) >= 8) sr[3] = (u32x4){0u, 0u, 0u, 0u};
                float t1 = 0.f, t2 = 0.f;
#pragma unroll
                for (int q = 0; q < 4; ++q)
#pragma unroll
                    for (int i = 0; i < 4; ++i) { t1 = dot2(sr[q][i], ONES, t1); t2 = dot2(sr[q][i], sr[q][i], t2); }
                t1 += xor32<1>(t1); t2 += xor32<1>(t2); t1 += xor32<2>(t1); t2 += xor32<2>(t2); t1 += xor32<4>(t1); t2 += xor32<4>(t2); t1 += xor32<8>(t1); t2 += xor32<8>(t2);
                const float mean = t1 * (1.f / D), var = __builtin_fmaxf(t2 * (1.f / D) - mean * mean, 0.f);
                const unsigned e = *(const unsigned*)(P.ws + WS_EP) + 1u;
                const __amdgpu_buffer_rsrc_t rs = __builtin_amdgcn_make_buffer_rsrc((void*)P.ws, 0, WS_END, 0x00020000);
                if ((lane & 15) == 0) gst(rs, (u32x4){f2u(mean), e, f2u(__builtin_amdgcn_rsqf(var + EPS)), e}, srow * 16, WS_ST1);
            }
            float cs[3][4] = {};
            const unsigned wrd2 = wrd + (w - 2) * 2 * 3072;
#pragma unroll 1
            for (int c = 0; c < 7; ++c) {
                ring_wait(c);
                if (c == LATE_TOUCH) {
                    unsigned long long pc; unsigned back;
                    asm volatile("s_getpc_b64 %0\nmk_code_here:\n\ts_mov_b32 %1, mk_code_here-_ZN2mk9fused_fwdENS_6ParamsE" : "=s"(pc), "=s"(back));
                    constexpr unsigned len = CODE_TOUCH_BYTES;
                    const unsigned oc = (slot * 18 + (lane < 18 ? lane : 0)) * 128 * (ISLANDS ? 1 : 1), o3 = (b * 64 + lane) * 128, o2 = (b * 16 + (lane & 15)) * 128;
                    const char* t0 = w == 2 ? (const char*)(pc - back) + (oc < len ? oc : 0) : (const char*)P.w3 + (o3 < 1605632 ? o3 : 0);
                    const char* t1 = w == 2 ? (lane < 16 ? (const char*)P.wo : lane < 32 ? (const char*)P.w1 : (const char*)P.w2) + (o2 < 401408 ? o2 : 0) : (const char*)P.w4 + (o3 < 1605632 ? o3 : 0);
                    asm volatile("s_mov_b32 m0, %2\n\ts_nop 0\n\tglobal_load_lds_dword %0, off\n\tglobal_load_lds_dword %1, off" :: "v"(t0), "v"(t1), "s"(L0 + L_MISC + 2048) : "memory");
                }
                const unsigned a = L0 + (c >= 5 ? c - 5 : c) * L_SLOT + (c < 6 ? wrd2 : wrd + w * 3072);
#pragma unroll
                for (int t = 0; t < 3; ++t) { const u32x4 f = wfrag(a + t * 256, a + 768 + t * 256);
#pragma unroll
                    for (int i = 0; i < 4; ++i) cs[t][i] = dot2(f[i], ONES, cs[t][i]); }
                if (c < 6) {
#pragma unroll
                    for (int t = 0; t < 3; ++t) { const u32x4 f = wfrag(a + 3072 + t * 256, a + 3072 + 768 + t * 256);
#pragma unroll
                        for (int i = 0; i < 4; ++i) cs[t][i] = dot2(f[i], ONES, cs[t][i]); }
                }
                WAIT_LGKM();
                ldsw_st(F_DONE + 2 + w, c + 1);
            }
#pragma unroll
            for (int t = 0; t < 3; ++t) { const float v = hsum((cs[t][0] + cs[t][1]) + (cs[t][2] + cs[t][3])); if (hh == 0) csp[(w - 2) * 96 + t * 32 + r32] = v; }
        }
        LBAR();
        auto stage = [&](const WSrc& s, unsigned lds, int n0, int n1) AI {
#pragma unroll 1
            for (int n = n0; n < n1; ++n) wpiece(s, lds, n);
        };
        LB(0);
        BAR();
        stage(wsrc_a((const char*)P.wo + (size_t)(112 * w) * 896 + tb * 64, 896, nt, lane), wst, 0, 28);
        LB(0);
        { const WSrc s_ = wsrc_a((const char*)P.w1 + (size_t)(112 * w) * 896 + tb * 64, 896, nt, lane); BAR(); stage(s_, wst, 0, HALF); BAR(); stage(s_, wst, HALF, 28); BAR(); LB(0); }
        { const WSrc s_ = wsrc_a((const char*)P.w2 + (size_t)(112 * w) * 896 + tb * 64, 896, nt, lane); BAR(); stage(s_, wst, 0, HALF); BAR(); stage(s_, wst, HALF, 28); BAR(); LB(0); }
        { const WSrc s_ = wsrc_a((const char*)P.w3 + (size_t)(112 * w) * 3584 + tb6 * 64, 3584, nt6, lane); BAR(); stage(s_, wst, 0, HALF); BAR(); stage(s_, wst, HALF, 28); BAR(); LB(0); }
        {
            float c[4] = {0.f, 0.f, 0.f, 0.f};
            const unsigned a0 = L0 + trq * 64 + gq * 8 + 2 * hh * 1024 + w * 256;
#pragma unroll 4
            for (int k = 0; k < 28; ++k) { const unsigned a = a0 + k * 4096; const u32x4 f = wfrag(a, a + 1024);
#pragma unroll
                for (int i = 0; i < 4; ++i) c[i] = dot2(f[i], ONES, c[i]); }
            const float cc = hsum((c[0] + c[1]) + (c[2] + c[3]));
            if (hh == 0) ((LAS float*)(uintptr_t)(L0 + L_M2 + 1024))[w * 32 + r32] = cc;
        }
        if (b == 0 && w == 3) {
            const unsigned e = *(const volatile GAS unsigned*)(P.ws + WS_EP) + 1u;
            const GAS unsigned* p = (const GAS unsigned*)(P.ws + flag_off(0, lane));
#pragma nounroll
            for (unsigned sp = 0; sp < SPIN_CAP; ++sp) {
                const unsigned v0 = __hip_atomic_load(p, __ATOMIC_RELAXED, __HIP_MEMORY_SCOPE_AGENT), v1 = __hip_atomic_load(p + 64 * 32, __ATOMIC_RELAXED, __HIP_MEMORY_SCOPE_AGENT);
                const unsigned v2 = __hip_atomic_load(p + 128 * 32, __ATOMIC_RELAXED, __HIP_MEMORY_SCOPE_AGENT), v3 = __hip_atomic_load(p + 192 * 32, __ATOMIC_RELAXED, __HIP_MEMORY_SCOPE_AGENT);
                if (wall(v0 == e && v1 == e && v2 == e && v3 == e)) break;
            }
            if (lane == 0) __hip_atomic_store((GAS unsigned*)(P.ws + WS_EP), e, __ATOMIC_RELAXED, __HIP_MEMORY_SCOPE_AGENT);
        }
        LBAR();
        const WSrc w4s = wsrc_b((const char*)P.w4 + (size_t)(448 * w) * 896 + g7 * 128, 896, lane);
        if (act7) { stage(w4s, L0 + L_X + w * 8192, 0, 8); stage(w4s, wst - 8 * 1024, 8, W4SPLIT); }
        BAR();
        if (act7) stage(w4s, wst - 8 * 1024, W4SPLIT, 36);
        LB(0);
        if (act7) {
            BAR();
            stage(w4s, wst - 36 * 1024, 36, 56);
            LB(0);
            BAR();
        }
        STAMP_FLUSH();
        return;
    }

    if (PRIO_C) __builtin_amdgcn_s_setprio(PRIO_C);
    const unsigned e = *(const unsigned*)(P.ws + WS_EP) + 1u;
    const __amdgpu_buffer_rsrc_t rs = __builtin_amdgcn_make_buffer_rsrc((void*)P.ws, 0, WS_END, 0x00020000);
    u32x4 qf0, qf1;
    {
        LANE_GEOM();
        const unsigned wrd = 16384 + hh * 6 * 256 + trq * 64 + ((shift + gq * 8) & 63);
        STAMP(15);
        f32x16 aq = {}, ak = {}, av = {};
        const unsigned xrd = (32 * w + r32) * 128 + ((hh ^ ((r32 >> 1) & 7)) << 4);
        struct Fr { u32x4 x, q, k, v; };
        Fr F[4];
        unsigned xb[3][4], wb3[3];
#pragma unroll
        for (int gsl = 0; gsl < 3; ++gsl) {
#pragma unroll
            for (int j = 0; j < 4; ++j) { xb[gsl][j] = L0 + (xrd ^ (j << 5)) + gsl * 2 * L_SLOT; PIN(xb[gsl][j]); }
            wb3[gsl] = L0 + wrd + gsl * 2 * L_SLOT; PIN(wb3[gsl]);
        }
        auto ldx = [&](int sl, int j) AI { F[j].x = lds128(xb[sl / 2][j] + (sl & 1) * L_SLOT); const unsigned a = wb3[sl / 2] + (sl & 1) * L_SLOT + j * 3072; F[j].q = wfrag(a, a + 768); };
        auto ldk = [&](int sl, int j) AI { const unsigned a = wb3[sl / 2] + (sl & 1) * L_SLOT + j * 3072; F[j].k = wfrag(a + 256, a + 1024); };
        auto ldv = [&](int sl, int j) AI { const unsigned a = wb3[sl / 2] + (sl & 1) * L_SLOT + j * 3072; F[j].v = wfrag(a + 512, a + 1280); };
        LAS float* stw = (LAS float*)(uintptr_t)(L0 + L_MISC + 3072 + w * 256);
        BAR();
        ring_wait(0);
        STAMP(16);
#pragma unroll
        for (int j = 0; j < 2; ++j) { ldx(0, j); ldk(0, j); ldv(0, j); }
        SB();
        u32x4 gv = {0u, 0u, 0u, 0u};
        const unsigned gvo = (128 * m + 32 * w + r32) * 16;
        sfor<7>([&](auto cc) AI {
            constexpr int c = decltype(cc)::value, sl = c >= 5 ? c - 5 : c, nsl = c >= 4 ? c - 4 : c + 1;
            u32x2 rdy;
            sfor<4>([&](auto jc) AI {
                constexpr int j = decltype(jc)::value, jn = (j + 2) & 3, lb = j < 2 ? sl : nsl;
                constexpr bool ld = j < 2 || c < 6;
                if constexpr (j == 2 && c < 6) { if (__builtin_amdgcn_readfirstlane(rdy.x & rdy.y) == 0) ring_wait(c + 1); SB(); }
                MFMA(aq, F[j].q, F[j].x, aq);
                if constexpr (ld) ldx(lb, jn);
                SB();
                MFMA(ak, F[j].k, F[j].x, ak);
                if constexpr (ld) ldk(lb, jn);
                if constexpr (j == 0 && c >= 1 && c <= 2) ldsw_st(F_DONE + w, c);
                if constexpr (j == 0 && c == 4) gv = gld(rs, gvo, WS_ST1);
                SB();
                MFMA(av, F[j].x, F[j].v, av);
                if constexpr (ld) ldv(lb, jn);
                if constexpr (j == 0 && c < 6) rdy = ldsw_ld2(F_RDY + 2 * (c + 1));
                SB();
            });
        });
#pragma nounroll
        for (unsigned sp2 = 0; sp2 < SPIN_CAP && !wall(gv.y == e && gv.w == e); ++sp2) { asm volatile("" ::: "memory"); gv = gld(rs, gvo, WS_ST1); }
        if (hh == 0) { stw[r32] = u2f(gv.x) * u2f(gv.z); stw[32 + r32] = u2f(gv.z); }
        STAMP(1);
        const float mean = u2f(gv.x), rstd = u2f(gv.z);
        LBAR();
        STAMP(17);
        const LAS float* csf = (const LAS float*)(uintptr_t)(L0 + L_MISC);
        const float cv = (csf[64 + r32] + csf[96 + 64 + r32]) + (csf[192 + 64 + r32] + csf[288 + 64 + r32]);
        const float qs = rstd * (0.18898223650461362f * 1.4426950408889634f);
#pragma unroll
        for (int q = 0; q < 4; ++q) {
            auto c4 = [&](int o) AI { return (*(const LAS f32x4*)(csf + o) + *(const LAS f32x4*)(csf + 96 + o)) + (*(const LAS f32x4*)(csf + 192 + o) + *(const LAS f32x4*)(csf + 288 + o)); };
            const f32x4 c1 = c4(8 * q + 4 * hh), c2 = c4(32 + 8 * q + 4 * hh);
            const f32x4 mr = *(const LAS f32x4*)(stw + 8 * q + 4 * hh), rr = *(const LAS f32x4*)(stw + 32 + 8 * q + 4 * hh);
#pragma unroll
            for (int i = 0; i < 4; ++i) {
                aq[4 * q + i] = (aq[4 * q + i] - mean * c1[i]) * qs;
                ak[4 * q + i] = (ak[4 * q + i] - mean * c2[i]) * rstd;
                av[4 * q + i] = av[4 * q + i] * rr[i] - mr[i] * cv;
            }
        }
        if (hh == 1) { aq[12] = 1.f; aq[13] = 0.f; aq[14] = 0.f; aq[15] = 0.f; ak[12] = 0.f; ak[13] = 0.f; ak[14] = 0.f; ak[15] = 0.f; }
        if (r32 >= 28) {
            const float pv = r32 == 28 ? 1.f : 0.f;
#pragma unroll
            for (int i = 0; i < 16; ++i) av[i] = pv;
        }
        const u32x4 k0 = pack8(ak, 0), k1 = pack8(ak, 1), v0 = pack8(av, 0), v1 = pack8(av, 1);
        const unsigned go = WS_KV + h * KVH + (4 * m + w) * 4096;
        gst(rs, k0, lane * 16, go); gst(rs, k1, lane * 16, go + 1024); gst(rs, v0, lane * 16, go + 2048); gst(rs, v1, lane * 16, go + 3072);
        const unsigned lo = L0 + L_OWNKV + w * 4096 + lane * 16;
        sts128(lo, k0); sts128(lo + 1024, k1); sts128(lo + 2048, v0); sts128(lo + 3072, v1);
        qf0 = pack8(aq, 0); qf1 = pack8(aq, 1);
        STAMP(18);
        WAIT_BAR(0);
        if (tid == 0) set_flag(P.ws, flag_off(0, m * 16 + h), e);
        STAMP(2);
    }
    {
        LANE_GEOM();
        const int mt = m - 2 + (w >> 1);
        u32x4 kv[8];
        if (mt >= 0) {
            wait_flags(P.ws, flag_off(0, mt * 16 + h), 1, e, lane);
            const unsigned go = WS_KV + h * KVH + (4 * m - 8 + 2 * w) * 4096;
#pragma unroll
            for (int i = 0; i < 8; ++i) kv[i] = gld(rs, lane * 16, go + i * 1024);
        } else {
#pragma unroll
            for (int i = 0; i < 8; ++i) kv[i] = (u32x4){0u, 0u, (i & 3) == 1 && hh == 1 ? 0xF149u : 0u, 0u};
        }
        f32x16 cdiag, cold;
#pragma unroll
        for (int i = 0; i < 16; ++i) { const int kr = (i & 3) + 8 * (i >> 2) + 4 * hh; cdiag[i] = kr <= r32 ? 0.f : -1e30f; cold[i] = kr > r32 ? 0.f : -1e30f; }
#pragma unroll
        for (int i = 0; i < 8; ++i) sts128(L0 + L_KV + 2 * w * 4096 + i * 1024 + lane * 16, kv[i]);
        LBAR();
        STAMP(3);
        unsigned kvb = L0 + L_KV + w * 4096 + lane * 16;
        PIN(kvb);
        u32x4 kf[9][2], vf[9][2];
        const f32x16 zero16 = {};
        auto ldK = [&](int i) AI { kf[i][0] = lds128(kvb + i * 4096); kf[i][1] = lds128(kvb + i * 4096 + 1024); };
        auto ldV = [&](int i) AI { vf[i][0] = lds128(kvb + i * 4096 + 2048); vf[i][1] = lds128(kvb + i * 4096 + 3072); };
        auto smax = [&](const f32x16& s, float a) AI {
#pragma unroll
            for (int i = 0; i < 16; i += 2) a = max3(a, s[i], s[i + 1]);
            return a; };
        ldK(0); ldK(1);
        float mx = -3e38f;
        f32x16 s[2];
        auto smax8 = [&](const f32x16& v, int o, float a) AI {
#pragma unroll
            for (int i = 0; i < 8; i += 2) a = max3(a, v[o + i], v[o + i + 1]);
            return a; };
        sfor<9>([&](auto ic) AI {
            constexpr int i = decltype(ic)::value;
            if constexpr (i + 2 < 9) ldK(i + 2);
            MFMA(s[i & 1], kf[i][0], qf0, i == 0 ? cold : i == 8 ? cdiag : zero16);
            SB();
            MFMA(s[i & 1], kf[i][1], qf1, s[i & 1]);
            SB();
            if constexpr (i > 0) { mx = smax8(s[(i - 1) & 1], 0, mx); mx = smax8(s[(i - 1) & 1], 8, mx); PIN(mx); }
            SB();
        });
        ldK(0); ldK(1); ldV(0);
        mx = hmax(smax(s[0], mx));
        STAMP(25);
        f32x16 o = {}, cm;
#pragma unroll
        for (int i = 0; i < 16; ++i) { cm[i] = -mx; cdiag[i] -= mx; cold[i] -= mx; }
        MFMA(s[0], kf[0][0], qf0, cold); MFMA(s[0], kf[0][1], qf1, s[0]);
        SB();
        sfor<9>([&](auto ic) AI {
            constexpr int i = decltype(ic)::value;
            f32x16& c = s[i & 1];
            f32x16& n = s[(i + 1) & 1];
            if constexpr (i + 2 < 9) ldK(i + 2);
            if constexpr (i + 1 < 9) ldV(i + 1);
            if constexpr (i + 1 < 9) MFMA(n, kf[i + 1][0], qf0, i + 1 == 8 ? cdiag : cm);
#pragma unroll
            for (int j = 0; j < 8; ++j) c[j] = __builtin_amdgcn_exp2f(c[j]);
            u32x4 p0 = pack8(c, 0);
            PIN(p0);
            SB();
            if constexpr (i + 1 < 9) MFMA(n, kf[i + 1][1], qf1, n);
            MFMA(o, vf[i][0], p0, o);
#pragma unroll
            for (int j = 8; j < 16; ++j) c[j] = __builtin_amdgcn_exp2f(c[j]);
            const u32x4 p1 = pack8(c, 1);
            MFMA(o, vf[i][1], p1, o);
            SB();
        });
        STAMP(26);
        auto lr = __builtin_amdgcn_permlane32_swap(f2u(o[12]), f2u(o[12]), false, false);
        const float inv = __builtin_amdgcn_rcpf(u2f(lr[1]));
#pragma unroll
        for (int i = 0; i < 16; ++i) o[i] *= inv;
        if (hh == 1) { o[12] = 0.f; o[13] = 0.f; o[14] = 0.f; o[15] = 0.f; }
        const unsigned vo = (hh * XR + r32) * 16, so = WS_O + ((4 * h) * XR + 128 * m + 32 * w) * 16;
        gst(rs, pack8(o, 0), vo, so); gst(rs, pack8(o, 1), vo, so + 2 * XROW);
        WAIT_BAR(0);
        if (tid == 0) set_flag(P.ws, flag_off(1, m * 16 + h), e);
        STAMP(4);
    }
    const bool act = w < nt;
    const int tw = tb + w;
#define GEMM_GEOM() LANE_GEOM(); \
    unsigned trW[2]; trW[0] = L0 + (w < nt ? w : nt - 1) * 256 + trq * 64 + gq * 8 + 2 * hh * 1024; trW[1] = trW[0] + 57344; PIN(trW[0]); PIN(trW[1]); \
    auto wimg = [&](int kq0, int kq1) AI { return wfrag(kq0 < 56 ? trW[0] + kq0 * 1024 : trW[1] + (kq0 - 56) * 1024, kq1 < 56 ? trW[0] + kq1 * 1024 : trW[1] + (kq1 - 56) * 1024); }; \
    unsigned xl = L0 + L_X + lane * 16; PIN(xl); \
    LAS float* m2 = (LAS float*)(uintptr_t)(L0 + L_M2 + w * 256); (void)m2
    auto gemm = [&](auto nk, const u32x4* wr, unsigned xl) AI -> f32x16 {
        constexpr int NK = decltype(nk)::value, PD = 4;
        f32x16 acc = {};
        u32x4 xf[NK];
#pragma unroll
        for (int k = 0; k < PD; ++k) xf[k] = lds128(xl + k * 1024);
        SB();
        sfor<NK>([&](auto kc) AI {
            constexpr int k = decltype(kc)::value;
            if constexpr (k + PD < NK) xf[k + PD] = lds128(xl + (k + PD) * 1024);
            MFMA(acc, wr[k], xf[k], acc);
            SB();
        });
        return acc;
    };
    u32x4 hp[2];
    float meanH, rstdH;
    {
        GEMM_GEOM();
        u32x4 xr[2];
        const char* xp = (const char*)P.x + (size_t)(32 * ti + r32) * 896 + 64 * (act ? tw : tb) + 16 * hh;
        xr[0] = *(const GAS u32x4*)xp; xr[1] = *(const GAS u32x4*)(xp + 32);
        u32x4 wr[32];
        sfor<32>([&](auto kc) AI { constexpr int k = decltype(kc)::value, kq = 7 * (k >> 1) + 4 * (k & 1);
            if constexpr (k < 31) wr[k] = wimg(kq, kq + 1);
            else { const u32x2 p = ldstr(trW[1] + (kq - 56) * 1024), q = ldstr(trW[1] + (kq - 56) * 1024 + (hh ? 0 : 1024)); wr[k] = (u32x4){p.x, p.y, q.x, q.y}; } });
        LBAR();
        STAMP(19);
        wait_flags(P.ws, flag_off(1, (ti >> 2) * 16 + 4 * w), 4, e, lane);
        STAMP(20);
        u32x4 xf[8];
#pragma unroll
        for (int k = 0; k < 8; ++k) xf[k] = gld(rs, vx, WS_O + ((16 * w + 2 * k) * XR + 32 * ti) * 16);
#pragma unroll
        for (int k = 0; k < 8; ++k) sts128(xl + (8 * w + k) * 1024, xf[k]);
        LBAR();
        STAMP(5);
        f32x16 r = gemm(IC(32), wr, xl);
        STAMP(21);
        float s1 = 0.f, s2 = 0.f;
#pragma unroll
        for (int s = 0; s < 2; ++s) {
#pragma unroll
            for (int i = 0; i < 4; ++i) {
                hp[s][i] = pk(r[8 * s + 2 * i] + blo(xr[s][i]), r[8 * s + 2 * i + 1] + bhi(xr[s][i]));
                s1 = dot2(hp[s][i], ONES, s1); s2 = dot2(hp[s][i], hp[s][i], s2);
            }
            if (act) gst(rs, hp[s], vx, WS_H + ((4 * tw + 2 * s) * XR + 32 * ti) * 16);
        }
        s1 = hsum(s1); s2 = hsum(s2);
        if (act && hh == 0) gst8(rs, (u32x2){f2u(s1), f2u(s2)}, r32 * 8, WS_STH + (tw * 2048 + 32 * ti) * 8);
        STAMP(22);
        WAIT_BAR(0);
        if (tid == 0) set_flag(P.ws, flag_off(2, ti * 4 + g), e);
        STAMP(6);
    }
#define LOAD_WR() u32x4 wr[28]; sfor<28>([&](auto kc) AI { constexpr int k = decltype(kc)::value; wr[k] = wimg(4 * k, 4 * k + 1); })
    const int fsn = w == 0 ? 1 : 2, fsl = w <= 1 ? 0 : w - 1;
    {
        GEMM_GEOM();
        BAR();
        LOAD_WR();
        LBAR();
        wait_flags(P.ws, flag_off(2, ti * 4), 4, e, lane);
        u32x4 xf[7];
#pragma unroll
        for (int k = 0; k < 7; ++k) xf[k] = gld(rs, vx, WS_H + ((14 * w + 2 * k) * XR + 32 * ti) * 16);
        u32x2 sv[7];
#pragma unroll
        for (int q = 0; q < 7; ++q) sv[q] = gld8(rs, (7 * hh * 2048 + r32) * 8, WS_STH + (q * 2048 + 32 * ti) * 8);
        SB();
        {
            float cs[4] = {0.f, 0.f, 0.f, 0.f};
#pragma unroll
            for (int k = 0; k < 28; ++k)
#pragma unroll
                for (int i = 0; i < 4; ++i) cs[i] = dot2(wr[k][i], ONES, cs[i]);
            const float c = hsum((cs[0] + cs[1]) + (cs[2] + cs[3]));
            if (hh == 0) m2[r32] = c;
        }
        SB();
#pragma unroll
        for (int k = 0; k < 7; ++k) sts128(xl + (7 * w + k) * 1024, xf[k]);
        LBAR();
        STAMP(7);
        f32x16 r = gemm(IC(28), wr, xl);
        float t1 = 0.f, t2 = 0.f;
#pragma unroll
        for (int q = 0; q < 7; ++q) { t1 += u2f(sv[q].x); t2 += u2f(sv[q].y); }
        t1 = hsum(t1); t2 = hsum(t2);
        meanH = t1 * (1.f / D);
        const float var = __builtin_fmaxf(t2 * (1.f / D) - meanH * meanH, 0.f);
        rstdH = __builtin_amdgcn_rsqf(var + EPS);
        const float sc = rstdH * __builtin_amdgcn_rsqf(var * rstdH * rstdH + EPS);
#pragma unroll
        for (int q = 0; q < 4; ++q) {
            const f32x4 c = *(const LAS f32x4*)(m2 + 8 * q + 4 * hh);
#pragma unroll
            for (int i = 0; i < 4; ++i) r[4 * q + i] = gelu_t((r[4 * q + i] - meanH * c[i]) * sc);
        }
#pragma unroll
        for (int s = 0; s < 2; ++s) if (act) gst(rs, pack8(r, s), vx, WS_HH + ((4 * tw + 2 * s) * XR + 32 * ti) * 16);
        WAIT_BAR(0);
        if (tid == 0) set_flag(P.ws, flag_off(3, ti * 4 + g), e);
        STAMP(8);
    }
    {
        GEMM_GEOM();
        BAR();
        LOAD_WR();
        LBAR();
        const int sh = w == 0 ? -1 : w == 1 ? 1 : w == 2 ? -2 : 2;
        const int tn = ti + (sh < 0 ? -1 : 1);
        {
            const bool nb = tn >= 0 && tn < 64;
            const int li = lane & 3;
            const GAS unsigned* p = (const GAS unsigned*)(P.ws + flag_off(3, (((li & 2) && nb) ? tn : ti) * 4 + fsl + ((li & 1) && fsn == 2 ? 1 : 0)));
            poll_word(p, e);
        }
        const int tok = 32 * ti + r32 + sh;
        const bool ok = tok >= 0 && tok < S;
        const unsigned vxs = ok ? (hh * XR + tok) * 16 : 0x7ffffff0u;
        u32x4 xf[7];
#pragma unroll
        for (int k = 0; k < 7; ++k) { xf[k] = gld(rs, vxs, WS_HH + (14 * w + 2 * k) * XROW); }
#pragma unroll
        for (int k = 0; k < 7; ++k) sts128(xl + (7 * w + k) * 1024, xf[k]);
        LBAR();
        STAMP(9);
        f32x16 r = gemm(IC(28), wr, xl);
        float s1 = 0.f, s2 = 0.f;
#pragma unroll
        for (int s = 0; s < 2; ++s) {
            u32x4 xp;
#pragma unroll
            for (int i = 0; i < 4; ++i) {
                xp[i] = pk(r[8 * s + 2 * i] + (blo(hp[s][i]) - meanH) * rstdH, r[8 * s + 2 * i + 1] + (bhi(hp[s][i]) - meanH) * rstdH);
                s1 = dot2(xp[i], ONES, s1); s2 = dot2(xp[i], xp[i], s2);
            }
            if (act) gst(rs, xp, vx, WS_X1 + ((4 * tw + 2 * s) * XR + 32 * ti) * 16);
        }
        s1 = hsum(s1); s2 = hsum(s2);
        if (act && hh == 0) gst8(rs, (u32x2){f2u(s1), f2u(s2)}, r32 * 8, WS_STX + (tw * 2048 + 32 * ti) * 8);
        WAIT_BAR(0);
        if (tid == 0) set_flag(P.ws, flag_off(4, ti * 4 + g), e);
        STAMP(10);
    }
    {
        LANE_GEOM();
        unsigned tr6[2];
        tr6[0] = L0 + trq * 64 + gq * 8 + 2 * hh * 1024; tr6[1] = tr6[0] + 57344; PIN(tr6[0]); PIN(tr6[1]);
        auto wf6 = [&](int t, int k) AI { const unsigned a = (k < 14 ? tr6[0] + k * 4096 : tr6[1] + (k - 14) * 4096) + t * 256; return wfrag(a, a + 1024); };
        const LAS float* cs6 = (const LAS float*)(uintptr_t)(L0 + L_M2 + 1024);
        wait_flags(P.ws, flag_off(4, (4 * m + w) * 4), 4, e, lane);
        STAMP(28);
        const int tk = 128 * m + 32 * w;
        auto ldx1 = [&](int k) AI { return gld(rs, vx, WS_X1 + (2 * k * XR + tk) * 16); };
        constexpr int PX = P6_PX, PW = 2;
        f32x16 acc[4] = {};
        u32x2 sv[7];
        {
            u32x4 xf[28], wf[28][4];
#pragma unroll
            for (int k = 0; k < PX; ++k) xf[k] = ldx1(k);
#pragma unroll
            for (int q = 0; q < 7; ++q) sv[q] = gld8(rs, (7 * hh * 2048 + r32) * 8, WS_STX + (q * 2048 + tk) * 8);
            BAR();
            STAMP(11);
#pragma unroll
            for (int k = 0; k < PW; ++k)
#pragma unroll
                for (int t = 0; t < 4; ++t) wf[k][t] = wf6(t, k);
            SB();
            sfor<28>([&](auto kc) AI {
                constexpr int k = decltype(kc)::value;
                MFMA(acc[0], wf[k][0], xf[k], acc[0]);
                if constexpr (k + PW < 28) { wf[k + PW][0] = wf6(0, k + PW); }
                SB();
                MFMA(acc[1], wf[k][1], xf[k], acc[1]);
                if constexpr (k + PW < 28) { wf[k + PW][1] = wf6(1, k + PW); }
                if constexpr (k + PX < 28) xf[k + PX] = ldx1(k + PX);
                SB();
                MFMA(acc[2], wf[k][2], xf[k], acc[2]);
                if constexpr (k + PW < 28) { wf[k + PW][2] = wf6(2, k + PW); }
                SB();
                MFMA(acc[3], wf[k][3], xf[k], acc[3]);
                if constexpr (k + PW < 28) { wf[k + PW][3] = wf6(3, k + PW); }
                SB();
            });
        }
        STAMP(23);
        LBAR();
        STAMP(24);
        float t1 = 0.f, t2 = 0.f;
#pragma unroll
        for (int q = 0; q < 7; ++q) { t1 += u2f(sv[q].x); t2 += u2f(sv[q].y); }
        t1 = hsum(t1); t2 = hsum(t2);
        const float mean = t1 * (1.f / D), var = __builtin_fmaxf(t2 * (1.f / D) - mean * mean, 0.f) + EPS, rstd = __builtin_amdgcn_rsqf(var);
        const GeluK gk = {-mean, GC1 * rstd * rstd * rstd, GC0 * rstd, var * rstd};
        const unsigned uo = WS_U + ((4 * tb6) * XR + tk) * 16;
        auto epi1 = [&](f32x16& y, int te) AI {
#pragma unroll
            for (int q = 0; q < 4; ++q) {
                const f32x4 c = *(const LAS f32x4*)(cs6 + te * 32 + 8 * q + 4 * hh);
#pragma unroll
                for (int i = 0; i < 4; ++i) {
                    const float v = __builtin_fmaf(gk.nmean, c[i], y[4 * q + i]);
                    const float z = v * __builtin_fmaf(v * v, gk.k1, gk.k0);
                    y[4 * q + i] = v * __builtin_amdgcn_rcpf(__builtin_fmaf(__builtin_amdgcn_exp2f(z), gk.irs, gk.irs));
                }
            }
            gst(rs, pack8(y, 0), vx, uo + 4 * te * XROW); gst(rs, pack8(y, 1), vx, uo + (4 * te + 2) * XROW);
            SB();
        };
        epi1(acc[0], 0); epi1(acc[1], 1); epi1(acc[2], 2);
        if (nt6 == 4) epi1(acc[3], 3);
        STAMP(29);
        WAIT_BAR(0);
        if (tid == 0) set_flag(P.ws, flag_off(5, m * 16 + s16), e);
        BAR();
        STAMP(12);
    }
    if (act7) {
        LANE_GEOM();
        const unsigned lanep = trq * 64 + gq * 8 + 2 * hh * 512;
        unsigned tA = L0 + L_X + w * 8192 + lanep, tB = wst + lanep; PIN(tA); PIN(tB);
        auto w4f = [&](int k, int t) AI { const unsigned a = (k < 4 ? tA + k * 2048 : k < 18 ? tB + (k - 4) * 2048 : tB + (k - 18) * 2048) + t * 256; return wfrag(a, a + 512); };
        u32x4 wr[28][2];
#pragma unroll
        for (int k = 0; k < 14; ++k) { wr[k][0] = w4f(k, 0); wr[k][1] = w4f(k, 1); }
        LBAR();
        const int tk = 64 * j7;
        const int tt = w >> 1, nt7 = w & 1;
        wait_flags(P.ws, flag_off(5, (j7 >> 1) * 16 + 4 * w), 4, e, lane);
        constexpr int PX = P7_PX;
        u32x4 xa[28][2];
#pragma unroll
        for (int k = 0; k < PX; ++k) { xa[k][0] = gld(rs, vx, WS_U + ((56 * w + 2 * k) * XR + tk) * 16); xa[k][1] = gld(rs, vx, WS_U + ((56 * w + 2 * k) * XR + tk + 32) * 16); }
        STAMP(13);
        SB();
        f32x16 acc[4] = {};
        u32x4 rh[2], rx[2];
        sfor<28>([&](auto kc) AI {
            constexpr int k = decltype(kc)::value;
            if constexpr (k == 13) { BAR(); SB(); }
            if constexpr (k + 3 >= 14 && k + 3 < 28) { wr[k + 3][0] = w4f(k + 3, 0); wr[k + 3][1] = w4f(k + 3, 1); }
            MFMA(acc[0], wr[k][0], xa[k][0], acc[0]); MFMA(acc[1], wr[k][1], xa[k][0], acc[1]);
            if constexpr (k + PX < 28) xa[k + PX][0] = gld(rs, vx, WS_U + ((56 * w + 2 * (k + PX)) * XR + tk) * 16);
            MFMA(acc[2], wr[k][0], xa[k][1], acc[2]); MFMA(acc[3], wr[k][1], xa[k][1], acc[3]);
            if constexpr (k + PX < 28) xa[k + PX][1] = gld(rs, vx, WS_U + ((56 * w + 2 * (k + PX)) * XR + tk + 32) * 16);
            if constexpr (k == 24 || k == 25) {
                constexpr int s = k - 24;
                const unsigned so = ((8 * g7 + 4 * nt7 + 2 * s) * XR + tk + 32 * tt) * 16;
                rh[s] = gld(rs, vx, WS_H + so); rx[s] = gld(rs, vx, WS_X1 + so);
            }
            SB();
        });
        STAMP(27);
#pragma unroll
        for (int t = 0; t < 4; ++t) {
            if (t != w) {
                const unsigned a = L0 + L_X + t * 12288 + (w < t ? w : w - 1) * 4096 + lane * 16;
#pragma unroll
                for (int q = 0; q < 4; ++q) stsf4(a + q * 1024, (f32x4){acc[t][4 * q], acc[t][4 * q + 1], acc[t][4 * q + 2], acc[t][4 * q + 3]});
            }
        }
        LBAR();
        f32x16 r = w == 0 ? acc[0] : w == 1 ? acc[1] : w == 2 ? acc[2] : acc[3];
        const unsigned a = L0 + L_X + w * 12288 + lane * 16;
#pragma unroll
        for (int j = 0; j < 3; ++j)
#pragma unroll
            for (int q = 0; q < 4; ++q) { const f32x4 v = ldsf4(a + j * 4096 + q * 1024); r[4 * q] += v[0]; r[4 * q + 1] += v[1]; r[4 * q + 2] += v[2]; r[4 * q + 3] += v[3]; }
        const unsigned oo = (tk + 32 * tt + r32) * 896 + 128 * g7 + 64 * nt7 + 16 * hh;
        char* op = (char*)P.out + oo;
        const __amdgpu_buffer_rsrc_t ro = __builtin_amdgcn_make_buffer_rsrc((void*)P.out, 0, S * D * 2, 0x00020000); (void)ro; (void)op;
#pragma unroll
        for (int s = 0; s < 2; ++s) {
            u32x4 ov;
#pragma unroll
            for (int i = 0; i < 4; ++i)
                ov[i] = pk(r[8 * s + 2 * i] + blo(rx[s][i]) + blo(rh[s][i]), r[8 * s + 2 * i + 1] + bhi(rx[s][i]) + bhi(rh[s][i]));
            *(GAS u32x4*)(op + 32 * s) = ov;
        }
        STAMP(14);
    }
    STAMP_FLUSH();
}
}

extern "C" void kernel_launch(void* const* d_in, const int* in_sizes, int n_in, void* d_out, int out_size, void* d_ws, size_t ws_size,
                              hipStream_t stream) {
    mk::Params P;
    P.x = (const u16*)d_in[0]; P.wqkv = (const u16*)d_in[1]; P.wo = (const u16*)d_in[2];
    P.w1 = (const u16*)d_in[4]; P.w2 = (const u16*)d_in[5]; P.w3 = (const u16*)d_in[6]; P.w4 = (const u16*)d_in[7];
    P.out = (u16*)d_out; P.ws = (char*)d_ws;
    mk::fused_fwd<<<256, 512, 0, stream>>>(P);
}
```

```cpp
#define EARLY_CODE 1
#define TOUCH_FLAGS 1
#define W4SPLIT 36
#define PRIO_C 2
#define PRIO_L 0
#define NT_W 0
#define NT_QKV 0
#define NT_X 0
#define OUT_MODE 0
#define P6_PX 10
#define P6_PW 3
#define P6_PG 6
#define P7_PX 5
#define HALF 12
#define POLL2 0
#define POLL_GAP 3
#define TOUCH_DELAY 12
#define EARLY_TOUCH 1
#define LATE_TOUCH 3
#include <hip/hip_runtime.h>
#include <hip/hip_bf16.h>
#include <stdint.h>
#include <type_traits>
#include <utility>

typedef unsigned short u16;
typedef __attribute__((ext_vector_type(8))) short bf16x8;
typedef __attribute__((ext_vector_type(4))) short s16x4;
typedef __attribute__((ext_vector_type(16))) float f32x16;
typedef __attribute__((ext_vector_type(4))) float f32x4;
typedef __attribute__((ext_vector_type(2))) float f32x2;
typedef __attribute__((ext_vector_type(4))) unsigned u32x4;
typedef __attribute__((ext_vector_type(2))) unsigned u32x2;
typedef __attribute__((ext_vector_type(2))) __bf16 bf2;
#define LAS __attribute__((address_space(3)))
#define GAS __attribute__((address_space(1)))
#define DEVI __device__ __forceinline__

#define MK_STAMP 0

namespace mk {
constexpr int S = 2048, D = 448;
constexpr unsigned XR = 2056, XROW = XR * 16;
constexpr unsigned KVH = 64 * 4096 + 128;
constexpr unsigned WS_EP = 0;
constexpr unsigned WS_FLAG = 4096;
constexpr unsigned WS_KV = 262144;
constexpr unsigned WS_O = WS_KV + 16 * KVH;
constexpr unsigned WS_H = WS_O + 64 * XROW;
constexpr unsigned WS_HH = WS_H + 56 * XROW;
constexpr unsigned WS_X1 = WS_HH + 56 * XROW;
constexpr unsigned WS_U = WS_X1 + 56 * XROW;
constexpr unsigned WS_STH = WS_U + 224 * XROW;
constexpr unsigned WS_STX = WS_STH + 262144;
constexpr unsigned WS_ST1 = WS_STX + 262144;
constexpr unsigned WS_SPARE = WS_ST1 + 32768;
constexpr unsigned WS_STAMP = WS_SPARE + 128;
constexpr unsigned WS_END = WS_STAMP + 16 * 512 * 8;
constexpr unsigned L_SLOT = 28672;
constexpr unsigned L_MISC = 143360;
constexpr unsigned L_KV = 114688;
constexpr unsigned L_OWNKV = L_KV + 8 * 4096;
constexpr unsigned L_WST = 28672;
constexpr unsigned L_X = 114688;
constexpr unsigned L_M2 = 147456;
constexpr unsigned CODE_TOUCH_BYTES = 36000;
constexpr float EPS = 1e-5f;
constexpr unsigned SPIN_CAP = 1u << 18;

struct Params {
    const u16* x; const u16* wqkv; const u16* wo; const u16* w1; const u16* w2; const u16* w3; const u16* w4;
    u16* out; char* ws;
};

DEVI unsigned f2u(float f) { return __builtin_bit_cast(unsigned, f); }
DEVI float u2f(unsigned u) { return __builtin_bit_cast(float, u); }
DEVI bool wall(bool p) { return __builtin_amdgcn_ballot_w64(!p) == 0; }
template <int X> DEVI float xor32(float v) { return u2f((unsigned)__builtin_amdgcn_ds_swizzle((int)f2u(v), (X << 10) | 0x1f)); }
DEVI unsigned pk(float lo, float hi) { bf2 v = {(__bf16)lo, (__bf16)hi}; return __builtin_bit_cast(unsigned, v); }
DEVI float blo(unsigned u) { return u2f(u << 16); }
DEVI float bhi(unsigned u) { return u2f(u & 0xffff0000u); }
DEVI float dot2(unsigned a, unsigned b, float c) { return __builtin_amdgcn_fdot2_f32_bf16(__builtin_bit_cast(bf2, a), __builtin_bit_cast(bf2, b), c, false); }
constexpr unsigned ONES = 0x3f803f80u;
DEVI float hsum(float v) { auto r = __builtin_amdgcn_permlane32_swap(f2u(v), f2u(v), false, false); return u2f(r[0]) + u2f(r[1]); }
DEVI float hmax(float v) { auto r = __builtin_amdgcn_permlane32_swap(f2u(v), f2u(v), false, false); return __builtin_fmaxf(u2f(r[0]), u2f(r[1])); }
DEVI f32x16 mfma(u32x4 a, u32x4 b, f32x16 c) { return __builtin_amdgcn_mfma_f32_32x32x16_bf16(__builtin_bit_cast(bf16x8, a), __builtin_bit_cast(bf16x8, b), c, 0, 0, 0); }
#define MFMA(acc, a, b, c) do { acc = mfma(a, b, c); asm volatile("" : "+v"(acc)); } while (0)
DEVI u32x4 lds128(unsigned a) { return *(const LAS u32x4*)(uintptr_t)a; }
DEVI f32x4 ldsf4(unsigned a) { return *(const LAS f32x4*)(uintptr_t)a; }
DEVI void sts128(unsigned a, u32x4 v) { *(LAS u32x4*)(uintptr_t)a = v; }
DEVI void stsf4(unsigned a, f32x4 v) { *(LAS f32x4*)(uintptr_t)a = v; }
DEVI u32x2 ldstr(unsigned a) { return __builtin_bit_cast(u32x2, __builtin_amdgcn_ds_read_tr16_b64_v4i16((LAS s16x4*)(uintptr_t)a)); }
DEVI u32x4 wfrag(unsigned a0, unsigned a1) { u32x2 p = ldstr(a0), q = ldstr(a1); return (u32x4){p.x, p.y, q.x, q.y}; }
DEVI u32x4 pack8(const f32x16& a, int s) { return (u32x4){pk(a[8 * s], a[8 * s + 1]), pk(a[8 * s + 2], a[8 * s + 3]), pk(a[8 * s + 4], a[8 * s + 5]), pk(a[8 * s + 6], a[8 * s + 7])}; }
constexpr int SC1 = 16;
DEVI u32x4 gld(__amdgpu_buffer_rsrc_t rs, unsigned voff, unsigned soff) { return __builtin_amdgcn_raw_buffer_load_b128(rs, voff, soff, SC1); }
DEVI u32x2 gld8(__amdgpu_buffer_rsrc_t rs, unsigned voff, unsigned soff) { return __builtin_amdgcn_raw_buffer_load_b64(rs, voff, soff, SC1); }
DEVI void gst(__amdgpu_buffer_rsrc_t rs, u32x4 v, unsigned voff, unsigned soff) { __builtin_amdgcn_raw_buffer_store_b128(v, rs, voff + soff, 0, SC1); }
DEVI void gst8(__amdgpu_buffer_rsrc_t rs, u32x2 v, unsigned voff, unsigned soff) { __builtin_amdgcn_raw_buffer_store_b64(v, rs, voff, soff, SC1); }
#define WAIT_VM(N) asm volatile("s_waitcnt vmcnt(" #N ")" ::: "memory")
#define WAIT_LGKM() asm volatile("s_waitcnt lgkmcnt(0)" ::: "memory")
#define WAIT_BAR(N) asm volatile("s_waitcnt vmcnt(" #N ") lgkmcnt(0)\n\ts_barrier" ::: "memory")
#define LBAR() asm volatile("s_waitcnt lgkmcnt(0)\n\ts_barrier" ::: "memory")
#define SB() __builtin_amdgcn_sched_barrier(0)
#define PIN(x) asm volatile("" : "+v"(x))
DEVI float fma1(float a, float b, float c) { return __builtin_fmaf(a, b, c); }
DEVI float mul1(float a, float b) { return a * b; }
DEVI float max3(float a, float b, float c) { return __builtin_fmaxf(__builtin_fmaxf(a, b), c); }
template <int... Is, class F> DEVI void sfor_impl(std::integer_sequence<int, Is...>, F&& f) { (f(std::integral_constant<int, Is>{}), ...); }
template <int N, class F> DEVI void sfor(F&& f) { sfor_impl(std::make_integer_sequence<int, N>{}, f); }
#define AI __attribute__((always_inline))
#define IC(n) std::integral_constant<int, n>{}

template <int NT = 0> DEVI void dma1(const char* sbase, unsigned voff, unsigned lds) {
    if (NT) asm volatile("s_mov_b32 m0, %2\n\ts_nop 0\n\tglobal_load_lds_dwordx4 %0, %1 nt" :: "v"(voff), "s"(sbase), "s"(lds) : "memory");
    else asm volatile("s_mov_b32 m0, %2\n\ts_nop 0\n\tglobal_load_lds_dwordx4 %0, %1" :: "v"(voff), "s"(sbase), "s"(lds) : "memory");
}
DEVI void touch(const char* sbase, unsigned voff, unsigned lds) {
    asm volatile("s_mov_b32 m0, %2\n\ts_nop 0\n\tglobal_load_lds_dword %0, %1" :: "v"(voff), "s"(sbase), "s"(lds) : "memory");
}
struct WSrc { const char* base; unsigned step, voff; };
DEVI void wpiece(const WSrc& s, unsigned lds, int n) { dma1<NT_W>(s.base + (size_t)n * s.step, s.voff, lds + n * 1024); }
DEVI WSrc wsrc_a(const char* base, unsigned rs, int nt, int lane) { const int t = (lane >> 4) < nt ? (lane >> 4) : nt - 1; return WSrc{base, 4 * rs, ((lane >> 2) & 3) * rs + t * 64 + (lane & 3) * 16}; }
DEVI WSrc wsrc_b(const char* base, unsigned rs, int lane) { return WSrc{base, 8 * rs, (4 * (lane >> 5) + ((lane >> 2) & 3)) * rs + ((lane >> 4) & 1) * 64 + (lane & 3) * 16}; }

DEVI void poll_word(const GAS unsigned* p, unsigned e) {
    unsigned a = __hip_atomic_load(p, __ATOMIC_RELAXED, __HIP_MEMORY_SCOPE_AGENT);
#pragma nounroll
    for (unsigned sp = 0; sp < SPIN_CAP; ++sp) {
        __builtin_amdgcn_s_sleep(POLL_GAP);
        const unsigned b2 = __hip_atomic_load(p, __ATOMIC_RELAXED, __HIP_MEMORY_SCOPE_AGENT);
        if (wall(a == e)) break;
        __builtin_amdgcn_s_sleep(POLL_GAP);
        a = __hip_atomic_load(p, __ATOMIC_RELAXED, __HIP_MEMORY_SCOPE_AGENT);
        if (wall(b2 == e)) break;
    }
    __builtin_amdgcn_fence(__ATOMIC_ACQUIRE, "wavefront");
}
DEVI void wait_flags(const char* ws, unsigned foff, int n, unsigned e, int lane) {
    const GAS unsigned* p = (const GAS unsigned*)(ws + foff) + (lane < n ? lane : 0) * 32;
    if (POLL2) poll_word(p, e);
    else {
#pragma nounroll
        for (unsigned sp = 0; sp < SPIN_CAP; ++sp) {
            unsigned v = __hip_atomic_load(p, __ATOMIC_RELAXED, __HIP_MEMORY_SCOPE_AGENT);
            if (wall(v == e)) break;
        }
        __builtin_amdgcn_fence(__ATOMIC_ACQUIRE, "wavefront");
    }
}
DEVI void set_flag(char* ws, unsigned foff, unsigned e) { __hip_atomic_store((GAS unsigned*)(ws + foff), e, __ATOMIC_RELAXED, __HIP_MEMORY_SCOPE_AGENT); }
DEVI unsigned flag_off(int ph, int idx) { return WS_FLAG + (ph * 256 + idx) * 128; }

constexpr float GC0 = -2.f * 0.7978845608028654f * 1.4426950408889634f, GC1 = GC0 * 0.044715f;
DEVI float gelu_t(float x) {
    float z = x * __builtin_fmaf(x * x, GC1, GC0);
    return x * __builtin_amdgcn_rcpf(1.f + __builtin_amdgcn_exp2f(z));
}
struct GeluK { float nmean, k1, k0, irs; };
DEVI void gelu_step(int o, float& y, float& z, float c, const GeluK& g) {
    if (o == 0) y = fma1(g.nmean, c, y);
    else if (o == 1) z = mul1(y, y);
    else if (o == 2) z = fma1(z, g.k1, g.k0);
    else if (o == 3) z = mul1(y, z);
    else if (o == 4) z = __builtin_amdgcn_exp2f(z);
    else if (o == 5) z = fma1(z, g.irs, g.irs);
    else if (o == 6) z = __builtin_amdgcn_rcpf(z);
    else y = mul1(y, z);
}

#define STAMP(k) do {} while (0)
#define STAMP_DECL
#define STAMP_FLUSH() do {} while (0)

#define LANE_GEOM() int lane_ = threadIdx.x & 63; asm volatile("" : "+v"(lane_)); const int lane = lane_, r32 = lane & 31, hh = lane >> 5; \
    const int trq = (lane & 15) >> 2, trQ_ = ((lane >> 4) & 1) * 4 + (lane & 3), gq = (trQ_ & 4) | ((trQ_ & 1) << 1) | ((trQ_ >> 1) & 1); \
    const unsigned vx = (hh * XR + r32) * 16; (void)trq; (void)gq; (void)vx; (void)r32; (void)hh
constexpr unsigned L_FLG = L_MISC + 1920, F_RDY = 0, F_DONE = 16;
DEVI unsigned ldsw_ld(unsigned i) { return *(volatile LAS unsigned*)(uintptr_t)(L_FLG + 4 * i); }
DEVI u32x2 ldsw_ld2(unsigned i) { return *(volatile LAS u32x2*)(uintptr_t)(L_FLG + 4 * i); }
DEVI void ldsw_st(unsigned i, unsigned v) { *(volatile LAS unsigned*)(uintptr_t)(L_FLG + 4 * i) = v; }
DEVI void ring_wait(int c) {
#pragma nounroll
    for (unsigned sp = 0; sp < SPIN_CAP * 16u; ++sp) { const u32x2 v = ldsw_ld2(F_RDY + 2 * c); if (__builtin_amdgcn_readfirstlane(v.x & v.y)) break; __builtin_amdgcn_s_sleep(1); }
}
DEVI void slot_wait(unsigned n, int lane) {
#pragma nounroll
    for (unsigned sp = 0; sp < SPIN_CAP * 16u; ++sp) { const unsigned v = ldsw_ld(F_DONE + (lane < 6 ? lane : 0)); if (wall(v >= n)) break; __builtin_amdgcn_s_sleep(1); }
}
#define LB(N) asm volatile("s_waitcnt vmcnt(" #N ")\n\ts_barrier" ::: "memory")
#define BAR() asm volatile("s_barrier" ::: "memory")

__global__ __launch_bounds__(512, 2) void fused_fwd(Params P) {
    __shared__ __attribute__((aligned(16))) char smem[163840];
    const unsigned L0 = (unsigned)(uintptr_t)(LAS char*)smem;
    const int tid = threadIdx.x, wv = __builtin_amdgcn_readfirstlane(tid >> 6), w = wv & 3;
    const int b = blockIdx.x, xcd = b & 7, slot = b >> 3;
    const int m = 2 * xcd + (slot >> 4), h = slot & 15;
    const int g = slot & 3, nt = g < 2 ? 4 : 3, tb = g < 2 ? 4 * g : 3 * g + 2;
    const int ti = 8 * xcd + (slot >> 2);
    const int s16 = slot & 15, pr = s16 >> 1;
    const int nt6 = ((s16 ^ pr) & 1) ? 3 : 4, tb6 = 7 * pr + ((s16 & 1) ? (pr & 1 ? 3 : 4) : 0);
    const bool act7 = slot < 28;
    const int j7 = 4 * xcd + (act7 ? slot / 7 : 0), g7 = act7 ? slot % 7 : 0;
    const unsigned wst = L0 + w * L_WST;
    const unsigned shift = (56 * h) & 15u;
    STAMP_DECL

    if (wv >= 4) {
        if (PRIO_L) __builtin_amdgcn_s_setprio(PRIO_L);
        LANE_GEOM();
        const unsigned wrd = 16384 + hh * 6 * 256 + trq * 64 + ((shift + gq * 8) & 63);
        LAS float* csp = (LAS float*)(uintptr_t)(L0 + L_MISC);
        if (w < 2) {
            const unsigned xoffE = (lane >> 3) * 896 + (((lane & 7) ^ (lane >> 4)) << 4);
            const unsigned xoffO = (lane >> 3) * 896 + (((lane & 7) ^ (4 + (lane >> 4))) << 4);
            const unsigned colb = (56 * h) & ~15u;
            unsigned woff[3];
#pragma unroll
            for (int j = 0; j < 3; ++j) {
                const int bl = 4 * j + (lane >> 4), kql = bl / 3, tile = bl % 3;
                woff[j] = (4 * kql + ((lane >> 2) & 3)) * 2688 + tile * 896 + colb + (lane & 3) * 16;
            }
            const char* xb = (const char*)P.x + (size_t)(128 * m + 64 * w) * 896;
            const char* wb = (const char*)P.wqkv + (size_t)(32 * w) * 2688;
            LBAR();
#pragma unroll 1
            for (int kc = 0; kc < 7; ++kc) {
                if (kc >= 5) slot_wait(kc - 4, lane);
                const unsigned base = L0 + (kc >= 5 ? kc - 5 : kc) * L_SLOT;
#pragma unroll
                for (int i = 0; i < 6; ++i) dma1<NT_QKV>(wb + (size_t)kc * 64 * 2688 + (i / 3) * 16 * 2688, woff[i % 3], base + 16384 + (6 * w + i) * 1024);
#pragma unroll
                for (int i = 0; i < 8; ++i) dma1<NT_X>(xb + kc * 128 + i * 8 * 896, (i & 1) ? xoffO : xoffE, base + (8 * w + i) * 1024);
                if (kc > 0) { WAIT_VM(14); ldsw_st(F_RDY + 2 * (kc - 1) + w, 1); }
            }
            WAIT_VM(0); ldsw_st(F_RDY + 12 + w, 1);
            {
                ring_wait(6);
                const unsigned a = L0 + L_SLOT + wrd + w * 3072;
#pragma unroll
                for (int t = 0; t < 3; ++t) { const u32x4 f = wfrag(a + t * 256, a + 768 + t * 256);
                    const float v = hsum((dot2(f[0], ONES, 0.f) + dot2(f[1], ONES, 0.f)) + (dot2(f[2], ONES, 0.f) + dot2(f[3], ONES, 0.f)));
                    if (hh == 0) csp[(2 + w) * 96 + t * 32 + r32] = v; }
            }
        } else {
            if (w == 2 && EARLY_CODE) {
                unsigned long long pc; unsigned back;
                asm volatile("s_getpc_b64 %0\nmk_code_here0:\n\ts_mov_b32 %1, mk_code_here0-_ZN2mk9fused_fwdENS_6ParamsE" : "=s"(pc), "=s"(back));
                const unsigned oc = (slot * 18 + (lane < 18 ? lane : 0)) * 128;
                const char* t0 = (const char*)(pc - back) + (oc < CODE_TOUCH_BYTES ? oc : 0);
                asm volatile("s_mov_b32 m0, %1\n\ts_nop 0\n\tglobal_load_lds_dword %0, off" :: "v"(t0), "s"(L0 + L_MISC + 2048) : "memory");
            }
            const int srow = 128 * m + 8 * h + 4 * (w - 2) + (lane >> 4);
            const char* sp = (const char*)P.x + (size_t)srow * 896 + (lane & 15) * 16;
            u32x4 sr[4];
            asm volatile("global_load_dwordx4 %0, %4, off\n\tglobal_load_dwordx4 %1, %4, off offset:256\n\tglobal_load_dwordx4 %2, %4, off offset:512\n\tglobal_load_dwordx4 %3, %5, off"
                         : "=&v"(sr[0]), "=&v"(sr[1]), "=&v"(sr[2]), "=&v"(sr[3]) : "v"(sp), "v"(sp + ((lane & 15) < 8 ? 768 : 0)) : "memory");
            if (w == 2 && lane < 32) ldsw_st(lane, 0);
            LBAR();
            {
                const unsigned lx = lane < 56 ? lane : 0, lw = b * 37 + (lane < 37 ? lane : 0);
                const unsigned fl = lane - 56;
                const int fidx = fl < 2 || fl == 5 ? m * 16 + h : ti * 4 + g;
                const char* tf = fl < 6 ? P.ws + flag_off(fl, fidx) : P.ws + WS_ST1 + (128 * m + 8 * h) * 16;
                const char* tp = !EARLY_TOUCH ? P.ws + WS_SPARE : w == 2 ? ((TOUCH_FLAGS && lane >= 56 && lane < 63) ? tf : (const char*)P.x + (size_t)(8 * b + lx / 7) * 896 + (lx % 7) * 128)
                                                                       : (const char*)P.wqkv + (size_t)(lw < 9408 ? lw : 0) * 128;
                if (TOUCH_DELAY) __builtin_amdgcn_s_sleep(TOUCH_DELAY);
                asm volatile("s_mov_b32 m0, %1\n\ts_nop 0\n\tglobal_load_lds_dword %0, off" :: "v"(tp), "s"(L0 + L_MISC + 2048) : "memory");
            }
            {
                asm volatile("s_waitcnt vmcnt(1)" : "+v"(sr[0]), "+v"(sr[1]), "+v"(sr[2]), "+v"(sr[3]) :: "memory");
                if ((lane & 15) >= 8) sr[3] = (u32x4){0u, 0u, 0u, 0u};
                float t1 = 0.f, t2 = 0.f;
#pragma unroll
                for (int q = 0; q < 4; ++q)
#pragma unroll
                    for (int i = 0; i < 4; ++i) { t1 = dot2(sr[q][i], ONES, t1); t2 = dot2(sr[q][i], sr[q][i], t2); }
                t1 += xor32<1>(t1); t2 += xor32<1>(t2); t1 += xor32<2>(t1); t2 += xor32<2>(t2); t1 += xor32<4>(t1); t2 += xor32<4>(t2); t1 += xor32<8>(t1); t2 += xor32<8>(t2);
                const float mean = t1 * (1.f / D), var = __builtin_fmaxf(t2 * (1.f / D) - mean * mean, 0.f);
                const unsigned e = *(const unsigned*)(P.ws + WS_EP) + 1u;
                const __amdgpu_buffer_rsrc_t rs = __builtin_amdgcn_make_buffer_rsrc((void*)P.ws, 0, WS_END, 0x00020000);
                if ((lane & 15) == 0) gst(rs, (u32x4){f2u(mean), e, f2u(__builtin_amdgcn_rsqf(var + EPS)), e}, srow * 16, WS_ST1);
            }
            float cs[3][4] = {};
            const unsigned wrd2 = wrd + (w - 2) * 2 * 3072;
#pragma unroll 1
            for (int c = 0; c < 7; ++c) {
                ring_wait(c);
                if (c == LATE_TOUCH) {
                    unsigned long long pc; unsigned back;
                    asm volatile("s_getpc_b64 %0\nmk_code_here:\n\ts_mov_b32 %1, mk_code_here-_ZN2mk9fused_fwdENS_6ParamsE" : "=s"(pc), "=s"(back));
                    constexpr unsigned len = CODE_TOUCH_BYTES;
                    const unsigned oc = (slot * 18 + (lane < 18 ? lane : 0)) * 128, o3 = (b * 64 + lane) * 128, o2 = (b * 16 + (lane & 15)) * 128;
                    const char* t0 = w == 2 ? (const char*)(pc - back) + (oc < len && !EARLY_CODE ? oc : 0) : (const char*)P.w3 + (o3 < 1605632 ? o3 : 0);
                    const char* t1 = w == 2 ? (lane < 16 ? (const char*)P.wo : lane < 32 ? (const char*)P.w1 : (const char*)P.w2) + (o2 < 401408 ? o2 : 0) : (const char*)P.w4 + (o3 < 1605632 ? o3 : 0);
                    asm volatile("s_mov_b32 m0, %2\n\ts_nop 0\n\tglobal_load_lds_dword %0, off\n\tglobal_load_lds_dword %1, off" :: "v"(t0), "v"(t1), "s"(L0 + L_MISC + 2048) : "memory");
                }
                const unsigned a = L0 + (c >= 5 ? c - 5 : c) * L_SLOT + (c < 6 ? wrd2 : wrd + w * 3072);
#pragma unroll
                for (int t = 0; t < 3; ++t) { const u32x4 f = wfrag(a + t * 256, a + 768 + t * 256);
#pragma unroll
                    for (int i = 0; i < 4; ++i) cs[t][i] = dot2(f[i], ONES, cs[t][i]); }
                if (c < 6) {
#pragma unroll
                    for (int t = 0; t < 3; ++t) { const u32x4 f = wfrag(a + 3072 + t * 256, a + 3072 + 768 + t * 256);
#pragma unroll
                        for (int i = 0; i < 4; ++i) cs[t][i] = dot2(f[i], ONES, cs[t][i]); }
                }
                WAIT_LGKM();
                ldsw_st(F_DONE + 2 + w, c + 1);
            }
#pragma unroll
            for (int t = 0; t < 3; ++t) { const float v = hsum((cs[t][0] + cs[t][1]) + (cs[t][2] + cs[t][3])); if (hh == 0) csp[(w - 2) * 96 + t * 32 + r32] = v; }
        }
        LBAR();
        auto stage = [&](const WSrc& s, unsigned lds, int n0, int n1) AI {
#pragma unroll 1
            for (int n = n0; n < n1; ++n) wpiece(s, lds, n);
        };
        LB(0);
        BAR();
        stage(wsrc_a((const char*)P.wo + (size_t)(112 * w) * 896 + tb * 64, 896, nt, lane), wst, 0, 28);
        LB(0);
        { const WSrc s_ = wsrc_a((const char*)P.w1 + (size_t)(112 * w) * 896 + tb * 64, 896, nt, lane); BAR(); stage(s_, wst, 0, HALF); BAR(); stage(s_, wst, HALF, 28); BAR(); LB(0); }
        { const WSrc s_ = wsrc_a((const char*)P.w2 + (size_t)(112 * w) * 896 + tb * 64, 896, nt, lane); BAR(); stage(s_, wst, 0, HALF); BAR(); stage(s_, wst, HALF, 28); BAR(); LB(0); }
        { const WSrc s_ = wsrc_a((const char*)P.w3 + (size_t)(112 * w) * 3584 + tb6 * 64, 3584, nt6, lane); BAR(); stage(s_, wst, 0, HALF); BAR(); stage(s_, wst, HALF, 28); BAR(); LB(0); }
        {
            float c[4] = {0.f, 0.f, 0.f, 0.f};
            const unsigned a0 = L0 + trq * 64 + gq * 8 + 2 * hh * 1024 + w * 256;
#pragma unroll 4
            for (int k = 0; k < 28; ++k) { const unsigned a = a0 + k * 4096; const u32x4 f = wfrag(a, a + 1024);
#pragma unroll
                for (int i = 0; i < 4; ++i) c[i] = dot2(f[i], ONES, c[i]); }
            const float cc = hsum((c[0] + c[1]) + (c[2] + c[3]));
            if (hh == 0) ((LAS float*)(uintptr_t)(L0 + L_M2 + 1024))[w * 32 + r32] = cc;
        }
        if (b == 0 && w == 3) {
            const unsigned e = *(const volatile GAS unsigned*)(P.ws + WS_EP) + 1u;
            const GAS unsigned* p = (const GAS unsigned*)(P.ws + flag_off(0, lane));
#pragma nounroll
            for (unsigned sp = 0; sp < SPIN_CAP; ++sp) {
                const unsigned v0 = __hip_atomic_load(p, __ATOMIC_RELAXED, __HIP_MEMORY_SCOPE_AGENT), v1 = __hip_atomic_load(p + 64 * 32, __ATOMIC_RELAXED, __HIP_MEMORY_SCOPE_AGENT);
                const unsigned v2 = __hip_atomic_load(p + 128 * 32, __ATOMIC_RELAXED, __HIP_MEMORY_SCOPE_AGENT), v3 = __hip_atomic_load(p + 192 * 32, __ATOMIC_RELAXED, __HIP_MEMORY_SCOPE_AGENT);
                if (wall(v0 == e && v1 == e && v2 == e && v3 == e)) break;
            }
            if (lane == 0) __hip_atomic_store((GAS unsigned*)(P.ws + WS_EP), e, __ATOMIC_RELAXED, __HIP_MEMORY_SCOPE_AGENT);
        }
        LBAR();
        const WSrc w4s = wsrc_b((const char*)P.w4 + (size_t)(448 * w) * 896 + g7 * 128, 896, lane);
        if (act7) { stage(w4s, L0 + L_X + w * 8192, 0, 8); stage(w4s, wst - 8 * 1024, 8, W4SPLIT); }
        BAR();
        if (act7) stage(w4s, wst - 8 * 1024, W4SPLIT, 36);
        LB(0);
        if (act7) {
            BAR();
            stage(w4s, wst - 36 * 1024, 36, 56);
            LB(0);
            BAR();
        }
        STAMP_FLUSH();
        return;
    }

    if (PRIO_C) __builtin_amdgcn_s_setprio(PRIO_C);
    const unsigned e = *(const unsigned*)(P.ws + WS_EP) + 1u;
    const __amdgpu_buffer_rsrc_t rs = __builtin_amdgcn_make_buffer_rsrc((void*)P.ws, 0, WS_END, 0x00020000);
    u32x4 qf0, qf1;
    {
        LANE_GEOM();
        const unsigned wrd = 16384 + hh * 6 * 256 + trq * 64 + ((shift + gq * 8) & 63);
        STAMP(15);
        f32x16 aq = {}, ak = {}, av = {};
        const unsigned xrd = (32 * w + r32) * 128 + ((hh ^ ((r32 >> 1) & 7)) << 4);
        struct Fr { u32x4 x, q, k, v; };
        Fr F[4];
        unsigned xb[3][4], wb3[3];
#pragma unroll
        for (int gsl = 0; gsl < 3; ++gsl) {
#pragma unroll
            for (int j = 0; j < 4; ++j) { xb[gsl][j] = L0 + (xrd ^ (j << 5)) + gsl * 2 * L_SLOT; PIN(xb[gsl][j]); }
            wb3[gsl] = L0 + wrd + gsl * 2 * L_SLOT; PIN(wb3[gsl]);
        }
        auto ldx = [&](int sl, int j) AI { F[j].x = lds128(xb[sl / 2][j] + (sl & 1) * L_SLOT); const unsigned a = wb3[sl / 2] + (sl & 1) * L_SLOT + j * 3072; F[j].q = wfrag(a, a + 768); };
        auto ldk = [&](int sl, int j) AI { const unsigned a = wb3[sl / 2] + (sl & 1) * L_SLOT + j * 3072; F[j].k = wfrag(a + 256, a + 1024); };
        auto ldv = [&](int sl, int j) AI { const unsigned a = wb3[sl / 2] + (sl & 1) * L_SLOT + j * 3072; F[j].v = wfrag(a + 512, a + 1280); };
        LAS float* stw = (LAS float*)(uintptr_t)(L0 + L_MISC + 3072 + w * 256);
        BAR();
        ring_wait(0);
        STAMP(16);
#pragma unroll
        for (int j = 0; j < 2; ++j) { ldx(0, j); ldk(0, j); ldv(0, j); }
        SB();
        u32x4 gv = {0u, 0u, 0u, 0u};
        const unsigned gvo = (128 * m + 32 * w + r32) * 16;
        sfor<7>([&](auto cc) AI {
            constexpr int c = decltype(cc)::value, sl = c >= 5 ? c - 5 : c, nsl = c >= 4 ? c - 4 : c + 1;
            u32x2 rdy;
            sfor<4>([&](auto jc) AI {
                constexpr int j = decltype(jc)::value, jn = (j + 2) & 3, lb = j < 2 ? sl : nsl;
                constexpr bool ld = j < 2 || c < 6;
                if constexpr (j == 2 && c < 6) { if (__builtin_amdgcn_readfirstlane(rdy.x & rdy.y) == 0) ring_wait(c + 1); SB(); }
                MFMA(aq, F[j].q, F[j].x, aq);
                if constexpr (ld) ldx(lb, jn);
                SB();
                MFMA(ak, F[j].k, F[j].x, ak);
                if constexpr (ld) ldk(lb, jn);
                if constexpr (j == 0 && c >= 1 && c <= 2) ldsw_st(F_DONE + w, c);
                if constexpr (j == 0 && c == 4) gv = gld(rs, gvo, WS_ST1);
                SB();
                MFMA(av, F[j].x, F[j].v, av);
                if constexpr (ld) ldv(lb, jn);
                if constexpr (j == 0 && c < 6) rdy = ldsw_ld2(F_RDY + 2 * (c + 1));
                SB();
            });
        });
#pragma nounroll
        for (unsigned sp2 = 0; sp2 < SPIN_CAP && !wall(gv.y == e && gv.w == e); ++sp2) { asm volatile("" ::: "memory"); gv = gld(rs, gvo, WS_ST1); }
        if (hh == 0) { stw[r32] = u2f(gv.x) * u2f(gv.z); stw[32 + r32] = u2f(gv.z); }
        STAMP(1);
        const float mean = u2f(gv.x), rstd = u2f(gv.z);
        LBAR();
        STAMP(17);
        const LAS float* csf = (const LAS float*)(uintptr_t)(L0 + L_MISC);
        const float cv = (csf[64 + r32] + csf[96 + 64 + r32]) + (csf[192 + 64 + r32] + csf[288 + 64 + r32]);
        const float qs = rstd * (0.18898223650461362f * 1.4426950408889634f);
#pragma unroll
        for (int q = 0; q < 4; ++q) {
            auto c4 = [&](int o) AI { return (*(const LAS f32x4*)(csf + o) + *(const LAS f32x4*)(csf + 96 + o)) + (*(const LAS f32x4*)(csf + 192 + o) + *(const LAS f32x4*)(csf + 288 + o)); };
            const f32x4 c1 = c4(8 * q + 4 * hh), c2 = c4(32 + 8 * q + 4 * hh);
            const f32x4 mr = *(const LAS f32x4*)(stw + 8 * q + 4 * hh), rr = *(const LAS f32x4*)(stw + 32 + 8 * q + 4 * hh);
#pragma unroll
            for (int i = 0; i < 4; ++i) {
                aq[4 * q + i] = (aq[4 * q + i] - mean * c1[i]) * qs;
                ak[4 * q + i] = (ak[4 * q + i] - mean * c2[i]) * rstd;
                av[4 * q + i] = av[4 * q + i] * rr[i] - mr[i] * cv;
            }
        }
        if (hh == 1) { aq[12] = 1.f; aq[13] = 0.f; aq[14] = 0.f; aq[15] = 0.f; ak[12] = 0.f; ak[13] = 0.f; ak[14] = 0.f; ak[15] = 0.f; }
        if (r32 >= 28) {
            const float pv = r32 == 28 ? 1.f : 0.f;
#pragma unroll
            for (int i = 0; i < 16; ++i) av[i] = pv;
        }
        const u32x4 k0 = pack8(ak, 0), k1 = pack8(ak, 1), v0 = pack8(av, 0), v1 = pack8(av, 1);
        const unsigned go = WS_KV + h * KVH + (4 * m + w) * 4096;
        gst(rs, k0, lane * 16, go); gst(rs, k1, lane * 16, go + 1024); gst(rs, v0, lane * 16, go + 2048); gst(rs, v1, lane * 16, go + 3072);
        const unsigned lo = L0 + L_OWNKV + w * 4096 + lane * 16;
        sts128(lo, k0); sts128(lo + 1024, k1); sts128(lo + 2048, v0); sts128(lo + 3072, v1);
        qf0 = pack8(aq, 0); qf1 = pack8(aq, 1);
        STAMP(18);
        WAIT_BAR(0);
        if (tid == 0) set_flag(P.ws, flag_off(0, m * 16 + h), e);
        STAMP(2);
    }
    {
        LANE_GEOM();
        const int mt = m - 2 + (w >> 1);
        u32x4 kv[8];
        if (mt >= 0) {
            wait_flags(P.ws, flag_off(0, mt * 16 + h), 1, e, lane);
            const unsigned go = WS_KV + h * KVH + (4 * m - 8 + 2 * w) * 4096;
#pragma unroll
            for (int i = 0; i < 8; ++i) kv[i] = gld(rs, lane * 16, go + i * 1024);
        } else {
#pragma unroll
            for (int i = 0; i < 8; ++i) kv[i] = (u32x4){0u, 0u, (i & 3) == 1 && hh == 1 ? 0xF149u : 0u, 0u};
        }
        f32x16 cdiag, cold;
#pragma unroll
        for (int i = 0; i < 16; ++i) { const int kr = (i & 3) + 8 * (i >> 2) + 4 * hh; cdiag[i] = kr <= r32 ? 0.f : -1e30f; cold[i] = kr > r32 ? 0.f : -1e30f; }
#pragma unroll
        for (int i = 0; i < 8; ++i) sts128(L0 + L_KV + 2 * w * 4096 + i * 1024 + lane * 16, kv[i]);
        LBAR();
        STAMP(3);
        unsigned kvb = L0 + L_KV + w * 4096 + lane * 16;
        PIN(kvb);
        u32x4 kf[9][2], vf[9][2];
        const f32x16 zero16 = {};
        auto ldK = [&](int i) AI { kf[i][0] = lds128(kvb + i * 4096); kf[i][1] = lds128(kvb + i * 4096 + 1024); };
        auto ldV = [&](int i) AI { vf[i][0] = lds128(kvb + i * 4096 + 2048); vf[i][1] = lds128(kvb + i * 4096 + 3072); };
        auto smax = [&](const f32x16& s, float a) AI {
#pragma unroll
            for (int i = 0; i < 16; i += 2) a = max3(a, s[i], s[i + 1]);
            return a; };
        ldK(0); ldK(1);
        float mx = -3e38f;
        f32x16 s[2];
        auto smax8 = [&](const f32x16& v, int o, float a) AI {
#pragma unroll
            for (int i = 0; i < 8; i += 2) a = max3(a, v[o + i], v[o + i + 1]);
            return a; };
        sfor<9>([&](auto ic) AI {
            constexpr int i = decltype(ic)::value;
            if constexpr (i + 2 < 9) ldK(i + 2);
            MFMA(s[i & 1], kf[i][0], qf0, i == 0 ? cold : i == 8 ? cdiag : zero16);
            SB();
            MFMA(s[i & 1], kf[i][1], qf1, s[i & 1]);
            SB();
            if constexpr (i > 0) { mx = smax8(s[(i - 1) & 1], 0, mx); mx = smax8(s[(i - 1) & 1], 8, mx); PIN(mx); }
            SB();
        });
        ldK(0); ldK(1); ldV(0);
        mx = hmax(smax(s[0], mx));
        STAMP(25);
        f32x16 o = {}, cm;
#pragma unroll
        for (int i = 0; i < 16; ++i) { cm[i] = -mx; cdiag[i] -= mx; cold[i] -= mx; }
        MFMA(s[0], kf[0][0], qf0, cold); MFMA(s[0], kf[0][1], qf1, s[0]);
        SB();
        sfor<9>([&](auto ic) AI {
            constexpr int i = decltype(ic)::value;
            f32x16& c = s[i & 1];
            f32x16& n = s[(i + 1) & 1];
            if constexpr (i + 2 < 9) ldK(i + 2);
            if constexpr (i + 1 < 9) ldV(i + 1);
            if constexpr (i + 1 < 9) MFMA(n, kf[i + 1][0], qf0, i + 1 == 8 ? cdiag : cm);
#pragma unroll
            for (int j = 0; j < 8; ++j) c[j] = __builtin_amdgcn_exp2f(c[j]);
            u32x4 p0 = pack8(c, 0);
            PIN(p0);
            SB();
            if constexpr (i + 1 < 9) MFMA(n, kf[i + 1][1], qf1, n);
            MFMA(o, vf[i][0], p0, o);
#pragma unroll
            for (int j = 8; j < 16; ++j) c[j] = __builtin_amdgcn_exp2f(c[j]);
            const u32x4 p1 = pack8(c, 1);
            MFMA(o, vf[i][1], p1, o);
            SB();
        });
        STAMP(26);
        auto lr = __builtin_amdgcn_permlane32_swap(f2u(o[12]), f2u(o[12]), false, false);
        const float inv = __builtin_amdgcn_rcpf(u2f(lr[1]));
#pragma unroll
        for (int i = 0; i < 16; ++i) o[i] *= inv;
        if (hh == 1) { o[12] = 0.f; o[13] = 0.f; o[14] = 0.f; o[15] = 0.f; }
        const unsigned vo = (hh * XR + r32) * 16, so = WS_O + ((4 * h) * XR + 128 * m + 32 * w) * 16;
        gst(rs, pack8(o, 0), vo, so); gst(rs, pack8(o, 1), vo, so + 2 * XROW);
        WAIT_BAR(0);
        if (tid == 0) set_flag(P.ws, flag_off(1, m * 16 + h), e);
        STAMP(4);
    }
    const bool act = w < nt;
    const int tw = tb + w;
#define GEMM_GEOM() LANE_GEOM(); \
    unsigned trW[2]; trW[0] = L0 + (w < nt ? w : nt - 1) * 256 + trq * 64 + gq * 8 + 2 * hh * 1024; trW[1] = trW[0] + 57344; PIN(trW[0]); PIN(trW[1]); \
    auto wimg = [&](int kq0, int kq1) AI { return wfrag(kq0 < 56 ? trW[0] + kq0 * 1024 : trW[1] + (kq0 - 56) * 1024, kq1 < 56 ? trW[0] + kq1 * 1024 : trW[1] + (kq1 - 56) * 1024); }; \
    unsigned xl = L0 + L_X + lane * 16; PIN(xl); \
    LAS float* m2 = (LAS float*)(uintptr_t)(L0 + L_M2 + w * 256); (void)m2
    auto gemm = [&](auto nk, const u32x4* wr, unsigned xl) AI -> f32x16 {
        constexpr int NK = decltype(nk)::value, PD = 4;
        f32x16 acc = {};
        u32x4 xf[NK];
#pragma unroll
        for (int k = 0; k < PD; ++k) xf[k] = lds128(xl + k * 1024);
        SB();
        sfor<NK>([&](auto kc) AI {
            constexpr int k = decltype(kc)::value;
            if constexpr (k + PD < NK) xf[k + PD] = lds128(xl + (k + PD) * 1024);
            MFMA(acc, wr[k], xf[k], acc);
            SB();
        });
        return acc;
    };
    u32x4 hp[2];
    float meanH, rstdH;
    {
        GEMM_GEOM();
        u32x4 xr[2];
        const char* xp = (const char*)P.x + (size_t)(32 * ti + r32) * 896 + 64 * (act ? tw : tb) + 16 * hh;
        xr[0] = *(const GAS u32x4*)xp; xr[1] = *(const GAS u32x4*)(xp + 32);
        u32x4 wr[32];
        sfor<32>([&](auto kc) AI { constexpr int k = decltype(kc)::value, kq = 7 * (k >> 1) + 4 * (k & 1);
            if constexpr (k < 31) wr[k] = wimg(kq, kq + 1);
            else { const u32x2 p = ldstr(trW[1] + (kq - 56) * 1024), q = ldstr(trW[1] + (kq - 56) * 1024 + (hh ? 0 : 1024)); wr[k] = (u32x4){p.x, p.y, q.x, q.y}; } });
        LBAR();
        STAMP(19);
        wait_flags(P.ws, flag_off(1, (ti >> 2) * 16 + 4 * w), 4, e, lane);
        STAMP(20);
        u32x4 xf[8];
#pragma unroll
        for (int k = 0; k < 8; ++k) xf[k] = gld(rs, vx, WS_O + ((16 * w + 2 * k) * XR + 32 * ti) * 16);
#pragma unroll
        for (int k = 0; k < 8; ++k) sts128(xl + (8 * w + k) * 1024, xf[k]);
        LBAR();
        STAMP(5);
        f32x16 r = gemm(IC(32), wr, xl);
        STAMP(21);
        float s1 = 0.f, s2 = 0.f;
#pragma unroll
        for (int s = 0; s < 2; ++s) {
#pragma unroll
            for (int i = 0; i < 4; ++i) {
                hp[s][i] = pk(r[8 * s + 2 * i] + blo(xr[s][i]), r[8 * s + 2 * i + 1] + bhi(xr[s][i]));
                s1 = dot2(hp[s][i], ONES, s1); s2 = dot2(hp[s][i], hp[s][i], s2);
            }
            if (act) gst(rs, hp[s], vx, WS_H + ((4 * tw + 2 * s) * XR + 32 * ti) * 16);
        }
        s1 = hsum(s1); s2 = hsum(s2);
        if (act && hh == 0) gst8(rs, (u32x2){f2u(s1), f2u(s2)}, r32 * 8, WS_STH + (tw * 2048 + 32 * ti) * 8);
        STAMP(22);
        WAIT_BAR(0);
        if (tid == 0) set_flag(P.ws, flag_off(2, ti * 4 + g), e);
        STAMP(6);
    }
#define LOAD_WR() u32x4 wr[28]; sfor<28>([&](auto kc) AI { constexpr int k = decltype(kc)::value; wr[k] = wimg(4 * k, 4 * k + 1); })
    const int fsn = w == 0 ? 1 : 2, fsl = w <= 1 ? 0 : w - 1;
    {
        GEMM_GEOM();
        BAR();
        LOAD_WR();
        LBAR();
        wait_flags(P.ws, flag_off(2, ti * 4), 4, e, lane);
        u32x4 xf[7];
#pragma unroll
        for (int k = 0; k < 7; ++k) xf[k] = gld(rs, vx, WS_H + ((14 * w + 2 * k) * XR + 32 * ti) * 16);
        u32x2 sv[7];
#pragma unroll
        for (int q = 0; q < 7; ++q) sv[q] = gld8(rs, (7 * hh * 2048 + r32) * 8, WS_STH + (q * 2048 + 32 * ti) * 8);
        SB();
        {
            float cs[4] = {0.f, 0.f, 0.f, 0.f};
#pragma unroll
            for (int k = 0; k < 28; ++k)
#pragma unroll
                for (int i = 0; i < 4; ++i) cs[i] = dot2(wr[k][i], ONES, cs[i]);
            const float c = hsum((cs[0] + cs[1]) + (cs[2] + cs[3]));
            if (hh == 0) m2[r32] = c;
        }
        SB();
#pragma unroll
        for (int k = 0; k < 7; ++k) sts128(xl + (7 * w + k) * 1024, xf[k]);
        LBAR();
        STAMP(7);
        f32x16 r = gemm(IC(28), wr, xl);
        float t1 = 0.f, t2 = 0.f;
#pragma unroll
        for (int q = 0; q < 7; ++q) { t1 += u2f(sv[q].x); t2 += u2f(sv[q].y); }
        t1 = hsum(t1); t2 = hsum(t2);
        meanH = t1 * (1.f / D);
        const float var = __builtin_fmaxf(t2 * (1.f / D) - meanH * meanH, 0.f);
        rstdH = __builtin_amdgcn_rsqf(var + EPS);
        const float sc = rstdH * __builtin_amdgcn_rsqf(var * rstdH * rstdH + EPS);
#pragma unroll
        for (int q = 0; q < 4; ++q) {
            const f32x4 c = *(const LAS f32x4*)(m2 + 8 * q + 4 * hh);
#pragma unroll
            for (int i = 0; i < 4; ++i) r[4 * q + i] = gelu_t((r[4 * q + i] - meanH * c[i]) * sc);
        }
#pragma unroll
        for (int s = 0; s < 2; ++s) if (act) gst(rs, pack8(r, s), vx, WS_HH + ((4 * tw + 2 * s) * XR + 32 * ti) * 16);
        WAIT_BAR(0);
        if (tid == 0) set_flag(P.ws, flag_off(3, ti * 4 + g), e);
        STAMP(8);
    }
    {
        GEMM_GEOM();
        BAR();
        LOAD_WR();
        LBAR();
        const int sh = w == 0 ? -1 : w == 1 ? 1 : w == 2 ? -2 : 2;
        const int tn = ti + (sh < 0 ? -1 : 1);
        {
            const bool nb = tn >= 0 && tn < 64;
            const int li = lane & 3;
            const GAS unsigned* p = (const GAS unsigned*)(P.ws + flag_off(3, (((li & 2) && nb) ? tn : ti) * 4 + fsl + ((li & 1) && fsn == 2 ? 1 : 0)));
            poll_word(p, e);
        }
        const int tok = 32 * ti + r32 + sh;
        const bool ok = tok >= 0 && tok < S;
        const unsigned vxs = ok ? (hh * XR + tok) * 16 : 0x7ffffff0u;
        u32x4 xf[7];
#pragma unroll
        for (int k = 0; k < 7; ++k) { xf[k] = gld(rs, vxs, WS_HH + (14 * w + 2 * k) * XROW); }
#pragma unroll
        for (int k = 0; k < 7; ++k) sts128(xl + (7 * w + k) * 1024, xf[k]);
        LBAR();
        STAMP(9);
        f32x16 r = gemm(IC(28), wr, xl);
        float s1 = 0.f, s2 = 0.f;
#pragma unroll
        for (int s = 0; s < 2; ++s) {
            u32x4 xp;
#pragma unroll
            for (int i = 0; i < 4; ++i) {
                xp[i] = pk(r[8 * s + 2 * i] + (blo(hp[s][i]) - meanH) * rstdH, r[8 * s + 2 * i + 1] + (bhi(hp[s][i]) - meanH) * rstdH);
                s1 = dot2(xp[i], ONES, s1); s2 = dot2(xp[i], xp[i], s2);
            }
            if (act) gst(rs, xp, vx, WS_X1 + ((4 * tw + 2 * s) * XR + 32 * ti) * 16);
        }
        s1 = hsum(s1); s2 = hsum(s2);
        if (act && hh == 0) gst8(rs, (u32x2){f2u(s1), f2u(s2)}, r32 * 8, WS_STX + (tw * 2048 + 32 * ti) * 8);
        WAIT_BAR(0);
        if (tid == 0) set_flag(P.ws, flag_off(4, ti * 4 + g), e);
        STAMP(10);
    }
    {
        LANE_GEOM();
        unsigned tr6[2];
        tr6[0] = L0 + trq * 64 + gq * 8 + 2 * hh * 1024; tr6[1] = tr6[0] + 57344; PIN(tr6[0]); PIN(tr6[1]);
        auto wf6 = [&](int t, int k) AI { const unsigned a = (k < 14 ? tr6[0] + k * 4096 : tr6[1] + (k - 14) * 4096) + t * 256; return wfrag(a, a + 1024); };
        const LAS float* cs6 = (const LAS float*)(uintptr_t)(L0 + L_M2 + 1024);
        wait_flags(P.ws, flag_off(4, (4 * m + w) * 4), 4, e, lane);
        STAMP(28);
        const int tk = 128 * m + 32 * w;
        auto ldx1 = [&](int k) AI { return gld(rs, vx, WS_X1 + (2 * k * XR + tk) * 16); };
        constexpr int PX = P6_PX, PW = 2;
        f32x16 acc[4] = {};
        u32x2 sv[7];
        {
            u32x4 xf[28], wf[28][4];
#pragma unroll
            for (int k = 0; k < PX; ++k) xf[k] = ldx1(k);
#pragma unroll
            for (int q = 0; q < 7; ++q) sv[q] = gld8(rs, (7 * hh * 2048 + r32) * 8, WS_STX + (q * 2048 + tk) * 8);
            BAR();
            STAMP(11);
#pragma unroll
            for (int k = 0; k < PW; ++k)
#pragma unroll
                for (int t = 0; t < 4; ++t) wf[k][t] = wf6(t, k);
            SB();
            sfor<28>([&](auto kc) AI {
                constexpr int k = decltype(kc)::value;
                MFMA(acc[0], wf[k][0], xf[k], acc[0]);
                if constexpr (k + PW < 28) { wf[k + PW][0] = wf6(0, k + PW); }
                SB();
                MFMA(acc[1], wf[k][1], xf[k], acc[1]);
                if constexpr (k + PW < 28) { wf[k + PW][1] = wf6(1, k + PW); }
                if constexpr (k + PX < 28) xf[k + PX] = ldx1(k + PX);
                SB();
                MFMA(acc[2], wf[k][2], xf[k], acc[2]);
                if constexpr (k + PW < 28) { wf[k + PW][2] = wf6(2, k + PW); }
                SB();
                MFMA(acc[3], wf[k][3], xf[k], acc[3]);
                if constexpr (k + PW < 28) { wf[k + PW][3] = wf6(3, k + PW); }
                SB();
            });
        }
        STAMP(23);
        LBAR();
        STAMP(24);
        float t1 = 0.f, t2 = 0.f;
#pragma unroll
        for (int q = 0; q < 7; ++q) { t1 += u2f(sv[q].x); t2 += u2f(sv[q].y); }
        t1 = hsum(t1); t2 = hsum(t2);
        const float mean = t1 * (1.f / D), var = __builtin_fmaxf(t2 * (1.f / D) - mean * mean, 0.f) + EPS, rstd = __builtin_amdgcn_rsqf(var);
        const GeluK gk = {-mean, GC1 * rstd * rstd * rstd, GC0 * rstd, var * rstd};
        const unsigned uo = WS_U + ((4 * tb6) * XR + tk) * 16;
        auto epi1 = [&](f32x16& y, int te) AI {
#pragma unroll
            for (int q = 0; q < 4; ++q) {
                const f32x4 c = *(const LAS f32x4*)(cs6 + te * 32 + 8 * q + 4 * hh);
#pragma unroll
                for (int i = 0; i < 4; ++i) {
                    const float v = __builtin_fmaf(gk.nmean, c[i], y[4 * q + i]);
                    const float z = v * __builtin_fmaf(v * v, gk.k1, gk.k0);
                    y[4 * q + i] = v * __builtin_amdgcn_rcpf(__builtin_fmaf(__builtin_amdgcn_exp2f(z), gk.irs, gk.irs));
                }
            }
            gst(rs, pack8(y, 0), vx, uo + 4 * te * XROW); gst(rs, pack8(y, 1), vx, uo + (4 * te + 2) * XROW);
            SB();
        };
        epi1(acc[0], 0); epi1(acc[1], 1); epi1(acc[2], 2);
        if (nt6 == 4) epi1(acc[3], 3);
        STAMP(29);
        WAIT_BAR(0);
        if (tid == 0) set_flag(P.ws, flag_off(5, m * 16 + s16), e);
        BAR();
        STAMP(12);
    }
    if (act7) {
        LANE_GEOM();
        const unsigned lanep = trq * 64 + gq * 8 + 2 * hh * 512;
        unsigned tA = L0 + L_X + w * 8192 + lanep, tB = wst + lanep; PIN(tA); PIN(tB);
        auto w4f = [&](int k, int t) AI { const unsigned a = (k < 4 ? tA + k * 2048 : k < 18 ? tB + (k - 4) * 2048 : tB + (k - 18) * 2048) + t * 256; return wfrag(a, a + 512); };
        u32x4 wr[28][2];
#pragma unroll
        for (int k = 0; k < 14; ++k) { wr[k][0] = w4f(k, 0); wr[k][1] = w4f(k, 1); }
        LBAR();
        const int tk = 64 * j7;
        const int tt = w >> 1, nt7 = w & 1;
        wait_flags(P.ws, flag_off(5, (j7 >> 1) * 16 + 4 * w), 4, e, lane);
        constexpr int PX = P7_PX;
        u32x4 xa[28][2];
#pragma unroll
        for (int k = 0; k < PX; ++k) { xa[k][0] = gld(rs, vx, WS_U + ((56 * w + 2 * k) * XR + tk) * 16); xa[k][1] = gld(rs, vx, WS_U + ((56 * w + 2 * k) * XR + tk + 32) * 16); }
        STAMP(13);
        SB();
        f32x16 acc[4] = {};
        u32x4 rh[2], rx[2];
        sfor<28>([&](auto kc) AI {
            constexpr int k = decltype(kc)::value;
            if constexpr (k == 13) { BAR(); SB(); }
            if constexpr (k + 3 >= 14 && k + 3 < 28) { wr[k + 3][0] = w4f(k + 3, 0); wr[k + 3][1] = w4f(k + 3, 1); }
            MFMA(acc[0], wr[k][0], xa[k][0], acc[0]); MFMA(acc[1], wr[k][1], xa[k][0], acc[1]);
            if constexpr (k + PX < 28) xa[k + PX][0] = gld(rs, vx, WS_U + ((56 * w + 2 * (k + PX)) * XR + tk) * 16);
            MFMA(acc[2], wr[k][0], xa[k][1], acc[2]); MFMA(acc[3], wr[k][1], xa[k][1], acc[3]);
            if constexpr (k + PX < 28) xa[k + PX][1] = gld(rs, vx, WS_U + ((56 * w + 2 * (k + PX)) * XR + tk + 32) * 16);
            if constexpr (k == 24 || k == 25) {
                constexpr int s = k - 24;
                const unsigned so = ((8 * g7 + 4 * nt7 + 2 * s) * XR + tk + 32 * tt) * 16;
                rh[s] = gld(rs, vx, WS_H + so); rx[s] = gld(rs, vx, WS_X1 + so);
            }
            SB();
        });
        STAMP(27);
#pragma unroll
        for (int t = 0; t < 4; ++t) {
            if (t != w) {
                const unsigned a = L0 + L_X + t * 12288 + (w < t ? w : w - 1) * 4096 + lane * 16;
#pragma unroll
                for (int q = 0; q < 4; ++q) stsf4(a + q * 1024, (f32x4){acc[t][4 * q], acc[t][4 * q + 1], acc[t][4 * q + 2], acc[t][4 * q + 3]});
            }
        }
        LBAR();
        f32x16 r = w == 0 ? acc[0] : w == 1 ? acc[1] : w == 2 ? acc[2] : acc[3];
        const unsigned a = L0 + L_X + w * 12288 + lane * 16;
#pragma unroll
        for (int j = 0; j < 3; ++j)
#pragma unroll
            for (int q = 0; q < 4; ++q) { const f32x4 v = ldsf4(a + j * 4096 + q * 1024); r[4 * q] += v[0]; r[4 * q + 1] += v[1]; r[4 * q + 2] += v[2]; r[4 * q + 3] += v[3]; }
        const unsigned oo = (tk + 32 * tt + r32) * 896 + 128 * g7 + 64 * nt7 + 16 * hh;
        char* op = (char*)P.out + oo;
        const __amdgpu_buffer_rsrc_t ro = __builtin_amdgcn_make_buffer_rsrc((void*)P.out, 0, S * D * 2, 0x00020000); (void)ro; (void)op;
#pragma unroll
        for (int s = 0; s < 2; ++s) {
            u32x4 ov;
#pragma unroll
            for (int i = 0; i < 4; ++i)
                ov[i] = pk(r[8 * s + 2 * i] + blo(rx[s][i]) + blo(rh[s][i]), r[8 * s + 2 * i + 1] + bhi(rx[s][i]) + bhi(rh[s][i]));
            *(GAS u32x4*)(op + 32 * s) = ov;
        }
        STAMP(14);
    }
    STAMP_FLUSH();
}
}

extern "C" void kernel_launch(void* const* d_in, const int* in_sizes, int n_in, void* d_out, int out_size, void* d_ws, size_t ws_size,
                              hipStream_t stream) {
    mk::Params P;
    P.x = (const u16*)d_in[0]; P.wqkv = (const u16*)d_in[1]; P.wo = (const u16*)d_in[2];
    P.w1 = (const u16*)d_in[4]; P.w2 = (const u16*)d_in[5]; P.w3 = (const u16*)d_in[6]; P.w4 = (const u16*)d_in[7];
    P.out = (u16*)d_out; P.ws = (char*)d_ws;
    mk::fused_fwd<<<256, 512, 0, stream>>>(P);
}
```

```cpp
#define P1_ROLLED 1
#define EARLY_CODE 1
#define TOUCH_FLAGS 1
#define W4SPLIT 36
#define PRIO_C 2
#define PRIO_L 0
#define NT_W 0
#define NT_QKV 0
#define NT_X 0
#define OUT_MODE 0
#define P6_PX 10
#define P6_PW 3
#define P6_PG 6
#define P7_PX 5
#define HALF 12
#define POLL2 0
#define POLL_GAP 3
#define TOUCH_DELAY 12
#define EARLY_TOUCH 1
#define LATE_TOUCH 3
#include <hip/hip_runtime.h>
#include <hip/hip_bf16.h>
#include <stdint.h>
#include <type_traits>
#include <utility>

typedef unsigned short u16;
typedef __attribute__((ext_vector_type(8))) short bf16x8;
typedef __attribute__((ext_vector_type(4))) short s16x4;
typedef __attribute__((ext_vector_type(16))) float f32x16;
typedef __attribute__((ext_vector_type(4))) float f32x4;
typedef __attribute__((ext_vector_type(2))) float f32x2;
typedef __attribute__((ext_vector_type(4))) unsigned u32x4;
typedef __attribute__((ext_vector_type(2))) unsigned u32x2;
typedef __attribute__((ext_vector_type(2))) __bf16 bf2;
#define LAS __attribute__((address_space(3)))
#define GAS __attribute__((address_space(1)))
#define DEVI __device__ __forceinline__

#define MK_STAMP 0

namespace mk {
constexpr int S = 2048, D = 448;
constexpr unsigned XR = 2056, XROW = XR * 16;
constexpr unsigned KVH = 64 * 4096 + 128;
constexpr unsigned WS_EP = 0;
constexpr unsigned WS_FLAG = 4096;
constexpr unsigned WS_KV = 262144;
constexpr unsigned WS_O = WS_KV + 16 * KVH;
constexpr unsigned WS_H = WS_O + 64 * XROW;
constexpr unsigned WS_HH = WS_H + 56 * XROW;
constexpr unsigned WS_X1 = WS_HH + 56 * XROW;
constexpr unsigned WS_U = WS_X1 + 56 * XROW;
constexpr unsigned WS_STH = WS_U + 224 * XROW;
constexpr unsigned WS_STX = WS_STH + 262144;
constexpr unsigned WS_ST1 = WS_STX + 262144;
constexpr unsigned WS_SPARE = WS_ST1 + 32768;
constexpr unsigned WS_STAMP = WS_SPARE + 128;
constexpr unsigned WS_END = WS_STAMP + 16 * 512 * 8;
constexpr unsigned L_SLOT = 28672;
constexpr unsigned L_MISC = 143360;
constexpr unsigned L_KV = 114688;
constexpr unsigned L_OWNKV = L_KV + 8 * 4096;
constexpr unsigned L_WST = 28672;
constexpr unsigned L_X = 114688;
constexpr unsigned L_M2 = 147456;
constexpr unsigned CODE_TOUCH_BYTES = 35000;
constexpr float EPS = 1e-5f;
constexpr unsigned SPIN_CAP = 1u << 18;

struct Params {
    const u16* x; const u16* wqkv; const u16* wo; const u16* w1; const u16* w2; const u16* w3; const u16* w4;
    u16* out; char* ws;
};

DEVI unsigned f2u(float f) { return __builtin_bit_cast(unsigned, f); }
DEVI float u2f(unsigned u) { return __builtin_bit_cast(float, u); }
DEVI bool wall(bool p) { return __builtin_amdgcn_ballot_w64(!p) == 0; }
template <int X> DEVI float xor32(float v) { return u2f((unsigned)__builtin_amdgcn_ds_swizzle((int)f2u(v), (X << 10) | 0x1f)); }
DEVI unsigned pk(float lo, float hi) { bf2 v = {(__bf16)lo, (__bf16)hi}; return __builtin_bit_cast(unsigned, v); }
DEVI float blo(unsigned u) { return u2f(u << 16); }
DEVI float bhi(unsigned u) { return u2f(u & 0xffff0000u); }
DEVI float dot2(unsigned a, unsigned b, float c) { return __builtin_amdgcn_fdot2_f32_bf16(__builtin_bit_cast(bf2, a), __builtin_bit_cast(bf2, b), c, false); }
constexpr unsigned ONES = 0x3f803f80u;
DEVI float hsum(float v) { auto r = __builtin_amdgcn_permlane32_swap(f2u(v), f2u(v), false, false); return u2f(r[0]) + u2f(r[1]); }
DEVI float hmax(float v) { auto r = __builtin_amdgcn_permlane32_swap(f2u(v), f2u(v), false, false); return __builtin_fmaxf(u2f(r[0]), u2f(r[1])); }
DEVI f32x16 mfma(u32x4 a, u32x4 b, f32x16 c) { return __builtin_amdgcn_mfma_f32_32x32x16_bf16(__builtin_bit_cast(bf16x8, a), __builtin_bit_cast(bf16x8, b), c, 0, 0, 0); }
#define MFMA(acc, a, b, c) do { acc = mfma(a, b, c); asm volatile("" : "+v"(acc)); } while (0)
DEVI u32x4 lds128(unsigned a) { return *(const LAS u32x4*)(uintptr_t)a; }
DEVI f32x4 ldsf4(unsigned a) { return *(const LAS f32x4*)(uintptr_t)a; }
DEVI void sts128(unsigned a, u32x4 v) { *(LAS u32x4*)(uintptr_t)a = v; }
DEVI void stsf4(unsigned a, f32x4 v) { *(LAS f32x4*)(uintptr_t)a = v; }
DEVI u32x2 ldstr(unsigned a) { return __builtin_bit_cast(u32x2, __builtin_amdgcn_ds_read_tr16_b64_v4i16((LAS s16x4*)(uintptr_t)a)); }
DEVI u32x4 wfrag(unsigned a0, unsigned a1) { u32x2 p = ldstr(a0), q = ldstr(a1); return (u32x4){p.x, p.y, q.x, q.y}; }
DEVI u32x4 pack8(const f32x16& a, int s) { return (u32x4){pk(a[8 * s], a[8 * s + 1]), pk(a[8 * s + 2], a[8 * s + 3]), pk(a[8 * s + 4], a[8 * s + 5]), pk(a[8 * s + 6], a[8 * s + 7])}; }
constexpr int SC1 = 16;
DEVI u32x4 gld(__amdgpu_buffer_rsrc_t rs, unsigned voff, unsigned soff) { return __builtin_amdgcn_raw_buffer_load_b128(rs, voff, soff, SC1); }
DEVI u32x2 gld8(__amdgpu_buffer_rsrc_t rs, unsigned voff, unsigned soff) { return __builtin_amdgcn_raw_buffer_load_b64(rs, voff, soff, SC1); }
DEVI void gst(__amdgpu_buffer_rsrc_t rs, u32x4 v, unsigned voff, unsigned soff) { __builtin_amdgcn_raw_buffer_store_b128(v, rs, voff + soff, 0, SC1); }
DEVI void gst8(__amdgpu_buffer_rsrc_t rs, u32x2 v, unsigned voff, unsigned soff) { __builtin_amdgcn_raw_buffer_store_b64(v, rs, voff, soff, SC1); }
#define WAIT_VM(N) asm volatile("s_waitcnt vmcnt(" #N ")" ::: "memory")
#define WAIT_LGKM() asm volatile("s_waitcnt lgkmcnt(0)" ::: "memory")
#define WAIT_BAR(N) asm volatile("s_waitcnt vmcnt(" #N ") lgkmcnt(0)\n\ts_barrier" ::: "memory")
#define LBAR() asm volatile("s_waitcnt lgkmcnt(0)\n\ts_barrier" ::: "memory")
#define SB() __builtin_amdgcn_sched_barrier(0)
#define PIN(x) asm volatile("" : "+v"(x))
DEVI float fma1(float a, float b, float c) { return __builtin_fmaf(a, b, c); }
DEVI float mul1(float a, float b) { return a * b; }
DEVI float max3(float a, float b, float c) { return __builtin_fmaxf(__builtin_fmaxf(a, b), c); }
template <int... Is, class F> DEVI void sfor_impl(std::integer_sequence<int, Is...>, F&& f) { (f(std::integral_constant<int, Is>{}), ...); }
template <int N, class F> DEVI void sfor(F&& f) { sfor_impl(std::make_integer_sequence<int, N>{}, f); }
#define AI __attribute__((always_inline))
#define IC(n) std::integral_constant<int, n>{}

template <int NT = 0> DEVI void dma1(const char* sbase, unsigned voff, unsigned lds) {
    if (NT) asm volatile("s_mov_b32 m0, %2\n\ts_nop 0\n\tglobal_load_lds_dwordx4 %0, %1 nt" :: "v"(voff), "s"(sbase), "s"(lds) : "memory");
    else asm volatile("s_mov_b32 m0, %2\n\ts_nop 0\n\tglobal_load_lds_dwordx4 %0, %1" :: "v"(voff), "s"(sbase), "s"(lds) : "memory");
}
DEVI void touch(const char* sbase, unsigned voff, unsigned lds) {
    asm volatile("s_mov_b32 m0, %2\n\ts_nop 0\n\tglobal_load_lds_dword %0, %1" :: "v"(voff), "s"(sbase), "s"(lds) : "memory");
}
struct WSrc { const char* base; unsigned step, voff; };
DEVI void wpiece(const WSrc& s, unsigned lds, int n) { dma1<NT_W>(s.base + (size_t)n * s.step, s.voff, lds + n * 1024); }
DEVI WSrc wsrc_a(const char* base, unsigned rs, int nt, int lane) { const int t = (lane >> 4) < nt ? (lane >> 4) : nt - 1; return WSrc{base, 4 * rs, ((lane >> 2) & 3) * rs + t * 64 + (lane & 3) * 16}; }
DEVI WSrc wsrc_b(const char* base, unsigned rs, int lane) { return WSrc{base, 8 * rs, (4 * (lane >> 5) + ((lane >> 2) & 3)) * rs + ((lane >> 4) & 1) * 64 + (lane & 3) * 16}; }

DEVI void poll_word(const GAS unsigned* p, unsigned e) {
    unsigned a = __hip_atomic_load(p, __ATOMIC_RELAXED, __HIP_MEMORY_SCOPE_AGENT);
#pragma nounroll
    for (unsigned sp = 0; sp < SPIN_CAP; ++sp) {
        __builtin_amdgcn_s_sleep(POLL_GAP);
        const unsigned b2 = __hip_atomic_load(p, __ATOMIC_RELAXED, __HIP_MEMORY_SCOPE_AGENT);
        if (wall(a == e)) break;
        __builtin_amdgcn_s_sleep(POLL_GAP);
        a = __hip_atomic_load(p, __ATOMIC_RELAXED, __HIP_MEMORY_SCOPE_AGENT);
        if (wall(b2 == e)) break;
    }
    __builtin_amdgcn_fence(__ATOMIC_ACQUIRE, "wavefront");
}
DEVI void wait_flags(const char* ws, unsigned foff, int n, unsigned e, int lane) {
    const GAS unsigned* p = (const GAS unsigned*)(ws + foff) + (lane < n ? lane : 0) * 32;
    if (POLL2) poll_word(p, e);
    else {
#pragma nounroll
        for (unsigned sp = 0; sp < SPIN_CAP; ++sp) {
            unsigned v = __hip_atomic_load(p, __ATOMIC_RELAXED, __HIP_MEMORY_SCOPE_AGENT);
            if (wall(v == e)) break;
        }
        __builtin_amdgcn_fence(__ATOMIC_ACQUIRE, "wavefront");
    }
}
DEVI void set_flag(char* ws, unsigned foff, unsigned e) { __hip_atomic_store((GAS unsigned*)(ws + foff), e, __ATOMIC_RELAXED, __HIP_MEMORY_SCOPE_AGENT); }
DEVI unsigned flag_off(int ph, int idx) { return WS_FLAG + (ph * 256 + idx) * 128; }

constexpr float GC0 = -2.f * 0.7978845608028654f * 1.4426950408889634f, GC1 = GC0 * 0.044715f;
DEVI float gelu_t(float x) {
    float z = x * __builtin_fmaf(x * x, GC1, GC0);
    return x * __builtin_amdgcn_rcpf(1.f + __builtin_amdgcn_exp2f(z));
}
struct GeluK { float nmean, k1, k0, irs; };
DEVI void gelu_step(int o, float& y, float& z, float c, const GeluK& g) {
    if (o == 0) y = fma1(g.nmean, c, y);
    else if (o == 1) z = mul1(y, y);
    else if (o == 2) z = fma1(z, g.k1, g.k0);
    else if (o == 3) z = mul1(y, z);
    else if (o == 4) z = __builtin_amdgcn_exp2f(z);
    else if (o == 5) z = fma1(z, g.irs, g.irs);
    else if (o == 6) z = __builtin_amdgcn_rcpf(z);
    else y = mul1(y, z);
}

#define STAMP(k) do {} while (0)
#define STAMP_DECL
#define STAMP_FLUSH() do {} while (0)

#define LANE_GEOM() int lane_ = threadIdx.x & 63; asm volatile("" : "+v"(lane_)); const int lane = lane_, r32 = lane & 31, hh = lane >> 5; \
    const int trq = (lane & 15) >> 2, trQ_ = ((lane >> 4) & 1) * 4 + (lane & 3), gq = (trQ_ & 4) | ((trQ_ & 1) << 1) | ((trQ_ >> 1) & 1); \
    const unsigned vx = (hh * XR + r32) * 16; (void)trq; (void)gq; (void)vx; (void)r32; (void)hh
constexpr unsigned L_FLG = L_MISC + 1920, F_RDY = 0, F_DONE = 16;
DEVI unsigned ldsw_ld(unsigned i) { return *(volatile LAS unsigned*)(uintptr_t)(L_FLG + 4 * i); }
DEVI u32x2 ldsw_ld2(unsigned i) { return *(volatile LAS u32x2*)(uintptr_t)(L_FLG + 4 * i); }
DEVI void ldsw_st(unsigned i, unsigned v) { *(volatile LAS unsigned*)(uintptr_t)(L_FLG + 4 * i) = v; }
DEVI void ring_wait(int c) {
#pragma nounroll
    for (unsigned sp = 0; sp < SPIN_CAP * 16u; ++sp) { const u32x2 v = ldsw_ld2(F_RDY + 2 * c); if (__builtin_amdgcn_readfirstlane(v.x & v.y)) break; __builtin_amdgcn_s_sleep(1); }
}
DEVI void slot_wait(unsigned n, int lane) {
#pragma nounroll
    for (unsigned sp = 0; sp < SPIN_CAP * 16u; ++sp) { const unsigned v = ldsw_ld(F_DONE + (lane < 6 ? lane : 0)); if (wall(v >= n)) break; __builtin_amdgcn_s_sleep(1); }
}
#define LB(N) asm volatile("s_waitcnt vmcnt(" #N ")\n\ts_barrier" ::: "memory")
#define BAR() asm volatile("s_barrier" ::: "memory")

__global__ __launch_bounds__(512, 2) void fused_fwd(Params P) {
    __shared__ __attribute__((aligned(16))) char smem[163840];
    const unsigned L0 = (unsigned)(uintptr_t)(LAS char*)smem;
    const int tid = threadIdx.x, wv = __builtin_amdgcn_readfirstlane(tid >> 6), w = wv & 3;
    const int b = blockIdx.x, xcd = b & 7, slot = b >> 3;
    const int m = 2 * xcd + (slot >> 4), h = slot & 15;
    const int g = slot & 3, nt = g < 2 ? 4 : 3, tb = g < 2 ? 4 * g : 3 * g + 2;
    const int ti = 8 * xcd + (slot >> 2);
    const int s16 = slot & 15, pr = s16 >> 1;
    const int nt6 = ((s16 ^ pr) & 1) ? 3 : 4, tb6 = 7 * pr + ((s16 & 1) ? (pr & 1 ? 3 : 4) : 0);
    const bool act7 = slot < 28;
    const int j7 = 4 * xcd + (act7 ? slot / 7 : 0), g7 = act7 ? slot % 7 : 0;
    const unsigned wst = L0 + w * L_WST;
    const unsigned shift = (56 * h) & 15u;
    STAMP_DECL

    if (wv >= 4) {
        if (PRIO_L) __builtin_amdgcn_s_setprio(PRIO_L);
        LANE_GEOM();
        const unsigned wrd = 16384 + hh * 6 * 256 + trq * 64 + ((shift + gq * 8) & 63);
        LAS float* csp = (LAS float*)(uintptr_t)(L0 + L_MISC);
        if (w < 2) {
            const unsigned xoffE = (lane >> 3) * 896 + (((lane & 7) ^ (lane >> 4)) << 4);
            const unsigned xoffO = (lane >> 3) * 896 + (((lane & 7) ^ (4 + (lane >> 4))) << 4);
            const unsigned colb = (56 * h) & ~15u;
            unsigned woff[3];
#pragma unroll
            for (int j = 0; j < 3; ++j) {
                const int bl = 4 * j + (lane >> 4), kql = bl / 3, tile = bl % 3;
                woff[j] = (4 * kql + ((lane >> 2) & 3)) * 2688 + tile * 896 + colb + (lane & 3) * 16;
            }
            const char* xb = (const char*)P.x + (size_t)(128 * m + 64 * w) * 896;
            const char* wb = (const char*)P.wqkv + (size_t)(32 * w) * 2688;
            LBAR();
#pragma unroll 1
            for (int kc = 0; kc < 7; ++kc) {
                if (kc >= 5) slot_wait(kc - 4, lane);
                const unsigned base = L0 + (kc >= 5 ? kc - 5 : kc) * L_SLOT;
#pragma unroll
                for (int i = 0; i < 6; ++i) dma1<NT_QKV>(wb + (size_t)kc * 64 * 2688 + (i / 3) * 16 * 2688, woff[i % 3], base + 16384 + (6 * w + i) * 1024);
#pragma unroll
                for (int i = 0; i < 8; ++i) dma1<NT_X>(xb + kc * 128 + i * 8 * 896, (i & 1) ? xoffO : xoffE, base + (8 * w + i) * 1024);
                if (kc > 0) { WAIT_VM(14); ldsw_st(F_RDY + 2 * (kc - 1) + w, 1); }
            }
            WAIT_VM(0); ldsw_st(F_RDY + 12 + w, 1);
            {
                ring_wait(6);
                const unsigned a = L0 + L_SLOT + wrd + w * 3072;
#pragma unroll
                for (int t = 0; t < 3; ++t) { const u32x4 f = wfrag(a + t * 256, a + 768 + t * 256);
                    const float v = hsum((dot2(f[0], ONES, 0.f) + dot2(f[1], ONES, 0.f)) + (dot2(f[2], ONES, 0.f) + dot2(f[3], ONES, 0.f)));
                    if (hh == 0) csp[(2 + w) * 96 + t * 32 + r32] = v; }
            }
        } else {
            if (w == 2 && EARLY_CODE) {
                unsigned long long pc; unsigned back;
                asm volatile("s_getpc_b64 %0\nmk_code_here0:\n\ts_mov_b32 %1, mk_code_here0-_ZN2mk9fused_fwdENS_6ParamsE" : "=s"(pc), "=s"(back));
                const unsigned oc = (slot * 18 + (lane < 18 ? lane : 0)) * 128;
                const char* t0 = (const char*)(pc - back) + (oc < CODE_TOUCH_BYTES ? oc : 0);
                asm volatile("s_mov_b32 m0, %1\n\ts_nop 0\n\tglobal_load_lds_dword %0, off" :: "v"(t0), "s"(L0 + L_MISC + 2048) : "memory");
            }
            const int srow = 128 * m + 8 * h + 4 * (w - 2) + (lane >> 4);
            const char* sp = (const char*)P.x + (size_t)srow * 896 + (lane & 15) * 16;
            u32x4 sr[4];
            asm volatile("global_load_dwordx4 %0, %4, off\n\tglobal_load_dwordx4 %1, %4, off offset:256\n\tglobal_load_dwordx4 %2, %4, off offset:512\n\tglobal_load_dwordx4 %3, %5, off"
                         : "=&v"(sr[0]), "=&v"(sr[1]), "=&v"(sr[2]), "=&v"(sr[3]) : "v"(sp), "v"(sp + ((lane & 15) < 8 ? 768 : 0)) : "memory");
            if (w == 2 && lane < 32) ldsw_st(lane, 0);
            LBAR();
            {
                const unsigned lx = lane < 56 ? lane : 0, lw = b * 37 + (lane < 37 ? lane : 0);
                const unsigned fl = lane - 56;
                const int fidx = fl < 2 || fl == 5 ? m * 16 + h : ti * 4 + g;
                const char* tf = fl < 6 ? P.ws + flag_off(fl, fidx) : P.ws + WS_ST1 + (128 * m + 8 * h) * 16;
                const char* tp = !EARLY_TOUCH ? P.ws + WS_SPARE : w == 2 ? ((TOUCH_FLAGS && lane >= 56 && lane < 63) ? tf : (const char*)P.x + (size_t)(8 * b + lx / 7) * 896 + (lx % 7) * 128)
                                                                       : (const char*)P.wqkv + (size_t)(lw < 9408 ? lw : 0) * 128;
                if (TOUCH_DELAY) __builtin_amdgcn_s_sleep(TOUCH_DELAY);
                asm volatile("s_mov_b32 m0, %1\n\ts_nop 0\n\tglobal_load_lds_dword %0, off" :: "v"(tp), "s"(L0 + L_MISC + 2048) : "memory");
            }
            {
                asm volatile("s_waitcnt vmcnt(1)" : "+v"(sr[0]), "+v"(sr[1]), "+v"(sr[2]), "+v"(sr[3]) :: "memory");
                if ((lane & 15) >= 8) sr[3] = (u32x4){0u, 0u, 0u, 0u};
                float t1 = 0.f, t2 = 0.f;
#pragma unroll
                for (int q = 0; q < 4; ++q)
#pragma unroll
                    for (int i = 0; i < 4; ++i) { t1 = dot2(sr[q][i], ONES, t1); t2 = dot2(sr[q][i], sr[q][i], t2); }
                t1 += xor32<1>(t1); t2 += xor32<1>(t2); t1 += xor32<2>(t1); t2 += xor32<2>(t2); t1 += xor32<4>(t1); t2 += xor32<4>(t2); t1 += xor32<8>(t1); t2 += xor32<8>(t2);
                const float mean = t1 * (1.f / D), var = __builtin_fmaxf(t2 * (1.f / D) - mean * mean, 0.f);
                const unsigned e = *(const unsigned*)(P.ws + WS_EP) + 1u;
                const __amdgpu_buffer_rsrc_t rs = __builtin_amdgcn_make_buffer_rsrc((void*)P.ws, 0, WS_END, 0x00020000);
                if ((lane & 15) == 0) gst(rs, (u32x4){f2u(mean), e, f2u(__builtin_amdgcn_rsqf(var + EPS)), e}, srow * 16, WS_ST1);
            }
            float cs[3][4] = {};
            const unsigned wrd2 = wrd + (w - 2) * 2 * 3072;
#pragma unroll 1
            for (int c = 0; c < 7; ++c) {
                ring_wait(c);
                if (c == LATE_TOUCH) {
                    unsigned long long pc; unsigned back;
                    asm volatile("s_getpc_b64 %0\nmk_code_here:\n\ts_mov_b32 %1, mk_code_here-_ZN2mk9fused_fwdENS_6ParamsE" : "=s"(pc), "=s"(back));
                    constexpr unsigned len = CODE_TOUCH_BYTES;
                    const unsigned oc = (slot * 18 + (lane < 18 ? lane : 0)) * 128, o3 = (b * 64 + lane) * 128, o2 = (b * 16 + (lane & 15)) * 128;
                    const char* t0 = w == 2 ? (const char*)(pc - back) + (oc < len && !EARLY_CODE ? oc : 0) : (const char*)P.w3 + (o3 < 1605632 ? o3 : 0);
                    const char* t1 = w == 2 ? (lane < 16 ? (const char*)P.wo : lane < 32 ? (const char*)P.w1 : (const char*)P.w2) + (o2 < 401408 ? o2 : 0) : (const char*)P.w4 + (o3 < 1605632 ? o3 : 0);
                    asm volatile("s_mov_b32 m0, %2\n\ts_nop 0\n\tglobal_load_lds_dword %0, off\n\tglobal_load_lds_dword %1, off" :: "v"(t0), "v"(t1), "s"(L0 + L_MISC + 2048) : "memory");
                }
                const unsigned a = L0 + (c >= 5 ? c - 5 : c) * L_SLOT + (c < 6 ? wrd2 : wrd + w * 3072);
#pragma unroll
                for (int t = 0; t < 3; ++t) { const u32x4 f = wfrag(a + t * 256, a + 768 + t * 256);
#pragma unroll
                    for (int i = 0; i < 4; ++i) cs[t][i] = dot2(f[i], ONES, cs[t][i]); }
                if (c < 6) {
#pragma unroll
                    for (int t = 0; t < 3; ++t) { const u32x4 f = wfrag(a + 3072 + t * 256, a + 3072 + 768 + t * 256);
#pragma unroll
                        for (int i = 0; i < 4; ++i) cs[t][i] = dot2(f[i], ONES, cs[t][i]); }
                }
                WAIT_LGKM();
                ldsw_st(F_DONE + 2 + w, c + 1);
            }
#pragma unroll
            for (int t = 0; t < 3; ++t) { const float v = hsum((cs[t][0] + cs[t][1]) + (cs[t][2] + cs[t][3])); if (hh == 0) csp[(w - 2) * 96 + t * 32 + r32] = v; }
        }
        LBAR();
        auto stage = [&](const WSrc& s, unsigned lds, int n0, int n1) AI {
#pragma unroll 1
            for (int n = n0; n < n1; ++n) wpiece(s, lds, n);
        };
        LB(0);
        BAR();
        stage(wsrc_a((const char*)P.wo + (size_t)(112 * w) * 896 + tb * 64, 896, nt, lane), wst, 0, 28);
        LB(0);
        { const WSrc s_ = wsrc_a((const char*)P.w1 + (size_t)(112 * w) * 896 + tb * 64, 896, nt, lane); BAR(); stage(s_, wst, 0, HALF); BAR(); stage(s_, wst, HALF, 28); BAR(); LB(0); }
        { const WSrc s_ = wsrc_a((const char*)P.w2 + (size_t)(112 * w) * 896 + tb * 64, 896, nt, lane); BAR(); stage(s_, wst, 0, HALF); BAR(); stage(s_, wst, HALF, 28); BAR(); LB(0); }
        { const WSrc s_ = wsrc_a((const char*)P.w3 + (size_t)(112 * w) * 3584 + tb6 * 64, 3584, nt6, lane); BAR(); stage(s_, wst, 0, HALF); BAR(); stage(s_, wst, HALF, 28); BAR(); LB(0); }
        {
            float c[4] = {0.f, 0.f, 0.f, 0.f};
            const unsigned a0 = L0 + trq * 64 + gq * 8 + 2 * hh * 1024 + w * 256;
#pragma unroll 4
            for (int k = 0; k < 28; ++k) { const unsigned a = a0 + k * 4096; const u32x4 f = wfrag(a, a + 1024);
#pragma unroll
                for (int i = 0; i < 4; ++i) c[i] = dot2(f[i], ONES, c[i]); }
            const float cc = hsum((c[0] + c[1]) + (c[2] + c[3]));
            if (hh == 0) ((LAS float*)(uintptr_t)(L0 + L_M2 + 1024))[w * 32 + r32] = cc;
        }
        if (b == 0 && w == 3) {
            const unsigned e = *(const volatile GAS unsigned*)(P.ws + WS_EP) + 1u;
            const GAS unsigned* p = (const GAS unsigned*)(P.ws + flag_off(0, lane));
#pragma nounroll
            for (unsigned sp = 0; sp < SPIN_CAP; ++sp) {
                const unsigned v0 = __hip_atomic_load(p, __ATOMIC_RELAXED, __HIP_MEMORY_SCOPE_AGENT), v1 = __hip_atomic_load(p + 64 * 32, __ATOMIC_RELAXED, __HIP_MEMORY_SCOPE_AGENT);
                const unsigned v2 = __hip_atomic_load(p + 128 * 32, __ATOMIC_RELAXED, __HIP_MEMORY_SCOPE_AGENT), v3 = __hip_atomic_load(p + 192 * 32, __ATOMIC_RELAXED, __HIP_MEMORY_SCOPE_AGENT);
                if (wall(v0 == e && v1 == e && v2 == e && v3 == e)) break;
            }
            if (lane == 0) __hip_atomic_store((GAS unsigned*)(P.ws + WS_EP), e, __ATOMIC_RELAXED, __HIP_MEMORY_SCOPE_AGENT);
        }
        LBAR();
        const WSrc w4s = wsrc_b((const char*)P.w4 + (size_t)(448 * w) * 896 + g7 * 128, 896, lane);
        if (act7) { stage(w4s, L0 + L_X + w * 8192, 0, 8); stage(w4s, wst - 8 * 1024, 8, W4SPLIT); }
        BAR();
        if (act7) stage(w4s, wst - 8 * 1024, W4SPLIT, 36);
        LB(0);
        if (act7) {
            BAR();
            stage(w4s, wst - 36 * 1024, 36, 56);
            LB(0);
            BAR();
        }
        STAMP_FLUSH();
        return;
    }

    if (PRIO_C) __builtin_amdgcn_s_setprio(PRIO_C);
    const unsigned e = *(const unsigned*)(P.ws + WS_EP) + 1u;
    const __amdgpu_buffer_rsrc_t rs = __builtin_amdgcn_make_buffer_rsrc((void*)P.ws, 0, WS_END, 0x00020000);
    u32x4 qf0, qf1;
    {
        LANE_GEOM();
        const unsigned wrd = 16384 + hh * 6 * 256 + trq * 64 + ((shift + gq * 8) & 63);
        STAMP(15);
        f32x16 aq = {}, ak = {}, av = {};
        const unsigned xrd = (32 * w + r32) * 128 + ((hh ^ ((r32 >> 1) & 7)) << 4);
        struct Fr { u32x4 x, q, k, v; };
        Fr F[4];
        unsigned xo[4];
#pragma unroll
        for (int j = 0; j < 4; ++j) { xo[j] = L0 + (xrd ^ (j << 5)); PIN(xo[j]); }
        unsigned wo_ = L0 + wrd; PIN(wo_);
        auto ldx = [&](unsigned base, int j) AI { F[j].x = lds128(xo[j] + base); const unsigned a = wo_ + base; F[j].q = wfrag(a + j * 3072, a + j * 3072 + 768); };
        auto ldk = [&](unsigned base, int j) AI { const unsigned a = wo_ + base; F[j].k = wfrag(a + j * 3072 + 256, a + j * 3072 + 1024); };
        auto ldv = [&](unsigned base, int j) AI { const unsigned a = wo_ + base; F[j].v = wfrag(a + j * 3072 + 512, a + j * 3072 + 1280); };
        LAS float* stw = (LAS float*)(uintptr_t)(L0 + L_MISC + 3072 + w * 256);
        BAR();
        ring_wait(0);
        STAMP(16);
#pragma unroll
        for (int j = 0; j < 2; ++j) { ldx(0, j); ldk(0, j); ldv(0, j); }
        SB();
        u32x4 gv = {0u, 0u, 0u, 0u};
        const unsigned gvo = (128 * m + 32 * w + r32) * 16;
#pragma unroll 1
        for (int c = 0; c < 7; ++c) {
            const unsigned base = (c >= 5 ? c - 5 : c) * L_SLOT, nbase = (c >= 4 ? c - 4 : c + 1) * L_SLOT;
            u32x2 rdy;
            if (c == 4) gv = gld(rs, gvo, WS_ST1);
            sfor<4>([&](auto jc) AI {
                constexpr int j = decltype(jc)::value, jn = (j + 2) & 3;
                const unsigned lb = j < 2 ? base : nbase;
                if constexpr (j == 2) { if (__builtin_amdgcn_readfirstlane(rdy.x & rdy.y) == 0) ring_wait(c + 1); SB(); }
                MFMA(aq, F[j].q, F[j].x, aq);
                ldx(lb, jn);
                SB();
                MFMA(ak, F[j].k, F[j].x, ak);
                ldk(lb, jn);
                if constexpr (j == 0) ldsw_st(F_DONE + w, c);
                SB();
                MFMA(av, F[j].x, F[j].v, av);
                ldv(lb, jn);
                if constexpr (j == 0) rdy = ldsw_ld2(F_RDY + 2 * (c < 6 ? c + 1 : 6));
                SB();
            });
        }
#pragma nounroll
        for (unsigned sp2 = 0; sp2 < SPIN_CAP && !wall(gv.y == e && gv.w == e); ++sp2) { asm volatile("" ::: "memory"); gv = gld(rs, gvo, WS_ST1); }
        if (hh == 0) { stw[r32] = u2f(gv.x) * u2f(gv.z); stw[32 + r32] = u2f(gv.z); }
        STAMP(1);
        const float mean = u2f(gv.x), rstd = u2f(gv.z);
        LBAR();
        STAMP(17);
        const LAS float* csf = (const LAS float*)(uintptr_t)(L0 + L_MISC);
        const float cv = (csf[64 + r32] + csf[96 + 64 + r32]) + (csf[192 + 64 + r32] + csf[288 + 64 + r32]);
        const float qs = rstd * (0.18898223650461362f * 1.4426950408889634f);
#pragma unroll
        for (int q = 0; q < 4; ++q) {
            auto c4 = [&](int o) AI { return (*(const LAS f32x4*)(csf + o) + *(const LAS f32x4*)(csf + 96 + o)) + (*(const LAS f32x4*)(csf + 192 + o) + *(const LAS f32x4*)(csf + 288 + o)); };
            const f32x4 c1 = c4(8 * q + 4 * hh), c2 = c4(32 + 8 * q + 4 * hh);
            const f32x4 mr = *(const LAS f32x4*)(stw + 8 * q + 4 * hh), rr = *(const LAS f32x4*)(stw + 32 + 8 * q + 4 * hh);
#pragma unroll
            for (int i = 0; i < 4; ++i) {
                aq[4 * q + i] = (aq[4 * q + i] - mean * c1[i]) * qs;
                ak[4 * q + i] = (ak[4 * q + i] - mean * c2[i]) * rstd;
                av[4 * q + i] = av[4 * q + i] * rr[i] - mr[i] * cv;
            }
        }
        if (hh == 1) { aq[12] = 1.f; aq[13] = 0.f; aq[14] = 0.f; aq[15] = 0.f; ak[12] = 0.f; ak[13] = 0.f; ak[14] = 0.f; ak[15] = 0.f; }
        if (r32 >= 28) {
            const float pv = r32 == 28 ? 1.f : 0.f;
#pragma unroll
            for (int i = 0; i < 16; ++i) av[i] = pv;
        }
        const u32x4 k0 = pack8(ak, 0), k1 = pack8(ak, 1), v0 = pack8(av, 0), v1 = pack8(av, 1);
        const unsigned go = WS_KV + h * KVH + (4 * m + w) * 4096;
        gst(rs, k0, lane * 16, go); gst(rs, k1, lane * 16, go + 1024); gst(rs, v0, lane * 16, go + 2048); gst(rs, v1, lane * 16, go + 3072);
        const unsigned lo = L0 + L_OWNKV + w * 4096 + lane * 16;
        sts128(lo, k0); sts128(lo + 1024, k1); sts128(lo + 2048, v0); sts128(lo + 3072, v1);
        qf0 = pack8(aq, 0); qf1 = pack8(aq, 1);
        STAMP(18);
        WAIT_BAR(0);
        if (tid == 0) set_flag(P.ws, flag_off(0, m * 16 + h), e);
        STAMP(2);
    }
    {
        LANE_GEOM();
        const int mt = m - 2 + (w >> 1);
        u32x4 kv[8];
        if (mt >= 0) {
            wait_flags(P.ws, flag_off(0, mt * 16 + h), 1, e, lane);
            const unsigned go = WS_KV + h * KVH + (4 * m - 8 + 2 * w) * 4096;
#pragma unroll
            for (int i = 0; i < 8; ++i) kv[i] = gld(rs, lane * 16, go + i * 1024);
        } else {
#pragma unroll
            for (int i = 0; i < 8; ++i) kv[i] = (u32x4){0u, 0u, (i & 3) == 1 && hh == 1 ? 0xF149u : 0u, 0u};
        }
        f32x16 cdiag, cold;
#pragma unroll
        for (int i = 0; i < 16; ++i) { const int kr = (i & 3) + 8 * (i >> 2) + 4 * hh; cdiag[i] = kr <= r32 ? 0.f : -1e30f; cold[i] = kr > r32 ? 0.f : -1e30f; }
#pragma unroll
        for (int i = 0; i < 8; ++i) sts128(L0 + L_KV + 2 * w * 4096 + i * 1024 + lane * 16, kv[i]);
        LBAR();
        STAMP(3);
        unsigned kvb = L0 + L_KV + w * 4096 + lane * 16;
        PIN(kvb);
        u32x4 kf[9][2], vf[9][2];
        const f32x16 zero16 = {};
        auto ldK = [&](int i) AI { kf[i][0] = lds128(kvb + i * 4096); kf[i][1] = lds128(kvb + i * 4096 + 1024); };
        auto ldV = [&](int i) AI { vf[i][0] = lds128(kvb + i * 4096 + 2048); vf[i][1] = lds128(kvb + i * 4096 + 3072); };
        auto smax = [&](const f32x16& s, float a) AI {
#pragma unroll
            for (int i = 0; i < 16; i += 2) a = max3(a, s[i], s[i + 1]);
            return a; };
        ldK(0); ldK(1);
        float mx = -3e38f;
        f32x16 s[2];
        auto smax8 = [&](const f32x16& v, int o, float a) AI {
#pragma unroll
            for (int i = 0; i < 8; i += 2) a = max3(a, v[o + i], v[o + i + 1]);
            return a; };
        sfor<9>([&](auto ic) AI {
            constexpr int i = decltype(ic)::value;
            if constexpr (i + 2 < 9) ldK(i + 2);
            MFMA(s[i & 1], kf[i][0], qf0, i == 0 ? cold : i == 8 ? cdiag : zero16);
            SB();
            MFMA(s[i & 1], kf[i][1], qf1, s[i & 1]);
            SB();
            if constexpr (i > 0) { mx = smax8(s[(i - 1) & 1], 0, mx); mx = smax8(s[(i - 1) & 1], 8, mx); PIN(mx); }
            SB();
        });
        ldK(0); ldK(1); ldV(0);
        mx = hmax(smax(s[0], mx));
        STAMP(25);
        f32x16 o = {}, cm;
#pragma unroll
        for (int i = 0; i < 16; ++i) { cm[i] = -mx; cdiag[i] -= mx; cold[i] -= mx; }
        MFMA(s[0], kf[0][0], qf0, cold); MFMA(s[0], kf[0][1], qf1, s[0]);
        SB();
        sfor<9>([&](auto ic) AI {
            constexpr int i = decltype(ic)::value;
            f32x16& c = s[i & 1];
            f32x16& n = s[(i + 1) & 1];
            if constexpr (i + 2 < 9) ldK(i + 2);
            if constexpr (i + 1 < 9) ldV(i + 1);
            if constexpr (i + 1 < 9) MFMA(n, kf[i + 1][0], qf0, i + 1 == 8 ? cdiag : cm);
#pragma unroll
            for (int j = 0; j < 8; ++j) c[j] = __builtin_amdgcn_exp2f(c[j]);
            u32x4 p0 = pack8(c, 0);
            PIN(p0);
            SB();
            if constexpr (i + 1 < 9) MFMA(n, kf[i + 1][1], qf1, n);
            MFMA(o, vf[i][0], p0, o);
#pragma unroll
            for (int j = 8; j < 16; ++j) c[j] = __builtin_amdgcn_exp2f(c[j]);
            const u32x4 p1 = pack8(c, 1);
            MFMA(o, vf[i][1], p1, o);
            SB();
        });
        STAMP(26);
        auto lr = __builtin_amdgcn_permlane32_swap(f2u(o[12]), f2u(o[12]), false, false);
        const float inv = __builtin_amdgcn_rcpf(u2f(lr[1]));
#pragma unroll
        for (int i = 0; i < 16; ++i) o[i] *= inv;
        if (hh == 1) { o[12] = 0.f; o[13] = 0.f; o[14] = 0.f; o[15] = 0.f; }
        const unsigned vo = (hh * XR + r32) * 16, so = WS_O + ((4 * h) * XR + 128 * m + 32 * w) * 16;
        gst(rs, pack8(o, 0), vo, so); gst(rs, pack8(o, 1), vo, so + 2 * XROW);
        WAIT_BAR(0);
        if (tid == 0) set_flag(P.ws, flag_off(1, m * 16 + h), e);
        STAMP(4);
    }
    const bool act = w < nt;
    const int tw = tb + w;
#define GEMM_GEOM() LANE_GEOM(); \
    unsigned trW[2]; trW[0] = L0 + (w < nt ? w : nt - 1) * 256 + trq * 64 + gq * 8 + 2 * hh * 1024; trW[1] = trW[0] + 57344; PIN(trW[0]); PIN(trW[1]); \
    auto wimg = [&](int kq0, int kq1) AI { return wfrag(kq0 < 56 ? trW[0] + kq0 * 1024 : trW[1] + (kq0 - 56) * 1024, kq1 < 56 ? trW[0] + kq1 * 1024 : trW[1] + (kq1 - 56) * 1024); }; \
    unsigned xl = L0 + L_X + lane * 16; PIN(xl); \
    LAS float* m2 = (LAS float*)(uintptr_t)(L0 + L_M2 + w * 256); (void)m2
    auto gemm = [&](auto nk, const u32x4* wr, unsigned xl) AI -> f32x16 {
        constexpr int NK = decltype(nk)::value, PD = 4;
        f32x16 acc = {};
        u32x4 xf[NK];
#pragma unroll
        for (int k = 0; k < PD; ++k) xf[k] = lds128(xl + k * 1024);
        SB();
        sfor<NK>([&](auto kc) AI {
            constexpr int k = decltype(kc)::value;
            if constexpr (k + PD < NK) xf[k + PD] = lds128(xl + (k + PD) * 1024);
            MFMA(acc, wr[k], xf[k], acc);
            SB();
        });
        return acc;
    };
    u32x4 hp[2];
    float meanH, rstdH;
    {
        GEMM_GEOM();
        u32x4 xr[2];
        const char* xp = (const char*)P.x + (size_t)(32 * ti + r32) * 896 + 64 * (act ? tw : tb) + 16 * hh;
        xr[0] = *(const GAS u32x4*)xp; xr[1] = *(const GAS u32x4*)(xp + 32);
        u32x4 wr[32];
        sfor<32>([&](auto kc) AI { constexpr int k = decltype(kc)::value, kq = 7 * (k >> 1) + 4 * (k & 1);
            if constexpr (k < 31) wr[k] = wimg(kq, kq + 1);
            else { const u32x2 p = ldstr(trW[1] + (kq - 56) * 1024), q = ldstr(trW[1] + (kq - 56) * 1024 + (hh ? 0 : 1024)); wr[k] = (u32x4){p.x, p.y, q.x, q.y}; } });
        LBAR();
        STAMP(19);
        wait_flags(P.ws, flag_off(1, (ti >> 2) * 16 + 4 * w), 4, e, lane);
        STAMP(20);
        u32x4 xf[8];
#pragma unroll
        for (int k = 0; k < 8; ++k) xf[k] = gld(rs, vx, WS_O + ((16 * w + 2 * k) * XR + 32 * ti) * 16);
#pragma unroll
        for (int k = 0; k < 8; ++k) sts128(xl + (8 * w + k) * 1024, xf[k]);
        LBAR();
        STAMP(5);
        f32x16 r = gemm(IC(32), wr, xl);
        STAMP(21);
        float s1 = 0.f, s2 = 0.f;
#pragma unroll
        for (int s = 0; s < 2; ++s) {
#pragma unroll
            for (int i = 0; i < 4; ++i) {
                hp[s][i] = pk(r[8 * s + 2 * i] + blo(xr[s][i]), r[8 * s + 2 * i + 1] + bhi(xr[s][i]));
                s1 = dot2(hp[s][i], ONES, s1); s2 = dot2(hp[s][i], hp[s][i], s2);
            }
            if (act) gst(rs, hp[s], vx, WS_H + ((4 * tw + 2 * s) * XR + 32 * ti) * 16);
        }
        s1 = hsum(s1); s2 = hsum(s2);
        if (act && hh == 0) gst8(rs, (u32x2){f2u(s1), f2u(s2)}, r32 * 8, WS_STH + (tw * 2048 + 32 * ti) * 8);
        STAMP(22);
        WAIT_BAR(0);
        if (tid == 0) set_flag(P.ws, flag_off(2, ti * 4 + g), e);
        STAMP(6);
    }
#define LOAD_WR() u32x4 wr[28]; sfor<28>([&](auto kc) AI { constexpr int k = decltype(kc)::value; wr[k] = wimg(4 * k, 4 * k + 1); })
    const int fsn = w == 0 ? 1 : 2, fsl = w <= 1 ? 0 : w - 1;
    {
        GEMM_GEOM();
        BAR();
        LOAD_WR();
        LBAR();
        wait_flags(P.ws, flag_off(2, ti * 4), 4, e, lane);
        u32x4 xf[7];
#pragma unroll
        for (int k = 0; k < 7; ++k) xf[k] = gld(rs, vx, WS_H + ((14 * w + 2 * k) * XR + 32 * ti) * 16);
        u32x2 sv[7];
#pragma unroll
        for (int q = 0; q < 7; ++q) sv[q] = gld8(rs, (7 * hh * 2048 + r32) * 8, WS_STH + (q * 2048 + 32 * ti) * 8);
        SB();
        {
            float cs[4] = {0.f, 0.f, 0.f, 0.f};
#pragma unroll
            for (int k = 0; k < 28; ++k)
#pragma unroll
                for (int i = 0; i < 4; ++i) cs[i] = dot2(wr[k][i], ONES, cs[i]);
            const float c = hsum((cs[0] + cs[1]) + (cs[2] + cs[3]));
            if (hh == 0) m2[r32] = c;
        }
        SB();
#pragma unroll
        for (int k = 0; k < 7; ++k) sts128(xl + (7 * w + k) * 1024, xf[k]);
        LBAR();
        STAMP(7);
        f32x16 r = gemm(IC(28), wr, xl);
        float t1 = 0.f, t2 = 0.f;
#pragma unroll
        for (int q = 0; q < 7; ++q) { t1 += u2f(sv[q].x); t2 += u2f(sv[q].y); }
        t1 = hsum(t1); t2 = hsum(t2);
        meanH = t1 * (1.f / D);
        const float var = __builtin_fmaxf(t2 * (1.f / D) - meanH * meanH, 0.f);
        rstdH = __builtin_amdgcn_rsqf(var + EPS);
        const float sc = rstdH * __builtin_amdgcn_rsqf(var * rstdH * rstdH + EPS);
#pragma unroll
        for (int q = 0; q < 4; ++q) {
            const f32x4 c = *(const LAS f32x4*)(m2 + 8 * q + 4 * hh);
#pragma unroll
            for (int i = 0; i < 4; ++i) r[4 * q + i] = gelu_t((r[4 * q + i] - meanH * c[i]) * sc);
        }
#pragma unroll
        for (int s = 0; s < 2; ++s) if (act) gst(rs, pack8(r, s), vx, WS_HH + ((4 * tw + 2 * s) * XR + 32 * ti) * 16);
        WAIT_BAR(0);
        if (tid == 0) set_flag(P.ws, flag_off(3, ti * 4 + g), e);
        STAMP(8);
    }
    {
        GEMM_GEOM();
        BAR();
        LOAD_WR();
        LBAR();
        const int sh = w == 0 ? -1 : w == 1 ? 1 : w == 2 ? -2 : 2;
        const int tn = ti + (sh < 0 ? -1 : 1);
        {
            const bool nb = tn >= 0 && tn < 64;
            const int li = lane & 3;
            const GAS unsigned* p = (const GAS unsigned*)(P.ws + flag_off(3, (((li & 2) && nb) ? tn : ti) * 4 + fsl + ((li & 1) && fsn == 2 ? 1 : 0)));
            poll_word(p, e);
        }
        const int tok = 32 * ti + r32 + sh;
        const bool ok = tok >= 0 && tok < S;
        const unsigned vxs = ok ? (hh * XR + tok) * 16 : 0x7ffffff0u;
        u32x4 xf[7];
#pragma unroll
        for (int k = 0; k < 7; ++k) { xf[k] = gld(rs, vxs, WS_HH + (14 * w + 2 * k) * XROW); }
#pragma unroll
        for (int k = 0; k < 7; ++k) sts128(xl + (7 * w + k) * 1024, xf[k]);
        LBAR();
        STAMP(9);
        f32x16 r = gemm(IC(28), wr, xl);
        float s1 = 0.f, s2 = 0.f;
#pragma unroll
        for (int s = 0; s < 2; ++s) {
            u32x4 xp;
#pragma unroll
            for (int i = 0; i < 4; ++i) {
                xp[i] = pk(r[8 * s + 2 * i] + (blo(hp[s][i]) - meanH) * rstdH, r[8 * s + 2 * i + 1] + (bhi(hp[s][i]) - meanH) * rstdH);
                s1 = dot2(xp[i], ONES, s1); s2 = dot2(xp[i], xp[i], s2);
            }
            if (act) gst(rs, xp, vx, WS_X1 + ((4 * tw + 2 * s) * XR + 32 * ti) * 16);
        }
        s1 = hsum(s1); s2 = hsum(s2);
        if (act && hh == 0) gst8(rs, (u32x2){f2u(s1), f2u(s2)}, r32 * 8, WS_STX + (tw * 2048 + 32 * ti) * 8);
        WAIT_BAR(0);
        if (tid == 0) set_flag(P.ws, flag_off(4, ti * 4 + g), e);
        STAMP(10);
    }
    {
        LANE_GEOM();
        unsigned tr6[2];
        tr6[0] = L0 + trq * 64 + gq * 8 + 2 * hh * 1024; tr6[1] = tr6[0] + 57344; PIN(tr6[0]); PIN(tr6[1]);
        auto wf6 = [&](int t, int k) AI { const unsigned a = (k < 14 ? tr6[0] + k * 4096 : tr6[1] + (k - 14) * 4096) + t * 256; return wfrag(a, a + 1024); };
        const LAS float* cs6 = (const LAS float*)(uintptr_t)(L0 + L_M2 + 1024);
        wait_flags(P.ws, flag_off(4, (4 * m + w) * 4), 4, e, lane);
        STAMP(28);
        const int tk = 128 * m + 32 * w;
        auto ldx1 = [&](int k) AI { return gld(rs, vx, WS_X1 + (2 * k * XR + tk) * 16); };
        constexpr int PX = P6_PX, PW = 2;
        f32x16 acc[4] = {};
        u32x2 sv[7];
        {
            u32x4 xf[28], wf[28][4];
#pragma unroll
            for (int k = 0; k < PX; ++k) xf[k] = ldx1(k);
#pragma unroll
            for (int q = 0; q < 7; ++q) sv[q] = gld8(rs, (7 * hh * 2048 + r32) * 8, WS_STX + (q * 2048 + tk) * 8);
            BAR();
            STAMP(11);
#pragma unroll
            for (int k = 0; k < PW; ++k)
#pragma unroll
                for (int t = 0; t < 4; ++t) wf[k][t] = wf6(t, k);
            SB();
            sfor<28>([&](auto kc) AI {
                constexpr int k = decltype(kc)::value;
                MFMA(acc[0], wf[k][0], xf[k], acc[0]);
                if constexpr (k + PW < 28) { wf[k + PW][0] = wf6(0, k + PW); }
                SB();
                MFMA(acc[1], wf[k][1], xf[k], acc[1]);
                if constexpr (k + PW < 28) { wf[k + PW][1] = wf6(1, k + PW); }
                if constexpr (k + PX < 28) xf[k + PX] = ldx1(k + PX);
                SB();
                MFMA(acc[2], wf[k][2], xf[k], acc[2]);
                if constexpr (k + PW < 28) { wf[k + PW][2] = wf6(2, k + PW); }
                SB();
                MFMA(acc[3], wf[k][3], xf[k], acc[3]);
                if constexpr (k + PW < 28) { wf[k + PW][3] = wf6(3, k + PW); }
                SB();
            });
        }
        STAMP(23);
        LBAR();
        STAMP(24);
        float t1 = 0.f, t2 = 0.f;
#pragma unroll
        for (int q = 0; q < 7; ++q) { t1 += u2f(sv[q].x); t2 += u2f(sv[q].y); }
        t1 = hsum(t1); t2 = hsum(t2);
        const float mean = t1 * (1.f / D), var = __builtin_fmaxf(t2 * (1.f / D) - mean * mean, 0.f) + EPS, rstd = __builtin_amdgcn_rsqf(var);
        const GeluK gk = {-mean, GC1 * rstd * rstd * rstd, GC0 * rstd, var * rstd};
        const unsigned uo = WS_U + ((4 * tb6) * XR + tk) * 16;
        auto epi1 = [&](f32x16& y, int te) AI {
#pragma unroll
            for (int q = 0; q < 4; ++q) {
                const f32x4 c = *(const LAS f32x4*)(cs6 + te * 32 + 8 * q + 4 * hh);
#pragma unroll
                for (int i = 0; i < 4; ++i) {
                    const float v = __builtin_fmaf(gk.nmean, c[i], y[4 * q + i]);
                    const float z = v * __builtin_fmaf(v * v, gk.k1, gk.k0);
                    y[4 * q + i] = v * __builtin_amdgcn_rcpf(__builtin_fmaf(__builtin_amdgcn_exp2f(z), gk.irs, gk.irs));
                }
            }
            gst(rs, pack8(y, 0), vx, uo + 4 * te * XROW); gst(rs, pack8(y, 1), vx, uo + (4 * te + 2) * XROW);
            SB();
        };
        epi1(acc[0], 0); epi1(acc[1], 1); epi1(acc[2], 2);
        if (nt6 == 4) epi1(acc[3], 3);
        STAMP(29);
        WAIT_BAR(0);
        if (tid == 0) set_flag(P.ws, flag_off(5, m * 16 + s16), e);
        BAR();
        STAMP(12);
    }
    if (act7) {
        LANE_GEOM();
        const unsigned lanep = trq * 64 + gq * 8 + 2 * hh * 512;
        unsigned tA = L0 + L_X + w * 8192 + lanep, tB = wst + lanep; PIN(tA); PIN(tB);
        auto w4f = [&](int k, int t) AI { const unsigned a = (k < 4 ? tA + k * 2048 : k < 18 ? tB + (k - 4) * 2048 : tB + (k - 18) * 2048) + t * 256; return wfrag(a, a + 512); };
        u32x4 wr[28][2];
#pragma unroll
        for (int k = 0; k < 14; ++k) { wr[k][0] = w4f(k, 0); wr[k][1] = w4f(k, 1); }
        LBAR();
        const int tk = 64 * j7;
        const int tt = w >> 1, nt7 = w & 1;
        wait_flags(P.ws, flag_off(5, (j7 >> 1) * 16 + 4 * w), 4, e, lane);
        constexpr int PX = P7_PX;
        u32x4 xa[28][2];
#pragma unroll
        for (int k = 0; k < PX; ++k) { xa[k][0] = gld(rs, vx, WS_U + ((56 * w + 2 * k) * XR + tk) * 16); xa[k][1] = gld(rs, vx, WS_U + ((56 * w + 2 * k) * XR + tk + 32) * 16); }
        STAMP(13);
        SB();
        f32x16 acc[4] = {};
        u32x4 rh[2], rx[2];
        sfor<28>([&](auto kc) AI {
            constexpr int k = decltype(kc)::value;
            if constexpr (k == 13) { BAR(); SB(); }
            if constexpr (k + 3 >= 14 && k + 3 < 28) { wr[k + 3][0] = w4f(k + 3, 0); wr[k + 3][1] = w4f(k + 3, 1); }
            MFMA(acc[0], wr[k][0], xa[k][0], acc[0]); MFMA(acc[1], wr[k][1], xa[k][0], acc[1]);
            if constexpr (k + PX < 28) xa[k + PX][0] = gld(rs, vx, WS_U + ((56 * w + 2 * (k + PX)) * XR + tk) * 16);
            MFMA(acc[2], wr[k][0], xa[k][1], acc[2]); MFMA(acc[3], wr[k][1], xa[k][1], acc[3]);
            if constexpr (k + PX < 28) xa[k + PX][1] = gld(rs, vx, WS_U + ((56 * w + 2 * (k + PX)) * XR + tk + 32) * 16);
            if constexpr (k == 24 || k == 25) {
                constexpr int s = k - 24;
                const unsigned so = ((8 * g7 + 4 * nt7 + 2 * s) * XR + tk + 32 * tt) * 16;
                rh[s] = gld(rs, vx, WS_H + so); rx[s] = gld(rs, vx, WS_X1 + so);
            }
            SB();
        });
        STAMP(27);
#pragma unroll
        for (int t = 0; t < 4; ++t) {
            if (t != w) {
                const unsigned a = L0 + L_X + t * 12288 + (w < t ? w : w - 1) * 4096 + lane * 16;
#pragma unroll
                for (int q = 0; q < 4; ++q) stsf4(a + q * 1024, (f32x4){acc[t][4 * q], acc[t][4 * q + 1], acc[t][4 * q + 2], acc[t][4 * q + 3]});
            }
        }
        LBAR();
        f32x16 r = w == 0 ? acc[0] : w == 1 ? acc[1] : w == 2 ? acc[2] : acc[3];
        const unsigned a = L0 + L_X + w * 12288 + lane * 16;
#pragma unroll
        for (int j = 0; j < 3; ++j)
#pragma unroll
            for (int q = 0; q < 4; ++q) { const f32x4 v = ldsf4(a + j * 4096 + q * 1024); r[4 * q] += v[0]; r[4 * q + 1] += v[1]; r[4 * q + 2] += v[2]; r[4 * q + 3] += v[3]; }
        const unsigned oo = (tk + 32 * tt + r32) * 896 + 128 * g7 + 64 * nt7 + 16 * hh;
        char* op = (char*)P.out + oo;
        const __amdgpu_buffer_rsrc_t ro = __builtin_amdgcn_make_buffer_rsrc((void*)P.out, 0, S * D * 2, 0x00020000); (void)ro; (void)op;
#pragma unroll
        for (int s = 0; s < 2; ++s) {
            u32x4 ov;
#pragma unroll
            for (int i = 0; i < 4; ++i)
                ov[i] = pk(r[8 * s + 2 * i] + blo(rx[s][i]) + blo(rh[s][i]), r[8 * s + 2 * i + 1] + bhi(rx[s][i]) + bhi(rh[s][i]));
            *(GAS u32x4*)(op + 32 * s) = ov;
        }
        STAMP(14);
    }
    STAMP_FLUSH();
}
}

extern "C" void kernel_launch(void* const* d_in, const int* in_sizes, int n_in, void* d_out, int out_size, void* d_ws, size_t ws_size,
                              hipStream_t stream) {
    mk::Params P;
    P.x = (const u16*)d_in[0]; P.wqkv = (const u16*)d_in[1]; P.wo = (const u16*)d_in[2];
    P.w1 = (const u16*)d_in[4]; P.w2 = (const u16*)d_in[5]; P.w3 = (const u16*)d_in[6]; P.w4 = (const u16*)d_in[7];
    P.out = (u16*)d_out; P.ws = (char*)d_ws;
    mk::fused_fwd<<<256, 512, 0, stream>>>(P);
}
```

```cpp
#define LOADER_FIRST 1
#define P1_ROLLED 1
#define EARLY_CODE 1
#define TOUCH_FLAGS 1
#define W4SPLIT 36
#define PRIO_C 2
#define PRIO_L 0
#define NT_W 0
#define NT_QKV 0
#define NT_X 0
#define OUT_MODE 0
#define P6_PX 10
#define P6_PW 3
#define P6_PG 6
#define P7_PX 5
#define HALF 12
#define POLL2 0
#define POLL_GAP 3
#define TOUCH_DELAY 12
#define EARLY_TOUCH 1
#define LATE_TOUCH 3
#include <hip/hip_runtime.h>
#include <hip/hip_bf16.h>
#include <stdint.h>
#include <type_traits>
#include <utility>

typedef unsigned short u16;
typedef __attribute__((ext_vector_type(8))) short bf16x8;
typedef __attribute__((ext_vector_type(4))) short s16x4;
typedef __attribute__((ext_vector_type(16))) float f32x16;
typedef __attribute__((ext_vector_type(4))) float f32x4;
typedef __attribute__((ext_vector_type(2))) float f32x2;
typedef __attribute__((ext_vector_type(4))) unsigned u32x4;
typedef __attribute__((ext_vector_type(2))) unsigned u32x2;
typedef __attribute__((ext_vector_type(2))) __bf16 bf2;
#define LAS __attribute__((address_space(3)))
#define GAS __attribute__((address_space(1)))
#define DEVI __device__ __forceinline__

#define MK_STAMP 0

namespace mk {
constexpr int S = 2048, D = 448;
constexpr unsigned XR = 2056, XROW = XR * 16;
constexpr unsigned KVH = 64 * 4096 + 128;
constexpr unsigned WS_EP = 0;
constexpr unsigned WS_FLAG = 4096;
constexpr unsigned WS_KV = 262144;
constexpr unsigned WS_O = WS_KV + 16 * KVH;
constexpr unsigned WS_H = WS_O + 64 * XROW;
constexpr unsigned WS_HH = WS_H + 56 * XROW;
constexpr unsigned WS_X1 = WS_HH + 56 * XROW;
constexpr unsigned WS_U = WS_X1 + 56 * XROW;
constexpr unsigned WS_STH = WS_U + 224 * XROW;
constexpr unsigned WS_STX = WS_STH + 262144;
constexpr unsigned WS_ST1 = WS_STX + 262144;
constexpr unsigned WS_SPARE = WS_ST1 + 32768;
constexpr unsigned WS_STAMP = WS_SPARE + 128;
constexpr unsigned WS_END = WS_STAMP + 16 * 512 * 8;
constexpr unsigned L_SLOT = 28672;
constexpr unsigned L_MISC = 143360;
constexpr unsigned L_KV = 114688;
constexpr unsigned L_OWNKV = L_KV + 8 * 4096;
constexpr unsigned L_WST = 28672;
constexpr unsigned L_X = 114688;
constexpr unsigned L_M2 = 147456;
constexpr unsigned CODE_TOUCH_BYTES = 35000;
constexpr float EPS = 1e-5f;
constexpr unsigned SPIN_CAP = 1u << 18;

struct Params {
    const u16* x; const u16* wqkv; const u16* wo; const u16* w1; const u16* w2; const u16* w3; const u16* w4;
    u16* out; char* ws;
};

DEVI unsigned f2u(float f) { return __builtin_bit_cast(unsigned, f); }
DEVI float u2f(unsigned u) { return __builtin_bit_cast(float, u); }
DEVI bool wall(bool p) { return __builtin_amdgcn_ballot_w64(!p) == 0; }
template <int X> DEVI float xor32(float v) { return u2f((unsigned)__builtin_amdgcn_ds_swizzle((int)f2u(v), (X << 10) | 0x1f)); }
DEVI unsigned pk(float lo, float hi) { bf2 v = {(__bf16)lo, (__bf16)hi}; return __builtin_bit_cast(unsigned, v); }
DEVI float blo(unsigned u) { return u2f(u << 16); }
DEVI float bhi(unsigned u) { return u2f(u & 0xffff0000u); }
DEVI float dot2(unsigned a, unsigned b, float c) { return __builtin_amdgcn_fdot2_f32_bf16(__builtin_bit_cast(bf2, a), __builtin_bit_cast(bf2, b), c, false); }
constexpr unsigned ONES = 0x3f803f80u;
DEVI float hsum(float v) { auto r = __builtin_amdgcn_permlane32_swap(f2u(v), f2u(v), false, false); return u2f(r[0]) + u2f(r[1]); }
DEVI float hmax(float v) { auto r = __builtin_amdgcn_permlane32_swap(f2u(v), f2u(v), false, false); return __builtin_fmaxf(u2f(r[0]), u2f(r[1])); }
DEVI f32x16 mfma(u32x4 a, u32x4 b, f32x16 c) { return __builtin_amdgcn_mfma_f32_32x32x16_bf16(__builtin_bit_cast(bf16x8, a), __builtin_bit_cast(bf16x8, b), c, 0, 0, 0); }
#define MFMA(acc, a, b, c) do { acc = mfma(a, b, c); asm volatile("" : "+v"(acc)); } while (0)
DEVI u32x4 lds128(unsigned a) { return *(const LAS u32x4*)(uintptr_t)a; }
DEVI f32x4 ldsf4(unsigned a) { return *(const LAS f32x4*)(uintptr_t)a; }
DEVI void sts128(unsigned a, u32x4 v) { *(LAS u32x4*)(uintptr_t)a = v; }
DEVI void stsf4(unsigned a, f32x4 v) { *(LAS f32x4*)(uintptr_t)a = v; }
DEVI u32x2 ldstr(unsigned a) { return __builtin_bit_cast(u32x2, __builtin_amdgcn_ds_read_tr16_b64_v4i16((LAS s16x4*)(uintptr_t)a)); }
DEVI u32x4 wfrag(unsigned a0, unsigned a1) { u32x2 p = ldstr(a0), q = ldstr(a1); return (u32x4){p.x, p.y, q.x, q.y}; }
DEVI u32x4 pack8(const f32x16& a, int s) { return (u32x4){pk(a[8 * s], a[8 * s + 1]), pk(a[8 * s + 2], a[8 * s + 3]), pk(a[8 * s + 4], a[8 * s + 5]), pk(a[8 * s + 6], a[8 * s + 7])}; }
constexpr int SC1 = 16;
DEVI u32x4 gld(__amdgpu_buffer_rsrc_t rs, unsigned voff, unsigned soff) { return __builtin_amdgcn_raw_buffer_load_b128(rs, voff, soff, SC1); }
DEVI u32x2 gld8(__amdgpu_buffer_rsrc_t rs, unsigned voff, unsigned soff) { return __builtin_amdgcn_raw_buffer_load_b64(rs, voff, soff, SC1); }
DEVI void gst(__amdgpu_buffer_rsrc_t rs, u32x4 v, unsigned voff, unsigned soff) { __builtin_amdgcn_raw_buffer_store_b128(v, rs, voff + soff, 0, SC1); }
DEVI void gst8(__amdgpu_buffer_rsrc_t rs, u32x2 v, unsigned voff, unsigned soff) { __builtin_amdgcn_raw_buffer_store_b64(v, rs, voff, soff, SC1); }
#define WAIT_VM(N) asm volatile("s_waitcnt vmcnt(" #N ")" ::: "memory")
#define WAIT_LGKM() asm volatile("s_waitcnt lgkmcnt(0)" ::: "memory")
#define WAIT_BAR(N) asm volatile("s_waitcnt vmcnt(" #N ") lgkmcnt(0)\n\ts_barrier" ::: "memory")
#define LBAR() asm volatile("s_waitcnt lgkmcnt(0)\n\ts_barrier" ::: "memory")
#define SB() __builtin_amdgcn_sched_barrier(0)
#define PIN(x) asm volatile("" : "+v"(x))
DEVI float fma1(float a, float b, float c) { return __builtin_fmaf(a, b, c); }
DEVI float mul1(float a, float b) { return a * b; }
DEVI float max3(float a, float b, float c) { return __builtin_fmaxf(__builtin_fmaxf(a, b), c); }
template <int... Is, class F> DEVI void sfor_impl(std::integer_sequence<int, Is...>, F&& f) { (f(std::integral_constant<int, Is>{}), ...); }
template <int N, class F> DEVI void sfor(F&& f) { sfor_impl(std::make_integer_sequence<int, N>{}, f); }
#define AI __attribute__((always_inline))
#define IC(n) std::integral_constant<int, n>{}

template <int NT = 0> DEVI void dma1(const char* sbase, unsigned voff, unsigned lds) {
    if (NT) asm volatile("s_mov_b32 m0, %2\n\ts_nop 0\n\tglobal_load_lds_dwordx4 %0, %1 nt" :: "v"(voff), "s"(sbase), "s"(lds) : "memory");
    else asm volatile("s_mov_b32 m0, %2\n\ts_nop 0\n\tglobal_load_lds_dwordx4 %0, %1" :: "v"(voff), "s"(sbase), "s"(lds) : "memory");
}
DEVI void touch(const char* sbase, unsigned voff, unsigned lds) {
    asm volatile("s_mov_b32 m0, %2\n\ts_nop 0\n\tglobal_load_lds_dword %0, %1" :: "v"(voff), "s"(sbase), "s"(lds) : "memory");
}
struct WSrc { const char* base; unsigned step, voff; };
DEVI void wpiece(const WSrc& s, unsigned lds, int n) { dma1<NT_W>(s.base + (size_t)n * s.step, s.voff, lds + n * 1024); }
DEVI WSrc wsrc_a(const char* base, unsigned rs, int nt, int lane) { const int t = (lane >> 4) < nt ? (lane >> 4) : nt - 1; return WSrc{base, 4 * rs, ((lane >> 2) & 3) * rs + t * 64 + (lane & 3) * 16}; }
DEVI WSrc wsrc_b(const char* base, unsigned rs, int lane) { return WSrc{base, 8 * rs, (4 * (lane >> 5) + ((lane >> 2) & 3)) * rs + ((lane >> 4) & 1) * 64 + (lane & 3) * 16}; }

DEVI void poll_word(const GAS unsigned* p, unsigned e) {
    unsigned a = __hip_atomic_load(p, __ATOMIC_RELAXED, __HIP_MEMORY_SCOPE_AGENT);
#pragma nounroll
    for (unsigned sp = 0; sp < SPIN_CAP; ++sp) {
        __builtin_amdgcn_s_sleep(POLL_GAP);
        const unsigned b2 = __hip_atomic_load(p, __ATOMIC_RELAXED, __HIP_MEMORY_SCOPE_AGENT);
        if (wall(a == e)) break;
        __builtin_amdgcn_s_sleep(POLL_GAP);
        a = __hip_atomic_load(p, __ATOMIC_RELAXED, __HIP_MEMORY_SCOPE_AGENT);
        if (wall(b2 == e)) break;
    }
    __builtin_amdgcn_fence(__ATOMIC_ACQUIRE, "wavefront");
}
DEVI void wait_flags(const char* ws, unsigned foff, int n, unsigned e, int lane) {
    const GAS unsigned* p = (const GAS unsigned*)(ws + foff) + (lane < n ? lane : 0) * 32;
    if (POLL2) poll_word(p, e);
    else {
#pragma nounroll
        for (unsigned sp = 0; sp < SPIN_CAP; ++sp) {
            unsigned v = __hip_atomic_load(p, __ATOMIC_RELAXED, __HIP_MEMORY_SCOPE_AGENT);
            if (wall(v == e)) break;
        }
        __builtin_amdgcn_fence(__ATOMIC_ACQUIRE, "wavefront");
    }
}
DEVI void set_flag(char* ws, unsigned foff, unsigned e) { __hip_atomic_store((GAS unsigned*)(ws + foff), e, __ATOMIC_RELAXED, __HIP_MEMORY_SCOPE_AGENT); }
DEVI unsigned flag_off(int ph, int idx) { return WS_FLAG + (ph * 256 + idx) * 128; }

constexpr float GC0 = -2.f * 0.7978845608028654f * 1.4426950408889634f, GC1 = GC0 * 0.044715f;
DEVI float gelu_t(float x) {
    float z = x * __builtin_fmaf(x * x, GC1, GC0);
    return x * __builtin_amdgcn_rcpf(1.f + __builtin_amdgcn_exp2f(z));
}
struct GeluK { float nmean, k1, k0, irs; };
DEVI void gelu_step(int o, float& y, float& z, float c, const GeluK& g) {
    if (o == 0) y = fma1(g.nmean, c, y);
    else if (o == 1) z = mul1(y, y);
    else if (o == 2) z = fma1(z, g.k1, g.k0);
    else if (o == 3) z = mul1(y, z);
    else if (o == 4) z = __builtin_amdgcn_exp2f(z);
    else if (o == 5) z = fma1(z, g.irs, g.irs);
    else if (o == 6) z = __builtin_amdgcn_rcpf(z);
    else y = mul1(y, z);
}

#define STAMP(k) do {} while (0)
#define STAMP_DECL
#define STAMP_FLUSH() do {} while (0)

#define LANE_GEOM() int lane_ = threadIdx.x & 63; asm volatile("" : "+v"(lane_)); const int lane = lane_, r32 = lane & 31, hh = lane >> 5; \
    const int trq = (lane & 15) >> 2, trQ_ = ((lane >> 4) & 1) * 4 + (lane & 3), gq = (trQ_ & 4) | ((trQ_ & 1) << 1) | ((trQ_ >> 1) & 1); \
    const unsigned vx = (hh * XR + r32) * 16; (void)trq; (void)gq; (void)vx; (void)r32; (void)hh
constexpr unsigned L_FLG = L_MISC + 1920, F_RDY = 0, F_DONE = 16;
DEVI unsigned ldsw_ld(unsigned i) { return *(volatile LAS unsigned*)(uintptr_t)(L_FLG + 4 * i); }
DEVI u32x2 ldsw_ld2(unsigned i) { return *(volatile LAS u32x2*)(uintptr_t)(L_FLG + 4 * i); }
DEVI void ldsw_st(unsigned i, unsigned v) { *(volatile LAS unsigned*)(uintptr_t)(L_FLG + 4 * i) = v; }
DEVI void ring_wait(int c) {
#pragma nounroll
    for (unsigned sp = 0; sp < SPIN_CAP * 16u; ++sp) { const u32x2 v = ldsw_ld2(F_RDY + 2 * c); if (__builtin_amdgcn_readfirstlane(v.x & v.y)) break; __builtin_amdgcn_s_sleep(1); }
}
DEVI void slot_wait(unsigned n, int lane) {
#pragma nounroll
    for (unsigned sp = 0; sp < SPIN_CAP * 16u; ++sp) { const unsigned v = ldsw_ld(F_DONE + (lane < 6 ? lane : 0)); if (wall(v >= n)) break; __builtin_amdgcn_s_sleep(1); }
}
#define LB(N) asm volatile("s_waitcnt vmcnt(" #N ")\n\ts_barrier" ::: "memory")
#define BAR() asm volatile("s_barrier" ::: "memory")

__global__ __launch_bounds__(512, 2) void fused_fwd(Params P) {
    __shared__ __attribute__((aligned(16))) char smem[163840];
    const unsigned L0 = (unsigned)(uintptr_t)(LAS char*)smem;
    const int tid = threadIdx.x, wv = __builtin_amdgcn_readfirstlane(tid >> 6), w = wv & 3;
    const int b = blockIdx.x, xcd = b & 7, slot = b >> 3;
    if (wv == 6 && EARLY_CODE) {
        unsigned long long pc; unsigned back;
        asm volatile("s_getpc_b64 %0\nmk_code_here0:\n\ts_mov_b32 %1, mk_code_here0-_ZN2mk9fused_fwdENS_6ParamsE" : "=s"(pc), "=s"(back));
        const unsigned l = tid & 63, oc = (slot * 18 + (l < 18 ? l : 0)) * 128;
        const char* t0 = (const char*)(pc - back) + (oc < CODE_TOUCH_BYTES ? oc : 0);
        asm volatile("s_mov_b32 m0, %1\n\ts_nop 0\n\tglobal_load_lds_dword %0, off" :: "v"(t0), "s"(L0 + L_MISC + 2048) : "memory");
    }
    const int m = 2 * xcd + (slot >> 4), h = slot & 15;
    const int g = slot & 3, nt = g < 2 ? 4 : 3, tb = g < 2 ? 4 * g : 3 * g + 2;
    const int ti = 8 * xcd + (slot >> 2);
    const int s16 = slot & 15, pr = s16 >> 1;
    const int nt6 = ((s16 ^ pr) & 1) ? 3 : 4, tb6 = 7 * pr + ((s16 & 1) ? (pr & 1 ? 3 : 4) : 0);
    const bool act7 = slot < 28;
    const int j7 = 4 * xcd + (act7 ? slot / 7 : 0), g7 = act7 ? slot % 7 : 0;
    const unsigned wst = L0 + w * L_WST;
    const unsigned shift = (56 * h) & 15u;
    STAMP_DECL

    if (__builtin_expect(wv >= 4, LOADER_FIRST)) {
        if (PRIO_L) __builtin_amdgcn_s_setprio(PRIO_L);
        LANE_GEOM();
        const unsigned wrd = 16384 + hh * 6 * 256 + trq * 64 + ((shift + gq * 8) & 63);
        LAS float* csp = (LAS float*)(uintptr_t)(L0 + L_MISC);
        if (__builtin_expect(w < 2, LOADER_FIRST)) {
            const unsigned xoffE = (lane >> 3) * 896 + (((lane & 7) ^ (lane >> 4)) << 4);
            const unsigned xoffO = (lane >> 3) * 896 + (((lane & 7) ^ (4 + (lane >> 4))) << 4);
            const unsigned colb = (56 * h) & ~15u;
            unsigned woff[3];
#pragma unroll
            for (int j = 0; j < 3; ++j) {
                const int bl = 4 * j + (lane >> 4), kql = bl / 3, tile = bl % 3;
                woff[j] = (4 * kql + ((lane >> 2) & 3)) * 2688 + tile * 896 + colb + (lane & 3) * 16;
            }
            const char* xb = (const char*)P.x + (size_t)(128 * m + 64 * w) * 896;
            const char* wb = (const char*)P.wqkv + (size_t)(32 * w) * 2688;
            LBAR();
#pragma unroll 1
            for (int kc = 0; kc < 7; ++kc) {
                if (kc >= 5) slot_wait(kc - 4, lane);
                const unsigned base = L0 + (kc >= 5 ? kc - 5 : kc) * L_SLOT;
#pragma unroll
                for (int i = 0; i < 6; ++i) dma1<NT_QKV>(wb + (size_t)kc * 64 * 2688 + (i / 3) * 16 * 2688, woff[i % 3], base + 16384 + (6 * w + i) * 1024);
#pragma unroll
                for (int i = 0; i < 8; ++i) dma1<NT_X>(xb + kc * 128 + i * 8 * 896, (i & 1) ? xoffO : xoffE, base + (8 * w + i) * 1024);
                if (kc > 0) { WAIT_VM(14); ldsw_st(F_RDY + 2 * (kc - 1) + w, 1); }
            }
            WAIT_VM(0); ldsw_st(F_RDY + 12 + w, 1);
            {
                ring_wait(6);
                const unsigned a = L0 + L_SLOT + wrd + w * 3072;
#pragma unroll
                for (int t = 0; t < 3; ++t) { const u32x4 f = wfrag(a + t * 256, a + 768 + t * 256);
                    const float v = hsum((dot2(f[0], ONES, 0.f) + dot2(f[1], ONES, 0.f)) + (dot2(f[2], ONES, 0.f) + dot2(f[3], ONES, 0.f)));
                    if (hh == 0) csp[(2 + w) * 96 + t * 32 + r32] = v; }
            }
        } else {
            const int srow = 128 * m + 8 * h + 4 * (w - 2) + (lane >> 4);
            const char* sp = (const char*)P.x + (size_t)srow * 896 + (lane & 15) * 16;
            u32x4 sr[4];
            asm volatile("global_load_dwordx4 %0, %4, off\n\tglobal_load_dwordx4 %1, %4, off offset:256\n\tglobal_load_dwordx4 %2, %4, off offset:512\n\tglobal_load_dwordx4 %3, %5, off"
                         : "=&v"(sr[0]), "=&v"(sr[1]), "=&v"(sr[2]), "=&v"(sr[3]) : "v"(sp), "v"(sp + ((lane & 15) < 8 ? 768 : 0)) : "memory");
            if (w == 2 && lane < 32) ldsw_st(lane, 0);
            LBAR();
            {
                const unsigned lx = lane < 56 ? lane : 0, lw = b * 37 + (lane < 37 ? lane : 0);
                const unsigned fl = lane - 56;
                const int fidx = fl < 2 || fl == 5 ? m * 16 + h : ti * 4 + g;
                const char* tf = fl < 6 ? P.ws + flag_off(fl, fidx) : P.ws + WS_ST1 + (128 * m + 8 * h) * 16;
                const char* tp = !EARLY_TOUCH ? P.ws + WS_SPARE : w == 2 ? ((TOUCH_FLAGS && lane >= 56 && lane < 63) ? tf : (const char*)P.x + (size_t)(8 * b + lx / 7) * 896 + (lx % 7) * 128)
                                                                       : (const char*)P.wqkv + (size_t)(lw < 9408 ? lw : 0) * 128;
                if (TOUCH_DELAY) __builtin_amdgcn_s_sleep(TOUCH_DELAY);
                asm volatile("s_mov_b32 m0, %1\n\ts_nop 0\n\tglobal_load_lds_dword %0, off" :: "v"(tp), "s"(L0 + L_MISC + 2048) : "memory");
            }
            {
                asm volatile("s_waitcnt vmcnt(1)" : "+v"(sr[0]), "+v"(sr[1]), "+v"(sr[2]), "+v"(sr[3]) :: "memory");
                if ((lane & 15) >= 8) sr[3] = (u32x4){0u, 0u, 0u, 0u};
                float t1 = 0.f, t2 = 0.f;
#pragma unroll
                for (int q = 0; q < 4; ++q)
#pragma unroll
                    for (int i = 0; i < 4; ++i) { t1 = dot2(sr[q][i], ONES, t1); t2 = dot2(sr[q][i], sr[q][i], t2); }
                t1 += xor32<1>(t1); t2 += xor32<1>(t2); t1 += xor32<2>(t1); t2 += xor32<2>(t2); t1 += xor32<4>(t1); t2 += xor32<4>(t2); t1 += xor32<8>(t1); t2 += xor32<8>(t2);
                const float mean = t1 * (1.f / D), var = __builtin_fmaxf(t2 * (1.f / D) - mean * mean, 0.f);
                const unsigned e = *(const unsigned*)(P.ws + WS_EP) + 1u;
                const __amdgpu_buffer_rsrc_t rs = __builtin_amdgcn_make_buffer_rsrc((void*)P.ws, 0, WS_END, 0x00020000);
                if ((lane & 15) == 0) gst(rs, (u32x4){f2u(mean), e, f2u(__builtin_amdgcn_rsqf(var + EPS)), e}, srow * 16, WS_ST1);
            }
            float cs[3][4] = {};
            const unsigned wrd2 = wrd + (w - 2) * 2 * 3072;
#pragma unroll 1
            for (int c = 0; c < 7; ++c) {
                ring_wait(c);
                if (c == LATE_TOUCH) {
                    unsigned long long pc; unsigned back;
                    asm volatile("s_getpc_b64 %0\nmk_code_here:\n\ts_mov_b32 %1, mk_code_here-_ZN2mk9fused_fwdENS_6ParamsE" : "=s"(pc), "=s"(back));
                    constexpr unsigned len = CODE_TOUCH_BYTES;
                    const unsigned oc = (slot * 18 + (lane < 18 ? lane : 0)) * 128, o3 = (b * 64 + lane) * 128, o2 = (b * 16 + (lane & 15)) * 128;
                    const char* t0 = w == 2 ? (const char*)(pc - back) + (oc < len && !EARLY_CODE ? oc : 0) : (const char*)P.w3 + (o3 < 1605632 ? o3 : 0);
                    const char* t1 = w == 2 ? (lane < 16 ? (const char*)P.wo : lane < 32 ? (const char*)P.w1 : (const char*)P.w2) + (o2 < 401408 ? o2 : 0) : (const char*)P.w4 + (o3 < 1605632 ? o3 : 0);
                    asm volatile("s_mov_b32 m0, %2\n\ts_nop 0\n\tglobal_load_lds_dword %0, off\n\tglobal_load_lds_dword %1, off" :: "v"(t0), "v"(t1), "s"(L0 + L_MISC + 2048) : "memory");
                }
                const unsigned a = L0 + (c >= 5 ? c - 5 : c) * L_SLOT + (c < 6 ? wrd2 : wrd + w * 3072);
#pragma unroll
                for (int t = 0; t < 3; ++t) { const u32x4 f = wfrag(a + t * 256, a + 768 + t * 256);
#pragma unroll
                    for (int i = 0; i < 4; ++i) cs[t][i] = dot2(f[i], ONES, cs[t][i]); }
                if (c < 6) {
#pragma unroll
                    for (int t = 0; t < 3; ++t) { const u32x4 f = wfrag(a + 3072 + t * 256, a + 3072 + 768 + t * 256);
#pragma unroll
                        for (int i = 0; i < 4; ++i) cs[t][i] = dot2(f[i], ONES, cs[t][i]); }
                }
                WAIT_LGKM();
                ldsw_st(F_DONE + 2 + w, c + 1);
            }
#pragma unroll
            for (int t = 0; t < 3; ++t) { const float v = hsum((cs[t][0] + cs[t][1]) + (cs[t][2] + cs[t][3])); if (hh == 0) csp[(w - 2) * 96 + t * 32 + r32] = v; }
        }
        LBAR();
        auto stage = [&](const WSrc& s, unsigned lds, int n0, int n1) AI {
#pragma unroll 1
            for (int n = n0; n < n1; ++n) wpiece(s, lds, n);
        };
        LB(0);
        BAR();
        stage(wsrc_a((const char*)P.wo + (size_t)(112 * w) * 896 + tb * 64, 896, nt, lane), wst, 0, 28);
        LB(0);
        { const WSrc s_ = wsrc_a((const char*)P.w1 + (size_t)(112 * w) * 896 + tb * 64, 896, nt, lane); BAR(); stage(s_, wst, 0, HALF); BAR(); stage(s_, wst, HALF, 28); BAR(); LB(0); }
        { const WSrc s_ = wsrc_a((const char*)P.w2 + (size_t)(112 * w) * 896 + tb * 64, 896, nt, lane); BAR(); stage(s_, wst, 0, HALF); BAR(); stage(s_, wst, HALF, 28); BAR(); LB(0); }
        { const WSrc s_ = wsrc_a((const char*)P.w3 + (size_t)(112 * w) * 3584 + tb6 * 64, 3584, nt6, lane); BAR(); stage(s_, wst, 0, HALF); BAR(); stage(s_, wst, HALF, 28); BAR(); LB(0); }
        {
            float c[4] = {0.f, 0.f, 0.f, 0.f};
            const unsigned a0 = L0 + trq * 64 + gq * 8 + 2 * hh * 1024 + w * 256;
#pragma unroll 4
            for (int k = 0; k < 28; ++k) { const unsigned a = a0 + k * 4096; const u32x4 f = wfrag(a, a + 1024);
#pragma unroll
                for (int i = 0; i < 4; ++i) c[i] = dot2(f[i], ONES, c[i]); }
            const float cc = hsum((c[0] + c[1]) + (c[2] + c[3]));
            if (hh == 0) ((LAS float*)(uintptr_t)(L0 + L_M2 + 1024))[w * 32 + r32] = cc;
        }
        if (b == 0 && w == 3) {
            const unsigned e = *(const volatile GAS unsigned*)(P.ws + WS_EP) + 1u;
            const GAS unsigned* p = (const GAS unsigned*)(P.ws + flag_off(0, lane));
#pragma nounroll
            for (unsigned sp = 0; sp < SPIN_CAP; ++sp) {
                const unsigned v0 = __hip_atomic_load(p, __ATOMIC_RELAXED, __HIP_MEMORY_SCOPE_AGENT), v1 = __hip_atomic_load(p + 64 * 32, __ATOMIC_RELAXED, __HIP_MEMORY_SCOPE_AGENT);
                const unsigned v2 = __hip_atomic_load(p + 128 * 32, __ATOMIC_RELAXED, __HIP_MEMORY_SCOPE_AGENT), v3 = __hip_atomic_load(p + 192 * 32, __ATOMIC_RELAXED, __HIP_MEMORY_SCOPE_AGENT);
                if (wall(v0 == e && v1 == e && v2 == e && v3 == e)) break;
            }
            if (lane == 0) __hip_atomic_store((GAS unsigned*)(P.ws + WS_EP), e, __ATOMIC_RELAXED, __HIP_MEMORY_SCOPE_AGENT);
        }
        LBAR();
        const WSrc w4s = wsrc_b((const char*)P.w4 + (size_t)(448 * w) * 896 + g7 * 128, 896, lane);
        if (act7) { stage(w4s, L0 + L_X + w * 8192, 0, 8); stage(w4s, wst - 8 * 1024, 8, W4SPLIT); }
        BAR();
        if (act7) stage(w4s, wst - 8 * 1024, W4SPLIT, 36);
        LB(0);
        if (act7) {
            BAR();
            stage(w4s, wst - 36 * 1024, 36, 56);
            LB(0);
            BAR();
        }
        STAMP_FLUSH();
        return;
    }

    if (PRIO_C) __builtin_amdgcn_s_setprio(PRIO_C);
    const unsigned e = *(const unsigned*)(P.ws + WS_EP) + 1u;
    const __amdgpu_buffer_rsrc_t rs = __builtin_amdgcn_make_buffer_rsrc((void*)P.ws, 0, WS_END, 0x00020000);
    u32x4 qf0, qf1;
    {
        LANE_GEOM();
        const unsigned wrd = 16384 + hh * 6 * 256 + trq * 64 + ((shift + gq * 8) & 63);
        STAMP(15);
        f32x16 aq = {}, ak = {}, av = {};
        const unsigned xrd = (32 * w + r32) * 128 + ((hh ^ ((r32 >> 1) & 7)) << 4);
        struct Fr { u32x4 x, q, k, v; };
        Fr F[4];
        unsigned xo[4];
#pragma unroll
        for (int j = 0; j < 4; ++j) { xo[j] = L0 + (xrd ^ (j << 5)); PIN(xo[j]); }
        unsigned wo_ = L0 + wrd; PIN(wo_);
        auto ldx = [&](unsigned base, int j) AI { F[j].x = lds128(xo[j] + base); const unsigned a = wo_ + base; F[j].q = wfrag(a + j * 3072, a + j * 3072 + 768); };
        auto ldk = [&](unsigned base, int j) AI { const unsigned a = wo_ + base; F[j].k = wfrag(a + j * 3072 + 256, a + j * 3072 + 1024); };
        auto ldv = [&](unsigned base, int j) AI { const unsigned a = wo_ + base; F[j].v = wfrag(a + j * 3072 + 512, a + j * 3072 + 1280); };
        LAS float* stw = (LAS float*)(uintptr_t)(L0 + L_MISC + 3072 + w * 256);
        BAR();
        ring_wait(0);
        STAMP(16);
#pragma unroll
        for (int j = 0; j < 2; ++j) { ldx(0, j); ldk(0, j); ldv(0, j); }
        SB();
        u32x4 gv = {0u, 0u, 0u, 0u};
        const unsigned gvo = (128 * m + 32 * w + r32) * 16;
#pragma unroll 1
        for (int c = 0; c < 7; ++c) {
            const unsigned base = (c >= 5 ? c - 5 : c) * L_SLOT, nbase = (c >= 4 ? c - 4 : c + 1) * L_SLOT;
            u32x2 rdy;
            if (c == 4) gv = gld(rs, gvo, WS_ST1);
            sfor<4>([&](auto jc) AI {
                constexpr int j = decltype(jc)::value, jn = (j + 2) & 3;
                const unsigned lb = j < 2 ? base : nbase;
                if constexpr (j == 2) { if (__builtin_amdgcn_readfirstlane(rdy.x & rdy.y) == 0) ring_wait(c + 1); SB(); }
                MFMA(aq, F[j].q, F[j].x, aq);
                ldx(lb, jn);
                SB();
                MFMA(ak, F[j].k, F[j].x, ak);
                ldk(lb, jn);
                if constexpr (j == 0) ldsw_st(F_DONE + w, c);
                SB();
                MFMA(av, F[j].x, F[j].v, av);
                ldv(lb, jn);
                if constexpr (j == 0) rdy = ldsw_ld2(F_RDY + 2 * (c < 6 ? c + 1 : 6));
                SB();
            });
        }
#pragma nounroll
        for (unsigned sp2 = 0; sp2 < SPIN_CAP && !wall(gv.y == e && gv.w == e); ++sp2) { asm volatile("" ::: "memory"); gv = gld(rs, gvo, WS_ST1); }
        if (hh == 0) { stw[r32] = u2f(gv.x) * u2f(gv.z); stw[32 + r32] = u2f(gv.z); }
        STAMP(1);
        const float mean = u2f(gv.x), rstd = u2f(gv.z);
        LBAR();
        STAMP(17);
        const LAS float* csf = (const LAS float*)(uintptr_t)(L0 + L_MISC);
        const float cv = (csf[64 + r32] + csf[96 + 64 + r32]) + (csf[192 + 64 + r32] + csf[288 + 64 + r32]);
        const float qs = rstd * (0.18898223650461362f * 1.4426950408889634f);
#pragma unroll
        for (int q = 0; q < 4; ++q) {
            auto c4 = [&](int o) AI { return (*(const LAS f32x4*)(csf + o) + *(const LAS f32x4*)(csf + 96 + o)) + (*(const LAS f32x4*)(csf + 192 + o) + *(const LAS f32x4*)(csf + 288 + o)); };
            const f32x4 c1 = c4(8 * q + 4 * hh), c2 = c4(32 + 8 * q + 4 * hh);
            const f32x4 mr = *(const LAS f32x4*)(stw + 8 * q + 4 * hh), rr = *(const LAS f32x4*)(stw + 32 + 8 * q + 4 * hh);
#pragma unroll
            for (int i = 0; i < 4; ++i) {
                aq[4 * q + i] = (aq[4 * q + i] - mean * c1[i]) * qs;
                ak[4 * q + i] = (ak[4 * q + i] - mean * c2[i]) * rstd;
                av[4 * q + i] = av[4 * q + i] * rr[i] - mr[i] * cv;
            }
        }
        if (hh == 1) { aq[12] = 1.f; aq[13] = 0.f; aq[14] = 0.f; aq[15] = 0.f; ak[12] = 0.f; ak[13] = 0.f; ak[14] = 0.f; ak[15] = 0.f; }
        if (r32 >= 28) {
            const float pv = r32 == 28 ? 1.f : 0.f;
#pragma unroll
            for (int i = 0; i < 16; ++i) av[i] = pv;
        }
        const u32x4 k0 = pack8(ak, 0), k1 = pack8(ak, 1), v0 = pack8(av, 0), v1 = pack8(av, 1);
        const unsigned go = WS_KV + h * KVH + (4 * m + w) * 4096;
        gst(rs, k0, lane * 16, go); gst(rs, k1, lane * 16, go + 1024); gst(rs, v0, lane * 16, go + 2048); gst(rs, v1, lane * 16, go + 3072);
        const unsigned lo = L0 + L_OWNKV + w * 4096 + lane * 16;
        sts128(lo, k0); sts128(lo + 1024, k1); sts128(lo + 2048, v0); sts128(lo + 3072, v1);
        qf0 = pack8(aq, 0); qf1 = pack8(aq, 1);
        STAMP(18);
        WAIT_BAR(0);
        if (tid == 0) set_flag(P.ws, flag_off(0, m * 16 + h), e);
        STAMP(2);
    }
    {
        LANE_GEOM();
        const int mt = m - 2 + (w >> 1);
        u32x4 kv[8];
        if (mt >= 0) {
            wait_flags(P.ws, flag_off(0, mt * 16 + h), 1, e, lane);
            const unsigned go = WS_KV + h * KVH + (4 * m - 8 + 2 * w) * 4096;
#pragma unroll
            for (int i = 0; i < 8; ++i) kv[i] = gld(rs, lane * 16, go + i * 1024);
        } else {
#pragma unroll
            for (int i = 0; i < 8; ++i) kv[i] = (u32x4){0u, 0u, (i & 3) == 1 && hh == 1 ? 0xF149u : 0u, 0u};
        }
        f32x16 cdiag, cold;
#pragma unroll
        for (int i = 0; i < 16; ++i) { const int kr = (i & 3) + 8 * (i >> 2) + 4 * hh; cdiag[i] = kr <= r32 ? 0.f : -1e30f; cold[i] = kr > r32 ? 0.f : -1e30f; }
#pragma unroll
        for (int i = 0; i < 8; ++i) sts128(L0 + L_KV + 2 * w * 4096 + i * 1024 + lane * 16, kv[i]);
        LBAR();
        STAMP(3);
        unsigned kvb = L0 + L_KV + w * 4096 + lane * 16;
        PIN(kvb);
        u32x4 kf[9][2], vf[9][2];
        const f32x16 zero16 = {};
        auto ldK = [&](int i) AI { kf[i][0] = lds128(kvb + i * 4096); kf[i][1] = lds128(kvb + i * 4096 + 1024); };
        auto ldV = [&](int i) AI { vf[i][0] = lds128(kvb + i * 4096 + 2048); vf[i][1] = lds128(kvb + i * 4096 + 3072); };
        auto smax = [&](const f32x16& s, float a) AI {
#pragma unroll
            for (int i = 0; i < 16; i += 2) a = max3(a, s[i], s[i + 1]);
            return a; };
        ldK(0); ldK(1);
        float mx = -3e38f;
        f32x16 s[2];
        auto smax8 = [&](const f32x16& v, int o, float a) AI {
#pragma unroll
            for (int i = 0; i < 8; i += 2) a = max3(a, v[o + i], v[o + i + 1]);
            return a; };
        sfor<9>([&](auto ic) AI {
            constexpr int i = decltype(ic)::value;
            if constexpr (i + 2 < 9) ldK(i + 2);
            MFMA(s[i & 1], kf[i][0], qf0, i == 0 ? cold : i == 8 ? cdiag : zero16);
            SB();
            MFMA(s[i & 1], kf[i][1], qf1, s[i & 1]);
            SB();
            if constexpr (i > 0) { mx = smax8(s[(i - 1) & 1], 0, mx); mx = smax8(s[(i - 1) & 1], 8, mx); PIN(mx); }
            SB();
        });
        ldK(0); ldK(1); ldV(0);
        mx = hmax(smax(s[0], mx));
        STAMP(25);
        f32x16 o = {}, cm;
#pragma unroll
        for (int i = 0; i < 16; ++i) { cm[i] = -mx; cdiag[i] -= mx; cold[i] -= mx; }
        MFMA(s[0], kf[0][0], qf0, cold); MFMA(s[0], kf[0][1], qf1, s[0]);
        SB();
        sfor<9>([&](auto ic) AI {
            constexpr int i = decltype(ic)::value;
            f32x16& c = s[i & 1];
            f32x16& n = s[(i + 1) & 1];
            if constexpr (i + 2 < 9) ldK(i + 2);
            if constexpr (i + 1 < 9) ldV(i + 1);
            if constexpr (i + 1 < 9) MFMA(n, kf[i + 1][0], qf0, i + 1 == 8 ? cdiag : cm);
#pragma unroll
            for (int j = 0; j < 8; ++j) c[j] = __builtin_amdgcn_exp2f(c[j]);
            u32x4 p0 = pack8(c, 0);
            PIN(p0);
            SB();
            if constexpr (i + 1 < 9) MFMA(n, kf[i + 1][1], qf1, n);
            MFMA(o, vf[i][0], p0, o);
#pragma unroll
            for (int j = 8; j < 16; ++j) c[j] = __builtin_amdgcn_exp2f(c[j]);
            const u32x4 p1 = pack8(c, 1);
            MFMA(o, vf[i][1], p1, o);
            SB();
        });
        STAMP(26);
        auto lr = __builtin_amdgcn_permlane32_swap(f2u(o[12]), f2u(o[12]), false, false);
        const float inv = __builtin_amdgcn_rcpf(u2f(lr[1]));
#pragma unroll
        for (int i = 0; i < 16; ++i) o[i] *= inv;
        if (hh == 1) { o[12] = 0.f; o[13] = 0.f; o[14] = 0.f; o[15] = 0.f; }
        const unsigned vo = (hh * XR + r32) * 16, so = WS_O + ((4 * h) * XR + 128 * m + 32 * w) * 16;
        gst(rs, pack8(o, 0), vo, so); gst(rs, pack8(o, 1), vo, so + 2 * XROW);
        WAIT_BAR(0);
        if (tid == 0) set_flag(P.ws, flag_off(1, m * 16 + h), e);
        STAMP(4);
    }
    const bool act = w < nt;
    const int tw = tb + w;
#define GEMM_GEOM() LANE_GEOM(); \
    unsigned trW[2]; trW[0] = L0 + (w < nt ? w : nt - 1) * 256 + trq * 64 + gq * 8 + 2 * hh * 1024; trW[1] = trW[0] + 57344; PIN(trW[0]); PIN(trW[1]); \
    auto wimg = [&](int kq0, int kq1) AI { return wfrag(kq0 < 56 ? trW[0] + kq0 * 1024 : trW[1] + (kq0 - 56) * 1024, kq1 < 56 ? trW[0] + kq1 * 1024 : trW[1] + (kq1 - 56) * 1024); }; \
    unsigned xl = L0 + L_X + lane * 16; PIN(xl); \
    LAS float* m2 = (LAS float*)(uintptr_t)(L0 + L_M2 + w * 256); (void)m2
    auto gemm = [&](auto nk, const u32x4* wr, unsigned xl) AI -> f32x16 {
        constexpr int NK = decltype(nk)::value, PD = 4;
        f32x16 acc = {};
        u32x4 xf[NK];
#pragma unroll
        for (int k = 0; k < PD; ++k) xf[k] = lds128(xl + k * 1024);
        SB();
        sfor<NK>([&](auto kc) AI {
            constexpr int k = decltype(kc)::value;
            if constexpr (k + PD < NK) xf[k + PD] = lds128(xl + (k + PD) * 1024);
            MFMA(acc, wr[k], xf[k], acc);
            SB();
        });
        return acc;
    };
    u32x4 hp[2];
    float meanH, rstdH;
    {
        GEMM_GEOM();
        u32x4 xr[2];
        const char* xp = (const char*)P.x + (size_t)(32 * ti + r32) * 896 + 64 * (act ? tw : tb) + 16 * hh;
        xr[0] = *(const GAS u32x4*)xp; xr[1] = *(const GAS u32x4*)(xp + 32);
        u32x4 wr[32];
        sfor<32>([&](auto kc) AI { constexpr int k = decltype(kc)::value, kq = 7 * (k >> 1) + 4 * (k & 1);
            if constexpr (k < 31) wr[k] = wimg(kq, kq + 1);
            else { const u32x2 p = ldstr(trW[1] + (kq - 56) * 1024), q = ldstr(trW[1] + (kq - 56) * 1024 + (hh ? 0 : 1024)); wr[k] = (u32x4){p.x, p.y, q.x, q.y}; } });
        LBAR();
        STAMP(19);
        wait_flags(P.ws, flag_off(1, (ti >> 2) * 16 + 4 * w), 4, e, lane);
        STAMP(20);
        u32x4 xf[8];
#pragma unroll
        for (int k = 0; k < 8; ++k) xf[k] = gld(rs, vx, WS_O + ((16 * w + 2 * k) * XR + 32 * ti) * 16);
#pragma unroll
        for (int k = 0; k < 8; ++k) sts128(xl + (8 * w + k) * 1024, xf[k]);
        LBAR();
        STAMP(5);
        f32x16 r = gemm(IC(32), wr, xl);
        STAMP(21);
        float s1 = 0.f, s2 = 0.f;
#pragma unroll
        for (int s = 0; s < 2; ++s) {
#pragma unroll
            for (int i = 0; i < 4; ++i) {
                hp[s][i] = pk(r[8 * s + 2 * i] + blo(xr[s][i]), r[8 * s + 2 * i + 1] + bhi(xr[s][i]));
                s1 = dot2(hp[s][i], ONES, s1); s2 = dot2(hp[s][i], hp[s][i], s2);
            }
            if (act) gst(rs, hp[s], vx, WS_H + ((4 * tw + 2 * s) * XR + 32 * ti) * 16);
        }
        s1 = hsum(s1); s2 = hsum(s2);
        if (act && hh == 0) gst8(rs, (u32x2){f2u(s1), f2u(s2)}, r32 * 8, WS_STH + (tw * 2048 + 32 * ti) * 8);
        STAMP(22);
        WAIT_BAR(0);
        if (tid == 0) set_flag(P.ws, flag_off(2, ti * 4 + g), e);
        STAMP(6);
    }
#define LOAD_WR() u32x4 wr[28]; sfor<28>([&](auto kc) AI { constexpr int k = decltype(kc)::value; wr[k] = wimg(4 * k, 4 * k + 1); })
    const int fsn = w == 0 ? 1 : 2, fsl = w <= 1 ? 0 : w - 1;
    {
        GEMM_GEOM();
        BAR();
        LOAD_WR();
        LBAR();
        wait_flags(P.ws, flag_off(2, ti * 4), 4, e, lane);
        u32x4 xf[7];
#pragma unroll
        for (int k = 0; k < 7; ++k) xf[k] = gld(rs, vx, WS_H + ((14 * w + 2 * k) * XR + 32 * ti) * 16);
        u32x2 sv[7];
#pragma unroll
        for (int q = 0; q < 7; ++q) sv[q] = gld8(rs, (7 * hh * 2048 + r32) * 8, WS_STH + (q * 2048 + 32 * ti) * 8);
        SB();
        {
            float cs[4] = {0.f, 0.f, 0.f, 0.f};
#pragma unroll
            for (int k = 0; k < 28; ++k)
#pragma unroll
                for (int i = 0; i < 4; ++i) cs[i] = dot2(wr[k][i], ONES, cs[i]);
            const float c = hsum((cs[0] + cs[1]) + (cs[2] + cs[3]));
            if (hh == 0) m2[r32] = c;
        }
        SB();
#pragma unroll
        for (int k = 0; k < 7; ++k) sts128(xl + (7 * w + k) * 1024, xf[k]);
        LBAR();
        STAMP(7);
        f32x16 r = gemm(IC(28), wr, xl);
        float t1 = 0.f, t2 = 0.f;
#pragma unroll
        for (int q = 0; q < 7; ++q) { t1 += u2f(sv[q].x); t2 += u2f(sv[q].y); }
        t1 = hsum(t1); t2 = hsum(t2);
        meanH = t1 * (1.f / D);
        const float var = __builtin_fmaxf(t2 * (1.f / D) - meanH * meanH, 0.f);
        rstdH = __builtin_amdgcn_rsqf(var + EPS);
        const float sc = rstdH * __builtin_amdgcn_rsqf(var * rstdH * rstdH + EPS);
#pragma unroll
        for (int q = 0; q < 4; ++q) {
            const f32x4 c = *(const LAS f32x4*)(m2 + 8 * q + 4 * hh);
#pragma unroll
            for (int i = 0; i < 4; ++i) r[4 * q + i] = gelu_t((r[4 * q + i] - meanH * c[i]) * sc);
        }
#pragma unroll
        for (int s = 0; s < 2; ++s) if (act) gst(rs, pack8(r, s), vx, WS_HH + ((4 * tw + 2 * s) * XR + 32 * ti) * 16);
        WAIT_BAR(0);
        if (tid == 0) set_flag(P.ws, flag_off(3, ti * 4 + g), e);
        STAMP(8);
    }
    {
        GEMM_GEOM();
        BAR();
        LOAD_WR();
        LBAR();
        const int sh = w == 0 ? -1 : w == 1 ? 1 : w == 2 ? -2 : 2;
        const int tn = ti + (sh < 0 ? -1 : 1);
        {
            const bool nb = tn >= 0 && tn < 64;
            const int li = lane & 3;
            const GAS unsigned* p = (const GAS unsigned*)(P.ws + flag_off(3, (((li & 2) && nb) ? tn : ti) * 4 + fsl + ((li & 1) && fsn == 2 ? 1 : 0)));
            poll_word(p, e);
        }
        const int tok = 32 * ti + r32 + sh;
        const bool ok = tok >= 0 && tok < S;
        const unsigned vxs = ok ? (hh * XR + tok) * 16 : 0x7ffffff0u;
        u32x4 xf[7];
#pragma unroll
        for (int k = 0; k < 7; ++k) { xf[k] = gld(rs, vxs, WS_HH + (14 * w + 2 * k) * XROW); }
#pragma unroll
        for (int k = 0; k < 7; ++k) sts128(xl + (7 * w + k) * 1024, xf[k]);
        LBAR();
        STAMP(9);
        f32x16 r = gemm(IC(28), wr, xl);
        float s1 = 0.f, s2 = 0.f;
#pragma unroll
        for (int s = 0; s < 2; ++s) {
            u32x4 xp;
#pragma unroll
            for (int i = 0; i < 4; ++i) {
                xp[i] = pk(r[8 * s + 2 * i] + (blo(hp[s][i]) - meanH) * rstdH, r[8 * s + 2 * i + 1] + (bhi(hp[s][i]) - meanH) * rstdH);
                s1 = dot2(xp[i], ONES, s1); s2 = dot2(xp[i], xp[i], s2);
            }
            if (act) gst(rs, xp, vx, WS_X1 + ((4 * tw + 2 * s) * XR + 32 * ti) * 16);
        }
        s1 = hsum(s1); s2 = hsum(s2);
        if (act && hh == 0) gst8(rs, (u32x2){f2u(s1), f2u(s2)}, r32 * 8, WS_STX + (tw * 2048 + 32 * ti) * 8);
        WAIT_BAR(0);
        if (tid == 0) set_flag(P.ws, flag_off(4, ti * 4 + g), e);
        STAMP(10);
    }
    {
        LANE_GEOM();
        unsigned tr6[2];
        tr6[0] = L0 + trq * 64 + gq * 8 + 2 * hh * 1024; tr6[1] = tr6[0] + 57344; PIN(tr6[0]); PIN(tr6[1]);
        auto wf6 = [&](int t, int k) AI { const unsigned a = (k < 14 ? tr6[0] + k * 4096 : tr6[1] + (k - 14) * 4096) + t * 256; return wfrag(a, a + 1024); };
        const LAS float* cs6 = (const LAS float*)(uintptr_t)(L0 + L_M2 + 1024);
        wait_flags(P.ws, flag_off(4, (4 * m + w) * 4), 4, e, lane);
        STAMP(28);
        const int tk = 128 * m + 32 * w;
        auto ldx1 = [&](int k) AI { return gld(rs, vx, WS_X1 + (2 * k * XR + tk) * 16); };
        constexpr int PX = P6_PX, PW = 2;
        f32x16 acc[4] = {};
        u32x2 sv[7];
        {
            u32x4 xf[28], wf[28][4];
#pragma unroll
            for (int k = 0; k < PX; ++k) xf[k] = ldx1(k);
#pragma unroll
            for (int q = 0; q < 7; ++q) sv[q] = gld8(rs, (7 * hh * 2048 + r32) * 8, WS_STX + (q * 2048 + tk) * 8);
            BAR();
            STAMP(11);
#pragma unroll
            for (int k = 0; k < PW; ++k)
#pragma unroll
                for (int t = 0; t < 4; ++t) wf[k][t] = wf6(t, k);
            SB();
            sfor<28>([&](auto kc) AI {
                constexpr int k = decltype(kc)::value;
                MFMA(acc[0], wf[k][0], xf[k], acc[0]);
                if constexpr (k + PW < 28) { wf[k + PW][0] = wf6(0, k + PW); }
                SB();
                MFMA(acc[1], wf[k][1], xf[k], acc[1]);
                if constexpr (k + PW < 28) { wf[k + PW][1] = wf6(1, k + PW); }
                if constexpr (k + PX < 28) xf[k + PX] = ldx1(k + PX);
                SB();
                MFMA(acc[2], wf[k][2], xf[k], acc[2]);
                if constexpr (k + PW < 28) { wf[k + PW][2] = wf6(2, k + PW); }
                SB();
                MFMA(acc[3], wf[k][3], xf[k], acc[3]);
                if constexpr (k + PW < 28) { wf[k + PW][3] = wf6(3, k + PW); }
                SB();
            });
        }
        STAMP(23);
        LBAR();
        STAMP(24);
        float t1 = 0.f, t2 = 0.f;
#pragma unroll
        for (int q = 0; q < 7; ++q) { t1 += u2f(sv[q].x); t2 += u2f(sv[q].y); }
        t1 = hsum(t1); t2 = hsum(t2);
        const float mean = t1 * (1.f / D), var = __builtin_fmaxf(t2 * (1.f / D) - mean * mean, 0.f) + EPS, rstd = __builtin_amdgcn_rsqf(var);
        const GeluK gk = {-mean, GC1 * rstd * rstd * rstd, GC0 * rstd, var * rstd};
        const unsigned uo = WS_U + ((4 * tb6) * XR + tk) * 16;
        auto epi1 = [&](f32x16& y, int te) AI {
#pragma unroll
            for (int q = 0; q < 4; ++q) {
                const f32x4 c = *(const LAS f32x4*)(cs6 + te * 32 + 8 * q + 4 * hh);
#pragma unroll
                for (int i = 0; i < 4; ++i) {
                    const float v = __builtin_fmaf(gk.nmean, c[i], y[4 * q + i]);
                    const float z = v * __builtin_fmaf(v * v, gk.k1, gk.k0);
                    y[4 * q + i] = v * __builtin_amdgcn_rcpf(__builtin_fmaf(__builtin_amdgcn_exp2f(z), gk.irs, gk.irs));
                }
            }
            gst(rs, pack8(y, 0), vx, uo + 4 * te * XROW); gst(rs, pack8(y, 1), vx, uo + (4 * te + 2) * XROW);
            SB();
        };
        epi1(acc[0], 0); epi1(acc[1], 1); epi1(acc[2], 2);
        if (nt6 == 4) epi1(acc[3], 3);
        STAMP(29);
        WAIT_BAR(0);
        if (tid == 0) set_flag(P.ws, flag_off(5, m * 16 + s16), e);
        BAR();
        STAMP(12);
    }
    if (act7) {
        LANE_GEOM();
        const unsigned lanep = trq * 64 + gq * 8 + 2 * hh * 512;
        unsigned tA = L0 + L_X + w * 8192 + lanep, tB = wst + lanep; PIN(tA); PIN(tB);
        auto w4f = [&](int k, int t) AI { const unsigned a = (k < 4 ? tA + k * 2048 : k < 18 ? tB + (k - 4) * 2048 : tB + (k - 18) * 2048) + t * 256; return wfrag(a, a + 512); };
        u32x4 wr[28][2];
#pragma unroll
        for (int k = 0; k < 14; ++k) { wr[k][0] = w4f(k, 0); wr[k][1] = w4f(k, 1); }
        LBAR();
        const int tk = 64 * j7;
        const int tt = w >> 1, nt7 = w & 1;
        wait_flags(P.ws, flag_off(5, (j7 >> 1) * 16 + 4 * w), 4, e, lane);
        constexpr int PX = P7_PX;
        u32x4 xa[28][2];
#pragma unroll
        for (int k = 0; k < PX; ++k) { xa[k][0] = gld(rs, vx, WS_U + ((56 * w + 2 * k) * XR + tk) * 16); xa[k][1] = gld(rs, vx, WS_U + ((56 * w + 2 * k) * XR + tk + 32) * 16); }
        STAMP(13);
        SB();
        f32x16 acc[4] = {};
        u32x4 rh[2], rx[2];
        sfor<28>([&](auto kc) AI {
            constexpr int k = decltype(kc)::value;
            if constexpr (k == 13) { BAR(); SB(); }
            if constexpr (k + 3 >= 14 && k + 3 < 28) { wr[k + 3][0] = w4f(k + 3, 0); wr[k + 3][1] = w4f(k + 3, 1); }
            MFMA(acc[0], wr[k][0], xa[k][0], acc[0]); MFMA(acc[1], wr[k][1], xa[k][0], acc[1]);
            if constexpr (k + PX < 28) xa[k + PX][0] = gld(rs, vx, WS_U + ((56 * w + 2 * (k + PX)) * XR + tk) * 16);
            MFMA(acc[2], wr[k][0], xa[k][1], acc[2]); MFMA(acc[3], wr[k][1], xa[k][1], acc[3]);
            if constexpr (k + PX < 28) xa[k + PX][1] = gld(rs, vx, WS_U + ((56 * w + 2 * (k + PX)) * XR + tk + 32) * 16);
            if constexpr (k == 24 || k == 25) {
                constexpr int s = k - 24;
                const unsigned so = ((8 * g7 + 4 * nt7 + 2 * s) * XR + tk + 32 * tt) * 16;
                rh[s] = gld(rs, vx, WS_H + so); rx[s] = gld(rs, vx, WS_X1 + so);
            }
            SB();
        });
        STAMP(27);
#pragma unroll
        for (int t = 0; t < 4; ++t) {
            if (t != w) {
                const unsigned a = L0 + L_X + t * 12288 + (w < t ? w : w - 1) * 4096 + lane * 16;
#pragma unroll
                for (int q = 0; q < 4; ++q) stsf4(a + q * 1024, (f32x4){acc[t][4 * q], acc[t][4 * q + 1], acc[t][4 * q + 2], acc[t][4 * q + 3]});
            }
        }
        LBAR();
        f32x16 r = w == 0 ? acc[0] : w == 1 ? acc[1] : w == 2 ? acc[2] : acc[3];
        const unsigned a = L0 + L_X + w * 12288 + lane * 16;
#pragma unroll
        for (int j = 0; j < 3; ++j)
#pragma unroll
            for (int q = 0; q < 4; ++q) { const f32x4 v = ldsf4(a + j * 4096 + q * 1024); r[4 * q] += v[0]; r[4 * q + 1] += v[1]; r[4 * q + 2] += v[2]; r[4 * q + 3] += v[3]; }
        const unsigned oo = (tk + 32 * tt + r32) * 896 + 128 * g7 + 64 * nt7 + 16 * hh;
        char* op = (char*)P.out + oo;
        const __amdgpu_buffer_rsrc_t ro = __builtin_amdgcn_make_buffer_rsrc((void*)P.out, 0, S * D * 2, 0x00020000); (void)ro; (void)op;
#pragma unroll
        for (int s = 0; s < 2; ++s) {
            u32x4 ov;
#pragma unroll
            for (int i = 0; i < 4; ++i)
                ov[i] = pk(r[8 * s + 2 * i] + blo(rx[s][i]) + blo(rh[s][i]), r[8 * s + 2 * i + 1] + bhi(rx[s][i]) + bhi(rh[s][i]));
            *(GAS u32x4*)(op + 32 * s) = ov;
        }
        STAMP(14);
    }
    STAMP_FLUSH();
}
}

extern "C" void kernel_launch(void* const* d_in, const int* in_sizes, int n_in, void* d_out, int out_size, void* d_ws, size_t ws_size,
                              hipStream_t stream) {
    mk::Params P;
    P.x = (const u16*)d_in[0]; P.wqkv = (const u16*)d_in[1]; P.wo = (const u16*)d_in[2];
    P.w1 = (const u16*)d_in[4]; P.w2 = (const u16*)d_in[5]; P.w3 = (const u16*)d_in[6]; P.w4 = (const u16*)d_in[7];
    P.out = (u16*)d_out; P.ws = (char*)d_ws;
    mk::fused_fwd<<<256, 512, 0, stream>>>(P);
}
```

```cpp
#define LOADER_FIRST 1
#define P1_ROLLED 1
#define EARLY_CODE 1
#define TOUCH_FLAGS 1
#define W4SPLIT 36
#define PRIO_C 2
#define PRIO_L 0
#define NT_W 0
#define NT_QKV 0
#define NT_X 0
#define OUT_MODE 0
#define P6_PX 10
#define P6_PW 3
#define P6_PG 6
#define P7_PX 5
#define HALF 12
#define POLL2 0
#define POLL_GAP 3
#define TOUCH_DELAY 12
#define EARLY_TOUCH 1
#define LATE_TOUCH 3
#include <hip/hip_runtime.h>
#include <hip/hip_bf16.h>
#include <stdint.h>
#include <type_traits>
#include <utility>

typedef unsigned short u16;
typedef __attribute__((ext_vector_type(8))) short bf16x8;
typedef __attribute__((ext_vector_type(4))) short s16x4;
typedef __attribute__((ext_vector_type(16))) float f32x16;
typedef __attribute__((ext_vector_type(4))) float f32x4;
typedef __attribute__((ext_vector_type(2))) float f32x2;
typedef __attribute__((ext_vector_type(4))) unsigned u32x4;
typedef __attribute__((ext_vector_type(2))) unsigned u32x2;
typedef __attribute__((ext_vector_type(2))) __bf16 bf2;
#define LAS __attribute__((address_space(3)))
#define GAS __attribute__((address_space(1)))
#define DEVI __device__ __forceinline__

#define MK_STAMP 0

namespace mk {
constexpr int S = 2048, D = 448;
constexpr unsigned XR = 2056, XROW = XR * 16;
constexpr unsigned KVH = 64 * 4096 + 128;
constexpr unsigned WS_EP = 0;
constexpr unsigned WS_FLAG = 4096;
constexpr unsigned WS_KV = 262144;
constexpr unsigned WS_O = WS_KV + 16 * KVH;
constexpr unsigned WS_H = WS_O + 64 * XROW;
constexpr unsigned WS_HH = WS_H + 56 * XROW;
constexpr unsigned WS_X1 = WS_HH + 56 * XROW;
constexpr unsigned WS_U = WS_X1 + 56 * XROW;
constexpr unsigned WS_STH = WS_U + 224 * XROW;
constexpr unsigned WS_STX = WS_STH + 262144;
constexpr unsigned WS_ST1 = WS_STX + 262144;
constexpr unsigned WS_SPARE = WS_ST1 + 32768;
constexpr unsigned WS_STAMP = WS_SPARE + 128;
constexpr unsigned WS_END = WS_STAMP + 16 * 512 * 8;
constexpr unsigned L_SLOT = 28672;
constexpr unsigned L_MISC = 143360;
constexpr unsigned L_KV = 114688;
constexpr unsigned L_OWNKV = L_KV + 8 * 4096;
constexpr unsigned L_WST = 28672;
constexpr unsigned L_X = 114688;
constexpr unsigned L_M2 = 147456;
constexpr unsigned CODE_TOUCH_BYTES = 35000;
constexpr float EPS = 1e-5f;
constexpr unsigned SPIN_CAP = 1u << 18;

struct Params {
    const u16* x; const u16* wqkv; const u16* wo; const u16* w1; const u16* w2; const u16* w3; const u16* w4;
    u16* out; char* ws;
};

DEVI unsigned f2u(float f) { return __builtin_bit_cast(unsigned, f); }
DEVI float u2f(unsigned u) { return __builtin_bit_cast(float, u); }
DEVI bool wall(bool p) { return __builtin_amdgcn_ballot_w64(!p) == 0; }
template <int X> DEVI float xor32(float v) { return u2f((unsigned)__builtin_amdgcn_ds_swizzle((int)f2u(v), (X << 10) | 0x1f)); }
DEVI unsigned pk(float lo, float hi) { bf2 v = {(__bf16)lo, (__bf16)hi}; return __builtin_bit_cast(unsigned, v); }
DEVI float blo(unsigned u) { return u2f(u << 16); }
DEVI float bhi(unsigned u) { return u2f(u & 0xffff0000u); }
DEVI float dot2(unsigned a, unsigned b, float c) { return __builtin_amdgcn_fdot2_f32_bf16(__builtin_bit_cast(bf2, a), __builtin_bit_cast(bf2, b), c, false); }
constexpr unsigned ONES = 0x3f803f80u;
DEVI float hsum(float v) { auto r = __builtin_amdgcn_permlane32_swap(f2u(v), f2u(v), false, false); return u2f(r[0]) + u2f(r[1]); }
DEVI float hmax(float v) { auto r = __builtin_amdgcn_permlane32_swap(f2u(v), f2u(v), false, false); return __builtin_fmaxf(u2f(r[0]), u2f(r[1])); }
DEVI f32x16 mfma(u32x4 a, u32x4 b, f32x16 c) { return __builtin_amdgcn_mfma_f32_32x32x16_bf16(__builtin_bit_cast(bf16x8, a), __builtin_bit_cast(bf16x8, b), c, 0, 0, 0); }
#define MFMA(acc, a, b, c) do { acc = mfma(a, b, c); asm volatile("" : "+v"(acc)); } while (0)
DEVI u32x4 lds128(unsigned a) { return *(const LAS u32x4*)(uintptr_t)a; }
DEVI f32x4 ldsf4(unsigned a) { return *(const LAS f32x4*)(uintptr_t)a; }
DEVI void sts128(unsigned a, u32x4 v) { *(LAS u32x4*)(uintptr_t)a = v; }
DEVI void stsf4(unsigned a, f32x4 v) { *(LAS f32x4*)(uintptr_t)a = v; }
DEVI u32x2 ldstr(unsigned a) { return __builtin_bit_cast(u32x2, __builtin_amdgcn_ds_read_tr16_b64_v4i16((LAS s16x4*)(uintptr_t)a)); }
DEVI u32x4 wfrag(unsigned a0, unsigned a1) { u32x2 p = ldstr(a0), q = ldstr(a1); return (u32x4){p.x, p.y, q.x, q.y}; }
DEVI u32x4 pack8(const f32x16& a, int s) { return (u32x4){pk(a[8 * s], a[8 * s + 1]), pk(a[8 * s + 2], a[8 * s + 3]), pk(a[8 * s + 4], a[8 * s + 5]), pk(a[8 * s + 6], a[8 * s + 7])}; }
constexpr int SC1 = 16;
DEVI u32x4 gld(__amdgpu_buffer_rsrc_t rs, unsigned voff, unsigned soff) { return __builtin_amdgcn_raw_buffer_load_b128(rs, voff, soff, SC1); }
DEVI u32x2 gld8(__amdgpu_buffer_rsrc_t rs, unsigned voff, unsigned soff) { return __builtin_amdgcn_raw_buffer_load_b64(rs, voff, soff, SC1); }
DEVI void gst(__amdgpu_buffer_rsrc_t rs, u32x4 v, unsigned voff, unsigned soff) { __builtin_amdgcn_raw_buffer_store_b128(v, rs, voff + soff, 0, SC1); }
DEVI void gst8(__amdgpu_buffer_rsrc_t rs, u32x2 v, unsigned voff, unsigned soff) { __builtin_amdgcn_raw_buffer_store_b64(v, rs, voff, soff, SC1); }
#define WAIT_VM(N) asm volatile("s_waitcnt vmcnt(" #N ")" ::: "memory")
#define WAIT_LGKM() asm volatile("s_waitcnt lgkmcnt(0)" ::: "memory")
#define WAIT_BAR(N) asm volatile("s_waitcnt vmcnt(" #N ") lgkmcnt(0)\n\ts_barrier" ::: "memory")
#define LBAR() asm volatile("s_waitcnt lgkmcnt(0)\n\ts_barrier" ::: "memory")
#define SB() __builtin_amdgcn_sched_barrier(0)
#define PIN(x) asm volatile("" : "+v"(x))
DEVI float fma1(float a, float b, float c) { return __builtin_fmaf(a, b, c); }
DEVI float mul1(float a, float b) { return a * b; }
DEVI float max3(float a, float b, float c) { return __builtin_fmaxf(__builtin_fmaxf(a, b), c); }
template <int... Is, class F> DEVI void sfor_impl(std::integer_sequence<int, Is...>, F&& f) { (f(std::integral_constant<int, Is>{}), ...); }
template <int N, class F> DEVI void sfor(F&& f) { sfor_impl(std::make_integer_sequence<int, N>{}, f); }
#define AI __attribute__((always_inline))
#define IC(n) std::integral_constant<int, n>{}

template <int NT = 0> DEVI void dma1(const char* sbase, unsigned voff, unsigned lds) {
    if (NT) asm volatile("s_mov_b32 m0, %2\n\ts_nop 0\n\tglobal_load_lds_dwordx4 %0, %1 nt" :: "v"(voff), "s"(sbase), "s"(lds) : "memory");
    else asm volatile("s_mov_b32 m0, %2\n\ts_nop 0\n\tglobal_load_lds_dwordx4 %0, %1" :: "v"(voff), "s"(sbase), "s"(lds) : "memory");
}
DEVI void touch(const char* sbase, unsigned voff, unsigned lds) {
    asm volatile("s_mov_b32 m0, %2\n\ts_nop 0\n\tglobal_load_lds_dword %0, %1" :: "v"(voff), "s"(sbase), "s"(lds) : "memory");
}
struct WSrc { const char* base; unsigned step, voff; };
DEVI void wpiece(const WSrc& s, unsigned lds, int n) { dma1<NT_W>(s.base + (size_t)n * s.step, s.voff, lds + n * 1024); }
DEVI WSrc wsrc_a(const char* base, unsigned rs, int nt, int lane) { const int t = (lane >> 4) < nt ? (lane >> 4) : nt - 1; return WSrc{base, 4 * rs, ((lane >> 2) & 3) * rs + t * 64 + (lane & 3) * 16}; }
DEVI WSrc wsrc_b(const char* base, unsigned rs, int lane) { return WSrc{base, 8 * rs, (4 * (lane >> 5) + ((lane >> 2) & 3)) * rs + ((lane >> 4) & 1) * 64 + (lane & 3) * 16}; }

DEVI void poll_word(const GAS unsigned* p, unsigned e) {
    unsigned a = __hip_atomic_load(p, __ATOMIC_RELAXED, __HIP_MEMORY_SCOPE_AGENT);
#pragma nounroll
    for (unsigned sp = 0; sp < SPIN_CAP; ++sp) {
        __builtin_amdgcn_s_sleep(POLL_GAP);
        const unsigned b2 = __hip_atomic_load(p, __ATOMIC_RELAXED, __HIP_MEMORY_SCOPE_AGENT);
        if (wall(a == e)) break;
        __builtin_amdgcn_s_sleep(POLL_GAP);
        a = __hip_atomic_load(p, __ATOMIC_RELAXED, __HIP_MEMORY_SCOPE_AGENT);
        if (wall(b2 == e)) break;
    }
    __builtin_amdgcn_fence(__ATOMIC_ACQUIRE, "wavefront");
}
DEVI void wait_flags(const char* ws, unsigned foff, int n, unsigned e, int lane) {
    const GAS unsigned* p = (const GAS unsigned*)(ws + foff) + (lane < n ? lane : 0) * 32;
    if (POLL2) poll_word(p, e);
    else {
#pragma nounroll
        for (unsigned sp = 0; sp < SPIN_CAP; ++sp) {
            unsigned v = __hip_atomic_load(p, __ATOMIC_RELAXED, __HIP_MEMORY_SCOPE_AGENT);
            if (wall(v == e)) break;
        }
        __builtin_amdgcn_fence(__ATOMIC_ACQUIRE, "wavefront");
    }
}
DEVI void set_flag(char* ws, unsigned foff, unsigned e) { __hip_atomic_store((GAS unsigned*)(ws + foff), e, __ATOMIC_RELAXED, __HIP_MEMORY_SCOPE_AGENT); }
DEVI unsigned flag_off(int ph, int idx) { return WS_FLAG + (ph * 256 + idx) * 128; }

constexpr float GC0 = -2.f * 0.7978845608028654f * 1.4426950408889634f, GC1 = GC0 * 0.044715f;
DEVI float gelu_t(float x) {
    float z = x * __builtin_fmaf(x * x, GC1, GC0);
    return x * __builtin_amdgcn_rcpf(1.f + __builtin_amdgcn_exp2f(z));
}
struct GeluK { float nmean, k1, k0, irs; };
DEVI void gelu_step(int o, float& y, float& z, float c, const GeluK& g) {
    if (o == 0) y = fma1(g.nmean, c, y);
    else if (o == 1) z = mul1(y, y);
    else if (o == 2) z = fma1(z, g.k1, g.k0);
    else if (o == 3) z = mul1(y, z);
    else if (o == 4) z = __builtin_amdgcn_exp2f(z);
    else if (o == 5) z = fma1(z, g.irs, g.irs);
    else if (o == 6) z = __builtin_amdgcn_rcpf(z);
    else y = mul1(y, z);
}

#define STAMP(k) do {} while (0)
#define STAMP_DECL
#define STAMP_FLUSH() do {} while (0)

#define LANE_GEOM() int lane_ = threadIdx.x & 63; asm volatile("" : "+v"(lane_)); const int lane = lane_, r32 = lane & 31, hh = lane >> 5; \
    const int trq = (lane & 15) >> 2, trQ_ = ((lane >> 4) & 1) * 4 + (lane & 3), gq = (trQ_ & 4) | ((trQ_ & 1) << 1) | ((trQ_ >> 1) & 1); \
    const unsigned vx = (hh * XR + r32) * 16; (void)trq; (void)gq; (void)vx; (void)r32; (void)hh
constexpr unsigned L_FLG = L_MISC + 1920, F_RDY = 0, F_DONE = 16;
DEVI unsigned ldsw_ld(unsigned i) { return *(volatile LAS unsigned*)(uintptr_t)(L_FLG + 4 * i); }
DEVI u32x2 ldsw_ld2(unsigned i) { return *(volatile LAS u32x2*)(uintptr_t)(L_FLG + 4 * i); }
DEVI void ldsw_st(unsigned i, unsigned v) { *(volatile LAS unsigned*)(uintptr_t)(L_FLG + 4 * i) = v; }
DEVI void ring_wait(int c) {
#pragma nounroll
    for (unsigned sp = 0; sp < SPIN_CAP * 16u; ++sp) { const u32x2 v = ldsw_ld2(F_RDY + 2 * c); if (__builtin_amdgcn_readfirstlane(v.x & v.y)) break; __builtin_amdgcn_s_sleep(1); }
}
DEVI void slot_wait(unsigned n, int lane) {
#pragma nounroll
    for (unsigned sp = 0; sp < SPIN_CAP * 16u; ++sp) { const unsigned v = ldsw_ld(F_DONE + (lane < 6 ? lane : 0)); if (wall(v >= n)) break; __builtin_amdgcn_s_sleep(1); }
}
#define LB(N) asm volatile("s_waitcnt vmcnt(" #N ")\n\ts_barrier" ::: "memory")
#define BAR() asm volatile("s_barrier" ::: "memory")

__global__ __launch_bounds__(512, 2) void fused_fwd(Params P) {
    __shared__ __attribute__((aligned(16))) char smem[163840];
    const unsigned L0 = (unsigned)(uintptr_t)(LAS char*)smem;
    const int tid = threadIdx.x, wv = __builtin_amdgcn_readfirstlane(tid >> 6), w = wv & 3;
    const int b = blockIdx.x, xcd = b & 7, slot = b >> 3;
    if (wv < 3) {
        unsigned long long pc; unsigned back;
        asm volatile("s_getpc_b64 %0\nmk_code_here0:\n\ts_mov_b32 %1, mk_code_here0-_ZN2mk9fused_fwdENS_6ParamsE" : "=s"(pc), "=s"(back));
        const unsigned l = tid & 63;
        const char* tp;
        if (wv == 0) { const unsigned oc = (slot * 18 + (l < 18 ? l : 0)) * 128; tp = (const char*)(pc - back) + (oc < CODE_TOUCH_BYTES ? oc : 0); }
        else if (wv == 1) {
            const unsigned lx = l < 56 ? l : 0, fl = l - 56, m_ = 2 * xcd + (slot >> 4), h_ = slot & 15;
            const unsigned fidx = fl < 2 || fl == 5 ? m_ * 16 + h_ : (8 * xcd + (slot >> 2)) * 4 + (slot & 3);
            tp = l < 56 || l == 63 ? (const char*)P.x + (size_t)(8 * b + lx / 7) * 896 + (lx % 7) * 128 : fl < 6 ? P.ws + flag_off(fl, fidx) : P.ws + WS_ST1 + (128 * m_ + 8 * h_) * 16;
        } else { const unsigned lw = b * 37 + (l < 37 ? l : 0); tp = (const char*)P.wqkv + (size_t)(lw < 9408 ? lw : 0) * 128; }
        asm volatile("s_mov_b32 m0, %1\n\ts_nop 0\n\tglobal_load_lds_dword %0, off" :: "v"(tp), "s"(L0 + L_MISC + 2048) : "memory");
    }
    const int m = 2 * xcd + (slot >> 4), h = slot & 15;
    const int g = slot & 3, nt = g < 2 ? 4 : 3, tb = g < 2 ? 4 * g : 3 * g + 2;
    const int ti = 8 * xcd + (slot >> 2);
    const int s16 = slot & 15, pr = s16 >> 1;
    const int nt6 = ((s16 ^ pr) & 1) ? 3 : 4, tb6 = 7 * pr + ((s16 & 1) ? (pr & 1 ? 3 : 4) : 0);
    const bool act7 = slot < 28;
    const int j7 = 4 * xcd + (act7 ? slot / 7 : 0), g7 = act7 ? slot % 7 : 0;
    const unsigned wst = L0 + w * L_WST;
    const unsigned shift = (56 * h) & 15u;
    STAMP_DECL

    if (__builtin_expect(wv >= 4, LOADER_FIRST)) {
        if (PRIO_L) __builtin_amdgcn_s_setprio(PRIO_L);
        LANE_GEOM();
        const unsigned wrd = 16384 + hh * 6 * 256 + trq * 64 + ((shift + gq * 8) & 63);
        LAS float* csp = (LAS float*)(uintptr_t)(L0 + L_MISC);
        if (__builtin_expect(w < 2, LOADER_FIRST)) {
            const unsigned xoffE = (lane >> 3) * 896 + (((lane & 7) ^ (lane >> 4)) << 4);
            const unsigned xoffO = (lane >> 3) * 896 + (((lane & 7) ^ (4 + (lane >> 4))) << 4);
            const unsigned colb = (56 * h) & ~15u;
            unsigned woff[3];
#pragma unroll
            for (int j = 0; j < 3; ++j) {
                const int bl = 4 * j + (lane >> 4), kql = bl / 3, tile = bl % 3;
                woff[j] = (4 * kql + ((lane >> 2) & 3)) * 2688 + tile * 896 + colb + (lane & 3) * 16;
            }
            const char* xb = (const char*)P.x + (size_t)(128 * m + 64 * w) * 896;
            const char* wb = (const char*)P.wqkv + (size_t)(32 * w) * 2688;
            LBAR();
#pragma unroll 1
            for (int kc = 0; kc < 7; ++kc) {
                if (kc >= 5) slot_wait(kc - 4, lane);
                const unsigned base = L0 + (kc >= 5 ? kc - 5 : kc) * L_SLOT;
#pragma unroll
                for (int i = 0; i < 6; ++i) dma1<NT_QKV>(wb + (size_t)kc * 64 * 2688 + (i / 3) * 16 * 2688, woff[i % 3], base + 16384 + (6 * w + i) * 1024);
#pragma unroll
                for (int i = 0; i < 8; ++i) dma1<NT_X>(xb + kc * 128 + i * 8 * 896, (i & 1) ? xoffO : xoffE, base + (8 * w + i) * 1024);
                if (kc > 0) { WAIT_VM(14); ldsw_st(F_RDY + 2 * (kc - 1) + w, 1); }
            }
            WAIT_VM(0); ldsw_st(F_RDY + 12 + w, 1);
            {
                ring_wait(6);
                const unsigned a = L0 + L_SLOT + wrd + w * 3072;
#pragma unroll
                for (int t = 0; t < 3; ++t) { const u32x4 f = wfrag(a + t * 256, a + 768 + t * 256);
                    const float v = hsum((dot2(f[0], ONES, 0.f) + dot2(f[1], ONES, 0.f)) + (dot2(f[2], ONES, 0.f) + dot2(f[3], ONES, 0.f)));
                    if (hh == 0) csp[(2 + w) * 96 + t * 32 + r32] = v; }
            }
        } else {
            const int srow = 128 * m + 8 * h + 4 * (w - 2) + (lane >> 4);
            const char* sp = (const char*)P.x + (size_t)srow * 896 + (lane & 15) * 16;
            u32x4 sr[4];
            asm volatile("global_load_dwordx4 %0, %4, off\n\tglobal_load_dwordx4 %1, %4, off offset:256\n\tglobal_load_dwordx4 %2, %4, off offset:512\n\tglobal_load_dwordx4 %3, %5, off"
                         : "=&v"(sr[0]), "=&v"(sr[1]), "=&v"(sr[2]), "=&v"(sr[3]) : "v"(sp), "v"(sp + ((lane & 15) < 8 ? 768 : 0)) : "memory");
            if (w == 2 && lane < 32) ldsw_st(lane, 0);
            LBAR();
            {
                asm volatile("s_waitcnt vmcnt(0)" : "+v"(sr[0]), "+v"(sr[1]), "+v"(sr[2]), "+v"(sr[3]) :: "memory");
                if ((lane & 15) >= 8) sr[3] = (u32x4){0u, 0u, 0u, 0u};
                float t1 = 0.f, t2 = 0.f;
#pragma unroll
                for (int q = 0; q < 4; ++q)
#pragma unroll
                    for (int i = 0; i < 4; ++i) { t1 = dot2(sr[q][i], ONES, t1); t2 = dot2(sr[q][i], sr[q][i], t2); }
                t1 += xor32<1>(t1); t2 += xor32<1>(t2); t1 += xor32<2>(t1); t2 += xor32<2>(t2); t1 += xor32<4>(t1); t2 += xor32<4>(t2); t1 += xor32<8>(t1); t2 += xor32<8>(t2);
                const float mean = t1 * (1.f / D), var = __builtin_fmaxf(t2 * (1.f / D) - mean * mean, 0.f);
                const unsigned e = *(const unsigned*)(P.ws + WS_EP) + 1u;
                const __amdgpu_buffer_rsrc_t rs = __builtin_amdgcn_make_buffer_rsrc((void*)P.ws, 0, WS_END, 0x00020000);
                if ((lane & 15) == 0) gst(rs, (u32x4){f2u(mean), e, f2u(__builtin_amdgcn_rsqf(var + EPS)), e}, srow * 16, WS_ST1);
            }
            float cs[3][4] = {};
            const unsigned wrd2 = wrd + (w - 2) * 2 * 3072;
#pragma unroll 1
            for (int c = 0; c < 7; ++c) {
                ring_wait(c);
                if (c == LATE_TOUCH) {
                    unsigned long long pc; unsigned back;
                    asm volatile("s_getpc_b64 %0\nmk_code_here:\n\ts_mov_b32 %1, mk_code_here-_ZN2mk9fused_fwdENS_6ParamsE" : "=s"(pc), "=s"(back));
                    constexpr unsigned len = CODE_TOUCH_BYTES;
                    const unsigned oc = (slot * 18 + (lane < 18 ? lane : 0)) * 128, o3 = (b * 64 + lane) * 128, o2 = (b * 16 + (lane & 15)) * 128;
                    const char* t0 = w == 2 ? (const char*)(pc - back) + (oc < len && !EARLY_CODE ? oc : 0) : (const char*)P.w3 + (o3 < 1605632 ? o3 : 0);
                    const char* t1 = w == 2 ? (lane < 16 ? (const char*)P.wo : lane < 32 ? (const char*)P.w1 : (const char*)P.w2) + (o2 < 401408 ? o2 : 0) : (const char*)P.w4 + (o3 < 1605632 ? o3 : 0);
                    asm volatile("s_mov_b32 m0, %2\n\ts_nop 0\n\tglobal_load_lds_dword %0, off\n\tglobal_load_lds_dword %1, off" :: "v"(t0), "v"(t1), "s"(L0 + L_MISC + 2048) : "memory");
                }
                const unsigned a = L0 + (c >= 5 ? c - 5 : c) * L_SLOT + (c < 6 ? wrd2 : wrd + w * 3072);
#pragma unroll
                for (int t = 0; t < 3; ++t) { const u32x4 f = wfrag(a + t * 256, a + 768 + t * 256);
#pragma unroll
                    for (int i = 0; i < 4; ++i) cs[t][i] = dot2(f[i], ONES, cs[t][i]); }
                if (c < 6) {
#pragma unroll
                    for (int t = 0; t < 3; ++t) { const u32x4 f = wfrag(a + 3072 + t * 256, a + 3072 + 768 + t * 256);
#pragma unroll
                        for (int i = 0; i < 4; ++i) cs[t][i] = dot2(f[i], ONES, cs[t][i]); }
                }
                WAIT_LGKM();
                ldsw_st(F_DONE + 2 + w, c + 1);
            }
#pragma unroll
            for (int t = 0; t < 3; ++t) { const float v = hsum((cs[t][0] + cs[t][1]) + (cs[t][2] + cs[t][3])); if (hh == 0) csp[(w - 2) * 96 + t * 32 + r32] = v; }
        }
        LBAR();
        auto stage = [&](const WSrc& s, unsigned lds, int n0, int n1) AI {
#pragma unroll 1
            for (int n = n0; n < n1; ++n) wpiece(s, lds, n);
        };
        LB(0);
        BAR();
        stage(wsrc_a((const char*)P.wo + (size_t)(112 * w) * 896 + tb * 64, 896, nt, lane), wst, 0, 28);
        LB(0);
        { const WSrc s_ = wsrc_a((const char*)P.w1 + (size_t)(112 * w) * 896 + tb * 64, 896, nt, lane); BAR(); stage(s_, wst, 0, HALF); BAR(); stage(s_, wst, HALF, 28); BAR(); LB(0); }
        { const WSrc s_ = wsrc_a((const char*)P.w2 + (size_t)(112 * w) * 896 + tb * 64, 896, nt, lane); BAR(); stage(s_, wst, 0, HALF); BAR(); stage(s_, wst, HALF, 28); BAR(); LB(0); }
        { const WSrc s_ = wsrc_a((const char*)P.w3 + (size_t)(112 * w) * 3584 + tb6 * 64, 3584, nt6, lane); BAR(); stage(s_, wst, 0, HALF); BAR(); stage(s_, wst, HALF, 28); BAR(); LB(0); }
        {
            float c[4] = {0.f, 0.f, 0.f, 0.f};
            const unsigned a0 = L0 + trq * 64 + gq * 8 + 2 * hh * 1024 + w * 256;
#pragma unroll 4
            for (int k = 0; k < 28; ++k) { const unsigned a = a0 + k * 4096; const u32x4 f = wfrag(a, a + 1024);
#pragma unroll
                for (int i = 0; i < 4; ++i) c[i] = dot2(f[i], ONES, c[i]); }
            const float cc = hsum((c[0] + c[1]) + (c[2] + c[3]));
            if (hh == 0) ((LAS float*)(uintptr_t)(L0 + L_M2 + 1024))[w * 32 + r32] = cc;
        }
        if (b == 0 && w == 3) {
            const unsigned e = *(const volatile GAS unsigned*)(P.ws + WS_EP) + 1u;
            const GAS unsigned* p = (const GAS unsigned*)(P.ws + flag_off(0, lane));
#pragma nounroll
            for (unsigned sp = 0; sp < SPIN_CAP; ++sp) {
                const unsigned v0 = __hip_atomic_load(p, __ATOMIC_RELAXED, __HIP_MEMORY_SCOPE_AGENT), v1 = __hip_atomic_load(p + 64 * 32, __ATOMIC_RELAXED, __HIP_MEMORY_SCOPE_AGENT);
                const unsigned v2 = __hip_atomic_load(p + 128 * 32, __ATOMIC_RELAXED, __HIP_MEMORY_SCOPE_AGENT), v3 = __hip_atomic_load(p + 192 * 32, __ATOMIC_RELAXED, __HIP_MEMORY_SCOPE_AGENT);
                if (wall(v0 == e && v1 == e && v2 == e && v3 == e)) break;
            }
            if (lane == 0) __hip_atomic_store((GAS unsigned*)(P.ws + WS_EP), e, __ATOMIC_RELAXED, __HIP_MEMORY_SCOPE_AGENT);
        }
        LBAR();
        const WSrc w4s = wsrc_b((const char*)P.w4 + (size_t)(448 * w) * 896 + g7 * 128, 896, lane);
        if (act7) { stage(w4s, L0 + L_X + w * 8192, 0, 8); stage(w4s, wst - 8 * 1024, 8, W4SPLIT); }
        BAR();
        if (act7) stage(w4s, wst - 8 * 1024, W4SPLIT, 36);
        LB(0);
        if (act7) {
            BAR();
            stage(w4s, wst - 36 * 1024, 36, 56);
            LB(0);
            BAR();
        }
        STAMP_FLUSH();
        return;
    }

    if (PRIO_C) __builtin_amdgcn_s_setprio(PRIO_C);
    const unsigned e = *(const unsigned*)(P.ws + WS_EP) + 1u;
    const __amdgpu_buffer_rsrc_t rs = __builtin_amdgcn_make_buffer_rsrc((void*)P.ws, 0, WS_END, 0x00020000);
    u32x4 qf0, qf1;
    {
        LANE_GEOM();
        const unsigned wrd = 16384 + hh * 6 * 256 + trq * 64 + ((shift + gq * 8) & 63);
        STAMP(15);
        f32x16 aq = {}, ak = {}, av = {};
        const unsigned xrd = (32 * w + r32) * 128 + ((hh ^ ((r32 >> 1) & 7)) << 4);
        struct Fr { u32x4 x, q, k, v; };
        Fr F[4];
        unsigned xo[4];
#pragma unroll
        for (int j = 0; j < 4; ++j) { xo[j] = L0 + (xrd ^ (j << 5)); PIN(xo[j]); }
        unsigned wo_ = L0 + wrd; PIN(wo_);
        auto ldx = [&](unsigned base, int j) AI { F[j].x = lds128(xo[j] + base); const unsigned a = wo_ + base; F[j].q = wfrag(a + j * 3072, a + j * 3072 + 768); };
        auto ldk = [&](unsigned base, int j) AI { const unsigned a = wo_ + base; F[j].k = wfrag(a + j * 3072 + 256, a + j * 3072 + 1024); };
        auto ldv = [&](unsigned base, int j) AI { const unsigned a = wo_ + base; F[j].v = wfrag(a + j * 3072 + 512, a + j * 3072 + 1280); };
        LAS float* stw = (LAS float*)(uintptr_t)(L0 + L_MISC + 3072 + w * 256);
        BAR();
        ring_wait(0);
        STAMP(16);
#pragma unroll
        for (int j = 0; j < 2; ++j) { ldx(0, j); ldk(0, j); ldv(0, j); }
        SB();
        u32x4 gv = {0u, 0u, 0u, 0u};
        const unsigned gvo = (128 * m + 32 * w + r32) * 16;
#pragma unroll 1
        for (int c = 0; c < 7; ++c) {
            const unsigned base = (c >= 5 ? c - 5 : c) * L_SLOT, nbase = (c >= 4 ? c - 4 : c + 1) * L_SLOT;
            u32x2 rdy;
            if (c == 4) gv = gld(rs, gvo, WS_ST1);
            sfor<4>([&](auto jc) AI {
                constexpr int j = decltype(jc)::value, jn = (j + 2) & 3;
                const unsigned lb = j < 2 ? base : nbase;
                if constexpr (j == 2) { if (__builtin_amdgcn_readfirstlane(rdy.x & rdy.y) == 0) ring_wait(c + 1); SB(); }
                MFMA(aq, F[j].q, F[j].x, aq);
                ldx(lb, jn);
                SB();
                MFMA(ak, F[j].k, F[j].x, ak);
                ldk(lb, jn);
                if constexpr (j == 0) ldsw_st(F_DONE + w, c);
                SB();
                MFMA(av, F[j].x, F[j].v, av);
                ldv(lb, jn);
                if constexpr (j == 0) rdy = ldsw_ld2(F_RDY + 2 * (c < 6 ? c + 1 : 6));
                SB();
            });
        }
#pragma nounroll
        for (unsigned sp2 = 0; sp2 < SPIN_CAP && !wall(gv.y == e && gv.w == e); ++sp2) { asm volatile("" ::: "memory"); gv = gld(rs, gvo, WS_ST1); }
        if (hh == 0) { stw[r32] = u2f(gv.x) * u2f(gv.z); stw[32 + r32] = u2f(gv.z); }
        STAMP(1);
        const float mean = u2f(gv.x), rstd = u2f(gv.z);
        LBAR();
        STAMP(17);
        const LAS float* csf = (const LAS float*)(uintptr_t)(L0 + L_MISC);
        const float cv = (csf[64 + r32] + csf[96 + 64 + r32]) + (csf[192 + 64 + r32] + csf[288 + 64 + r32]);
        const float qs = rstd * (0.18898223650461362f * 1.4426950408889634f);
#pragma unroll
        for (int q = 0; q < 4; ++q) {
            auto c4 = [&](int o) AI { return (*(const LAS f32x4*)(csf + o) + *(const LAS f32x4*)(csf + 96 + o)) + (*(const LAS f32x4*)(csf + 192 + o) + *(const LAS f32x4*)(csf + 288 + o)); };
            const f32x4 c1 = c4(8 * q + 4 * hh), c2 = c4(32 + 8 * q + 4 * hh);
            const f32x4 mr = *(const LAS f32x4*)(stw + 8 * q + 4 * hh), rr = *(const LAS f32x4*)(stw + 32 + 8 * q + 4 * hh);
#pragma unroll
            for (int i = 0; i < 4; ++i) {
                aq[4 * q + i] = (aq[4 * q + i] - mean * c1[i]) * qs;
                ak[4 * q + i] = (ak[4 * q + i] - mean * c2[i]) * rstd;
                av[4 * q + i] = av[4 * q + i] * rr[i] - mr[i] * cv;
            }
        }
        if (hh == 1) { aq[12] = 1.f; aq[13] = 0.f; aq[14] = 0.f; aq[15] = 0.f; ak[12] = 0.f; ak[13] = 0.f; ak[14] = 0.f; ak[15] = 0.f; }
        if (r32 >= 28) {
            const float pv = r32 == 28 ? 1.f : 0.f;
#pragma unroll
            for (int i = 0; i < 16; ++i) av[i] = pv;
        }
        const u32x4 k0 = pack8(ak, 0), k1 = pack8(ak, 1), v0 = pack8(av, 0), v1 = pack8(av, 1);
        const unsigned go = WS_KV + h * KVH + (4 * m + w) * 4096;
        gst(rs, k0, lane * 16, go); gst(rs, k1, lane * 16, go + 1024); gst(rs, v0, lane * 16, go + 2048); gst(rs, v1, lane * 16, go + 3072);
        const unsigned lo = L0 + L_OWNKV + w * 4096 + lane * 16;
        sts128(lo, k0); sts128(lo + 1024, k1); sts128(lo + 2048, v0); sts128(lo + 3072, v1);
        qf0 = pack8(aq, 0); qf1 = pack8(aq, 1);
        STAMP(18);
        WAIT_BAR(0);
        if (tid == 0) set_flag(P.ws, flag_off(0, m * 16 + h), e);
        STAMP(2);
    }
    {
        LANE_GEOM();
        const int mt = m - 2 + (w >> 1);
        u32x4 kv[8];
        if (mt >= 0) {
            wait_flags(P.ws, flag_off(0, mt * 16 + h), 1, e, lane);
            const unsigned go = WS_KV + h * KVH + (4 * m - 8 + 2 * w) * 4096;
#pragma unroll
            for (int i = 0; i < 8; ++i) kv[i] = gld(rs, lane * 16, go + i * 1024);
        } else {
#pragma unroll
            for (int i = 0; i < 8; ++i) kv[i] = (u32x4){0u, 0u, (i & 3) == 1 && hh == 1 ? 0xF149u : 0u, 0u};
        }
        f32x16 cdiag, cold;
#pragma unroll
        for (int i = 0; i < 16; ++i) { const int kr = (i & 3) + 8 * (i >> 2) + 4 * hh; cdiag[i] = kr <= r32 ? 0.f : -1e30f; cold[i] = kr > r32 ? 0.f : -1e30f; }
#pragma unroll
        for (int i = 0; i < 8; ++i) sts128(L0 + L_KV + 2 * w * 4096 + i * 1024 + lane * 16, kv[i]);
        LBAR();
        STAMP(3);
        unsigned kvb = L0 + L_KV + w * 4096 + lane * 16;
        PIN(kvb);
        u32x4 kf[9][2], vf[9][2];
        const f32x16 zero16 = {};
        auto ldK = [&](int i) AI { kf[i][0] = lds128(kvb + i * 4096); kf[i][1] = lds128(kvb + i * 4096 + 1024); };
        auto ldV = [&](int i) AI { vf[i][0] = lds128(kvb + i * 4096 + 2048); vf[i][1] = lds128(kvb + i * 4096 + 3072); };
        auto smax = [&](const f32x16& s, float a) AI {
#pragma unroll
            for (int i = 0; i < 16; i += 2) a = max3(a, s[i], s[i + 1]);
            return a; };
        ldK(0); ldK(1);
        float mx = -3e38f;
        f32x16 s[2];
        auto smax8 = [&](const f32x16& v, int o, float a) AI {
#pragma unroll
            for (int i = 0; i < 8; i += 2) a = max3(a, v[o + i], v[o + i + 1]);
            return a; };
        sfor<9>([&](auto ic) AI {
            constexpr int i = decltype(ic)::value;
            if constexpr (i + 2 < 9) ldK(i + 2);
            MFMA(s[i & 1], kf[i][0], qf0, i == 0 ? cold : i == 8 ? cdiag : zero16);
            SB();
            MFMA(s[i & 1], kf[i][1], qf1, s[i & 1]);
            SB();
            if constexpr (i > 0) { mx = smax8(s[(i - 1) & 1], 0, mx); mx = smax8(s[(i - 1) & 1], 8, mx); PIN(mx); }
            SB();
        });
        ldK(0); ldK(1); ldV(0);
        mx = hmax(smax(s[0], mx));
        STAMP(25);
        f32x16 o = {}, cm;
#pragma unroll
        for (int i = 0; i < 16; ++i) { cm[i] = -mx; cdiag[i] -= mx; cold[i] -= mx; }
        MFMA(s[0], kf[0][0], qf0, cold); MFMA(s[0], kf[0][1], qf1, s[0]);
        SB();
        sfor<9>([&](auto ic) AI {
            constexpr int i = decltype(ic)::value;
            f32x16& c = s[i & 1];
            f32x16& n = s[(i + 1) & 1];
            if constexpr (i + 2 < 9) ldK(i + 2);
            if constexpr (i + 1 < 9) ldV(i + 1);
            if constexpr (i + 1 < 9) MFMA(n, kf[i + 1][0], qf0, i + 1 == 8 ? cdiag : cm);
#pragma unroll
            for (int j = 0; j < 8; ++j) c[j] = __builtin_amdgcn_exp2f(c[j]);
            u32x4 p0 = pack8(c, 0);
            PIN(p0);
            SB();
            if constexpr (i + 1 < 9) MFMA(n, kf[i + 1][1], qf1, n);
            MFMA(o, vf[i][0], p0, o);
#pragma unroll
            for (int j = 8; j < 16; ++j) c[j] = __builtin_amdgcn_exp2f(c[j]);
            const u32x4 p1 = pack8(c, 1);
            MFMA(o, vf[i][1], p1, o);
            SB();
        });
        STAMP(26);
        auto lr = __builtin_amdgcn_permlane32_swap(f2u(o[12]), f2u(o[12]), false, false);
        const float inv = __builtin_amdgcn_rcpf(u2f(lr[1]));
#pragma unroll
        for (int i = 0; i < 16; ++i) o[i] *= inv;
        if (hh == 1) { o[12] = 0.f; o[13] = 0.f; o[14] = 0.f; o[15] = 0.f; }
        const unsigned vo = (hh * XR + r32) * 16, so = WS_O + ((4 * h) * XR + 128 * m + 32 * w) * 16;
        gst(rs, pack8(o, 0), vo, so); gst(rs, pack8(o, 1), vo, so + 2 * XROW);
        WAIT_BAR(0);
        if (tid == 0) set_flag(P.ws, flag_off(1, m * 16 + h), e);
        STAMP(4);
    }
    const bool act = w < nt;
    const int tw = tb + w;
#define GEMM_GEOM() LANE_GEOM(); \
    unsigned trW[2]; trW[0] = L0 + (w < nt ? w : nt - 1) * 256 + trq * 64 + gq * 8 + 2 * hh * 1024; trW[1] = trW[0] + 57344; PIN(trW[0]); PIN(trW[1]); \
    auto wimg = [&](int kq0, int kq1) AI { return wfrag(kq0 < 56 ? trW[0] + kq0 * 1024 : trW[1] + (kq0 - 56) * 1024, kq1 < 56 ? trW[0] + kq1 * 1024 : trW[1] + (kq1 - 56) * 1024); }; \
    unsigned xl = L0 + L_X + lane * 16; PIN(xl); \
    LAS float* m2 = (LAS float*)(uintptr_t)(L0 + L_M2 + w * 256); (void)m2
    auto gemm = [&](auto nk, const u32x4* wr, unsigned xl) AI -> f32x16 {
        constexpr int NK = decltype(nk)::value, PD = 4;
        f32x16 acc = {};
        u32x4 xf[NK];
#pragma unroll
        for (int k = 0; k < PD; ++k) xf[k] = lds128(xl + k * 1024);
        SB();
        sfor<NK>([&](auto kc) AI {
            constexpr int k = decltype(kc)::value;
            if constexpr (k + PD < NK) xf[k + PD] = lds128(xl + (k + PD) * 1024);
            MFMA(acc, wr[k], xf[k], acc);
            SB();
        });
        return acc;
    };
    u32x4 hp[2];
    float meanH, rstdH;
    {
        GEMM_GEOM();
        u32x4 xr[2];
        const char* xp = (const char*)P.x + (size_t)(32 * ti + r32) * 896 + 64 * (act ? tw : tb) + 16 * hh;
        xr[0] = *(const GAS u32x4*)xp; xr[1] = *(const GAS u32x4*)(xp + 32);
        u32x4 wr[32];
        sfor<32>([&](auto kc) AI { constexpr int k = decltype(kc)::value, kq = 7 * (k >> 1) + 4 * (k & 1);
            if constexpr (k < 31) wr[k] = wimg(kq, kq + 1);
            else { const u32x2 p = ldstr(trW[1] + (kq - 56) * 1024), q = ldstr(trW[1] + (kq - 56) * 1024 + (hh ? 0 : 1024)); wr[k] = (u32x4){p.x, p.y, q.x, q.y}; } });
        LBAR();
        STAMP(19);
        wait_flags(P.ws, flag_off(1, (ti >> 2) * 16 + 4 * w), 4, e, lane);
        STAMP(20);
        u32x4 xf[8];
#pragma unroll
        for (int k = 0; k < 8; ++k) xf[k] = gld(rs, vx, WS_O + ((16 * w + 2 * k) * XR + 32 * ti) * 16);
#pragma unroll
        for (int k = 0; k < 8; ++k) sts128(xl + (8 * w + k) * 1024, xf[k]);
        LBAR();
        STAMP(5);
        f32x16 r = gemm(IC(32), wr, xl);
        STAMP(21);
        float s1 = 0.f, s2 = 0.f;
#pragma unroll
        for (int s = 0; s < 2; ++s) {
#pragma unroll
            for (int i = 0; i < 4; ++i) {
                hp[s][i] = pk(r[8 * s + 2 * i] + blo(xr[s][i]), r[8 * s + 2 * i + 1] + bhi(xr[s][i]));
                s1 = dot2(hp[s][i], ONES, s1); s2 = dot2(hp[s][i], hp[s][i], s2);
            }
            if (act) gst(rs, hp[s], vx, WS_H + ((4 * tw + 2 * s) * XR + 32 * ti) * 16);
        }
        s1 = hsum(s1); s2 = hsum(s2);
        if (act && hh == 0) gst8(rs, (u32x2){f2u(s1), f2u(s2)}, r32 * 8, WS_STH + (tw * 2048 + 32 * ti) * 8);
        STAMP(22);
        WAIT_BAR(0);
        if (tid == 0) set_flag(P.ws, flag_off(2, ti * 4 + g), e);
        STAMP(6);
    }
#define LOAD_WR() u32x4 wr[28]; sfor<28>([&](auto kc) AI { constexpr int k = decltype(kc)::value; wr[k] = wimg(4 * k, 4 * k + 1); })
    const int fsn = w == 0 ? 1 : 2, fsl = w <= 1 ? 0 : w - 1;
    {
        GEMM_GEOM();
        BAR();
        LOAD_WR();
        LBAR();
        wait_flags(P.ws, flag_off(2, ti * 4), 4, e, lane);
        u32x4 xf[7];
#pragma unroll
        for (int k = 0; k < 7; ++k) xf[k] = gld(rs, vx, WS_H + ((14 * w + 2 * k) * XR + 32 * ti) * 16);
        u32x2 sv[7];
#pragma unroll
        for (int q = 0; q < 7; ++q) sv[q] = gld8(rs, (7 * hh * 2048 + r32) * 8, WS_STH + (q * 2048 + 32 * ti) * 8);
        SB();
        {
            float cs[4] = {0.f, 0.f, 0.f, 0.f};
#pragma unroll
            for (int k = 0; k < 28; ++k)
#pragma unroll
                for (int i = 0; i < 4; ++i) cs[i] = dot2(wr[k][i], ONES, cs[i]);
            const float c = hsum((cs[0] + cs[1]) + (cs[2] + cs[3]));
            if (hh == 0) m2[r32] = c;
        }
        SB();
#pragma unroll
        for (int k = 0; k < 7; ++k) sts128(xl + (7 * w + k) * 1024, xf[k]);
        LBAR();
        STAMP(7);
        f32x16 r = gemm(IC(28), wr, xl);
        float t1 = 0.f, t2 = 0.f;
#pragma unroll
        for (int q = 0; q < 7; ++q) { t1 += u2f(sv[q].x); t2 += u2f(sv[q].y); }
        t1 = hsum(t1); t2 = hsum(t2);
        meanH = t1 * (1.f / D);
        const float var = __builtin_fmaxf(t2 * (1.f / D) - meanH * meanH, 0.f);
        rstdH = __builtin_amdgcn_rsqf(var + EPS);
        const float sc = rstdH * __builtin_amdgcn_rsqf(var * rstdH * rstdH + EPS);
#pragma unroll
        for (int q = 0; q < 4; ++q) {
            const f32x4 c = *(const LAS f32x4*)(m2 + 8 * q + 4 * hh);
#pragma unroll
            for (int i = 0; i < 4; ++i) r[4 * q + i] = gelu_t((r[4 * q + i] - meanH * c[i]) * sc);
        }
#pragma unroll
        for (int s = 0; s < 2; ++s) if (act) gst(rs, pack8(r, s), vx, WS_HH + ((4 * tw + 2 * s) * XR + 32 * ti) * 16);
        WAIT_BAR(0);
        if (tid == 0) set_flag(P.ws, flag_off(3, ti * 4 + g), e);
        STAMP(8);
    }
    {
        GEMM_GEOM();
        BAR();
        LOAD_WR();
        LBAR();
        const int sh = w == 0 ? -1 : w == 1 ? 1 : w == 2 ? -2 : 2;
        const int tn = ti + (sh < 0 ? -1 : 1);
        {
            const bool nb = tn >= 0 && tn < 64;
            const int li = lane & 3;
            const GAS unsigned* p = (const GAS unsigned*)(P.ws + flag_off(3, (((li & 2) && nb) ? tn : ti) * 4 + fsl + ((li & 1) && fsn == 2 ? 1 : 0)));
            poll_word(p, e);
        }
        const int tok = 32 * ti + r32 + sh;
        const bool ok = tok >= 0 && tok < S;
        const unsigned vxs = ok ? (hh * XR + tok) * 16 : 0x7ffffff0u;
        u32x4 xf[7];
#pragma unroll
        for (int k = 0; k < 7; ++k) { xf[k] = gld(rs, vxs, WS_HH + (14 * w + 2 * k) * XROW); }
#pragma unroll
        for (int k = 0; k < 7; ++k) sts128(xl + (7 * w + k) * 1024, xf[k]);
        LBAR();
        STAMP(9);
        f32x16 r = gemm(IC(28), wr, xl);
        float s1 = 0.f, s2 = 0.f;
#pragma unroll
        for (int s = 0; s < 2; ++s) {
            u32x4 xp;
#pragma unroll
            for (int i = 0; i < 4; ++i) {
                xp[i] = pk(r[8 * s + 2 * i] + (blo(hp[s][i]) - meanH) * rstdH, r[8 * s + 2 * i + 1] + (bhi(hp[s][i]) - meanH) * rstdH);
                s1 = dot2(xp[i], ONES, s1); s2 = dot2(xp[i], xp[i], s2);
            }
            if (act) gst(rs, xp, vx, WS_X1 + ((4 * tw + 2 * s) * XR + 32 * ti) * 16);
        }
        s1 = hsum(s1); s2 = hsum(s2);
        if (act && hh == 0) gst8(rs, (u32x2){f2u(s1), f2u(s2)}, r32 * 8, WS_STX + (tw * 2048 + 32 * ti) * 8);
        WAIT_BAR(0);
        if (tid == 0) set_flag(P.ws, flag_off(4, ti * 4 + g), e);
        STAMP(10);
    }
    {
        LANE_GEOM();
        unsigned tr6[2];
        tr6[0] = L0 + trq * 64 + gq * 8 + 2 * hh * 1024; tr6[1] = tr6[0] + 57344; PIN(tr6[0]); PIN(tr6[1]);
        auto wf6 = [&](int t, int k) AI { const unsigned a = (k < 14 ? tr6[0] + k * 4096 : tr6[1] + (k - 14) * 4096) + t * 256; return wfrag(a, a + 1024); };
        const LAS float* cs6 = (const LAS float*)(uintptr_t)(L0 + L_M2 + 1024);
        wait_flags(P.ws, flag_off(4, (4 * m + w) * 4), 4, e, lane);
        STAMP(28);
        const int tk = 128 * m + 32 * w;
        auto ldx1 = [&](int k) AI { return gld(rs, vx, WS_X1 + (2 * k * XR + tk) * 16); };
        constexpr int PX = P6_PX, PW = 2;
        f32x16 acc[4] = {};
        u32x2 sv[7];
        {
            u32x4 xf[28], wf[28][4];
#pragma unroll
            for (int k = 0; k < PX; ++k) xf[k] = ldx1(k);
#pragma unroll
            for (int q = 0; q < 7; ++q) sv[q] = gld8(rs, (7 * hh * 2048 + r32) * 8, WS_STX + (q * 2048 + tk) * 8);
            BAR();
            STAMP(11);
#pragma unroll
            for (int k = 0; k < PW; ++k)
#pragma unroll
                for (int t = 0; t < 4; ++t) wf[k][t] = wf6(t, k);
            SB();
            sfor<28>([&](auto kc) AI {
                constexpr int k = decltype(kc)::value;
                MFMA(acc[0], wf[k][0], xf[k], acc[0]);
                if constexpr (k + PW < 28) { wf[k + PW][0] = wf6(0, k + PW); }
                SB();
                MFMA(acc[1], wf[k][1], xf[k], acc[1]);
                if constexpr (k + PW < 28) { wf[k + PW][1] = wf6(1, k + PW); }
                if constexpr (k + PX < 28) xf[k + PX] = ldx1(k + PX);
                SB();
                MFMA(acc[2], wf[k][2], xf[k], acc[2]);
                if constexpr (k + PW < 28) { wf[k + PW][2] = wf6(2, k + PW); }
                SB();
                MFMA(acc[3], wf[k][3], xf[k], acc[3]);
                if constexpr (k + PW < 28) { wf[k + PW][3] = wf6(3, k + PW); }
                SB();
            });
        }
        STAMP(23);
        LBAR();
        STAMP(24);
        float t1 = 0.f, t2 = 0.f;
#pragma unroll
        for (int q = 0; q < 7; ++q) { t1 += u2f(sv[q].x); t2 += u2f(sv[q].y); }
        t1 = hsum(t1); t2 = hsum(t2);
        const float mean = t1 * (1.f / D), var = __builtin_fmaxf(t2 * (1.f / D) - mean * mean, 0.f) + EPS, rstd = __builtin_amdgcn_rsqf(var);
        const GeluK gk = {-mean, GC1 * rstd * rstd * rstd, GC0 * rstd, var * rstd};
        const unsigned uo = WS_U + ((4 * tb6) * XR + tk) * 16;
        auto epi1 = [&](f32x16& y, int te) AI {
#pragma unroll
            for (int q = 0; q < 4; ++q) {
                const f32x4 c = *(const LAS f32x4*)(cs6 + te * 32 + 8 * q + 4 * hh);
#pragma unroll
                for (int i = 0; i < 4; ++i) {
                    const float v = __builtin_fmaf(gk.nmean, c[i], y[4 * q + i]);
                    const float z = v * __builtin_fmaf(v * v, gk.k1, gk.k0);
                    y[4 * q + i] = v * __builtin_amdgcn_rcpf(__builtin_fmaf(__builtin_amdgcn_exp2f(z), gk.irs, gk.irs));
                }
            }
            gst(rs, pack8(y, 0), vx, uo + 4 * te * XROW); gst(rs, pack8(y, 1), vx, uo + (4 * te + 2) * XROW);
            SB();
        };
        epi1(acc[0], 0); epi1(acc[1], 1); epi1(acc[2], 2);
        if (nt6 == 4) epi1(acc[3], 3);
        STAMP(29);
        WAIT_BAR(0);
        if (tid == 0) set_flag(P.ws, flag_off(5, m * 16 + s16), e);
        BAR();
        STAMP(12);
    }
    if (act7) {
        LANE_GEOM();
        const unsigned lanep = trq * 64 + gq * 8 + 2 * hh * 512;
        unsigned tA = L0 + L_X + w * 8192 + lanep, tB = wst + lanep; PIN(tA); PIN(tB);
        auto w4f = [&](int k, int t) AI { const unsigned a = (k < 4 ? tA + k * 2048 : k < 18 ? tB + (k - 4) * 2048 : tB + (k - 18) * 2048) + t * 256; return wfrag(a, a + 512); };
        u32x4 wr[28][2];
#pragma unroll
        for (int k = 0; k < 14; ++k) { wr[k][0] = w4f(k, 0); wr[k][1] = w4f(k, 1); }
        LBAR();
        const int tk = 64 * j7;
        const int tt = w >> 1, nt7 = w & 1;
        wait_flags(P.ws, flag_off(5, (j7 >> 1) * 16 + 4 * w), 4, e, lane);
        constexpr int PX = P7_PX;
        u32x4 xa[28][2];
#pragma unroll
        for (int k = 0; k < PX; ++k) { xa[k][0] = gld(rs, vx, WS_U + ((56 * w + 2 * k) * XR + tk) * 16); xa[k][1] = gld(rs, vx, WS_U + ((56 * w + 2 * k) * XR + tk + 32) * 16); }
        STAMP(13);
        SB();
        f32x16 acc[4] = {};
        u32x4 rh[2], rx[2];
        sfor<28>([&](auto kc) AI {
            constexpr int k = decltype(kc)::value;
            if constexpr (k == 13) { BAR(); SB(); }
            if constexpr (k + 3 >= 14 && k + 3 < 28) { wr[k + 3][0] = w4f(k + 3, 0); wr[k + 3][1] = w4f(k + 3, 1); }
            MFMA(acc[0], wr[k][0], xa[k][0], acc[0]); MFMA(acc[1], wr[k][1], xa[k][0], acc[1]);
            if constexpr (k + PX < 28) xa[k + PX][0] = gld(rs, vx, WS_U + ((56 * w + 2 * (k + PX)) * XR + tk) * 16);
            MFMA(acc[2], wr[k][0], xa[k][1], acc[2]); MFMA(acc[3], wr[k][1], xa[k][1], acc[3]);
            if constexpr (k + PX < 28) xa[k + PX][1] = gld(rs, vx, WS_U + ((56 * w + 2 * (k + PX)) * XR + tk + 32) * 16);
            if constexpr (k == 24 || k == 25) {
                constexpr int s = k - 24;
                const unsigned so = ((8 * g7 + 4 * nt7 + 2 * s) * XR + tk + 32 * tt) * 16;
                rh[s] = gld(rs, vx, WS_H + so); rx[s] = gld(rs, vx, WS_X1 + so);
            }
            SB();
        });
        STAMP(27);
#pragma unroll
        for (int t = 0; t < 4; ++t) {
            if (t != w) {
                const unsigned a = L0 + L_X + t * 12288 + (w < t ? w : w - 1) * 4096 + lane * 16;
#pragma unroll
                for (int q = 0; q < 4; ++q) stsf4(a + q * 1024, (f32x4){acc[t][4 * q], acc[t][4 * q + 1], acc[t][4 * q + 2], acc[t][4 * q + 3]});
            }
        }
        LBAR();
        f32x16 r = w == 0 ? acc[0] : w == 1 ? acc[1] : w == 2 ? acc[2] : acc[3];
        const unsigned a = L0 + L_X + w * 12288 + lane * 16;
#pragma unroll
        for (int j = 0; j < 3; ++j)
#pragma unroll
            for (int q = 0; q < 4; ++q) { const f32x4 v = ldsf4(a + j * 4096 + q * 1024); r[4 * q] += v[0]; r[4 * q + 1] += v[1]; r[4 * q + 2] += v[2]; r[4 * q + 3] += v[3]; }
        const unsigned oo = (tk + 32 * tt + r32) * 896 + 128 * g7 + 64 * nt7 + 16 * hh;
        char* op = (char*)P.out + oo;
        const __amdgpu_buffer_rsrc_t ro = __builtin_amdgcn_make_buffer_rsrc((void*)P.out, 0, S * D * 2, 0x00020000); (void)ro; (void)op;
#pragma unroll
        for (int s = 0; s < 2; ++s) {
            u32x4 ov;
#pragma unroll
            for (int i = 0; i < 4; ++i)
                ov[i] = pk(r[8 * s + 2 * i] + blo(rx[s][i]) + blo(rh[s][i]), r[8 * s + 2 * i + 1] + bhi(rx[s][i]) + bhi(rh[s][i]));
            *(GAS u32x4*)(op + 32 * s) = ov;
        }
        STAMP(14);
    }
    STAMP_FLUSH();
}
}

extern "C" void kernel_launch(void* const* d_in, const int* in_sizes, int n_in, void* d_out, int out_size, void* d_ws, size_t ws_size,
                              hipStream_t stream) {
    mk::Params P;
    P.x = (const u16*)d_in[0]; P.wqkv = (const u16*)d_in[1]; P.wo = (const u16*)d_in[2];
    P.w1 = (const u16*)d_in[4]; P.w2 = (const u16*)d_in[5]; P.w3 = (const u16*)d_in[6]; P.w4 = (const u16*)d_in[7];
    P.out = (u16*)d_out; P.ws = (char*)d_ws;
    mk::fused_fwd<<<256, 512, 0, stream>>>(P);
}
```

```cpp
#include <hip/hip_runtime.h>
#include <hip/hip_bf16.h>
#include <stdint.h>
#include <type_traits>
#include <utility>

typedef unsigned short u16;
typedef __attribute__((ext_vector_type(8))) short bf16x8;
typedef __attribute__((ext_vector_type(4))) short s16x4;
typedef __attribute__((ext_vector_type(16))) float f32x16;
typedef __attribute__((ext_vector_type(4))) float f32x4;
typedef __attribute__((ext_vector_type(2))) float f32x2;
typedef __attribute__((ext_vector_type(4))) unsigned u32x4;
typedef __attribute__((ext_vector_type(2))) unsigned u32x2;
typedef __attribute__((ext_vector_type(2))) __bf16 bf2;
#define LAS __attribute__((address_space(3)))
#define GAS __attribute__((address_space(1)))
#define DEVI __device__ __forceinline__

namespace mk {
constexpr int S = 2048, D = 448;
constexpr unsigned XR = 2048, XROW = XR * 16;
constexpr unsigned KVH = 64 * 4096;
constexpr unsigned WS_EP = 0;
constexpr unsigned WS_FLAG = 4096;
constexpr unsigned WS_KV = 262144;
constexpr unsigned WS_O = WS_KV + 16 * KVH;
constexpr unsigned WS_H = WS_O + 64 * XROW;
constexpr unsigned WS_HH = WS_H + 56 * XROW;
constexpr unsigned WS_X1 = WS_HH + 56 * XROW;
constexpr unsigned WS_U = WS_X1 + 56 * XROW;
constexpr unsigned WS_STH = WS_U + 224 * XROW;
constexpr unsigned WS_STX = WS_STH + 262144;
constexpr unsigned WS_ST1 = WS_STX + 262144;
constexpr unsigned WS_END = WS_ST1 + 32768;
constexpr unsigned L_SLOT = 28672;
constexpr unsigned L_MISC = 143360;
constexpr unsigned L_KV = 114688;
constexpr unsigned L_OWNKV = L_KV + 8 * 4096;
constexpr unsigned L_WST = 28672;
constexpr unsigned L_X = 114688;
constexpr unsigned L_M2 = 147456;
constexpr unsigned CODE_TOUCH_BYTES = 36000;
constexpr float EPS = 1e-5f;
constexpr unsigned SPIN_CAP = 1u << 18;

struct Params {
    const u16* x; const u16* wqkv; const u16* wo; const u16* w1; const u16* w2; const u16* w3; const u16* w4;
    u16* out; char* ws;
};

DEVI unsigned f2u(float f) { return __builtin_bit_cast(unsigned, f); }
DEVI float u2f(unsigned u) { return __builtin_bit_cast(float, u); }
DEVI bool wall(bool p) { return __builtin_amdgcn_ballot_w64(!p) == 0; }
template <int X> DEVI float xor32(float v) { return u2f((unsigned)__builtin_amdgcn_ds_swizzle((int)f2u(v), (X << 10) | 0x1f)); }
DEVI unsigned pk(float lo, float hi) { bf2 v = {(__bf16)lo, (__bf16)hi}; return __builtin_bit_cast(unsigned, v); }
DEVI float blo(unsigned u) { return u2f(u << 16); }
DEVI float bhi(unsigned u) { return u2f(u & 0xffff0000u); }
DEVI float dot2(unsigned a, unsigned b, float c) { return __builtin_amdgcn_fdot2_f32_bf16(__builtin_bit_cast(bf2, a), __builtin_bit_cast(bf2, b), c, false); }
constexpr unsigned ONES = 0x3f803f80u;
DEVI float hsum(float v) { auto r = __builtin_amdgcn_permlane32_swap(f2u(v), f2u(v), false, false); return u2f(r[0]) + u2f(r[1]); }
DEVI float hmax(float v) { auto r = __builtin_amdgcn_permlane32_swap(f2u(v), f2u(v), false, false); return __builtin_fmaxf(u2f(r[0]), u2f(r[1])); }
DEVI f32x16 mfma(u32x4 a, u32x4 b, f32x16 c) { return __builtin_amdgcn_mfma_f32_32x32x16_bf16(__builtin_bit_cast(bf16x8, a), __builtin_bit_cast(bf16x8, b), c, 0, 0, 0); }
#define MFMA(acc, a, b, c) do { acc = mfma(a, b, c); asm volatile("" : "+v"(acc)); } while (0)
DEVI u32x4 lds128(unsigned a) { return *(const LAS u32x4*)(uintptr_t)a; }
DEVI f32x4 ldsf4(unsigned a) { return *(const LAS f32x4*)(uintptr_t)a; }
DEVI void sts128(unsigned a, u32x4 v) { *(LAS u32x4*)(uintptr_t)a = v; }
DEVI void stsf4(unsigned a, f32x4 v) { *(LAS f32x4*)(uintptr_t)a = v; }
DEVI u32x2 ldstr(unsigned a) { return __builtin_bit_cast(u32x2, __builtin_amdgcn_ds_read_tr16_b64_v4i16((LAS s16x4*)(uintptr_t)a)); }
DEVI u32x4 wfrag(unsigned a0, unsigned a1) { u32x2 p = ldstr(a0), q = ldstr(a1); return (u32x4){p.x, p.y, q.x, q.y}; }
DEVI u32x4 pack8(const f32x16& a, int s) { return (u32x4){pk(a[8 * s], a[8 * s + 1]), pk(a[8 * s + 2], a[8 * s + 3]), pk(a[8 * s + 4], a[8 * s + 5]), pk(a[8 * s + 6], a[8 * s + 7])}; }
constexpr int SC1 = 16;
DEVI u32x4 gld(__amdgpu_buffer_rsrc_t rs, unsigned voff, unsigned soff) { return __builtin_amdgcn_raw_buffer_load_b128(rs, voff, soff, SC1); }
DEVI u32x2 gld8(__amdgpu_buffer_rsrc_t rs, unsigned voff, unsigned soff) { return __builtin_amdgcn_raw_buffer_load_b64(rs, voff, soff, SC1); }
DEVI void gst(__amdgpu_buffer_rsrc_t rs, u32x4 v, unsigned voff, unsigned soff) { __builtin_amdgcn_raw_buffer_store_b128(v, rs, voff + soff, 0, SC1); }
DEVI void gst8(__amdgpu_buffer_rsrc_t rs, u32x2 v, unsigned voff, unsigned soff) { __builtin_amdgcn_raw_buffer_store_b64(v, rs, voff, soff, SC1); }
#define WAIT_VM(N) asm volatile("s_waitcnt vmcnt(" #N ")" ::: "memory")
#define WAIT_LGKM() asm volatile("s_waitcnt lgkmcnt(0)" ::: "memory")
#define WAIT_BAR(N) asm volatile("s_waitcnt vmcnt(" #N ") lgkmcnt(0)\n\ts_barrier" ::: "memory")
#define LBAR() asm volatile("s_waitcnt lgkmcnt(0)\n\ts_barrier" ::: "memory")
#define SB() __builtin_amdgcn_sched_barrier(0)
#define PIN(x) asm volatile("" : "+v"(x))
DEVI float max3(float a, float b, float c) { return __builtin_fmaxf(__builtin_fmaxf(a, b), c); }
template <int... Is, class F> DEVI void sfor_impl(std::integer_sequence<int, Is...>, F&& f) { (f(std::integral_constant<int, Is>{}), ...); }
template <int N, class F> DEVI void sfor(F&& f) { sfor_impl(std::make_integer_sequence<int, N>{}, f); }
#define AI __attribute__((always_inline))
#define IC(n) std::integral_constant<int, n>{}

DEVI void dma1(const char* sbase, unsigned voff, unsigned lds) {
    asm volatile("s_mov_b32 m0, %2\n\ts_nop 0\n\tglobal_load_lds_dwordx4 %0, %1" :: "v"(voff), "s"(sbase), "s"(lds) : "memory");
}
struct WSrc { const char* base; unsigned step, voff; };
DEVI void wpiece(const WSrc& s, unsigned lds, int n) { dma1(s.base + (size_t)n * s.step, s.voff, lds + n * 1024); }
DEVI WSrc wsrc_a(const char* base, unsigned rs, int nt, int lane) { const int t = (lane >> 4) < nt ? (lane >> 4) : nt - 1; return WSrc{base, 4 * rs, ((lane >> 2) & 3) * rs + t * 64 + (lane & 3) * 16}; }
DEVI WSrc wsrc_b(const char* base, unsigned rs, int lane) { return WSrc{base, 8 * rs, (4 * (lane >> 5) + ((lane >> 2) & 3)) * rs + ((lane >> 4) & 1) * 64 + (lane & 3) * 16}; }

DEVI void poll_word(const GAS unsigned* p, unsigned e) {
#pragma nounroll
    for (unsigned sp = 0; sp < SPIN_CAP; ++sp) {
        const unsigned v = __hip_atomic_load(p, __ATOMIC_RELAXED, __HIP_MEMORY_SCOPE_AGENT);
        if (wall(v == e)) break;
    }
    __builtin_amdgcn_fence(__ATOMIC_ACQUIRE, "wavefront");
}
DEVI void wait_flags(const char* ws, unsigned foff, int n, unsigned e, int lane) {
    poll_word((const GAS unsigned*)(ws + foff) + (lane < n ? lane : 0) * 32, e);
}
DEVI void set_flag(char* ws, unsigned foff, unsigned e) { __hip_atomic_store((GAS unsigned*)(ws + foff), e, __ATOMIC_RELAXED, __HIP_MEMORY_SCOPE_AGENT); }
DEVI unsigned flag_off(int ph, int idx) { return WS_FLAG + (ph * 256 + idx) * 128; }

constexpr float GC0 = -2.f * 0.7978845608028654f * 1.4426950408889634f, GC1 = GC0 * 0.044715f;
DEVI float gelu_t(float x) {
    float z = x * __builtin_fmaf(x * x, GC1, GC0);
    return x * __builtin_amdgcn_rcpf(1.f + __builtin_amdgcn_exp2f(z));
}
struct GeluK { float nmean, k1, k0, irs; };

#define LANE_GEOM() int lane_ = threadIdx.x & 63; asm volatile("" : "+v"(lane_)); const int lane = lane_, r32 = lane & 31, hh = lane >> 5; \
    const int trq = (lane & 15) >> 2, trQ_ = ((lane >> 4) & 1) * 4 + (lane & 3), gq = (trQ_ & 4) | ((trQ_ & 1) << 1) | ((trQ_ >> 1) & 1); \
    const unsigned vx = (hh * XR + r32) * 16; (void)trq; (void)gq; (void)vx; (void)r32; (void)hh
constexpr unsigned L_FLG = L_MISC + 1920, F_RDY = 0, F_DONE = 16;
DEVI unsigned ldsw_ld(unsigned i) { return *(volatile LAS unsigned*)(uintptr_t)(L_FLG + 4 * i); }
DEVI u32x2 ldsw_ld2(unsigned i) { return *(volatile LAS u32x2*)(uintptr_t)(L_FLG + 4 * i); }
DEVI void ldsw_st(unsigned i, unsigned v) { *(volatile LAS unsigned*)(uintptr_t)(L_FLG + 4 * i) = v; }
DEVI void ring_wait(int c) {
#pragma nounroll
    for (unsigned sp = 0; sp < SPIN_CAP * 16u; ++sp) { const u32x2 v = ldsw_ld2(F_RDY + 2 * c); if (__builtin_amdgcn_readfirstlane(v.x & v.y)) break; __builtin_amdgcn_s_sleep(1); }
}
DEVI void slot_wait(unsigned n, int lane) {
#pragma nounroll
    for (unsigned sp = 0; sp < SPIN_CAP * 16u; ++sp) { const unsigned v = ldsw_ld(F_DONE + (lane < 6 ? lane : 0)); if (wall(v >= n)) break; __builtin_amdgcn_s_sleep(1); }
}
#define LB(N) asm volatile("s_waitcnt vmcnt(" #N ")\n\ts_barrier" ::: "memory")
#define BAR() asm volatile("s_barrier" ::: "memory")

__global__ __launch_bounds__(512, 2) void fused_fwd(Params P) {
    __shared__ __attribute__((aligned(16))) char smem[163840];
    const unsigned L0 = (unsigned)(uintptr_t)(LAS char*)smem;
    const int tid = threadIdx.x, wv = __builtin_amdgcn_readfirstlane(tid >> 6), w = wv & 3;
    const int b = blockIdx.x, xcd = b & 7, slot = b >> 3;
    const int m = 2 * xcd + (slot >> 4), h = slot & 15;
    const int g = slot & 3, nt = g < 2 ? 4 : 3, tb = g < 2 ? 4 * g : 3 * g + 2;
    const int ti = 8 * xcd + (slot >> 2);
    const int s16 = slot & 15, pr = s16 >> 1;
    const int nt6 = ((s16 ^ pr) & 1) ? 3 : 4, tb6 = 7 * pr + ((s16 & 1) ? (pr & 1 ? 3 : 4) : 0);
    const bool act7 = slot < 28;
    const int j7 = 4 * xcd + (act7 ? slot / 7 : 0), g7 = act7 ? slot % 7 : 0;
    const unsigned wst = L0 + w * L_WST;
    const unsigned shift = (56 * h) & 15u;

    if (wv >= 4) {
        LANE_GEOM();
        const unsigned wrd = 16384 + hh * 6 * 256 + trq * 64 + ((shift + gq * 8) & 63);
        LAS float* csp = (LAS float*)(uintptr_t)(L0 + L_MISC);
        if (w < 2) {
            const unsigned xoffE = (lane >> 3) * 896 + (((lane & 7) ^ (lane >> 4)) << 4);
            const unsigned xoffO = (lane >> 3) * 896 + (((lane & 7) ^ (4 + (lane >> 4))) << 4);
            const unsigned colb = (56 * h) & ~15u;
            unsigned woff[3];
#pragma unroll
            for (int j = 0; j < 3; ++j) {
                const int bl = 4 * j + (lane >> 4), kql = bl / 3, tile = bl % 3;
                woff[j] = (4 * kql + ((lane >> 2) & 3)) * 2688 + tile * 896 + colb + (lane & 3) * 16;
            }
            const char* xb = (const char*)P.x + (size_t)(128 * m + 64 * w) * 896;
            const char* wb = (const char*)P.wqkv + (size_t)(32 * w) * 2688;
            LBAR();
#pragma unroll 1
            for (int kc = 0; kc < 7; ++kc) {
                if (kc >= 5) slot_wait(kc - 4, lane);
                const unsigned base = L0 + (kc >= 5 ? kc - 5 : kc) * L_SLOT;
#pragma unroll
                for (int i = 0; i < 6; ++i) dma1(wb + (size_t)kc * 64 * 2688 + (i / 3) * 16 * 2688, woff[i % 3], base + 16384 + (6 * w + i) * 1024);
#pragma unroll
                for (int i = 0; i < 8; ++i) dma1(xb + kc * 128 + i * 8 * 896, (i & 1) ? xoffO : xoffE, base + (8 * w + i) * 1024);
                if (kc > 0) { WAIT_VM(14); ldsw_st(F_RDY + 2 * (kc - 1) + w, 1); }
            }
            WAIT_VM(0); ldsw_st(F_RDY + 12 + w, 1);
            {
                ring_wait(6);
                const unsigned a = L0 + L_SLOT + wrd + w * 3072;
#pragma unroll
                for (int t = 0; t < 3; ++t) { const u32x4 f = wfrag(a + t * 256, a + 768 + t * 256);
                    const float v = hsum((dot2(f[0], ONES, 0.f) + dot2(f[1], ONES, 0.f)) + (dot2(f[2], ONES, 0.f) + dot2(f[3], ONES, 0.f)));
                    if (hh == 0) csp[(2 + w) * 96 + t * 32 + r32] = v; }
            }
        } else {
            const int srow = 128 * m + 8 * h + 4 * (w - 2) + (lane >> 4);
            const char* sp = (const char*)P.x + (size_t)srow * 896 + (lane & 15) * 16;
            u32x4 sr[4];
            asm volatile("global_load_dwordx4 %0, %4, off\n\tglobal_load_dwordx4 %1, %4, off offset:256\n\tglobal_load_dwordx4 %2, %4, off offset:512\n\tglobal_load_dwordx4 %3, %5, off"
                         : "=&v"(sr[0]), "=&v"(sr[1]), "=&v"(sr[2]), "=&v"(sr[3]) : "v"(sp), "v"(sp + ((lane & 15) < 8 ? 768 : 0)) : "memory");
            if (w == 2 && lane < 32) ldsw_st(lane, 0);
            LBAR();
            {
                const unsigned lx = lane < 56 ? lane : 0, lw = b * 37 + (lane < 37 ? lane : 0);
                const char* tp = w == 2 ? (const char*)P.x + (size_t)(8 * b + lx / 7) * 896 + (lx % 7) * 128 : (const char*)P.wqkv + (size_t)(lw < 9408 ? lw : 0) * 128;
                __builtin_amdgcn_s_sleep(12);
                asm volatile("s_mov_b32 m0, %1\n\ts_nop 0\n\tglobal_load_lds_dword %0, off" :: "v"(tp), "s"(L0 + L_MISC + 2048) : "memory");
            }
            {
                asm volatile("s_waitcnt vmcnt(1)" : "+v"(sr[0]), "+v"(sr[1]), "+v"(sr[2]), "+v"(sr[3]) :: "memory");
                if ((lane & 15) >= 8) sr[3] = (u32x4){0u, 0u, 0u, 0u};
                float t1 = 0.f, t2 = 0.f;
#pragma unroll
                for (int q = 0; q < 4; ++q)
#pragma unroll
                    for (int i = 0; i < 4; ++i) { t1 = dot2(sr[q][i], ONES, t1); t2 = dot2(sr[q][i], sr[q][i], t2); }
                t1 += xor32<1>(t1); t2 += xor32<1>(t2); t1 += xor32<2>(t1); t2 += xor32<2>(t2); t1 += xor32<4>(t1); t2 += xor32<4>(t2); t1 += xor32<8>(t1); t2 += xor32<8>(t2);
                const float mean = t1 * (1.f / D), var = __builtin_fmaxf(t2 * (1.f / D) - mean * mean, 0.f);
                const unsigned e = *(const unsigned*)(P.ws + WS_EP) + 1u;
                const __amdgpu_buffer_rsrc_t rs = __builtin_amdgcn_make_buffer_rsrc((void*)P.ws, 0, WS_END, 0x00020000);
                if ((lane & 15) == 0) gst(rs, (u32x4){f2u(mean), e, f2u(__builtin_amdgcn_rsqf(var + EPS)), e}, srow * 16, WS_ST1);
            }
            float cs[3][4] = {};
            const unsigned wrd2 = wrd + (w - 2) * 2 * 3072;
#pragma unroll 1
            for (int c = 0; c < 7; ++c) {
                ring_wait(c);
                if (c == 3) {
                    unsigned long long pc; unsigned back;
                    asm volatile("s_getpc_b64 %0\nmk_code_here:\n\ts_mov_b32 %1, mk_code_here-_ZN2mk9fused_fwdENS_6ParamsE" : "=s"(pc), "=s"(back));
                    constexpr unsigned len = CODE_TOUCH_BYTES;
                    const unsigned oc = (slot * 18 + (lane < 18 ? lane : 0)) * 128, o3 = (b * 64 + lane) * 128, o2 = (b * 16 + (lane & 15)) * 128;
                    const char* t0 = w == 2 ? (const char*)(pc - back) + (oc < len ? oc : 0) : (const char*)P.w3 + (o3 < 1605632 ? o3 : 0);
                    const char* t1 = w == 2 ? (lane < 16 ? (const char*)P.wo : lane < 32 ? (const char*)P.w1 : (const char*)P.w2) + (o2 < 401408 ? o2 : 0) : (const char*)P.w4 + (o3 < 1605632 ? o3 : 0);
                    asm volatile("s_mov_b32 m0, %2\n\ts_nop 0\n\tglobal_load_lds_dword %0, off\n\tglobal_load_lds_dword %1, off" :: "v"(t0), "v"(t1), "s"(L0 + L_MISC + 2048) : "memory");
                }
                const unsigned a = L0 + (c >= 5 ? c - 5 : c) * L_SLOT + (c < 6 ? wrd2 : wrd + w * 3072);
#pragma unroll
                for (int t = 0; t < 3; ++t) { const u32x4 f = wfrag(a + t * 256, a + 768 + t * 256);
#pragma unroll
                    for (int i = 0; i < 4; ++i) cs[t][i] = dot2(f[i], ONES, cs[t][i]); }
                if (c < 6) {
#pragma unroll
                    for (int t = 0; t < 3; ++t) { const u32x4 f = wfrag(a + 3072 + t * 256, a + 3072 + 768 + t * 256);
#pragma unroll
                        for (int i = 0; i < 4; ++i) cs[t][i] = dot2(f[i], ONES, cs[t][i]); }
                }
                WAIT_LGKM();
                ldsw_st(F_DONE + 2 + w, c + 1);
            }
#pragma unroll
            for (int t = 0; t < 3; ++t) { const float v = hsum((cs[t][0] + cs[t][1]) + (cs[t][2] + cs[t][3])); if (hh == 0) csp[(w - 2) * 96 + t * 32 + r32] = v; }
        }
        LBAR();
        auto stage = [&](const WSrc& s, unsigned lds, int n0, int n1) AI {
#pragma unroll 1
            for (int n = n0; n < n1; ++n) wpiece(s, lds, n);
        };
        LB(0);
        BAR();
        stage(wsrc_a((const char*)P.wo + (size_t)(112 * w) * 896 + tb * 64, 896, nt, lane), wst, 0, 28);
        LB(0);
        { const WSrc s_ = wsrc_a((const char*)P.w1 + (size_t)(112 * w) * 896 + tb * 64, 896, nt, lane); BAR(); stage(s_, wst, 0, 12); BAR(); stage(s_, wst, 12, 28); BAR(); LB(0); }
        { const WSrc s_ = wsrc_a((const char*)P.w2 + (size_t)(112 * w) * 896 + tb * 64, 896, nt, lane); BAR(); stage(s_, wst, 0, 12); BAR(); stage(s_, wst, 12, 28); BAR(); LB(0); }
        { const WSrc s_ = wsrc_a((const char*)P.w3 + (size_t)(112 * w) * 3584 + tb6 * 64, 3584, nt6, lane); BAR(); stage(s_, wst, 0, 12); BAR(); stage(s_, wst, 12, 28); BAR(); LB(0); }
        {
            float c[4] = {0.f, 0.f, 0.f, 0.f};
            const unsigned a0 = L0 + trq * 64 + gq * 8 + 2 * hh * 1024 + w * 256;
#pragma unroll 4
            for (int k = 0; k < 28; ++k) { const unsigned a = a0 + k * 4096; const u32x4 f = wfrag(a, a + 1024);
#pragma unroll
                for (int i = 0; i < 4; ++i) c[i] = dot2(f[i], ONES, c[i]); }
            const float cc = hsum((c[0] + c[1]) + (c[2] + c[3]));
            if (hh == 0) ((LAS float*)(uintptr_t)(L0 + L_M2 + 1024))[w * 32 + r32] = cc;
        }
        if (b == 0 && w == 3) {
            const unsigned e = *(const volatile GAS unsigned*)(P.ws + WS_EP) + 1u;
            const GAS unsigned* p = (const GAS unsigned*)(P.ws + flag_off(0, lane));
#pragma nounroll
            for (unsigned sp = 0; sp < SPIN_CAP; ++sp) {
                const unsigned v0 = __hip_atomic_load(p, __ATOMIC_RELAXED, __HIP_MEMORY_SCOPE_AGENT), v1 = __hip_atomic_load(p + 64 * 32, __ATOMIC_RELAXED, __HIP_MEMORY_SCOPE_AGENT);
                const unsigned v2 = __hip_atomic_load(p + 128 * 32, __ATOMIC_RELAXED, __HIP_MEMORY_SCOPE_AGENT), v3 = __hip_atomic_load(p + 192 * 32, __ATOMIC_RELAXED, __HIP_MEMORY_SCOPE_AGENT);
                if (wall(v0 == e && v1 == e && v2 == e && v3 == e)) break;
            }
            if (lane == 0) __hip_atomic_store((GAS unsigned*)(P.ws + WS_EP), e, __ATOMIC_RELAXED, __HIP_MEMORY_SCOPE_AGENT);
        }
        LBAR();
        const WSrc w4s = wsrc_b((const char*)P.w4 + (size_t)(448 * w) * 896 + g7 * 128, 896, lane);
        if (act7) { stage(w4s, L0 + L_X + w * 8192, 0, 8); stage(w4s, wst - 8 * 1024, 8, 36); }
        BAR();
        LB(0);
        if (act7) {
            BAR();
            stage(w4s, wst - 36 * 1024, 36, 56);
            LB(0);
            BAR();
        }
        return;
    }

    __builtin_amdgcn_s_setprio(2);
    const unsigned e = *(const unsigned*)(P.ws + WS_EP) + 1u;
    const __amdgpu_buffer_rsrc_t rs = __builtin_amdgcn_make_buffer_rsrc((void*)P.ws, 0, WS_END, 0x00020000);
    u32x4 qf0, qf1;
    {
        LANE_GEOM();
        const unsigned wrd = 16384 + hh * 6 * 256 + trq * 64 + ((shift + gq * 8) & 63);
        f32x16 aq = {}, ak = {}, av = {};
        const unsigned xrd = (32 * w + r32) * 128 + ((hh ^ ((r32 >> 1) & 7)) << 4);
        struct Fr { u32x4 x, q, k, v; };
        Fr F[4];
        unsigned xb[3][4], wb3[3];
#pragma unroll
        for (int gsl = 0; gsl < 3; ++gsl) {
#pragma unroll
            for (int j = 0; j < 4; ++j) { xb[gsl][j] = L0 + (xrd ^ (j << 5)) + gsl * 2 * L_SLOT; PIN(xb[gsl][j]); }
            wb3[gsl] = L0 + wrd + gsl * 2 * L_SLOT; PIN(wb3[gsl]);
        }
        auto ldx = [&](int sl, int j) AI { F[j].x = lds128(xb[sl / 2][j] + (sl & 1) * L_SLOT); const unsigned a = wb3[sl / 2] + (sl & 1) * L_SLOT + j * 3072; F[j].q = wfrag(a, a + 768); };
        auto ldk = [&](int sl, int j) AI { const unsigned a = wb3[sl / 2] + (sl & 1) * L_SLOT + j * 3072; F[j].k = wfrag(a + 256, a + 1024); };
        auto ldv = [&](int sl, int j) AI { const unsigned a = wb3[sl / 2] + (sl & 1) * L_SLOT + j * 3072; F[j].v = wfrag(a + 512, a + 1280); };
        LAS float* stw = (LAS float*)(uintptr_t)(L0 + L_MISC + 3072 + w * 256);
        BAR();
        ring_wait(0);
#pragma unroll
        for (int j = 0; j < 2; ++j) { ldx(0, j); ldk(0, j); ldv(0, j); }
        SB();
        u32x4 gv = {0u, 0u, 0u, 0u};
        const unsigned gvo = (128 * m + 32 * w + r32) * 16;
        sfor<7>([&](auto cc) AI {
            constexpr int c = decltype(cc)::value, sl = c >= 5 ? c - 5 : c, nsl = c >= 4 ? c - 4 : c + 1;
            u32x2 rdy;
            sfor<4>([&](auto jc) AI {
                constexpr int j = decltype(jc)::value, jn = (j + 2) & 3, lb = j < 2 ? sl : nsl;
                constexpr bool ld = j < 2 || c < 6;
                if constexpr (j == 2 && c < 6) { if (__builtin_amdgcn_readfirstlane(rdy.x & rdy.y) == 0) ring_wait(c + 1); SB(); }
                MFMA(aq, F[j].q, F[j].x, aq);
                if constexpr (ld) ldx(lb, jn);
                SB();
                MFMA(ak, F[j].k, F[j].x, ak);
                if constexpr (ld) ldk(lb, jn);
                if constexpr (j == 0 && c >= 1 && c <= 2) ldsw_st(F_DONE + w, c);
                if constexpr (j == 0 && c == 4) gv = gld(rs, gvo, WS_ST1);
                SB();
                MFMA(av, F[j].x, F[j].v, av);
                if constexpr (ld) ldv(lb, jn);
                if constexpr (j == 0 && c < 6) rdy = ldsw_ld2(F_RDY + 2 * (c + 1));
                SB();
            });
        });
#pragma nounroll
        for (unsigned sp2 = 0; sp2 < SPIN_CAP && !wall(gv.y == e && gv.w == e); ++sp2) { asm volatile("" ::: "memory"); gv = gld(rs, gvo, WS_ST1); }
        if (hh == 0) { stw[r32] = u2f(gv.x) * u2f(gv.z); stw[32 + r32] = u2f(gv.z); }
        const float mean = u2f(gv.x), rstd = u2f(gv.z);
        LBAR();
        const LAS float* csf = (const LAS float*)(uintptr_t)(L0 + L_MISC);
        const float cv = (csf[64 + r32] + csf[96 + 64 + r32]) + (csf[192 + 64 + r32] + csf[288 + 64 + r32]);
        const float qs = rstd * (0.18898223650461362f * 1.4426950408889634f);
#pragma unroll
        for (int q = 0; q < 4; ++q) {
            auto c4 = [&](int o) AI { return (*(const LAS f32x4*)(csf + o) + *(const LAS f32x4*)(csf + 96 + o)) + (*(const LAS f32x4*)(csf + 192 + o) + *(const LAS f32x4*)(csf + 288 + o)); };
            const f32x4 c1 = c4(8 * q + 4 * hh), c2 = c4(32 + 8 * q + 4 * hh);
            const f32x4 mr = *(const LAS f32x4*)(stw + 8 * q + 4 * hh), rr = *(const LAS f32x4*)(stw + 32 + 8 * q + 4 * hh);
#pragma unroll
            for (int i = 0; i < 4; ++i) {
                aq[4 * q + i] = (aq[4 * q + i] - mean * c1[i]) * qs;
                ak[4 * q + i] = (ak[4 * q + i] - mean * c2[i]) * rstd;
                av[4 * q + i] = av[4 * q + i] * rr[i] - mr[i] * cv;
            }
        }
        if (hh == 1) { aq[12] = 1.f; aq[13] = 0.f; aq[14] = 0.f; aq[15] = 0.f; ak[12] = 0.f; ak[13] = 0.f; ak[14] = 0.f; ak[15] = 0.f; }
        if (r32 >= 28) {
            const float pv = r32 == 28 ? 1.f : 0.f;
#pragma unroll
            for (int i = 0; i < 16; ++i) av[i] = pv;
        }
        const u32x4 k0 = pack8(ak, 0), k1 = pack8(ak, 1), v0 = pack8(av, 0), v1 = pack8(av, 1);
        const unsigned go = WS_KV + h * KVH + (4 * m + w) * 4096;
        gst(rs, k0, lane * 16, go); gst(rs, k1, lane * 16, go + 1024); gst(rs, v0, lane * 16, go + 2048); gst(rs, v1, lane * 16, go + 3072);
        const unsigned lo = L0 + L_OWNKV + w * 4096 + lane * 16;
        sts128(lo, k0); sts128(lo + 1024, k1); sts128(lo + 2048, v0); sts128(lo + 3072, v1);
        qf0 = pack8(aq, 0); qf1 = pack8(aq, 1);
        WAIT_BAR(0);
        if (tid == 0) set_flag(P.ws, flag_off(0, m * 16 + h), e);
    }
    {
        LANE_GEOM();
        const int mt = m - 2 + (w >> 1);
        u32x4 kv[8];
        if (mt >= 0) {
            wait_flags(P.ws, flag_off(0, mt * 16 + h), 1, e, lane);
            const unsigned go = WS_KV + h * KVH + (4 * m - 8 + 2 * w) * 4096;
#pragma unroll
            for (int i = 0; i < 8; ++i) kv[i] = gld(rs, lane * 16, go + i * 1024);
        } else {
#pragma unroll
            for (int i = 0; i < 8; ++i) kv[i] = (u32x4){0u, 0u, (i & 3) == 1 && hh == 1 ? 0xF149u : 0u, 0u};
        }
        f32x16 cdiag, cold;
#pragma unroll
        for (int i = 0; i < 16; ++i) { const int kr = (i & 3) + 8 * (i >> 2) + 4 * hh; cdiag[i] = kr <= r32 ? 0.f : -1e30f; cold[i] = kr > r32 ? 0.f : -1e30f; }
#pragma unroll
        for (int i = 0; i < 8; ++i) sts128(L0 + L_KV + 2 * w * 4096 + i * 1024 + lane * 16, kv[i]);
        LBAR();
        unsigned kvb = L0 + L_KV + w * 4096 + lane * 16;
        PIN(kvb);
        u32x4 kf[9][2], vf[9][2];
        const f32x16 zero16 = {};
        auto ldK = [&](int i) AI { kf[i][0] = lds128(kvb + i * 4096); kf[i][1] = lds128(kvb + i * 4096 + 1024); };
        auto ldV = [&](int i) AI { vf[i][0] = lds128(kvb + i * 4096 + 2048); vf[i][1] = lds128(kvb + i * 4096 + 3072); };
        auto smax = [&](const f32x16& s, float a) AI {
#pragma unroll
            for (int i = 0; i < 16; i += 2) a = max3(a, s[i], s[i + 1]);
            return a; };
        ldK(0); ldK(1);
        float mx = -3e38f;
        f32x16 s[2];
        auto smax8 = [&](const f32x16& v, int o, float a) AI {
#pragma unroll
            for (int i = 0; i < 8; i += 2) a = max3(a, v[o + i], v[o + i + 1]);
            return a; };
        sfor<9>([&](auto ic) AI {
            constexpr int i = decltype(ic)::value;
            if constexpr (i + 2 < 9) ldK(i + 2);
            MFMA(s[i & 1], kf[i][0], qf0, i == 0 ? cold : i == 8 ? cdiag : zero16);
            SB();
            MFMA(s[i & 1], kf[i][1], qf1, s[i & 1]);
            SB();
            if constexpr (i > 0) { mx = smax8(s[(i - 1) & 1], 0, mx); mx = smax8(s[(i - 1) & 1], 8, mx); PIN(mx); }
            SB();
        });
        ldK(0); ldK(1); ldV(0);
        mx = hmax(smax(s[0], mx));
        f32x16 o = {}, cm;
#pragma unroll
        for (int i = 0; i < 16; ++i) { cm[i] = -mx; cdiag[i] -= mx; cold[i] -= mx; }
        MFMA(s[0], kf[0][0], qf0, cold); MFMA(s[0], kf[0][1], qf1, s[0]);
        SB();
        sfor<9>([&](auto ic) AI {
            constexpr int i = decltype(ic)::value;
            f32x16& c = s[i & 1];
            f32x16& n = s[(i + 1) & 1];
            if constexpr (i + 2 < 9) ldK(i + 2);
            if constexpr (i + 1 < 9) ldV(i + 1);
            if constexpr (i + 1 < 9) MFMA(n, kf[i + 1][0], qf0, i + 1 == 8 ? cdiag : cm);
#pragma unroll
            for (int j = 0; j < 8; ++j) c[j] = __builtin_amdgcn_exp2f(c[j]);
            u32x4 p0 = pack8(c, 0);
            PIN(p0);
            SB();
            if constexpr (i + 1 < 9) MFMA(n, kf[i + 1][1], qf1, n);
            MFMA(o, vf[i][0], p0, o);
#pragma unroll
            for (int j = 8; j < 16; ++j) c[j] = __builtin_amdgcn_exp2f(c[j]);
            const u32x4 p1 = pack8(c, 1);
            MFMA(o, vf[i][1], p1, o);
            SB();
        });
        auto lr = __builtin_amdgcn_permlane32_swap(f2u(o[12]), f2u(o[12]), false, false);
        const float inv = __builtin_amdgcn_rcpf(u2f(lr[1]));
#pragma unroll
        for (int i = 0; i < 16; ++i) o[i] *= inv;
        if (hh == 1) { o[12] = 0.f; o[13] = 0.f; o[14] = 0.f; o[15] = 0.f; }
        const unsigned vo = (hh * XR + r32) * 16, so = WS_O + ((4 * h) * XR + 128 * m + 32 * w) * 16;
        gst(rs, pack8(o, 0), vo, so); gst(rs, pack8(o, 1), vo, so + 2 * XROW);
        WAIT_BAR(0);
        if (tid == 0) set_flag(P.ws, flag_off(1, m * 16 + h), e);
    }
    const bool act = w < nt;
    const int tw = tb + w;
#define GEMM_GEOM() LANE_GEOM(); \
    unsigned trW[2]; trW[0] = L0 + (w < nt ? w : nt - 1) * 256 + trq * 64 + gq * 8 + 2 * hh * 1024; trW[1] = trW[0] + 57344; PIN(trW[0]); PIN(trW[1]); \
    auto wimg = [&](int kq0, int kq1) AI { return wfrag(kq0 < 56 ? trW[0] + kq0 * 1024 : trW[1] + (kq0 - 56) * 1024, kq1 < 56 ? trW[0] + kq1 * 1024 : trW[1] + (kq1 - 56) * 1024); }; \
    unsigned xl = L0 + L_X + lane * 16; PIN(xl); \
    LAS float* m2 = (LAS float*)(uintptr_t)(L0 + L_M2 + w * 256); (void)m2
    auto gemm = [&](auto nk, const u32x4* wr, unsigned xl) AI -> f32x16 {
        constexpr int NK = decltype(nk)::value, PD = 4;
        f32x16 acc = {};
        u32x4 xf[NK];
#pragma unroll
        for (int k = 0; k < PD; ++k) xf[k] = lds128(xl + k * 1024);
        SB();
        sfor<NK>([&](auto kc) AI {
            constexpr int k = decltype(kc)::value;
            if constexpr (k + PD < NK) xf[k + PD] = lds128(xl + (k + PD) * 1024);
            MFMA(acc, wr[k], xf[k], acc);
            SB();
        });
        return acc;
    };
    u32x4 hp[2];
    float meanH, rstdH;
    {
        GEMM_GEOM();
        u32x4 xr[2];
        const char* xp = (const char*)P.x + (size_t)(32 * ti + r32) * 896 + 64 * (act ? tw : tb) + 16 * hh;
        xr[0] = *(const GAS u32x4*)xp; xr[1] = *(const GAS u32x4*)(xp + 32);
        u32x4 wr[32];
        sfor<32>([&](auto kc) AI { constexpr int k = decltype(kc)::value, kq = 7 * (k >> 1) + 4 * (k & 1);
            if constexpr (k < 31) wr[k] = wimg(kq, kq + 1);
            else { const u32x2 p = ldstr(trW[1] + (kq - 56) * 1024), q = ldstr(trW[1] + (kq - 56) * 1024 + (hh ? 0 : 1024)); wr[k] = (u32x4){p.x, p.y, q.x, q.y}; } });
        LBAR();
        wait_flags(P.ws, flag_off(1, (ti >> 2) * 16 + 4 * w), 4, e, lane);
        u32x4 xf[8];
#pragma unroll
        for (int k = 0; k < 8; ++k) xf[k] = gld(rs, vx, WS_O + ((16 * w + 2 * k) * XR + 32 * ti) * 16);
#pragma unroll
        for (int k = 0; k < 8; ++k) sts128(xl + (8 * w + k) * 1024, xf[k]);
        LBAR();
        f32x16 r = gemm(IC(32), wr, xl);
        float s1 = 0.f, s2 = 0.f;
#pragma unroll
        for (int s = 0; s < 2; ++s) {
#pragma unroll
            for (int i = 0; i < 4; ++i) {
                hp[s][i] = pk(r[8 * s + 2 * i] + blo(xr[s][i]), r[8 * s + 2 * i + 1] + bhi(xr[s][i]));
                s1 = dot2(hp[s][i], ONES, s1); s2 = dot2(hp[s][i], hp[s][i], s2);
            }
            if (act) gst(rs, hp[s], vx, WS_H + ((4 * tw + 2 * s) * XR + 32 * ti) * 16);
        }
        s1 = hsum(s1); s2 = hsum(s2);
        if (act && hh == 0) gst8(rs, (u32x2){f2u(s1), f2u(s2)}, r32 * 8, WS_STH + (tw * 2048 + 32 * ti) * 8);
        WAIT_BAR(0);
        if (tid == 0) set_flag(P.ws, flag_off(2, ti * 4 + g), e);
    }
#define LOAD_WR() u32x4 wr[28]; sfor<28>([&](auto kc) AI { constexpr int k = decltype(kc)::value; wr[k] = wimg(4 * k, 4 * k + 1); })
    const int fsn = w == 0 ? 1 : 2, fsl = w <= 1 ? 0 : w - 1;
    {
        GEMM_GEOM();
        BAR();
        LOAD_WR();
        LBAR();
        wait_flags(P.ws, flag_off(2, ti * 4), 4, e, lane);
        u32x4 xf[7];
#pragma unroll
        for (int k = 0; k < 7; ++k) xf[k] = gld(rs, vx, WS_H + ((14 * w + 2 * k) * XR + 32 * ti) * 16);
        u32x2 sv[7];
#pragma unroll
        for (int q = 0; q < 7; ++q) sv[q] = gld8(rs, (7 * hh * 2048 + r32) * 8, WS_STH + (q * 2048 + 32 * ti) * 8);
        SB();
        {
            float cs[4] = {0.f, 0.f, 0.f, 0.f};
#pragma unroll
            for (int k = 0; k < 28; ++k)
#pragma unroll
                for (int i = 0; i < 4; ++i) cs[i] = dot2(wr[k][i], ONES, cs[i]);
            const float c = hsum((cs[0] + cs[1]) + (cs[2] + cs[3]));
            if (hh == 0) m2[r32] = c;
        }
        SB();
#pragma unroll
        for (int k = 0; k < 7; ++k) sts128(xl + (7 * w + k) * 1024, xf[k]);
        LBAR();
        f32x16 r = gemm(IC(28), wr, xl);
        float t1 = 0.f, t2 = 0.f;
#pragma unroll
        for (int q = 0; q < 7; ++q) { t1 += u2f(sv[q].x); t2 += u2f(sv[q].y); }
        t1 = hsum(t1); t2 = hsum(t2);
        meanH = t1 * (1.f / D);
        const float var = __builtin_fmaxf(t2 * (1.f / D) - meanH * meanH, 0.f);
        rstdH = __builtin_amdgcn_rsqf(var + EPS);
        const float sc = rstdH * __builtin_amdgcn_rsqf(var * rstdH * rstdH + EPS);
#pragma unroll
        for (int q = 0; q < 4; ++q) {
            const f32x4 c = *(const LAS f32x4*)(m2 + 8 * q + 4 * hh);
#pragma unroll
            for (int i = 0; i < 4; ++i) r[4 * q + i] = gelu_t((r[4 * q + i] - meanH * c[i]) * sc);
        }
#pragma unroll
        for (int s = 0; s < 2; ++s) if (act) gst(rs, pack8(r, s), vx, WS_HH + ((4 * tw + 2 * s) * XR + 32 * ti) * 16);
        WAIT_BAR(0);
        if (tid == 0) set_flag(P.ws, flag_off(3, ti * 4 + g), e);
    }
    {
        GEMM_GEOM();
        BAR();
        LOAD_WR();
        LBAR();
        const int sh = w == 0 ? -1 : w == 1 ? 1 : w == 2 ? -2 : 2;
        const int tn = ti + (sh < 0 ? -1 : 1);
        {
            const bool nb = tn >= 0 && tn < 64;
            const int li = lane & 3;
            const GAS unsigned* p = (const GAS unsigned*)(P.ws + flag_off(3, (((li & 2) && nb) ? tn : ti) * 4 + fsl + ((li & 1) && fsn == 2 ? 1 : 0)));
            poll_word(p, e);
        }
        const int tok = 32 * ti + r32 + sh;
        const bool ok = tok >= 0 && tok < S;
        const unsigned vxs = ok ? (hh * XR + tok) * 16 : 0x7ffffff0u;
        u32x4 xf[7];
#pragma unroll
        for (int k = 0; k < 7; ++k) { xf[k] = gld(rs, vxs, WS_HH + (14 * w + 2 * k) * XROW); }
#pragma unroll
        for (int k = 0; k < 7; ++k) sts128(xl + (7 * w + k) * 1024, xf[k]);
        LBAR();
        f32x16 r = gemm(IC(28), wr, xl);
        float s1 = 0.f, s2 = 0.f;
#pragma unroll
        for (int s = 0; s < 2; ++s) {
            u32x4 xp;
#pragma unroll
            for (int i = 0; i < 4; ++i) {
                xp[i] = pk(r[8 * s + 2 * i] + (blo(hp[s][i]) - meanH) * rstdH, r[8 * s + 2 * i + 1] + (bhi(hp[s][i]) - meanH) * rstdH);
                s1 = dot2(xp[i], ONES, s1); s2 = dot2(xp[i], xp[i], s2);
            }
            if (act) gst(rs, xp, vx, WS_X1 + ((4 * tw + 2 * s) * XR + 32 * ti) * 16);
        }
        s1 = hsum(s1); s2 = hsum(s2);
        if (act && hh == 0) gst8(rs, (u32x2){f2u(s1), f2u(s2)}, r32 * 8, WS_STX + (tw * 2048 + 32 * ti) * 8);
        WAIT_BAR(0);
        if (tid == 0) set_flag(P.ws, flag_off(4, ti * 4 + g), e);
    }
    {
        LANE_GEOM();
        unsigned tr6[2];
        tr6[0] = L0 + trq * 64 + gq * 8 + 2 * hh * 1024; tr6[1] = tr6[0] + 57344; PIN(tr6[0]); PIN(tr6[1]);
        auto wf6 = [&](int t, int k) AI { const unsigned a = (k < 14 ? tr6[0] + k * 4096 : tr6[1] + (k - 14) * 4096) + t * 256; return wfrag(a, a + 1024); };
        const LAS float* cs6 = (const LAS float*)(uintptr_t)(L0 + L_M2 + 1024);
        wait_flags(P.ws, flag_off(4, (4 * m + w) * 4), 4, e, lane);
        const int tk = 128 * m + 32 * w;
        auto ldx1 = [&](int k) AI { return gld(rs, vx, WS_X1 + (2 * k * XR + tk) * 16); };
        constexpr int PX = 10, PW = 2;
        f32x16 acc[4] = {};
        u32x2 sv[7];
        {
            u32x4 xf[28], wf[28][4];
#pragma unroll
            for (int k = 0; k < PX; ++k) xf[k] = ldx1(k);
#pragma unroll
            for (int q = 0; q < 7; ++q) sv[q] = gld8(rs, (7 * hh * 2048 + r32) * 8, WS_STX + (q * 2048 + tk) * 8);
            BAR();
#pragma unroll
            for (int k = 0; k < PW; ++k)
#pragma unroll
                for (int t = 0; t < 4; ++t) wf[k][t] = wf6(t, k);
            SB();
            sfor<28>([&](auto kc) AI {
                constexpr int k = decltype(kc)::value;
                MFMA(acc[0], wf[k][0], xf[k], acc[0]);
                if constexpr (k + PW < 28) { wf[k + PW][0] = wf6(0, k + PW); }
                SB();
                MFMA(acc[1], wf[k][1], xf[k], acc[1]);
                if constexpr (k + PW < 28) { wf[k + PW][1] = wf6(1, k + PW); }
                if constexpr (k + PX < 28) xf[k + PX] = ldx1(k + PX);
                SB();
                MFMA(acc[2], wf[k][2], xf[k], acc[2]);
                if constexpr (k + PW < 28) { wf[k + PW][2] = wf6(2, k + PW); }
                SB();
                MFMA(acc[3], wf[k][3], xf[k], acc[3]);
                if constexpr (k + PW < 28) { wf[k + PW][3] = wf6(3, k + PW); }
                SB();
            });
        }
        LBAR();
        float t1 = 0.f, t2 = 0.f;
#pragma unroll
        for (int q = 0; q < 7; ++q) { t1 += u2f(sv[q].x); t2 += u2f(sv[q].y); }
        t1 = hsum(t1); t2 = hsum(t2);
        const float mean = t1 * (1.f / D), var = __builtin_fmaxf(t2 * (1.f / D) - mean * mean, 0.f) + EPS, rstd = __builtin_amdgcn_rsqf(var);
        const GeluK gk = {-mean, GC1 * rstd * rstd * rstd, GC0 * rstd, var * rstd};
        const unsigned uo = WS_U + ((4 * tb6) * XR + tk) * 16;
        auto epi1 = [&](f32x16& y, int te) AI {
#pragma unroll
            for (int q = 0; q < 4; ++q) {
                const f32x4 c = *(const LAS f32x4*)(cs6 + te * 32 + 8 * q + 4 * hh);
#pragma unroll
                for (int i = 0; i < 4; ++i) {
                    const float v = __builtin_fmaf(gk.nmean, c[i], y[4 * q + i]);
                    const float z = v * __builtin_fmaf(v * v, gk.k1, gk.k0);
                    y[4 * q + i] = v * __builtin_amdgcn_rcpf(__builtin_fmaf(__builtin_amdgcn_exp2f(z), gk.irs, gk.irs));
                }
            }
            gst(rs, pack8(y, 0), vx, uo + 4 * te * XROW); gst(rs, pack8(y, 1), vx, uo + (4 * te + 2) * XROW);
            SB();
        };
        epi1(acc[0], 0); epi1(acc[1], 1); epi1(acc[2], 2);
        if (nt6 == 4) epi1(acc[3], 3);
        WAIT_BAR(0);
        if (tid == 0) set_flag(P.ws, flag_off(5, m * 16 + s16), e);
        BAR();
    }
    if (act7) {
        LANE_GEOM();
        const unsigned lanep = trq * 64 + gq * 8 + 2 * hh * 512;
        unsigned tA = L0 + L_X + w * 8192 + lanep, tB = wst + lanep; PIN(tA); PIN(tB);
        auto w4f = [&](int k, int t) AI { const unsigned a = (k < 4 ? tA + k * 2048 : k < 18 ? tB + (k - 4) * 2048 : tB + (k - 18) * 2048) + t * 256; return wfrag(a, a + 512); };
        u32x4 wr[28][2];
#pragma unroll
        for (int k = 0; k < 14; ++k) { wr[k][0] = w4f(k, 0); wr[k][1] = w4f(k, 1); }
        LBAR();
        const int tk = 64 * j7;
        const int tt = w >> 1, nt7 = w & 1;
        wait_flags(P.ws, flag_off(5, (j7 >> 1) * 16 + 4 * w), 4, e, lane);
        constexpr int PX = 5;
        u32x4 xa[28][2];
#pragma unroll
        for (int k = 0; k < PX; ++k) { xa[k][0] = gld(rs, vx, WS_U + ((56 * w + 2 * k) * XR + tk) * 16); xa[k][1] = gld(rs, vx, WS_U + ((56 * w + 2 * k) * XR + tk + 32) * 16); }
        SB();
        f32x16 acc[4] = {};
        u32x4 rh[2], rx[2];
        sfor<28>([&](auto kc) AI {
            constexpr int k = decltype(kc)::value;
            if constexpr (k == 13) { BAR(); SB(); }
            if constexpr (k + 3 >= 14 && k + 3 < 28) { wr[k + 3][0] = w4f(k + 3, 0); wr[k + 3][1] = w4f(k + 3, 1); }
            MFMA(acc[0], wr[k][0], xa[k][0], acc[0]); MFMA(acc[1], wr[k][1], xa[k][0], acc[1]);
            if constexpr (k + PX < 28) xa[k + PX][0] = gld(rs, vx, WS_U + ((56 * w + 2 * (k + PX)) * XR + tk) * 16);
            MFMA(acc[2], wr[k][0], xa[k][1], acc[2]); MFMA(acc[3], wr[k][1], xa[k][1], acc[3]);
            if constexpr (k + PX < 28) xa[k + PX][1] = gld(rs, vx, WS_U + ((56 * w + 2 * (k + PX)) * XR + tk + 32) * 16);
            if constexpr (k == 24 || k == 25) {
                constexpr int s = k - 24;
                const unsigned so = ((8 * g7 + 4 * nt7 + 2 * s) * XR + tk + 32 * tt) * 16;
                rh[s] = gld(rs, vx, WS_H + so); rx[s] = gld(rs, vx, WS_X1 + so);
            }
            SB();
        });
#pragma unroll
        for (int t = 0; t < 4; ++t) {
            if (t != w) {
                const unsigned a = L0 + L_X + t * 12288 + (w < t ? w : w - 1) * 4096 + lane * 16;
#pragma unroll
                for (int q = 0; q < 4; ++q) stsf4(a + q * 1024, (f32x4){acc[t][4 * q], acc[t][4 * q + 1], acc[t][4 * q + 2], acc[t][4 * q + 3]});
            }
        }
        LBAR();
        f32x16 r = w == 0 ? acc[0] : w == 1 ? acc[1] : w == 2 ? acc[2] : acc[3];
        const unsigned a = L0 + L_X + w * 12288 + lane * 16;
#pragma unroll
        for (int j = 0; j < 3; ++j)
#pragma unroll
            for (int q = 0; q < 4; ++q) { const f32x4 v = ldsf4(a + j * 4096 + q * 1024); r[4 * q] += v[0]; r[4 * q + 1] += v[1]; r[4 * q + 2] += v[2]; r[4 * q + 3] += v[3]; }
        char* op = (char*)P.out + (size_t)(tk + 32 * tt + r32) * 896 + 128 * g7 + 64 * nt7 + 16 * hh;
#pragma unroll
        for (int s = 0; s < 2; ++s) {
            u32x4 ov;
#pragma unroll
            for (int i = 0; i < 4; ++i)
                ov[i] = pk(r[8 * s + 2 * i] + blo(rx[s][i]) + blo(rh[s][i]), r[8 * s + 2 * i + 1] + bhi(rx[s][i]) + bhi(rh[s][i]));
            *(GAS u32x4*)(op + 32 * s) = ov;
        }
    }
}
}


extern "C" void kernel_launch(void* const* d_in, const int* in_sizes, int n_in, void* d_out, int out_size, void* d_ws, size_t ws_size,
                              hipStream_t stream) {
    mk::Params P;
    P.x = (const u16*)d_in[0]; P.wqkv = (const u16*)d_in[1]; P.wo = (const u16*)d_in[2];
    P.w1 = (const u16*)d_in[4]; P.w2 = (const u16*)d_in[5]; P.w3 = (const u16*)d_in[6]; P.w4 = (const u16*)d_in[7];
    P.out = (u16*)d_out; P.ws = (char*)d_ws;
    mk::fused_fwd<<<256, 512, 0, stream>>>(P);
}
```
